```python
import torch

def _softmax(x, axis=-1):
    return torch.softmax(x, dim=axis)

def _gelu(x):
    return torch.nn.functional.gelu(x, approximate='tanh')

def _silu(x):
    return torch.nn.functional.silu(x)

def _sigmoid(x):
    return torch.sigmoid(x)

def _layernorm(x, axis=-1, eps=1e-05):
    m = x.mean(dim=axis, keepdim=True)
    v = x.var(dim=axis, keepdim=True, unbiased=False)
    return (x - m) / torch.sqrt(v + eps)

def _scatter_add(tgt, idx, src, axis=0):
    return tgt.clone().index_add_(axis, idx, src)

def _segment_sum(src, seg, n):
    out = torch.zeros((n,) + src.shape[1:], dtype=src.dtype)
    return out.index_add_(0, seg, src)

def _pad(x, pw):
    flat = tuple((v for pair in reversed(pw) for v in pair))
    return torch.nn.functional.pad(x, flat)
kernel_name = 'concat_heads__memory_aug_recurrent_mem_tr__spectral_gfnet__prompt'

def setup_inputs():

    _w = lambda a: (lambda t: t.to(torch.bfloat16) if t.is_floating_point() else t)(torch.as_tensor(a))

    def _A_setup():
        B, S, Dm = (1, 2048, 1024)
        H, Dh = (32, 128)
        M, seg = (8, 32)
        seg = min(seg, S)
        S = S // seg * seg
        return {'x': _w((lambda *s: torch.randn(*s, dtype=torch.bfloat16) * 0.05)(B, S, Dm)), 'mem0': _w((lambda *s: torch.randn(*s, dtype=torch.bfloat16) * 0.4)(B, M, Dm)), 'Wq': _w((lambda *s: torch.randn(*s, dtype=torch.bfloat16) * 6.4)(Dm, H, Dh) * Dm ** (-0.5)), 'Wk': _w((lambda *s: torch.randn(*s, dtype=torch.bfloat16) * 6.4)(Dm, H, Dh) * Dm ** (-0.5)), 'Wv': _w((lambda *s: torch.randn(*s, dtype=torch.bfloat16) * 0.4)(Dm, H, Dh) * Dm ** (-0.5)), 'Wo': _w((lambda *s: torch.randn(*s, dtype=torch.bfloat16) * 0.4)(H, Dh, Dm) * (H * Dh) ** (-0.5)), 'seg_len': seg}
    _A_in = _A_setup()

    def _B_setup():
        B, S, D = (1, 2048, 1024)
        Df = D // 2 + 1
        return {'x': _w((lambda *s: torch.randn(*s, dtype=torch.bfloat16) * 0.05)(B, S, D)), 'w_r': _w((lambda *s: torch.randn(*s, dtype=torch.bfloat16) * 3.2)(S, Df) * 0.02), 'w_i': _w((lambda *s: torch.randn(*s, dtype=torch.bfloat16) * 3.2)(S, Df) * 0.02)}
    _B_in = _B_setup()
    return {'A_x': _A_in['x'], 'A_mem0': _A_in['mem0'], 'A_Wq': _A_in['Wq'], 'A_Wk': _A_in['Wk'], 'A_Wv': _A_in['Wv'], 'A_Wo': _A_in['Wo'], 'A_seg_len': _A_in['seg_len'], 'B_x': _B_in['x'], 'B_w_r': _B_in['w_r'], 'B_w_i': _B_in['w_i']}

def reference(A_x, A_mem0, A_Wq, A_Wk, A_Wv, A_Wo, A_seg_len, B_x, B_w_r, B_w_i):

    def _A_ref(x, mem0, Wq, Wk, Wv, Wo, seg_len):
        B, S, Dm = x.shape
        H, Dh = (Wq.shape[1], Wq.shape[2])
        M = mem0.shape[1]
        scale = Dh ** (-0.5)
        n_seg = S // seg_len
        L = M + seg_len
        idx = torch.arange(L)
        mask = (idx[:, None] < M) | (idx[:, None] >= idx[None, :])
        mem = mem0
        outs = []
        for s in range(n_seg):
            xs = x[:, s * seg_len:(s + 1) * seg_len, :].clone()
            h = torch.concatenate([mem, xs], axis=1)
            q = torch.einsum('btd,dhe->bhte', h, Wq)
            k = torch.einsum('btd,dhe->bhte', h, Wk)
            v = torch.einsum('btd,dhe->bhte', h, Wv)
            scores = torch.einsum('bhqe,bhke->bhqk', q, k) * scale
            scores = torch.where(mask[None, None], scores, -1000000000.0)
            p = _softmax(scores, axis=-1)
            o = torch.einsum('bhqk,bhke->bhqe', p, v)
            o = torch.einsum('bhte,hed->btd', o, Wo)
            mem = o[:, :M, :].clone()
            outs.append(o[:, M:, :])
        return torch.concatenate(outs, axis=1)

    def _B_ref(x, w_r, w_i):
        B, S, D = x.shape
        X = (lambda x, axes=(-2, -1): torch.fft.rfft2(x.float(), dim=axes))(x, axes=(-2, -1))
        W = w_r + 1j * w_i
        X = X * W
        y = (lambda x, s, axes=(-2, -1): torch.fft.irfft2(x, s=s, dim=axes).to(torch.bfloat16))(X, (S, D), axes=(-2, -1))
        return y
    a = _A_ref(A_x, A_mem0, A_Wq, A_Wk, A_Wv, A_Wo, A_seg_len)
    b = _B_ref(B_x, B_w_r, B_w_i)
    return torch.concatenate([a, b], axis=-1)
```

```cpp
#if defined(__HIP_DEVICE_COMPILE__)
#pragma clang attribute push (__attribute__((target("no-packed-fp32-ops"))), apply_to = function)
#endif
#include <hip/hip_runtime.h>
#include <hip/hip_bf16.h>
#include <stdint.h>

typedef unsigned short u16;
#define TIDX ((int)__builtin_amdgcn_workitem_id_x())
#define BIDX ((int)__builtin_amdgcn_workgroup_id_x())
__device__ __forceinline__ int lane_now() { int t; asm volatile("v_mbcnt_lo_u32_b32 %0, -1, 0\n\tv_mbcnt_hi_u32_b32 %0, -1, %0" : "=v"(t)); return t; }
__device__ __forceinline__ int tid_now(int w) { return w * 64 + lane_now(); }
typedef __attribute__((ext_vector_type(8))) short bf16x8;
typedef __attribute__((ext_vector_type(4))) float f32x4;

constexpr int S_LEN = 2048, DM = 1024, NH = 32, DH = 128, SEG = 32;
constexpr int NSEG = S_LEN / SEG;
constexpr int HD = NH * DH;
constexpr int OUTW = 2048;

typedef unsigned v4u __attribute__((ext_vector_type(4)));
typedef unsigned v2u __attribute__((ext_vector_type(2)));
typedef short v4i16 __attribute__((ext_vector_type(4)));
typedef __amdgpu_buffer_rsrc_t rsrc_t;
#define AUX_SC1 16
__device__ __forceinline__ void st_sc1s(rsrc_t rs, int voff, int soff, v4u v) { asm volatile("buffer_store_dwordx4 %0, %1, %2, %3 offen sc1\n\ts_nop 1" :: "v"(v), "v"(voff), "s"(rs), "s"(soff) : "memory"); }
__device__ __forceinline__ void st_sc1(rsrc_t rs, int off, v4u v) { __builtin_amdgcn_raw_buffer_store_b128(v, rs, off, 0, AUX_SC1); }
typedef __bf16 bf16x2_t __attribute__((ext_vector_type(2)));
typedef float f32x2_t __attribute__((ext_vector_type(2)));
__device__ __forceinline__ unsigned cvt_pk_bf16(float lo, float hi) {
    f32x2_t f = {lo, hi};
    return __builtin_bit_cast(unsigned, __builtin_convertvector(f, bf16x2_t));
}
#define SENT 0xFFBFFFBF
constexpr int MB2 = 2097152;
constexpr int YX_OFF = 16777216, KV_OFF = 50331648, Q_OFF = 83886080, ARENA_BYTES = Q_OFF + 16 * MB2, CAL_OFF = 24 * 16384;
__device__ __forceinline__ char* arena(char* slots) { return (char*)(((uintptr_t)slots + 2097151u) & ~(uintptr_t)2097151u); }
constexpr int PL = 0, VNEAR = 1, YXC = 0;
__device__ __forceinline__ int hcol(int h) { return PL == 0 ? ((h & 3) * 8 + (h >> 2)) * 128 : ((h & 7) * 4 + (h >> 3)) * 128; }
__device__ __forceinline__ int cls_s(int h) { return PL == 0 ? h >> 4 : 0; }
__device__ __forceinline__ int cls_v(int h, int half) { return PL == 0 ? (VNEAR ? half : (h >> 4) ^ 1) : PL == 1 ? 1 : 0; }
__device__ __forceinline__ int cls_r(int j) { return PL == 0 ? j >> 5 : 0; }

struct Args {
    const u16* x; const u16* mem0; const u16* Wq; const u16* Wk; const u16* Wv; const u16* Wo;
    const u16* bx; const u16* wr; const u16* wi;
    u16* out;
    char* slots;
    unsigned* sync;
    unsigned long long* boot;
    void* F; void* G; unsigned* Wt;
    unsigned* stamps;
};
__device__ __forceinline__ Args get_args() {
    typedef __attribute__((address_space(4))) const Args* cargs;
    cargs p = (cargs)__builtin_amdgcn_kernarg_segment_ptr();
    asm volatile("" : "+s"(p));
#if defined(__HIP_DEVICE_COMPILE__)
    return *p;
#else
    return Args{};
#endif
}
constexpr int SY_DONE = 0, SY_FLAGS = 16;
typedef __attribute__((address_space(1))) unsigned gu32;
typedef __attribute__((address_space(1))) unsigned long long gu64;
__device__ __forceinline__ unsigned ld_flag(const unsigned* p) { return __hip_atomic_load((gu32*)p, __ATOMIC_RELAXED, __HIP_MEMORY_SCOPE_AGENT); }
__device__ __forceinline__ unsigned long long boot_tag(const unsigned long long* boot) {
    return __hip_atomic_load((gu64*)(boot + 144), __ATOMIC_RELAXED, __HIP_MEMORY_SCOPE_AGENT) + 1ull;
}
__device__ __forceinline__ unsigned long long boot_wait(const unsigned long long* boot, unsigned* tmo) {
    const int l = lane_now();
    unsigned long long tag = 0, x = 0;
#pragma nounroll
    for (int spin = 0;; ++spin) {
        asm volatile("" ::: "memory");
        x = __hip_atomic_load((gu64*)(boot + l), __ATOMIC_RELAXED, __HIP_MEMORY_SCOPE_AGENT);
        tag = boot_tag(boot);
        if (__all((x | 1u) == ((tag << 1) | 1u))) break;
        if (spin > 100000) { __hip_atomic_store((gu32*)tmo, 9000u, __ATOMIC_RELAXED, __HIP_MEMORY_SCOPE_AGENT); break; }
        __builtin_amdgcn_s_sleep(16);
    }
    asm volatile("" ::: "memory");
    return (tag << 8) | (__popcll(__ballot(x & 1u)) > 32 ? 128u : 0u);
}
__device__ __forceinline__ void wg_wait_acquire(const int w, const unsigned* p, unsigned* tmo, unsigned code) {
    if (w == 0) {
#pragma nounroll
        for (int spin = 0;; ++spin) {
            asm volatile("" ::: "memory");
            if (ld_flag(p) >= 1u) break;
            if (spin > 25000) { __hip_atomic_store((gu32*)tmo, code, __ATOMIC_RELAXED, __HIP_MEMORY_SCOPE_AGENT); break; }
            __builtin_amdgcn_s_sleep(16);
        }
        __builtin_amdgcn_fence(__ATOMIC_ACQUIRE, "agent");
        asm volatile("s_waitcnt vmcnt(0)" ::: "memory");
    }
    __syncthreads();
}
__device__ __forceinline__ void wg_publish(const int w, unsigned* sync, const int f, const unsigned need) {
    asm volatile("s_waitcnt vmcnt(0)" ::: "memory");
    __syncthreads();
    if (w == 0) {
        const int l = lane_now();
        unsigned old = 0;
        if (l == 0) old = __hip_atomic_fetch_add((gu32*)(sync + (SY_FLAGS + f) * 32), 1u, __ATOMIC_RELAXED, __HIP_MEMORY_SCOPE_AGENT);
        if (__builtin_amdgcn_readfirstlane(old) == need - 1u && l < 8) __hip_atomic_store((gu32*)(sync + (f + l) * 32), 16u, __ATOMIC_RELAXED, __HIP_MEMORY_SCOPE_AGENT);
    }
}
__device__ __forceinline__ void touch(const void* p, unsigned lds_dst) {
    unsigned keep;
    asm volatile("s_mov_b32 %0, m0\n\ts_mov_b32 m0, %2\n\ts_nop 0\n\tglobal_load_lds_dword %1, off\n\ts_mov_b32 m0, %0" : "=&s"(keep) : "v"(p), "s"(lds_dst) : "memory");
}
__device__ __forceinline__ void touch_sc1(const void* p, unsigned lds_dst) {
    unsigned keep;
    asm volatile("s_mov_b32 %0, m0\n\ts_mov_b32 m0, %2\n\ts_nop 0\n\tglobal_load_lds_dword %1, off sc1\n\ts_mov_b32 m0, %0" : "=&s"(keep) : "v"(p), "s"(lds_dst) : "memory");
}
#define C10(a) "v" #a "0", "v" #a "1", "v" #a "2", "v" #a "3", "v" #a "4", "v" #a "5", "v" #a "6", "v" #a "7", "v" #a "8", "v" #a "9"
#define ALLV C10(), C10(1), C10(2), C10(3), C10(4), C10(5), C10(6), C10(7), C10(8), C10(9), C10(10), C10(11), C10(12), C10(13), C10(14), C10(15), C10(16), C10(17), C10(18), C10(19), C10(20), C10(21), C10(22), \
    C10(23), C10(24), "v250", "v251", "v252", "v253", "v254", "v255"
#define S10(a) "s" #a "0", "s" #a "1", "s" #a "2", "s" #a "3", "s" #a "4", "s" #a "5", "s" #a "6", "s" #a "7", "s" #a "8", "s" #a "9"
#define ALLS "s44", "s45", "s46", "s47", "s48", "s49", S10(5), S10(6), S10(7), S10(8), "s90", "s91", "s92", "s93", "s94", "s95", "s96", "s97", "s98", "s99" STCL
#define RSRC(p, n) __builtin_amdgcn_make_buffer_rsrc((void*)(p), 0, (n), 0x00020000)
#define REP_S(x) ((x) & 1)
#define REP_V(x) (2 + ((x) & 1))
#define KW 4
#define MULTI 1
#define FLA 1
#define OOP 0
#define STOFF 244
#define FCW 6
#define FAB 0
#define FD1 0
#define FD2 90
#define FD3 120
#define CODEWARM if (wu >= 4) { if ((idx & 3) == 0) warm<0>(wu - 4, l); else if ((idx & 3) == 1) warm<1>(wu - 4, l); else if ((idx & 3) == 2) warm<3>(wu - 4, l); else warm<2>(wu - 4, l); }
#define XSL 127
#define XD 4
constexpr int XFAST = 12;
#define PREFETCH
#define WST(n)
#define FST(n)
#define FWT

#define PROG(n) RSRC(FLA ? arena(a.slots) + (MULTI ? ((n) >> 5) * MB2 + 32 * 16384 + ((n) & 31) * 8320 : 32 * 16384 + (n) * 128) : (char*)(a.boot + 256 + 1040 * (n)), 65 * 128)
#define TW(c) (20 * 16384 + (c) * 8192)
#define STCL
#define STIN(u)
#define XSTIN

__device__ __forceinline__ void score_wg(const int w, const int h, const int rep) {
    const Args a = get_args();
    const int co = hcol(h) + 64 * ((w >> 1) & 1);
    asm volatile(
        ".Lcode_s0:\n\t"
        "v_mbcnt_lo_u32_b32 v52, -1, 0\n\t"
        "v_mbcnt_hi_u32_b32 v52, -1, v52\n\t"
        "s_load_dwordx2 s[54:55], %[ep], 0\n\t"
        "s_mov_b32 s79, 0\n\t"
        "s_mov_b32 s44, -1\n\t"
        "s_mov_b32 s45, 0\n\t"
        "s_mov_b32 s46, 200000\n\t"
        "s_mov_b32 s51, 0\n\t"
        "v_lshlrev_b32 v0, 4, v52\n\t"
        "v_lshrrev_b32 v53, 3, v52\n\t"
        "v_and_b32 v54, 7, v52\n\t"
        "v_and_b32 v8, 2, v53\n\t"
        "v_xor_b32 v54, v54, v8\n\t"
        "v_lshlrev_b32 v54, 4, v54\n\t"
        "v_lshl_add_u32 v8, v53, 13, v54\n\t"
        "v_xor_b32 v9, 64, v8\n\t"
        "s_lshl_b32 s68, %[w], 20\n\t"
        "s_lshl_b32 s69, %[w], 12\n\t"
        "s_lshl_b32 s71, %[w], 8\n\t"
        "v_bfe_u32 v10, v52, 1, 3\n\t"
        "v_lshlrev_b32 v10, 11, v10\n\t"
        "v_lshrrev_b32 v11, 4, v52\n\t"
        "v_lshl_add_u32 v10, v11, 5, v10\n\t"
        "v_and_b32 v53, 1, v52\n\t"
        "v_lshl_add_u32 v10, v53, 2, v10\n\t"
        "s_add_u32 m0, s69, 65536\n\t"
        "s_xor_b32 s70, s68, 0x0\n\t"
        "buffer_load_dwordx4 v8, %[rw], s70 offen offset:0 lds\n\t"
        "s_add_u32 s70, s70, 64512\n\t"
        "buffer_load_dwordx4 v9, %[rw], s70 offen offset:1024 lds\n\t"
        "s_add_u32 s70, s70, 64512\n\t"
        "buffer_load_dwordx4 v8, %[rw], s70 offen offset:2048 lds\n\t"
        "s_add_u32 s70, s70, 64512\n\t"
        "buffer_load_dwordx4 v9, %[rw], s70 offen offset:3072 lds\n\t"
        "buffer_load_dword v12, v10, %[rm], s71 offen offset:0\n\t"
        "buffer_load_dword v13, v10, %[rm], s71 offen offset:8\n\t"
        "buffer_load_dword v14, v10, %[rm], s71 offen offset:16\n\t"
        "buffer_load_dword v15, v10, %[rm], s71 offen offset:24\n\t"
        "buffer_load_dword v16, v10, %[rm], s71 offen offset:128\n\t"
        "buffer_load_dword v17, v10, %[rm], s71 offen offset:136\n\t"
        "buffer_load_dword v18, v10, %[rm], s71 offen offset:144\n\t"
        "buffer_load_dword v19, v10, %[rm], s71 offen offset:152\n\t"
        "v_mov_b32 v20, 0\n\t"
        "s_mov_b32 s72, 52428544\n\t"
        "s_mov_b32 s73, 85982976\n\t"
        "s_mov_b32 s74, 376832\n\t"
        "buffer_load_dword v21, v20, %[rf], 0 offen sc1\n\t"
        "buffer_load_dword v21, v20, %[rs], s72 offen sc1\n\t"
        "buffer_load_dword v21, v20, %[rs], s73 offen sc1\n\t"
        "buffer_load_dword v21, v20, %[rs], s74 offen sc1\n\t"
        "s_add_u32 m0, s69, 98304\n\t"
        "s_xor_b32 s70, s68, 0x40000\n\t"
        "buffer_load_dwordx4 v8, %[rw], s70 offen offset:0 lds\n\t"
        "s_add_u32 s70, s70, 64512\n\t"
        "buffer_load_dwordx4 v9, %[rw], s70 offen offset:1024 lds\n\t"
        "s_add_u32 s70, s70, 64512\n\t"
        "buffer_load_dwordx4 v8, %[rw], s70 offen offset:2048 lds\n\t"
        "s_add_u32 s70, s70, 64512\n\t"
        "buffer_load_dwordx4 v9, %[rw], s70 offen offset:3072 lds\n\t"
        "v_lshrrev_b32 v4, 2, v52\n\t"
        "v_lshl_add_u32 v4, v11, 2, v4\n\t"
        "v_lshlrev_b32 v4, 7, v4\n\t"
        "v_and_b32 v5, 3, v52\n\t"
        "v_lshl_add_u32 v4, v5, 3, v4\n\t"
        "s_and_b32 s70, %[w], 1\n\t"
        "s_lshl_b32 s70, s70, 1\n\t"
        "v_bfe_u32 v5, v52, 3, 2\n\t"
        "v_xor_b32 v5, s70, v5\n\t"
        "v_lshl_add_u32 v4, v5, 5, v4\n\t"
        "v_add_u32 v4, s69, v4\n\t"
        "v_add_u32 v4, 65536, v4\n\t"
        "v_xor_b32 v5, 32, v4\n\t"
        "v_xor_b32 v6, 0x1000, v4\n\t"
        "v_xor_b32 v7, 0x1000, v5\n\t"
        "v_mov_b32 v53, 0x4444\n\t"
        "v_mov_b32 v54, 0xeeee\n\t"
        "v_and_b32 v1, 1, v52\n\t"
        "v_cmp_eq_u32 vcc, 0, v1\n\t"
        "s_lshl_b32 s70, %[w], 11\n\t"
        "v_add_u32 v3, s70, v0\n\t"
        "v_cndmask_b32 v1, v54, v53, vcc\n\t"
        "v_and_b32 v2, 0x1fff, v3\n\t"
        "s_waitcnt lgkmcnt(0)\n\t"
        "s_add_u32 s54, s54, 1\n\t"
        "s_addc_u32 s55, s55, 0\n\t"
        "s_lshl_b64 s[54:55], s[54:55], 8\n\t"
        "s_cmp_ge_u32 %[w], 4\n\t"
        "s_cbranch_scc1 .Ls_stk_%=\n\t"
        "s_waitcnt vmcnt(4)\n\t"
        "s_barrier\n\t"
        "s_add_u32 m0, s69, 131072\n\t"
        "s_xor_b32 s70, s68, 0x80000\n\t"
        "buffer_load_dwordx4 v8, %[rw], s70 offen offset:0 lds\n\t"
        "s_add_u32 s70, s70, 64512\n\t"
        "buffer_load_dwordx4 v9, %[rw], s70 offen offset:1024 lds\n\t"
        "s_add_u32 s70, s70, 64512\n\t"
        "buffer_load_dwordx4 v8, %[rw], s70 offen offset:2048 lds\n\t"
        "s_add_u32 s70, s70, 64512\n\t"
        "buffer_load_dwordx4 v9, %[rw], s70 offen offset:3072 lds\n\t"
        "ds_read_b64_tr_b16 v[56:57], v4\n\t"
        "ds_read_b64_tr_b16 v[58:59], v4 offset:512\n\t"
        "ds_read_b64_tr_b16 v[64:65], v5\n\t"
        "ds_read_b64_tr_b16 v[66:67], v5 offset:512\n\t"
        "ds_read_b64_tr_b16 v[88:89], v6\n\t"
        "ds_read_b64_tr_b16 v[90:91], v6 offset:512\n\t"
        "ds_read_b64_tr_b16 v[28:29], v7\n\t"
        "ds_read_b64_tr_b16 v[30:31], v7 offset:512\n\t"
        "s_waitcnt lgkmcnt(0)\n\t"
        "v_add_u32 v4, 32768, v4\n\t"
        "v_add_u32 v5, 32768, v5\n\t"
        "v_add_u32 v6, 32768, v6\n\t"
        "v_add_u32 v7, 32768, v7\n\t"
        "s_waitcnt vmcnt(4)\n\t"
        "s_barrier\n\t"
        "s_add_u32 m0, s69, 65536\n\t"
        "s_xor_b32 s70, s68, 0xc0000\n\t"
        "buffer_load_dwordx4 v8, %[rw], s70 offen offset:0 lds\n\t"
        "s_add_u32 s70, s70, 64512\n\t"
        "buffer_load_dwordx4 v9, %[rw], s70 offen offset:1024 lds\n\t"
        "s_add_u32 s70, s70, 64512\n\t"
        "buffer_load_dwordx4 v8, %[rw], s70 offen offset:2048 lds\n\t"
        "s_add_u32 s70, s70, 64512\n\t"
        "buffer_load_dwordx4 v9, %[rw], s70 offen offset:3072 lds\n\t"
        "ds_read_b64_tr_b16 v[60:61], v4\n\t"
        "ds_read_b64_tr_b16 v[62:63], v4 offset:512\n\t"
        "ds_read_b64_tr_b16 v[68:69], v5\n\t"
        "ds_read_b64_tr_b16 v[70:71], v5 offset:512\n\t"
        "ds_read_b64_tr_b16 v[92:93], v6\n\t"
        "ds_read_b64_tr_b16 v[94:95], v6 offset:512\n\t"
        "ds_read_b64_tr_b16 v[32:33], v7\n\t"
        "ds_read_b64_tr_b16 v[34:35], v7 offset:512\n\t"
        "s_waitcnt lgkmcnt(0)\n\t"
        "v_add_u32 v4, 32768, v4\n\t"
        "v_add_u32 v5, 32768, v5\n\t"
        "v_add_u32 v6, 32768, v6\n\t"
        "v_add_u32 v7, 32768, v7\n\t"
        "s_waitcnt vmcnt(4)\n\t"
        "s_barrier\n\t"
        "s_add_u32 m0, s69, 98304\n\t"
        "s_xor_b32 s70, s68, 0x200000\n\t"
        "buffer_load_dwordx4 v8, %[rw], s70 offen offset:0 lds\n\t"
        "s_add_u32 s70, s70, 64512\n\t"
        "buffer_load_dwordx4 v9, %[rw], s70 offen offset:1024 lds\n\t"
        "s_add_u32 s70, s70, 64512\n\t"
        "buffer_load_dwordx4 v8, %[rw], s70 offen offset:2048 lds\n\t"
        "s_add_u32 s70, s70, 64512\n\t"
        "buffer_load_dwordx4 v9, %[rw], s70 offen offset:3072 lds\n\t"
        "ds_read_b64_tr_b16 v[72:73], v4\n\t"
        "ds_read_b64_tr_b16 v[74:75], v4 offset:512\n\t"
        "ds_read_b64_tr_b16 v[80:81], v5\n\t"
        "ds_read_b64_tr_b16 v[82:83], v5 offset:512\n\t"
        "ds_read_b64_tr_b16 v[96:97], v6\n\t"
        "ds_read_b64_tr_b16 v[98:99], v6 offset:512\n\t"
        "ds_read_b64_tr_b16 v[104:105], v7\n\t"
        "ds_read_b64_tr_b16 v[106:107], v7 offset:512\n\t"
        "s_waitcnt lgkmcnt(0)\n\t"
        "v_add_u32 v4, -65536, v4\n\t"
        "v_add_u32 v5, -65536, v5\n\t"
        "v_add_u32 v6, -65536, v6\n\t"
        "v_add_u32 v7, -65536, v7\n\t"
        "s_waitcnt vmcnt(4)\n\t"
        "s_barrier\n\t"
        "s_add_u32 m0, s69, 131072\n\t"
        "s_xor_b32 s70, s68, 0x240000\n\t"
        "buffer_load_dwordx4 v8, %[rw], s70 offen offset:0 lds\n\t"
        "s_add_u32 s70, s70, 64512\n\t"
        "buffer_load_dwordx4 v9, %[rw], s70 offen offset:1024 lds\n\t"
        "s_add_u32 s70, s70, 64512\n\t"
        "buffer_load_dwordx4 v8, %[rw], s70 offen offset:2048 lds\n\t"
        "s_add_u32 s70, s70, 64512\n\t"
        "buffer_load_dwordx4 v9, %[rw], s70 offen offset:3072 lds\n\t"
        "ds_read_b64_tr_b16 v[76:77], v4\n\t"
        "ds_read_b64_tr_b16 v[78:79], v4 offset:512\n\t"
        "ds_read_b64_tr_b16 v[84:85], v5\n\t"
        "ds_read_b64_tr_b16 v[86:87], v5 offset:512\n\t"
        "ds_read_b64_tr_b16 v[100:101], v6\n\t"
        "ds_read_b64_tr_b16 v[102:103], v6 offset:512\n\t"
        "ds_read_b64_tr_b16 v[108:109], v7\n\t"
        "ds_read_b64_tr_b16 v[110:111], v7 offset:512\n\t"
        "s_waitcnt lgkmcnt(0)\n\t"
        "v_add_u32 v4, 32768, v4\n\t"
        "v_add_u32 v5, 32768, v5\n\t"
        "v_add_u32 v6, 32768, v6\n\t"
        "v_add_u32 v7, 32768, v7\n\t"
        "s_waitcnt vmcnt(4)\n\t"
        "s_barrier\n\t"
        "s_add_u32 m0, s69, 65536\n\t"
        "s_xor_b32 s70, s68, 0x280000\n\t"
        "buffer_load_dwordx4 v8, %[rw], s70 offen offset:0 lds\n\t"
        "s_add_u32 s70, s70, 64512\n\t"
        "buffer_load_dwordx4 v9, %[rw], s70 offen offset:1024 lds\n\t"
        "s_add_u32 s70, s70, 64512\n\t"
        "buffer_load_dwordx4 v8, %[rw], s70 offen offset:2048 lds\n\t"
        "s_add_u32 s70, s70, 64512\n\t"
        "buffer_load_dwordx4 v9, %[rw], s70 offen offset:3072 lds\n\t"
        "ds_read_b64_tr_b16 v[112:113], v4\n\t"
        "ds_read_b64_tr_b16 v[114:115], v4 offset:512\n\t"
        "ds_read_b64_tr_b16 v[36:37], v5\n\t"
        "ds_read_b64_tr_b16 v[38:39], v5 offset:512\n\t"
        "ds_read_b64_tr_b16 v[136:137], v6\n\t"
        "ds_read_b64_tr_b16 v[138:139], v6 offset:512\n\t"
        "ds_read_b64_tr_b16 v[44:45], v7\n\t"
        "ds_read_b64_tr_b16 v[46:47], v7 offset:512\n\t"
        "s_waitcnt lgkmcnt(0)\n\t"
        "v_add_u32 v4, 32768, v4\n\t"
        "v_add_u32 v5, 32768, v5\n\t"
        "v_add_u32 v6, 32768, v6\n\t"
        "v_add_u32 v7, 32768, v7\n\t"
        "s_waitcnt vmcnt(4)\n\t"
        "s_barrier\n\t"
        "s_add_u32 m0, s69, 98304\n\t"
        "s_xor_b32 s70, s68, 0x2c0000\n\t"
        "buffer_load_dwordx4 v8, %[rw], s70 offen offset:0 lds\n\t"
        "s_add_u32 s70, s70, 64512\n\t"
        "buffer_load_dwordx4 v9, %[rw], s70 offen offset:1024 lds\n\t"
        "s_add_u32 s70, s70, 64512\n\t"
        "buffer_load_dwordx4 v8, %[rw], s70 offen offset:2048 lds\n\t"
        "s_add_u32 s70, s70, 64512\n\t"
        "buffer_load_dwordx4 v9, %[rw], s70 offen offset:3072 lds\n\t"
        "ds_read_b64_tr_b16 v[116:117], v4\n\t"
        "ds_read_b64_tr_b16 v[118:119], v4 offset:512\n\t"
        "ds_read_b64_tr_b16 v[40:41], v5\n\t"
        "ds_read_b64_tr_b16 v[42:43], v5 offset:512\n\t"
        "ds_read_b64_tr_b16 v[140:141], v6\n\t"
        "ds_read_b64_tr_b16 v[142:143], v6 offset:512\n\t"
        "ds_read_b64_tr_b16 v[48:49], v7\n\t"
        "ds_read_b64_tr_b16 v[50:51], v7 offset:512\n\t"
        "s_waitcnt lgkmcnt(0)\n\t"
        "v_add_u32 v4, -65536, v4\n\t"
        "v_add_u32 v5, -65536, v5\n\t"
        "v_add_u32 v6, -65536, v6\n\t"
        "v_add_u32 v7, -65536, v7\n\t"
        "s_waitcnt vmcnt(4)\n\t"
        "s_barrier\n\t"
        "s_add_u32 m0, s69, 131072\n\t"
        "s_xor_b32 s70, s68, 0x400000\n\t"
        "buffer_load_dwordx4 v8, %[rw], s70 offen offset:0 lds\n\t"
        "s_add_u32 s70, s70, 64512\n\t"
        "buffer_load_dwordx4 v9, %[rw], s70 offen offset:1024 lds\n\t"
        "s_add_u32 s70, s70, 64512\n\t"
        "buffer_load_dwordx4 v8, %[rw], s70 offen offset:2048 lds\n\t"
        "s_add_u32 s70, s70, 64512\n\t"
        "buffer_load_dwordx4 v9, %[rw], s70 offen offset:3072 lds\n\t"
        "ds_read_b64_tr_b16 v[120:121], v4\n\t"
        "ds_read_b64_tr_b16 v[122:123], v4 offset:512\n\t"
        "ds_read_b64_tr_b16 v[128:129], v5\n\t"
        "ds_read_b64_tr_b16 v[130:131], v5 offset:512\n\t"
        "ds_read_b64_tr_b16 v[144:145], v6\n\t"
        "ds_read_b64_tr_b16 v[146:147], v6 offset:512\n\t"
        "ds_read_b64_tr_b16 v[152:153], v7\n\t"
        "ds_read_b64_tr_b16 v[154:155], v7 offset:512\n\t"
        "s_waitcnt lgkmcnt(0)\n\t"
        "v_add_u32 v4, 32768, v4\n\t"
        "v_add_u32 v5, 32768, v5\n\t"
        "v_add_u32 v6, 32768, v6\n\t"
        "v_add_u32 v7, 32768, v7\n\t"
        "s_waitcnt vmcnt(4)\n\t"
        "s_barrier\n\t"
        "s_add_u32 m0, s69, 65536\n\t"
        "s_xor_b32 s70, s68, 0x440000\n\t"
        "buffer_load_dwordx4 v8, %[rw], s70 offen offset:0 lds\n\t"
        "s_add_u32 s70, s70, 64512\n\t"
        "buffer_load_dwordx4 v9, %[rw], s70 offen offset:1024 lds\n\t"
        "s_add_u32 s70, s70, 64512\n\t"
        "buffer_load_dwordx4 v8, %[rw], s70 offen offset:2048 lds\n\t"
        "s_add_u32 s70, s70, 64512\n\t"
        "buffer_load_dwordx4 v9, %[rw], s70 offen offset:3072 lds\n\t"
        "ds_read_b64_tr_b16 v[124:125], v4\n\t"
        "ds_read_b64_tr_b16 v[126:127], v4 offset:512\n\t"
        "ds_read_b64_tr_b16 v[132:133], v5\n\t"
        "ds_read_b64_tr_b16 v[134:135], v5 offset:512\n\t"
        "ds_read_b64_tr_b16 v[148:149], v6\n\t"
        "ds_read_b64_tr_b16 v[150:151], v6 offset:512\n\t"
        "ds_read_b64_tr_b16 v[156:157], v7\n\t"
        "ds_read_b64_tr_b16 v[158:159], v7 offset:512\n\t"
        "s_waitcnt lgkmcnt(0)\n\t"
        "v_add_u32 v4, 32768, v4\n\t"
        "v_add_u32 v5, 32768, v5\n\t"
        "v_add_u32 v6, 32768, v6\n\t"
        "v_add_u32 v7, 32768, v7\n\t"
        "s_waitcnt vmcnt(4)\n\t"
        "s_barrier\n\t"
        "s_add_u32 m0, s69, 98304\n\t"
        "s_xor_b32 s70, s68, 0x480000\n\t"
        "buffer_load_dwordx4 v8, %[rw], s70 offen offset:0 lds\n\t"
        "s_add_u32 s70, s70, 64512\n\t"
        "buffer_load_dwordx4 v9, %[rw], s70 offen offset:1024 lds\n\t"
        "s_add_u32 s70, s70, 64512\n\t"
        "buffer_load_dwordx4 v8, %[rw], s70 offen offset:2048 lds\n\t"
        "s_add_u32 s70, s70, 64512\n\t"
        "buffer_load_dwordx4 v9, %[rw], s70 offen offset:3072 lds\n\t"
        "ds_read_b64_tr_b16 v[160:161], v4\n\t"
        "ds_read_b64_tr_b16 v[162:163], v4 offset:512\n\t"
        "ds_read_b64_tr_b16 v[168:169], v5\n\t"
        "ds_read_b64_tr_b16 v[170:171], v5 offset:512\n\t"
        "ds_read_b64_tr_b16 v[184:185], v6\n\t"
        "ds_read_b64_tr_b16 v[186:187], v6 offset:512\n\t"
        "ds_read_b64_tr_b16 v[192:193], v7\n\t"
        "ds_read_b64_tr_b16 v[194:195], v7 offset:512\n\t"
        "s_waitcnt lgkmcnt(0)\n\t"
        "v_add_u32 v4, -65536, v4\n\t"
        "v_add_u32 v5, -65536, v5\n\t"
        "v_add_u32 v6, -65536, v6\n\t"
        "v_add_u32 v7, -65536, v7\n\t"
        "s_waitcnt vmcnt(4)\n\t"
        "s_barrier\n\t"
        "s_add_u32 m0, s69, 131072\n\t"
        "s_xor_b32 s70, s68, 0x4c0000\n\t"
        "buffer_load_dwordx4 v8, %[rw], s70 offen offset:0 lds\n\t"
        "s_add_u32 s70, s70, 64512\n\t"
        "buffer_load_dwordx4 v9, %[rw], s70 offen offset:1024 lds\n\t"
        "s_add_u32 s70, s70, 64512\n\t"
        "buffer_load_dwordx4 v8, %[rw], s70 offen offset:2048 lds\n\t"
        "s_add_u32 s70, s70, 64512\n\t"
        "buffer_load_dwordx4 v9, %[rw], s70 offen offset:3072 lds\n\t"
        "ds_read_b64_tr_b16 v[164:165], v4\n\t"
        "ds_read_b64_tr_b16 v[166:167], v4 offset:512\n\t"
        "ds_read_b64_tr_b16 v[172:173], v5\n\t"
        "ds_read_b64_tr_b16 v[174:175], v5 offset:512\n\t"
        "ds_read_b64_tr_b16 v[188:189], v6\n\t"
        "ds_read_b64_tr_b16 v[190:191], v6 offset:512\n\t"
        "ds_read_b64_tr_b16 v[196:197], v7\n\t"
        "ds_read_b64_tr_b16 v[198:199], v7 offset:512\n\t"
        "s_waitcnt lgkmcnt(0)\n\t"
        "v_add_u32 v4, 32768, v4\n\t"
        "v_add_u32 v5, 32768, v5\n\t"
        "v_add_u32 v6, 32768, v6\n\t"
        "v_add_u32 v7, 32768, v7\n\t"
        "s_waitcnt vmcnt(4)\n\t"
        "s_barrier\n\t"
        "s_add_u32 m0, s69, 65536\n\t"
        "s_xor_b32 s70, s68, 0x600000\n\t"
        "buffer_load_dwordx4 v8, %[rw], s70 offen offset:0 lds\n\t"
        "s_add_u32 s70, s70, 64512\n\t"
        "buffer_load_dwordx4 v9, %[rw], s70 offen offset:1024 lds\n\t"
        "s_add_u32 s70, s70, 64512\n\t"
        "buffer_load_dwordx4 v8, %[rw], s70 offen offset:2048 lds\n\t"
        "s_add_u32 s70, s70, 64512\n\t"
        "buffer_load_dwordx4 v9, %[rw], s70 offen offset:3072 lds\n\t"
        "ds_read_b64_tr_b16 v[176:177], v4\n\t"
        "ds_read_b64_tr_b16 v[178:179], v4 offset:512\n\t"
        "ds_read_b64_tr_b16 v[52:53], v5\n\t"
        "ds_read_b64_tr_b16 v[54:55], v5 offset:512\n\t"
        "ds_read_b64_tr_b16 v[200:201], v6\n\t"
        "ds_read_b64_tr_b16 v[202:203], v6 offset:512\n\t"
        "ds_read_b64_tr_b16 v[20:21], v7\n\t"
        "ds_read_b64_tr_b16 v[22:23], v7 offset:512\n\t"
        "s_waitcnt lgkmcnt(0)\n\t"
        "v_add_u32 v4, 32768, v4\n\t"
        "v_add_u32 v5, 32768, v5\n\t"
        "v_add_u32 v6, 32768, v6\n\t"
        "v_add_u32 v7, 32768, v7\n\t"
        "ds_write_b128 v3, v[52:55] offset:16384\n\t"
        "ds_write_b128 v3, v[20:23] offset:32768\n\t"
        "s_waitcnt vmcnt(4)\n\t"
        "s_barrier\n\t"
        "s_add_u32 m0, s69, 98304\n\t"
        "s_xor_b32 s70, s68, 0x640000\n\t"
        "buffer_load_dwordx4 v8, %[rw], s70 offen offset:0 lds\n\t"
        "s_add_u32 s70, s70, 64512\n\t"
        "buffer_load_dwordx4 v9, %[rw], s70 offen offset:1024 lds\n\t"
        "s_add_u32 s70, s70, 64512\n\t"
        "buffer_load_dwordx4 v8, %[rw], s70 offen offset:2048 lds\n\t"
        "s_add_u32 s70, s70, 64512\n\t"
        "buffer_load_dwordx4 v9, %[rw], s70 offen offset:3072 lds\n\t"
        "ds_read_b64_tr_b16 v[180:181], v4\n\t"
        "ds_read_b64_tr_b16 v[182:183], v4 offset:512\n\t"
        "ds_read_b64_tr_b16 v[52:53], v5\n\t"
        "ds_read_b64_tr_b16 v[54:55], v5 offset:512\n\t"
        "ds_read_b64_tr_b16 v[204:205], v6\n\t"
        "ds_read_b64_tr_b16 v[206:207], v6 offset:512\n\t"
        "ds_read_b64_tr_b16 v[20:21], v7\n\t"
        "ds_read_b64_tr_b16 v[22:23], v7 offset:512\n\t"
        "s_waitcnt lgkmcnt(0)\n\t"
        "v_add_u32 v4, -65536, v4\n\t"
        "v_add_u32 v5, -65536, v5\n\t"
        "v_add_u32 v6, -65536, v6\n\t"
        "v_add_u32 v7, -65536, v7\n\t"
        "ds_write_b128 v3, v[52:55] offset:17408\n\t"
        "ds_write_b128 v3, v[20:23] offset:33792\n\t"
        "s_waitcnt vmcnt(4)\n\t"
        "s_barrier\n\t"
        "s_add_u32 m0, s69, 131072\n\t"
        "s_xor_b32 s70, s68, 0x680000\n\t"
        "buffer_load_dwordx4 v8, %[rw], s70 offen offset:0 lds\n\t"
        "s_add_u32 s70, s70, 64512\n\t"
        "buffer_load_dwordx4 v9, %[rw], s70 offen offset:1024 lds\n\t"
        "s_add_u32 s70, s70, 64512\n\t"
        "buffer_load_dwordx4 v8, %[rw], s70 offen offset:2048 lds\n\t"
        "s_add_u32 s70, s70, 64512\n\t"
        "buffer_load_dwordx4 v9, %[rw], s70 offen offset:3072 lds\n\t"
        "ds_read_b64_tr_b16 v[208:209], v4\n\t"
        "ds_read_b64_tr_b16 v[210:211], v4 offset:512\n\t"
        "ds_read_b64_tr_b16 v[216:217], v5\n\t"
        "ds_read_b64_tr_b16 v[218:219], v5 offset:512\n\t"
        "ds_read_b64_tr_b16 v[232:233], v6\n\t"
        "ds_read_b64_tr_b16 v[234:235], v6 offset:512\n\t"
        "ds_read_b64_tr_b16 v[240:241], v7\n\t"
        "ds_read_b64_tr_b16 v[242:243], v7 offset:512\n\t"
        "s_waitcnt lgkmcnt(0)\n\t"
        "v_add_u32 v4, 32768, v4\n\t"
        "v_add_u32 v5, 32768, v5\n\t"
        "v_add_u32 v6, 32768, v6\n\t"
        "v_add_u32 v7, 32768, v7\n\t"
        "s_waitcnt vmcnt(4)\n\t"
        "s_barrier\n\t"
        "s_add_u32 m0, s69, 65536\n\t"
        "s_xor_b32 s70, s68, 0x6c0000\n\t"
        "buffer_load_dwordx4 v8, %[rw], s70 offen offset:0 lds\n\t"
        "s_add_u32 s70, s70, 64512\n\t"
        "buffer_load_dwordx4 v9, %[rw], s70 offen offset:1024 lds\n\t"
        "s_add_u32 s70, s70, 64512\n\t"
        "buffer_load_dwordx4 v8, %[rw], s70 offen offset:2048 lds\n\t"
        "s_add_u32 s70, s70, 64512\n\t"
        "buffer_load_dwordx4 v9, %[rw], s70 offen offset:3072 lds\n\t"
        "ds_read_b64_tr_b16 v[212:213], v4\n\t"
        "ds_read_b64_tr_b16 v[214:215], v4 offset:512\n\t"
        "ds_read_b64_tr_b16 v[220:221], v5\n\t"
        "ds_read_b64_tr_b16 v[222:223], v5 offset:512\n\t"
        "ds_read_b64_tr_b16 v[236:237], v6\n\t"
        "ds_read_b64_tr_b16 v[238:239], v6 offset:512\n\t"
        "ds_read_b64_tr_b16 v[244:245], v7\n\t"
        "ds_read_b64_tr_b16 v[246:247], v7 offset:512\n\t"
        "s_waitcnt lgkmcnt(0)\n\t"
        "v_add_u32 v4, 32768, v4\n\t"
        "v_add_u32 v5, 32768, v5\n\t"
        "v_add_u32 v6, 32768, v6\n\t"
        "v_add_u32 v7, 32768, v7\n\t"
        "s_waitcnt vmcnt(4)\n\t"
        "s_barrier\n\t"
        "v_mov_b32 v10, 128\n\t"
        "buffer_load_dwordx2 v[10:11], v10, %[rf], 0 offen sc1\n\t"
        "ds_read_b64_tr_b16 v[224:225], v4\n\t"
        "ds_read_b64_tr_b16 v[226:227], v4 offset:512\n\t"
        "ds_read_b64_tr_b16 v[52:53], v5\n\t"
        "ds_read_b64_tr_b16 v[54:55], v5 offset:512\n\t"
        "ds_read_b64_tr_b16 v[248:249], v6\n\t"
        "ds_read_b64_tr_b16 v[250:251], v6 offset:512\n\t"
        "ds_read_b64_tr_b16 v[20:21], v7\n\t"
        "ds_read_b64_tr_b16 v[22:23], v7 offset:512\n\t"
        "s_waitcnt lgkmcnt(0)\n\t"
        "v_add_u32 v4, -65536, v4\n\t"
        "v_add_u32 v5, -65536, v5\n\t"
        "v_add_u32 v6, -65536, v6\n\t"
        "v_add_u32 v7, -65536, v7\n\t"
        "ds_write_b128 v3, v[52:55] offset:49152\n\t"
        "s_waitcnt vmcnt(0)\n\t"
        "s_barrier\n\t"
        "ds_read_b64_tr_b16 v[228:229], v4\n\t"
        "ds_read_b64_tr_b16 v[230:231], v4 offset:512\n\t"
        "ds_read_b64_tr_b16 v[52:53], v5\n\t"
        "ds_read_b64_tr_b16 v[54:55], v5 offset:512\n\t"
        "ds_read_b64_tr_b16 v[252:253], v6\n\t"
        "ds_read_b64_tr_b16 v[254:255], v6 offset:512\n\t"
        "ds_read_b64_tr_b16 v[24:25], v7\n\t"
        "ds_read_b64_tr_b16 v[26:27], v7 offset:512\n\t"
        "s_waitcnt lgkmcnt(0)\n\t"
        "v_add_u32 v4, 32768, v4\n\t"
        "v_add_u32 v5, 32768, v5\n\t"
        "v_add_u32 v6, 32768, v6\n\t"
        "v_add_u32 v7, 32768, v7\n\t"
        "ds_write_b128 v3, v[52:55] offset:50176\n\t"
        "s_branch .Ls_std_%=\n\t"
        ".Ls_stk_%=:\n\t"
        "s_waitcnt vmcnt(4)\n\t"
        "s_barrier\n\t"
        "s_add_u32 m0, s69, 131072\n\t"
        "s_xor_b32 s70, s68, 0x80000\n\t"
        "buffer_load_dwordx4 v8, %[rw], s70 offen offset:0 lds\n\t"
        "s_add_u32 s70, s70, 64512\n\t"
        "buffer_load_dwordx4 v9, %[rw], s70 offen offset:1024 lds\n\t"
        "s_add_u32 s70, s70, 64512\n\t"
        "buffer_load_dwordx4 v8, %[rw], s70 offen offset:2048 lds\n\t"
        "s_add_u32 s70, s70, 64512\n\t"
        "buffer_load_dwordx4 v9, %[rw], s70 offen offset:3072 lds\n\t"
        "ds_read_b64_tr_b16 v[56:57], v4\n\t"
        "ds_read_b64_tr_b16 v[58:59], v4 offset:512\n\t"
        "ds_read_b64_tr_b16 v[64:65], v5\n\t"
        "ds_read_b64_tr_b16 v[66:67], v5 offset:512\n\t"
        "ds_read_b64_tr_b16 v[88:89], v6\n\t"
        "ds_read_b64_tr_b16 v[90:91], v6 offset:512\n\t"
        "ds_read_b64_tr_b16 v[28:29], v7\n\t"
        "ds_read_b64_tr_b16 v[30:31], v7 offset:512\n\t"
        "s_waitcnt lgkmcnt(0)\n\t"
        "v_add_u32 v4, 32768, v4\n\t"
        "v_add_u32 v5, 32768, v5\n\t"
        "v_add_u32 v6, 32768, v6\n\t"
        "v_add_u32 v7, 32768, v7\n\t"
        "s_waitcnt vmcnt(4)\n\t"
        "s_barrier\n\t"
        "s_add_u32 m0, s69, 65536\n\t"
        "s_xor_b32 s70, s68, 0xc0000\n\t"
        "buffer_load_dwordx4 v8, %[rw], s70 offen offset:0 lds\n\t"
        "s_add_u32 s70, s70, 64512\n\t"
        "buffer_load_dwordx4 v9, %[rw], s70 offen offset:1024 lds\n\t"
        "s_add_u32 s70, s70, 64512\n\t"
        "buffer_load_dwordx4 v8, %[rw], s70 offen offset:2048 lds\n\t"
        "s_add_u32 s70, s70, 64512\n\t"
        "buffer_load_dwordx4 v9, %[rw], s70 offen offset:3072 lds\n\t"
        "ds_read_b64_tr_b16 v[60:61], v4\n\t"
        "ds_read_b64_tr_b16 v[62:63], v4 offset:512\n\t"
        "ds_read_b64_tr_b16 v[68:69], v5\n\t"
        "ds_read_b64_tr_b16 v[70:71], v5 offset:512\n\t"
        "ds_read_b64_tr_b16 v[92:93], v6\n\t"
        "ds_read_b64_tr_b16 v[94:95], v6 offset:512\n\t"
        "ds_read_b64_tr_b16 v[32:33], v7\n\t"
        "ds_read_b64_tr_b16 v[34:35], v7 offset:512\n\t"
        "s_waitcnt lgkmcnt(0)\n\t"
        "v_add_u32 v4, 32768, v4\n\t"
        "v_add_u32 v5, 32768, v5\n\t"
        "v_add_u32 v6, 32768, v6\n\t"
        "v_add_u32 v7, 32768, v7\n\t"
        "s_waitcnt vmcnt(4)\n\t"
        "s_barrier\n\t"
        "s_add_u32 m0, s69, 98304\n\t"
        "s_xor_b32 s70, s68, 0x200000\n\t"
        "buffer_load_dwordx4 v8, %[rw], s70 offen offset:0 lds\n\t"
        "s_add_u32 s70, s70, 64512\n\t"
        "buffer_load_dwordx4 v9, %[rw], s70 offen offset:1024 lds\n\t"
        "s_add_u32 s70, s70, 64512\n\t"
        "buffer_load_dwordx4 v8, %[rw], s70 offen offset:2048 lds\n\t"
        "s_add_u32 s70, s70, 64512\n\t"
        "buffer_load_dwordx4 v9, %[rw], s70 offen offset:3072 lds\n\t"
        "ds_read_b64_tr_b16 v[72:73], v4\n\t"
        "ds_read_b64_tr_b16 v[74:75], v4 offset:512\n\t"
        "ds_read_b64_tr_b16 v[80:81], v5\n\t"
        "ds_read_b64_tr_b16 v[82:83], v5 offset:512\n\t"
        "ds_read_b64_tr_b16 v[96:97], v6\n\t"
        "ds_read_b64_tr_b16 v[98:99], v6 offset:512\n\t"
        "ds_read_b64_tr_b16 v[104:105], v7\n\t"
        "ds_read_b64_tr_b16 v[106:107], v7 offset:512\n\t"
        "s_waitcnt lgkmcnt(0)\n\t"
        "v_add_u32 v4, -65536, v4\n\t"
        "v_add_u32 v5, -65536, v5\n\t"
        "v_add_u32 v6, -65536, v6\n\t"
        "v_add_u32 v7, -65536, v7\n\t"
        "s_waitcnt vmcnt(4)\n\t"
        "s_barrier\n\t"
        "s_add_u32 m0, s69, 131072\n\t"
        "s_xor_b32 s70, s68, 0x240000\n\t"
        "buffer_load_dwordx4 v8, %[rw], s70 offen offset:0 lds\n\t"
        "s_add_u32 s70, s70, 64512\n\t"
        "buffer_load_dwordx4 v9, %[rw], s70 offen offset:1024 lds\n\t"
        "s_add_u32 s70, s70, 64512\n\t"
        "buffer_load_dwordx4 v8, %[rw], s70 offen offset:2048 lds\n\t"
        "s_add_u32 s70, s70, 64512\n\t"
        "buffer_load_dwordx4 v9, %[rw], s70 offen offset:3072 lds\n\t"
        "ds_read_b64_tr_b16 v[76:77], v4\n\t"
        "ds_read_b64_tr_b16 v[78:79], v4 offset:512\n\t"
        "ds_read_b64_tr_b16 v[84:85], v5\n\t"
        "ds_read_b64_tr_b16 v[86:87], v5 offset:512\n\t"
        "ds_read_b64_tr_b16 v[100:101], v6\n\t"
        "ds_read_b64_tr_b16 v[102:103], v6 offset:512\n\t"
        "ds_read_b64_tr_b16 v[108:109], v7\n\t"
        "ds_read_b64_tr_b16 v[110:111], v7 offset:512\n\t"
        "s_waitcnt lgkmcnt(0)\n\t"
        "v_add_u32 v4, 32768, v4\n\t"
        "v_add_u32 v5, 32768, v5\n\t"
        "v_add_u32 v6, 32768, v6\n\t"
        "v_add_u32 v7, 32768, v7\n\t"
        "s_waitcnt vmcnt(4)\n\t"
        "s_barrier\n\t"
        "s_add_u32 m0, s69, 65536\n\t"
        "s_xor_b32 s70, s68, 0x280000\n\t"
        "buffer_load_dwordx4 v8, %[rw], s70 offen offset:0 lds\n\t"
        "s_add_u32 s70, s70, 64512\n\t"
        "buffer_load_dwordx4 v9, %[rw], s70 offen offset:1024 lds\n\t"
        "s_add_u32 s70, s70, 64512\n\t"
        "buffer_load_dwordx4 v8, %[rw], s70 offen offset:2048 lds\n\t"
        "s_add_u32 s70, s70, 64512\n\t"
        "buffer_load_dwordx4 v9, %[rw], s70 offen offset:3072 lds\n\t"
        "ds_read_b64_tr_b16 v[112:113], v4\n\t"
        "ds_read_b64_tr_b16 v[114:115], v4 offset:512\n\t"
        "ds_read_b64_tr_b16 v[36:37], v5\n\t"
        "ds_read_b64_tr_b16 v[38:39], v5 offset:512\n\t"
        "ds_read_b64_tr_b16 v[136:137], v6\n\t"
        "ds_read_b64_tr_b16 v[138:139], v6 offset:512\n\t"
        "ds_read_b64_tr_b16 v[44:45], v7\n\t"
        "ds_read_b64_tr_b16 v[46:47], v7 offset:512\n\t"
        "s_waitcnt lgkmcnt(0)\n\t"
        "v_add_u32 v4, 32768, v4\n\t"
        "v_add_u32 v5, 32768, v5\n\t"
        "v_add_u32 v6, 32768, v6\n\t"
        "v_add_u32 v7, 32768, v7\n\t"
        "s_waitcnt vmcnt(4)\n\t"
        "s_barrier\n\t"
        "s_add_u32 m0, s69, 98304\n\t"
        "s_xor_b32 s70, s68, 0x2c0000\n\t"
        "buffer_load_dwordx4 v8, %[rw], s70 offen offset:0 lds\n\t"
        "s_add_u32 s70, s70, 64512\n\t"
        "buffer_load_dwordx4 v9, %[rw], s70 offen offset:1024 lds\n\t"
        "s_add_u32 s70, s70, 64512\n\t"
        "buffer_load_dwordx4 v8, %[rw], s70 offen offset:2048 lds\n\t"
        "s_add_u32 s70, s70, 64512\n\t"
        "buffer_load_dwordx4 v9, %[rw], s70 offen offset:3072 lds\n\t"
        "ds_read_b64_tr_b16 v[116:117], v4\n\t"
        "ds_read_b64_tr_b16 v[118:119], v4 offset:512\n\t"
        "ds_read_b64_tr_b16 v[40:41], v5\n\t"
        "ds_read_b64_tr_b16 v[42:43], v5 offset:512\n\t"
        "ds_read_b64_tr_b16 v[140:141], v6\n\t"
        "ds_read_b64_tr_b16 v[142:143], v6 offset:512\n\t"
        "ds_read_b64_tr_b16 v[48:49], v7\n\t"
        "ds_read_b64_tr_b16 v[50:51], v7 offset:512\n\t"
        "s_waitcnt lgkmcnt(0)\n\t"
        "v_add_u32 v4, -65536, v4\n\t"
        "v_add_u32 v5, -65536, v5\n\t"
        "v_add_u32 v6, -65536, v6\n\t"
        "v_add_u32 v7, -65536, v7\n\t"
        "s_waitcnt vmcnt(4)\n\t"
        "s_barrier\n\t"
        "s_add_u32 m0, s69, 131072\n\t"
        "s_xor_b32 s70, s68, 0x400000\n\t"
        "buffer_load_dwordx4 v8, %[rw], s70 offen offset:0 lds\n\t"
        "s_add_u32 s70, s70, 64512\n\t"
        "buffer_load_dwordx4 v9, %[rw], s70 offen offset:1024 lds\n\t"
        "s_add_u32 s70, s70, 64512\n\t"
        "buffer_load_dwordx4 v8, %[rw], s70 offen offset:2048 lds\n\t"
        "s_add_u32 s70, s70, 64512\n\t"
        "buffer_load_dwordx4 v9, %[rw], s70 offen offset:3072 lds\n\t"
        "ds_read_b64_tr_b16 v[120:121], v4\n\t"
        "ds_read_b64_tr_b16 v[122:123], v4 offset:512\n\t"
        "ds_read_b64_tr_b16 v[128:129], v5\n\t"
        "ds_read_b64_tr_b16 v[130:131], v5 offset:512\n\t"
        "ds_read_b64_tr_b16 v[144:145], v6\n\t"
        "ds_read_b64_tr_b16 v[146:147], v6 offset:512\n\t"
        "ds_read_b64_tr_b16 v[52:53], v7\n\t"
        "ds_read_b64_tr_b16 v[54:55], v7 offset:512\n\t"
        "s_waitcnt lgkmcnt(0)\n\t"
        "v_add_u32 v4, 32768, v4\n\t"
        "v_add_u32 v5, 32768, v5\n\t"
        "v_add_u32 v6, 32768, v6\n\t"
        "v_add_u32 v7, 32768, v7\n\t"
        "ds_write_b128 v3, v[52:55] offset:16384\n\t"
        "s_waitcnt vmcnt(4)\n\t"
        "s_barrier\n\t"
        "s_add_u32 m0, s69, 65536\n\t"
        "s_xor_b32 s70, s68, 0x440000\n\t"
        "buffer_load_dwordx4 v8, %[rw], s70 offen offset:0 lds\n\t"
        "s_add_u32 s70, s70, 64512\n\t"
        "buffer_load_dwordx4 v9, %[rw], s70 offen offset:1024 lds\n\t"
        "s_add_u32 s70, s70, 64512\n\t"
        "buffer_load_dwordx4 v8, %[rw], s70 offen offset:2048 lds\n\t"
        "s_add_u32 s70, s70, 64512\n\t"
        "buffer_load_dwordx4 v9, %[rw], s70 offen offset:3072 lds\n\t"
        "ds_read_b64_tr_b16 v[124:125], v4\n\t"
        "ds_read_b64_tr_b16 v[126:127], v4 offset:512\n\t"
        "ds_read_b64_tr_b16 v[132:133], v5\n\t"
        "ds_read_b64_tr_b16 v[134:135], v5 offset:512\n\t"
        "ds_read_b64_tr_b16 v[148:149], v6\n\t"
        "ds_read_b64_tr_b16 v[150:151], v6 offset:512\n\t"
        "ds_read_b64_tr_b16 v[52:53], v7\n\t"
        "ds_read_b64_tr_b16 v[54:55], v7 offset:512\n\t"
        "s_waitcnt lgkmcnt(0)\n\t"
        "v_add_u32 v4, 32768, v4\n\t"
        "v_add_u32 v5, 32768, v5\n\t"
        "v_add_u32 v6, 32768, v6\n\t"
        "v_add_u32 v7, 32768, v7\n\t"
        "ds_write_b128 v3, v[52:55] offset:17408\n\t"
        "s_waitcnt vmcnt(4)\n\t"
        "s_barrier\n\t"
        "s_add_u32 m0, s69, 98304\n\t"
        "s_xor_b32 s70, s68, 0x480000\n\t"
        "buffer_load_dwordx4 v8, %[rw], s70 offen offset:0 lds\n\t"
        "s_add_u32 s70, s70, 64512\n\t"
        "buffer_load_dwordx4 v9, %[rw], s70 offen offset:1024 lds\n\t"
        "s_add_u32 s70, s70, 64512\n\t"
        "buffer_load_dwordx4 v8, %[rw], s70 offen offset:2048 lds\n\t"
        "s_add_u32 s70, s70, 64512\n\t"
        "buffer_load_dwordx4 v9, %[rw], s70 offen offset:3072 lds\n\t"
        "ds_read_b64_tr_b16 v[152:153], v4\n\t"
        "ds_read_b64_tr_b16 v[154:155], v4 offset:512\n\t"
        "ds_read_b64_tr_b16 v[52:53], v5\n\t"
        "ds_read_b64_tr_b16 v[54:55], v5 offset:512\n\t"
        "ds_read_b64_tr_b16 v[168:169], v6\n\t"
        "ds_read_b64_tr_b16 v[170:171], v6 offset:512\n\t"
        "ds_read_b64_tr_b16 v[20:21], v7\n\t"
        "ds_read_b64_tr_b16 v[22:23], v7 offset:512\n\t"
        "s_waitcnt lgkmcnt(0)\n\t"
        "v_add_u32 v4, -65536, v4\n\t"
        "v_add_u32 v5, -65536, v5\n\t"
        "v_add_u32 v6, -65536, v6\n\t"
        "v_add_u32 v7, -65536, v7\n\t"
        "ds_write_b128 v3, v[52:55] offset:32768\n\t"
        "s_waitcnt vmcnt(4)\n\t"
        "s_barrier\n\t"
        "s_add_u32 m0, s69, 131072\n\t"
        "s_xor_b32 s70, s68, 0x4c0000\n\t"
        "buffer_load_dwordx4 v8, %[rw], s70 offen offset:0 lds\n\t"
        "s_add_u32 s70, s70, 64512\n\t"
        "buffer_load_dwordx4 v9, %[rw], s70 offen offset:1024 lds\n\t"
        "s_add_u32 s70, s70, 64512\n\t"
        "buffer_load_dwordx4 v8, %[rw], s70 offen offset:2048 lds\n\t"
        "s_add_u32 s70, s70, 64512\n\t"
        "buffer_load_dwordx4 v9, %[rw], s70 offen offset:3072 lds\n\t"
        "ds_read_b64_tr_b16 v[156:157], v4\n\t"
        "ds_read_b64_tr_b16 v[158:159], v4 offset:512\n\t"
        "ds_read_b64_tr_b16 v[52:53], v5\n\t"
        "ds_read_b64_tr_b16 v[54:55], v5 offset:512\n\t"
        "ds_read_b64_tr_b16 v[172:173], v6\n\t"
        "ds_read_b64_tr_b16 v[174:175], v6 offset:512\n\t"
        "ds_read_b64_tr_b16 v[24:25], v7\n\t"
        "ds_read_b64_tr_b16 v[26:27], v7 offset:512\n\t"
        "s_waitcnt lgkmcnt(0)\n\t"
        "v_add_u32 v4, 32768, v4\n\t"
        "v_add_u32 v5, 32768, v5\n\t"
        "v_add_u32 v6, 32768, v6\n\t"
        "v_add_u32 v7, 32768, v7\n\t"
        "ds_write_b128 v3, v[52:55] offset:33792\n\t"
        "s_waitcnt vmcnt(4)\n\t"
        "s_barrier\n\t"
        "s_add_u32 m0, s69, 65536\n\t"
        "s_xor_b32 s70, s68, 0x600000\n\t"
        "buffer_load_dwordx4 v8, %[rw], s70 offen offset:0 lds\n\t"
        "s_add_u32 s70, s70, 64512\n\t"
        "buffer_load_dwordx4 v9, %[rw], s70 offen offset:1024 lds\n\t"
        "s_add_u32 s70, s70, 64512\n\t"
        "buffer_load_dwordx4 v8, %[rw], s70 offen offset:2048 lds\n\t"
        "s_add_u32 s70, s70, 64512\n\t"
        "buffer_load_dwordx4 v9, %[rw], s70 offen offset:3072 lds\n\t"
        "ds_read_b64_tr_b16 v[160:161], v4\n\t"
        "ds_read_b64_tr_b16 v[162:163], v4 offset:512\n\t"
        "ds_read_b64_tr_b16 v[52:53], v5\n\t"
        "ds_read_b64_tr_b16 v[54:55], v5 offset:512\n\t"
        "ds_read_b64_tr_b16 v[176:177], v6\n\t"
        "ds_read_b64_tr_b16 v[178:179], v6 offset:512\n\t"
        "ds_read_b64_tr_b16 v[184:185], v7\n\t"
        "ds_read_b64_tr_b16 v[186:187], v7 offset:512\n\t"
        "s_waitcnt lgkmcnt(0)\n\t"
        "v_add_u32 v4, 32768, v4\n\t"
        "v_add_u32 v5, 32768, v5\n\t"
        "v_add_u32 v6, 32768, v6\n\t"
        "v_add_u32 v7, 32768, v7\n\t"
        "ds_write_b128 v3, v[52:55] offset:49152\n\t"
        "s_waitcnt vmcnt(4)\n\t"
        "s_barrier\n\t"
        "s_add_u32 m0, s69, 98304\n\t"
        "s_xor_b32 s70, s68, 0x640000\n\t"
        "buffer_load_dwordx4 v8, %[rw], s70 offen offset:0 lds\n\t"
        "s_add_u32 s70, s70, 64512\n\t"
        "buffer_load_dwordx4 v9, %[rw], s70 offen offset:1024 lds\n\t"
        "s_add_u32 s70, s70, 64512\n\t"
        "buffer_load_dwordx4 v8, %[rw], s70 offen offset:2048 lds\n\t"
        "s_add_u32 s70, s70, 64512\n\t"
        "buffer_load_dwordx4 v9, %[rw], s70 offen offset:3072 lds\n\t"
        "ds_read_b64_tr_b16 v[164:165], v4\n\t"
        "ds_read_b64_tr_b16 v[166:167], v4 offset:512\n\t"
        "ds_read_b64_tr_b16 v[52:53], v5\n\t"
        "ds_read_b64_tr_b16 v[54:55], v5 offset:512\n\t"
        "ds_read_b64_tr_b16 v[180:181], v6\n\t"
        "ds_read_b64_tr_b16 v[182:183], v6 offset:512\n\t"
        "ds_read_b64_tr_b16 v[188:189], v7\n\t"
        "ds_read_b64_tr_b16 v[190:191], v7 offset:512\n\t"
        "s_waitcnt lgkmcnt(0)\n\t"
        "v_add_u32 v4, -65536, v4\n\t"
        "v_add_u32 v5, -65536, v5\n\t"
        "v_add_u32 v6, -65536, v6\n\t"
        "v_add_u32 v7, -65536, v7\n\t"
        "ds_write_b128 v3, v[52:55] offset:50176\n\t"
        "s_waitcnt vmcnt(4)\n\t"
        "s_barrier\n\t"
        "s_add_u32 m0, s69, 131072\n\t"
        "s_xor_b32 s70, s68, 0x680000\n\t"
        "buffer_load_dwordx4 v8, %[rw], s70 offen offset:0 lds\n\t"
        "s_add_u32 s70, s70, 64512\n\t"
        "buffer_load_dwordx4 v9, %[rw], s70 offen offset:1024 lds\n\t"
        "s_add_u32 s70, s70, 64512\n\t"
        "buffer_load_dwordx4 v8, %[rw], s70 offen offset:2048 lds\n\t"
        "s_add_u32 s70, s70, 64512\n\t"
        "buffer_load_dwordx4 v9, %[rw], s70 offen offset:3072 lds\n\t"
        "ds_read_b64_tr_b16 v[192:193], v4\n\t"
        "ds_read_b64_tr_b16 v[194:195], v4 offset:512\n\t"
        "ds_read_b64_tr_b16 v[200:201], v5\n\t"
        "ds_read_b64_tr_b16 v[202:203], v5 offset:512\n\t"
        "ds_read_b64_tr_b16 v[224:225], v6\n\t"
        "ds_read_b64_tr_b16 v[226:227], v6 offset:512\n\t"
        "ds_read_b64_tr_b16 v[232:233], v7\n\t"
        "ds_read_b64_tr_b16 v[234:235], v7 offset:512\n\t"
        "s_waitcnt lgkmcnt(0)\n\t"
        "v_add_u32 v4, 32768, v4\n\t"
        "v_add_u32 v5, 32768, v5\n\t"
        "v_add_u32 v6, 32768, v6\n\t"
        "v_add_u32 v7, 32768, v7\n\t"
        "s_waitcnt vmcnt(4)\n\t"
        "s_barrier\n\t"
        "s_add_u32 m0, s69, 65536\n\t"
        "s_xor_b32 s70, s68, 0x6c0000\n\t"
        "buffer_load_dwordx4 v8, %[rw], s70 offen offset:0 lds\n\t"
        "s_add_u32 s70, s70, 64512\n\t"
        "buffer_load_dwordx4 v9, %[rw], s70 offen offset:1024 lds\n\t"
        "s_add_u32 s70, s70, 64512\n\t"
        "buffer_load_dwordx4 v8, %[rw], s70 offen offset:2048 lds\n\t"
        "s_add_u32 s70, s70, 64512\n\t"
        "buffer_load_dwordx4 v9, %[rw], s70 offen offset:3072 lds\n\t"
        "ds_read_b64_tr_b16 v[196:197], v4\n\t"
        "ds_read_b64_tr_b16 v[198:199], v4 offset:512\n\t"
        "ds_read_b64_tr_b16 v[204:205], v5\n\t"
        "ds_read_b64_tr_b16 v[206:207], v5 offset:512\n\t"
        "ds_read_b64_tr_b16 v[228:229], v6\n\t"
        "ds_read_b64_tr_b16 v[230:231], v6 offset:512\n\t"
        "ds_read_b64_tr_b16 v[236:237], v7\n\t"
        "ds_read_b64_tr_b16 v[238:239], v7 offset:512\n\t"
        "s_waitcnt lgkmcnt(0)\n\t"
        "v_add_u32 v4, 32768, v4\n\t"
        "v_add_u32 v5, 32768, v5\n\t"
        "v_add_u32 v6, 32768, v6\n\t"
        "v_add_u32 v7, 32768, v7\n\t"
        "s_waitcnt vmcnt(4)\n\t"
        "s_barrier\n\t"
        "v_mov_b32 v10, 128\n\t"
        "buffer_load_dwordx2 v[10:11], v10, %[rf], 0 offen sc1\n\t"
        "ds_read_b64_tr_b16 v[208:209], v4\n\t"
        "ds_read_b64_tr_b16 v[210:211], v4 offset:512\n\t"
        "ds_read_b64_tr_b16 v[216:217], v5\n\t"
        "ds_read_b64_tr_b16 v[218:219], v5 offset:512\n\t"
        "ds_read_b64_tr_b16 v[240:241], v6\n\t"
        "ds_read_b64_tr_b16 v[242:243], v6 offset:512\n\t"
        "ds_read_b64_tr_b16 v[248:249], v7\n\t"
        "ds_read_b64_tr_b16 v[250:251], v7 offset:512\n\t"
        "s_waitcnt lgkmcnt(0)\n\t"
        "v_add_u32 v4, -65536, v4\n\t"
        "v_add_u32 v5, -65536, v5\n\t"
        "v_add_u32 v6, -65536, v6\n\t"
        "v_add_u32 v7, -65536, v7\n\t"
        "s_waitcnt vmcnt(0)\n\t"
        "s_barrier\n\t"
        "ds_read_b64_tr_b16 v[212:213], v4\n\t"
        "ds_read_b64_tr_b16 v[214:215], v4 offset:512\n\t"
        "ds_read_b64_tr_b16 v[220:221], v5\n\t"
        "ds_read_b64_tr_b16 v[222:223], v5 offset:512\n\t"
        "ds_read_b64_tr_b16 v[244:245], v6\n\t"
        "ds_read_b64_tr_b16 v[246:247], v6 offset:512\n\t"
        "ds_read_b64_tr_b16 v[252:253], v7\n\t"
        "ds_read_b64_tr_b16 v[254:255], v7 offset:512\n\t"
        "s_waitcnt lgkmcnt(0)\n\t"
        "v_add_u32 v4, 32768, v4\n\t"
        "v_add_u32 v5, 32768, v5\n\t"
        "v_add_u32 v6, 32768, v6\n\t"
        "v_add_u32 v7, 32768, v7\n\t"
        ".Ls_std_%=:\n\t"
        "s_waitcnt lgkmcnt(0)\n\t"
        "s_barrier\n\t"
        "v_add_u32 v53, 65536, v3\n\t"
        "ds_write_b128 v53, v[28:31] offset:0\n\t"
        "ds_write_b128 v53, v[32:35] offset:1024\n\t"
        "ds_write_b128 v53, v[36:39] offset:16384\n\t"
        "ds_write_b128 v53, v[40:43] offset:17408\n\t"
        "ds_write_b128 v53, v[44:47] offset:32768\n\t"
        "ds_write_b128 v53, v[48:51] offset:33792\n\t"
        "ds_write_b128 v53, v[20:23] offset:49152\n\t"
        "ds_write_b128 v53, v[24:27] offset:50176\n\t"
        "v_xor_b32 v3, 0x1800, v2\n\t"
        "s_mov_b32 s56, 0x55555555\n\t"
        "s_mov_b32 s57, 0x55555555\n\t"
        "s_mov_b32 s66, 0x3e0293ee\n\t"
        "s_mov_b32 s58, 0xfffcffff\n\t"
        "s_mov_b32 s59, 0xffc0fff0\n\t"
        "s_mov_b32 s60, 0xfc00ff00\n\t"
        "s_mov_b32 s61, 0xc000f000\n\t"
        "s_mov_b32 s62, 0xfff8fffe\n\t"
        "s_mov_b32 s63, 0xff80ffe0\n\t"
        "s_mov_b32 s64, 0xf800fe00\n\t"
        "s_mov_b32 s65, 0x8000e000\n\t"
        "s_memtime s[92:93]\n\t"
        "s_memrealtime s[94:95]\n\t"
        "s_mov_b32 s98, 0x3d380000\n\t"
        "s_waitcnt lgkmcnt(0)\n\t"
        "s_sub_u32 s96, s92, 4500\n\t"
        "s_sub_u32 s97, s94, 200\n\t"
        "s_waitcnt vmcnt(0)\n\t"
        "v_readfirstlane_b32 s68, v10\n\t"
        "v_readfirstlane_b32 s69, v11\n\t"
        "s_xor_b32 s68, s68, s54\n\t"
        "s_xor_b32 s69, s69, s55\n\t"
        "s_lshr_b32 s70, s68, 8\n\t"
        "s_or_b32 s70, s70, s69\n\t"
        "s_cmp_eq_u32 s70, 0\n\t"
        "s_cselect_b32 s68, s68, 0\n\t"
        "s_and_b32 s69, s68, 127\n\t"
        "s_max_u32 s51, s51, s69\n\t"
        "s_cmp_ge_u32 s51, 1\n\t"
        "s_cbranch_scc0 .Ls_f0p_%=\n\t"
        ".Ls_f0k_%=:\n\t"
        "s_lshr_b32 s48, s68, 7\n\t"
        "s_lshl_b32 s48, s48, 13\n\t"
        "s_lshl_b32 s70, %[cs], 13\n\t"
        "s_xor_b32 s70, s70, s48\n\t"
        "s_lshr_b32 s71, %[w], 2\n\t"
        "s_lshl_b32 s71, s71, 14\n\t"
        "s_and_b32 s72, %[w], 3\n\t"
        "s_lshl_b32 s73, s72, 11\n\t"
        "s_add_u32 s71, s71, s73\n\t"
        "s_add_u32 s47, s71, %[ra]\n\t"
        "s_add_u32 s47, s47, s70\n\t"
        "s_xor_b32 s47, s47, 0x2000\n\t"
        "s_sub_u32 s47, s47, 2097152\n\t"
        "s_lshl_b32 s71, %[h], 14\n\t"
        "s_add_u32 s49, s71, s70\n\t"
        "s_mov_b32 s50, s48\n\t"
        "s_lshr_b32 s71, %[h], 4\n\t"
        "s_lshl_b32 s74, s71, 14\n\t"
        "s_and_b32 s71, %[h], 15\n\t"
        "s_lshl_b32 s71, s71, 9\n\t"
        "s_add_u32 s74, s74, s71\n\t"
        "s_lshr_b32 s71, %[h], 3\n\t"
        "s_lshl_b32 s75, s71, 10\n\t"
        "s_bfe_u32 s72, %[h], 0x20001\n\t"
        "s_lshl_b32 s73, s72, 8\n\t"
        "s_add_u32 s75, s75, s73\n\t"
        "s_and_b32 s73, %[h], 1\n\t"
        "s_lshl_b32 s73, s73, 3\n\t"
        "s_add_u32 s75, s75, s73\n\t"
        "s_add_u32 s75, s75, 32768\n\t"
        "s_lshl_b32 s71, s71, 15\n\t"
        "s_add_u32 s71, s71, 196608\n\t"
        "s_and_b32 s70, %[h], 7\n\t"
        "s_lshl_b32 s76, s70, 10\n\t"
        "s_add_u32 s76, s76, s71\n\t"
        "s_lshl_b32 s77, s72, 10\n\t"
        "s_add_u32 s77, s77, s73\n\t"
        "s_add_u32 s77, s77, s71\n\t"
        "s_add_u32 s77, s77, 16384\n\t"
        "s_lshl_b32 s78, s70, 8\n\t"
        "s_add_u32 s78, s78, s71\n\t"
        "s_add_u32 s78, s78, 20480\n\t"
        "v_mbcnt_lo_u32_b32 v52, -1, 0\n\t"
        "v_mbcnt_hi_u32_b32 v52, -1, v52\n\t"
        "v_mov_b64 v[4:5], 0\n\t"
        "v_mov_b64 v[6:7], 0\n\t"
        "v_mov_b64 v[8:9], 0\n\t"
        "v_mov_b64 v[10:11], 0\n\t"
        "v_lshrrev_b32 v53, 4, v52\n\t"
        "v_lshlrev_b32 v53, 3, v53\n\t"
        "s_cmp_ge_u32 %[w], 4\n\t"
        "s_cbranch_scc1 .Ls_kpro_%=\n\t"
        "s_mov_b32 s52, 147456\n\t"
        "s_add_u32 s49, s49, 83886080\n\t"
        "s_sub_u32 s70, %[w], 1\n\t"
        "s_lshl_b32 s70, s70, 12\n\t"
        "s_add_u32 s49, s49, s70\n\t"
        "v_mov_b32 v20, v12\n\t"
        "v_mov_b32 v21, v13\n\t"
        "v_mov_b32 v22, v14\n\t"
        "v_mov_b32 v23, v15\n\t"
        "v_mov_b32 v24, v16\n\t"
        "v_mov_b32 v25, v17\n\t"
        "v_mov_b32 v26, v18\n\t"
        "v_mov_b32 v27, v19\n\t"
        "s_and_b32 s70, s45, 1\n\t"
        "s_lshl_b32 s70, s70, 13\n\t"
        "s_add_u32 s71, s70, %[kx]\n\t"
        "v_bfe_u32 v12, v0, 4, 1\n\t"
        "v_bfe_u32 v13, v0, 8, 1\n\t"
        "v_lshrrev_b32 v14, 9, v0\n\t"
        "v_and_b32 v15, 0xe0, v0\n\t"
        "v_and_b32 v16, 0xf0, v0\n\t"
        "v_bfe_u32 v17, v0, 6, 2\n\t"
        "v_lshrrev_b32 v18, 8, v0\n\t"
        "v_lshrrev_b32 v4, 1, v15\n\t"
        "v_lshl_add_u32 v4, v12, 9, v4\n\t"
        "v_lshl_add_u32 v4, v13, 8, v4\n\t"
        "v_lshl_add_u32 v4, v14, 10, v4\n\t"
        "v_add_u32 v4, s71, v4\n\t"
        "v_bfe_u32 v5, v0, 4, 4\n\t"
        "v_xor_b32 v5, v5, v18\n\t"
        "v_lshlrev_b32 v5, 3, v5\n\t"
        "v_mul_u32_u24 v6, 384, v18\n\t"
        "v_add3_u32 v5, v5, v6, s52\n\t"
        "s_lshl_b32 s71, %[w], 2\n\t"
        "s_add_u32 s71, s71, 156672\n\t"
        "v_add_u32 v6, s71, v0\n\t"
        "v_lshl_add_u32 v7, v12, 2, v18\n\t"
        "v_xor_b32 v7, v7, v17\n\t"
        "v_lshlrev_b32 v7, 3, v7\n\t"
        "v_lshl_add_u32 v7, v15, 2, v7\n\t"
        "v_lshl_add_u32 v7, v17, 7, v7\n\t"
        "s_mul_i32 s71, %[w], 1536\n\t"
        "s_add_u32 s71, s71, s52\n\t"
        "v_add_u32 v7, s71, v7\n\t"
        "v_lshl_add_u32 v8, v13, 3, v16\n\t"
        "v_lshl_add_u32 v8, v14, 8, v8\n\t"
        "v_lshl_add_u32 v9, v13, 4, v15\n\t"
        "v_lshl_add_u32 v9, v14, 2, v9\n\t"
        "v_lshlrev_b32 v10, 13, v12\n\t"
        "s_and_b32 s71, s50, 0x2000\n\t"
        "v_xor_b32 v10, s71, v10\n\t"
        "v_add_u32 v9, v9, v10\n\t"
        "s_lshl_b32 s71, %[w], 10\n\t"
        "s_add_u32 s71, s71, 158720\n\t"
        "v_add_u32 v55, s71, v0\n\t"
        "v_add_u32 v14, 162816, v0\n\t"
        "v_and_b32 v10, 0x1f0, v0\n\t"
        "v_lshlrev_b32 v10, 1, v10\n\t"
        "ds_write_b128 v55, v[4:7]\n\t"
        "ds_write_b128 v14, v[8:11]\n\t"
        "s_xor_b32 s48, s50, %[tw]\n\t"
        "v_mov_b32 v12, 0xffbfffbf\n\t"
        "v_mov_b32 v13, v12\n\t"
        "ds_write_b64 v7, v[12:13]\n\t"
        "ds_write_b64 v7, v[12:13] offset:64\n\t"
        "s_add_u32 s72, s50, s74\n\t"
        "s_xor_b32 s73, s72, 0x2000\n\t"
        "s_andn2_b32 s67, s50, 0x2000\n\t"
        "s_add_u32 s67, s67, s75\n\t"
        "v_mov_b32 v12, v20\n\t"
        "v_mov_b32 v13, v21\n\t"
        "v_mov_b32 v14, v22\n\t"
        "v_mov_b32 v15, v23\n\t"
        "v_mov_b32 v16, v24\n\t"
        "v_mov_b32 v17, v25\n\t"
        "v_mov_b32 v18, v26\n\t"
        "v_mov_b32 v19, v27\n\t"
        "s_cmp_eq_u32 %[w], 3\n\t"
        "s_cbranch_scc0 .Ls_na_%=\n\t"
        "s_add_u32 s70, s50, 2097152\n\t"
        "s_xor_b32 s70, s70, 0x2000\n\t"
        "v_mov_b32 v24, 0xffbfffbf\n\t"
        "v_mov_b32 v25, v24\n\t"
        "v_mov_b32 v26, v24\n\t"
        "v_mov_b32 v27, v24\n\t"
        "s_bitcmp0_b32 %[h], 0\n\t"
        "s_cselect_b32 s71, s70, 0x7f000000\n\t"
        "v_add_u32 v20, s74, v0\n\t"
        "v_and_b32 v21, 0xf0, v0\n\t"
        "v_bfe_u32 v22, v0, 8, 1\n\t"
        "v_lshl_add_u32 v21, v22, 11, v21\n\t"
        "v_add_u32 v21, s78, v21\n\t"
        "v_cmp_gt_u32 vcc, 512, v0\n\t"
        "v_lshrrev_b32 v22, 8, v0\n\t"
        "v_lshl_add_u32 v22, v22, 29, v0\n\t"
        "v_cndmask_b32 v20, v21, v20, vcc\n\t"
        "s_add_u32 s72, s70, s76\n\t"
        "s_add_u32 s73, s71, s77\n\t"
        "buffer_store_dwordx4 v[24:27], v0, %[rs], s72 offen sc1\n\t"
        "buffer_store_dwordx4 v[24:27], v0, %[rs], s73 offen sc1\n\t"
        "s_add_u32 s72, s71, s75\n\t"
        "buffer_store_dwordx4 v[24:27], v20, %[rs], s70 offen sc1\n\t"
        "buffer_store_dwordx4 v[24:27], v22, %[rs], s72 offen sc1\n\t"
        "s_xor_b32 s70, s70, 0x2000\n\t"
        "s_xor_b32 s71, s71, 0x2000\n\t"
        "s_add_u32 s72, s70, s76\n\t"
        "s_add_u32 s73, s71, s77\n\t"
        "buffer_store_dwordx4 v[24:27], v0, %[rs], s72 offen sc1\n\t"
        "buffer_store_dwordx4 v[24:27], v0, %[rs], s73 offen sc1\n\t"
        "s_add_u32 s72, s71, s75\n\t"
        "buffer_store_dwordx4 v[24:27], v20, %[rs], s70 offen sc1\n\t"
        "buffer_store_dwordx4 v[24:27], v22, %[rs], s72 offen sc1\n\t"
        "s_waitcnt vmcnt(0)\n\t"
        ".Ls_na_%=:\n\t"
        "v_mov_b64 v[4:5], 0\n\t"
        "v_mov_b64 v[6:7], 0\n\t"
        "v_mov_b64 v[8:9], 0\n\t"
        "v_mov_b64 v[10:11], 0\n\t"
        "v_xor_b32 v52, 0x800, v2\n\t"
        "v_xor_b32 v53, 0x1000, v2\n\t"
        "s_setprio 2\n\t"
        "s_waitcnt lgkmcnt(0)\n\t"
        "s_branch .Ls_q0r_%=\n\t"
        ".p2align 6\n\t"
        ".Ls_qloop_%=:\n\t"
        "s_barrier\n\t"
        "buffer_load_dwordx4 v[12:15], v0, %[rs], s47 offen sc1\n\t"
        "s_nop 4\n\t"
        "buffer_load_dwordx4 v[16:19], v0, %[rs], s47 offen offset:1024 sc1\n\t"
        ".Ls_q0r_%=:\n\t"
        "s_waitcnt vmcnt(1)\n\t"
        "ds_write_b128 v2, v[12:15] offset:0\n\t"
        "v_smfmac_f32_16x16x64_bf16 v[4:7], v[12:15], v[56:63], v1\n\t"
        "v_max_u32 v54, v12, v13\n\t"
        "v_smfmac_f32_16x16x64_bf16 v[8:11], v[12:15], v[64:71], v1\n\t"
        "v_max3_u32 v54, v54, v14, v15\n\t"
        "s_waitcnt vmcnt(0)\n\t"
        "ds_write_b128 v2, v[16:19] offset:1024\n\t"
        "v_max3_u32 v54, v54, v16, v17\n\t"
        "v_max3_u32 v54, v54, v18, v19\n\t"
        "v_smfmac_f32_16x16x64_bf16 v[4:7], v[16:19], v[72:79], v1\n\t"
        "v_cmp_eq_u32 vcc, 0xffbfffbf, v54\n\t"
        "v_smfmac_f32_16x16x64_bf16 v[8:11], v[16:19], v[80:87], v1\n\t"
        "s_waitcnt lgkmcnt(0)\n\t"
        "s_cbranch_vccnz .Ls_q0f_%=\n\t"
        ".Ls_q0k_%=:\n\t"
        "s_barrier\n\t"
        "ds_read_b128 v[20:23], v52 offset:0\n\t"
        "ds_read_b128 v[24:27], v52 offset:1024\n\t"
        "ds_read_b128 v[12:15], v53 offset:0\n\t"
        "ds_read_b128 v[16:19], v53 offset:1024\n\t"
        "s_cmp_eq_u32 s45, 63\n\t"
        "s_cbranch_scc1 .Ls_qlast_%=\n\t"
        "s_waitcnt lgkmcnt(3)\n\t"
        "v_smfmac_f32_16x16x64_bf16 v[4:7], v[20:23], v[88:95], v1\n\t"
        "v_smfmac_f32_16x16x64_bf16 v[8:11], v[20:23], v[28:35], v1\n\t"
        "ds_read_b128 v[20:23], v3 offset:0\n\t"
        "s_waitcnt lgkmcnt(3)\n\t"
        "v_smfmac_f32_16x16x64_bf16 v[4:7], v[24:27], v[96:103], v1\n\t"
        "v_smfmac_f32_16x16x64_bf16 v[8:11], v[24:27], v[104:111], v1\n\t"
        "ds_read_b128 v[28:31], v2 offset:16384\n\t"
        "ds_read_b128 v[32:35], v2 offset:17408\n\t"
        "ds_read_b128 v[24:27], v3 offset:1024\n\t"
        "s_waitcnt lgkmcnt(5)\n\t"
        "v_smfmac_f32_16x16x64_bf16 v[4:7], v[12:15], v[112:119], v1\n\t"
        "v_smfmac_f32_16x16x64_bf16 v[8:11], v[12:15], v[36:43], v1\n\t"
        "ds_read_b128 v[12:15], v2 offset:8192\n\t"
        "s_waitcnt lgkmcnt(5)\n\t"
        "v_smfmac_f32_16x16x64_bf16 v[4:7], v[16:19], v[120:127], v1\n\t"
        "v_smfmac_f32_16x16x64_bf16 v[8:11], v[16:19], v[128:135], v1\n\t"
        "ds_read_b128 v[36:39], v2 offset:32768\n\t"
        "ds_read_b128 v[40:43], v2 offset:33792\n\t"
        "ds_read_b128 v[16:19], v2 offset:9216\n\t"
        "s_waitcnt lgkmcnt(7)\n\t"
        "v_smfmac_f32_16x16x64_bf16 v[4:7], v[20:23], v[136:143], v1\n\t"
        "v_smfmac_f32_16x16x64_bf16 v[8:11], v[20:23], v[44:51], v1\n\t"
        "ds_read_b128 v[20:23], v52 offset:8192\n\t"
        "s_waitcnt lgkmcnt(5)\n\t"
        "v_smfmac_f32_16x16x64_bf16 v[4:7], v[24:27], v[144:151], v1\n\t"
        "v_smfmac_f32_16x16x64_bf16 v[8:11], v[24:27], v[152:159], v1\n\t"
        "ds_read_b128 v[44:47], v2 offset:49152\n\t"
        "ds_read_b128 v[48:51], v2 offset:50176\n\t"
        "ds_read_b128 v[24:27], v52 offset:9216\n\t"
        "s_waitcnt lgkmcnt(7)\n\t"
        "v_smfmac_f32_16x16x64_bf16 v[4:7], v[12:15], v[160:167], v1\n\t"
        "v_smfmac_f32_16x16x64_bf16 v[8:11], v[12:15], v[168:175], v1\n\t"
        "ds_read_b128 v[12:15], v53 offset:8192\n\t"
        "s_waitcnt lgkmcnt(5)\n\t"
        "v_smfmac_f32_16x16x64_bf16 v[4:7], v[16:19], v[176:183], v1\n\t"
        "v_smfmac_f32_16x16x64_bf16 v[8:11], v[16:19], v[28:35], v1\n\t"
        "v_add_u32 v54, 114688, v2\n\t"
        "ds_read_b128 v[28:31], v54 offset:0\n\t"
        "ds_read_b128 v[32:35], v54 offset:1024\n\t"
        "ds_read_b128 v[16:19], v53 offset:9216\n\t"
        "ds_read_b96 v[52:54], v55\n\t"
        "s_waitcnt lgkmcnt(8)\n\t"
        "v_smfmac_f32_16x16x64_bf16 v[4:7], v[20:23], v[184:191], v1\n\t"
        "v_smfmac_f32_16x16x64_bf16 v[8:11], v[20:23], v[192:199], v1\n\t"
        "ds_read_b128 v[20:23], v3 offset:8192\n\t"
        "s_waitcnt lgkmcnt(6)\n\t"
        "v_smfmac_f32_16x16x64_bf16 v[4:7], v[24:27], v[200:207], v1\n\t"
        "v_smfmac_f32_16x16x64_bf16 v[8:11], v[24:27], v[36:43], v1\n\t"
        "ds_read_b128 v[24:27], v3 offset:9216\n\t"
        "s_waitcnt lgkmcnt(6)\n\t"
        "v_smfmac_f32_16x16x64_bf16 v[4:7], v[12:15], v[208:215], v1\n\t"
        "v_smfmac_f32_16x16x64_bf16 v[8:11], v[12:15], v[216:223], v1\n\t"
        "s_waitcnt lgkmcnt(3)\n\t"
        "v_smfmac_f32_16x16x64_bf16 v[4:7], v[16:19], v[224:231], v1\n\t"
        "v_mov_b64 v[12:13], 0\n\t"
        "v_smfmac_f32_16x16x64_bf16 v[8:11], v[16:19], v[44:51], v1\n\t"
        "v_mov_b64 v[14:15], 0\n\t"
        "s_waitcnt lgkmcnt(2)\n\t"
        "ds_read_b128 v[36:39], v52 offset:0\n\t"
        "ds_read_b128 v[40:43], v52 offset:2048\n\t"
        "ds_read_b128 v[16:19], v55 offset:4096\n\t"
        "ds_read_b32 v55, v55 offset:12\n\t"
        "s_waitcnt lgkmcnt(5)\n\t"
        "v_smfmac_f32_16x16x64_bf16 v[4:7], v[20:23], v[232:239], v1\n\t"
        "v_smfmac_f32_16x16x64_bf16 v[8:11], v[20:23], v[240:247], v1\n\t"
        "s_waitcnt lgkmcnt(4)\n\t"
        "v_smfmac_f32_16x16x64_bf16 v[4:7], v[24:27], v[248:255], v1\n\t"
        "v_mov_b64 v[20:21], 0\n\t"
        "v_smfmac_f32_16x16x64_bf16 v[8:11], v[24:27], v[28:35], v1\n\t"
        "v_mov_b64 v[22:23], 0\n\t"
        "s_nop 4\n\t"
        "v_add_f32 v4, v4, v5\n\t"
        "v_add_f32 v6, v6, v7\n\t"
        "v_mul_f32 v4, s66, v4\n\t"
        "v_add_f32 v8, v8, v9\n\t"
        "v_add_f32 v10, v10, v11\n\t"
        "v_mul_f32 v6, s66, v6\n\t"
        "v_mul_f32 v8, s66, v8\n\t"
        "v_mul_f32 v10, s66, v10\n\t"
        "v_cvt_pk_bf16_f32 v4, v4, v4\n\t"
        "v_cvt_pk_bf16_f32 v5, v6, v6\n\t"
        "v_cvt_pk_bf16_f32 v8, v8, v8\n\t"
        "v_cvt_pk_bf16_f32 v9, v10, v10\n\t"
        "s_waitcnt lgkmcnt(0)\n\t"
        "ds_write_b64 v55, v[4:5]\n\t"
        "ds_write_b64 v55, v[8:9] offset:64\n\t"
        "s_waitcnt lgkmcnt(0)\n\t"
        "s_barrier\n\t"
        ".Ls_xs_%=:\n\t"
        "ds_read_b64_tr_b16 v[28:29], v53 offset:0\n\t"
        "ds_read_b64_tr_b16 v[30:31], v53 offset:128\n\t"
        "ds_read_b64_tr_b16 v[32:33], v53 offset:1536\n\t"
        "ds_read_b64_tr_b16 v[34:35], v53 offset:1664\n\t"
        "ds_read_b64_tr_b16 v[44:45], v53 offset:3072\n\t"
        "ds_read_b64_tr_b16 v[46:47], v53 offset:3200\n\t"
        "ds_read_b64_tr_b16 v[48:49], v53 offset:4608\n\t"
        "ds_read_b64_tr_b16 v[50:51], v53 offset:4736\n\t"
        "s_waitcnt lgkmcnt(4)\n\t"
        "v_smfmac_f32_16x16x64_bf16 v[12:15], v[36:39], v[28:35], v1\n\t"
        "s_waitcnt lgkmcnt(0)\n\t"
        "v_smfmac_f32_16x16x64_bf16 v[12:15], v[40:43], v[44:51], v1\n\t"
        "s_nop 7\n\t"
        "v_cndmask_b32_e64 v8, v14, v12, s[56:57]\n\t"
        "v_cndmask_b32_e64 v9, v15, v13, s[56:57]\n\t"
        "v_add_f32 v8, v8, v9\n\t"
        "v_exp_f32 v9, v8\n\t"
        "v_cmp_u_f32 vcc, v8, v8\n\t"
        "s_cmp_eq_u32 %[w], 0\n\t"
        ".Ls_xk_%=:\n\t"
        "ds_write2st64_b32 v54, v9, v8 offset0:0 offset1:4\n\t"
        "s_cbranch_scc0 .Ls_qoth_%=\n\t"
        "buffer_load_dword v36, v18, %[rs], s48 offen sc1\n\t"
        "s_setprio 3\n\t"
        "s_waitcnt lgkmcnt(0)\n\t"
        "s_barrier\n\t"
        "ds_read_b64_tr_b16 v[4:5], v53 offset:6144\n\t"
        "ds_read_b64_tr_b16 v[6:7], v53 offset:6272\n\t"
        "ds_read_b64_tr_b16 v[8:9], v53 offset:7680\n\t"
        "ds_read_b64_tr_b16 v[10:11], v53 offset:7808\n\t"
        "ds_read_b128 v[24:27], v54\n\t"
        "s_waitcnt lgkmcnt(3)\n\t"
        "v_smfmac_f32_16x16x64_bf16 v[20:23], v[4:7], v[28:35], v1\n\t"
        "s_waitcnt lgkmcnt(1)\n\t"
        "v_smfmac_f32_16x16x64_bf16 v[20:23], v[8:11], v[44:51], v1\n\t"
        "s_waitcnt lgkmcnt(0)\n\t"
        "v_add_f32 v8, v24, v25\n\t"
        "v_add_f32 v9, v26, v27\n\t"
        "v_add_f32 v8, v8, v9\n\t"
        "s_nop 1\n\t"
        "v_add_f32_dpp v8, v8, v8 quad_perm:[1,0,3,2] row_mask:0xf bank_mask:0xf\n\t"
        "s_nop 0\n\t"
        "v_add_f32 v4, v20, v21\n\t"
        "v_add_f32 v6, v22, v23\n\t"
        "v_exp_f32 v5, v4\n\t"
        "v_exp_f32 v7, v6\n\t"
        ".Ls_psum_%=:\n\t"
        "v_add_f32 v8, v8, v5\n\t"
        "v_add_f32 v9, v8, v7\n\t"
        "v_add_f32 v10, v8, v7\n\t"
        "s_nop 1\n\t"
        "v_permlane16_swap_b32 v9, v10\n\t"
        "v_add_f32 v8, v9, v10\n\t"
        "v_add_f32 v11, v9, v10\n\t"
        "s_nop 1\n\t"
        "v_permlane32_swap_b32 v8, v11\n\t"
        "v_add_f32 v8, v8, v11\n\t"
        "v_rcp_f32 v9, v8\n\t"
        "v_subrev_u32 v10, 0x0d800000, v8\n\t"
        "v_cmp_lt_u32 vcc, 0x64000000, v10\n\t"
        "v_mul_f32 v24, v24, v9\n\t"
        "v_mul_f32 v25, v25, v9\n\t"
        "v_mul_f32 v26, v26, v9\n\t"
        "v_mul_f32 v27, v27, v9\n\t"
        "s_cbranch_vccnz .Ls_pmax_%=\n\t"
        ".Ls_pok_%=:\n\t"
        "v_cvt_pk_bf16_f32 v24, v24, v25\n\t"
        "v_cvt_pk_bf16_f32 v25, v26, v27\n\t"
        "v_mul_f32 v5, v5, v9\n\t"
        "buffer_store_dwordx2 v[24:25], v16, %[rs], s72 offen sc1\n\t"
        "v_mul_f32 v7, v7, v9\n\t"
        "buffer_store_dwordx2 v[24:25], v16, %[rs], s73 offen sc1\n\t"
        "v_cvt_pk_bf16_f32 v5, v5, v7\n\t"
        "buffer_store_dword v5, v17, %[rs], s67 offen sc1\n\t"
        "s_sub_u32 s68, s92, s96\n\t"
        "s_sub_u32 s69, s94, s97\n\t"
        "s_mov_b32 s96, s92\n\t"
        "s_mov_b32 s97, s94\n\t"
        "v_cvt_f32_u32 v39, s68\n\t"
        "v_cvt_f32_u32 v38, s69\n\t"
        "v_rcp_f32 v39, v39\n\t"
        "s_nop 0\n\t"
        "v_mul_f32 v39, v39, v38\n\t"
        "v_max_f32 v39, 0x3d2aaaab, v39\n\t"
        "v_min_f32 v39, 0x3d638e39, v39\n\t"
        "v_subrev_f32 v39, s98, v39\n\t"
        "v_mov_b32 v38, s98\n\t"
        "v_fmac_f32 v38, 0x3f000000, v39\n\t"
        "v_mul_f32 v39, 0x44200000, v38\n\t"
        "v_cvt_u32_f32 v39, v39\n\t"
        "v_readfirstlane_b32 s98, v38\n\t"
        ".Ls_twm_%=:\n\t"
        "s_waitcnt vmcnt(3)\n\t"
        "v_cmp_eq_u32 s[68:69], 0, v36\n\t"
        "v_max_u32_dpp v36, v36, v36 row_shr:1 row_mask:0xf bank_mask:0xf\n\t"
        "s_nop 1\n\t"
        "v_max_u32_dpp v36, v36, v36 row_shr:2 row_mask:0xf bank_mask:0xf\n\t"
        "s_nop 1\n\t"
        "v_max_u32_dpp v36, v36, v36 row_shr:4 row_mask:0xf bank_mask:0xf\n\t"
        "s_nop 1\n\t"
        "v_max_u32_dpp v36, v36, v36 row_shr:8 row_mask:0xf bank_mask:0xf\n\t"
        "s_nop 1\n\t"
        "v_max_u32_dpp v36, v36, v36 row_bcast:15 row_mask:0xf bank_mask:0xf\n\t"
        "s_nop 1\n\t"
        "v_readlane_b32 s70, v36, 63\n\t"
        "s_cmp_lg_u64 s[68:69], 0\n\t"
        "v_readfirstlane_b32 s68, v39\n\t"
        "s_cbranch_scc1 .Ls_twx_%=\n\t"
        ".Ls_twy_%=:\n\t"
        "s_add_i32 s70, s70, 49\n\t"
        "s_add_u32 s48, s70, s68\n\t"
        "s_mov_b32 s99, s48\n\t"
        "v_add_u32 v54, 65536, v2\n\t"
        "ds_read_b128 v[28:31], v54 offset:0\n\t"
        "ds_read_b128 v[32:35], v54 offset:1024\n\t"
        "ds_read_b128 v[36:39], v54 offset:16384\n\t"
        "ds_read_b128 v[40:43], v54 offset:17408\n\t"
        "ds_read_b128 v[44:47], v54 offset:32768\n\t"
        "ds_read_b128 v[48:51], v54 offset:33792\n\t"
        "v_xor_b32 v52, 0x800, v2\n\t"
        "v_xor_b32 v53, 0x1000, v2\n\t"
        "v_xor_b32 v3, 0x1800, v2\n\t"
        "s_add_u32 s45, s45, 1\n\t"
        "s_add_u32 s47, s47, 2097152\n\t"
        "s_and_b32 s47, s47, 0xffffff\n\t"
        "s_xor_b32 s47, s47, 0x2000\n\t"
        "s_add_u32 s50, s50, 2097152\n\t"
        "s_and_b32 s50, s50, 0xffffff\n\t"
        "s_xor_b32 s50, s50, 0x2000\n\t"
        "s_add_u32 s49, s49, 0x80000\n\t"
        "s_add_u32 s70, s45, 0\n\t"
        "s_and_b32 s70, s70, 3\n\t"
        "s_cselect_b32 s70, 0, 0x2000\n\t"
        "s_xor_b32 s49, s49, s70\n\t"
        "s_and_b32 s70, s45, 1\n\t"
        "s_lshl_b32 s70, s70, 13\n\t"
        "s_add_u32 s71, s70, %[kx]\n\t"
        "v_bfe_u32 v12, v0, 4, 1\n\t"
        "v_bfe_u32 v13, v0, 8, 1\n\t"
        "v_lshrrev_b32 v14, 9, v0\n\t"
        "v_and_b32 v15, 0xe0, v0\n\t"
        "v_and_b32 v16, 0xf0, v0\n\t"
        "v_bfe_u32 v17, v0, 6, 2\n\t"
        "v_lshrrev_b32 v18, 8, v0\n\t"
        "v_lshrrev_b32 v4, 1, v15\n\t"
        "v_lshl_add_u32 v4, v12, 9, v4\n\t"
        "v_lshl_add_u32 v4, v13, 8, v4\n\t"
        "v_lshl_add_u32 v4, v14, 10, v4\n\t"
        "v_add_u32 v4, s71, v4\n\t"
        "v_bfe_u32 v5, v0, 4, 4\n\t"
        "v_xor_b32 v5, v5, v18\n\t"
        "v_lshlrev_b32 v5, 3, v5\n\t"
        "v_mul_u32_u24 v6, 384, v18\n\t"
        "v_add3_u32 v5, v5, v6, s52\n\t"
        "s_lshl_b32 s71, %[w], 2\n\t"
        "s_add_u32 s71, s71, 156672\n\t"
        "v_add_u32 v6, s71, v0\n\t"
        "v_lshl_add_u32 v7, v12, 2, v18\n\t"
        "v_xor_b32 v7, v7, v17\n\t"
        "v_lshlrev_b32 v7, 3, v7\n\t"
        "v_lshl_add_u32 v7, v15, 2, v7\n\t"
        "v_lshl_add_u32 v7, v17, 7, v7\n\t"
        "s_mul_i32 s71, %[w], 1536\n\t"
        "s_add_u32 s71, s71, s52\n\t"
        "v_add_u32 v7, s71, v7\n\t"
        "v_lshl_add_u32 v8, v13, 3, v16\n\t"
        "v_lshl_add_u32 v8, v14, 8, v8\n\t"
        "v_lshl_add_u32 v9, v13, 4, v15\n\t"
        "v_lshl_add_u32 v9, v14, 2, v9\n\t"
        "v_lshlrev_b32 v10, 13, v12\n\t"
        "s_and_b32 s71, s50, 0x2000\n\t"
        "v_xor_b32 v10, s71, v10\n\t"
        "v_add_u32 v9, v9, v10\n\t"
        "s_lshl_b32 s71, %[w], 10\n\t"
        "s_add_u32 s71, s71, 158720\n\t"
        "v_add_u32 v55, s71, v0\n\t"
        "v_add_u32 v14, 162816, v0\n\t"
        "v_and_b32 v10, 0x1f0, v0\n\t"
        "v_lshlrev_b32 v10, 1, v10\n\t"
        "ds_write_b128 v55, v[4:7]\n\t"
        "ds_write_b128 v14, v[8:11]\n\t"
        "s_xor_b32 s48, s50, %[tw]\n\t"
        "v_mov_b32 v12, 0xffbfffbf\n\t"
        "v_mov_b32 v13, v12\n\t"
        "ds_write_b64 v7, v[12:13]\n\t"
        "ds_write_b64 v7, v[12:13] offset:64\n\t"
        "s_add_u32 s72, s50, s74\n\t"
        "s_xor_b32 s73, s72, 0x2000\n\t"
        "s_andn2_b32 s67, s50, 0x2000\n\t"
        "s_add_u32 s67, s67, s75\n\t"
        "s_nop 0\n\t"
        "v_mov_b64 v[4:5], 0\n\t"
        "v_mov_b64 v[6:7], 0\n\t"
        "v_mov_b64 v[8:9], 0\n\t"
        "v_mov_b64 v[10:11], 0\n\t"
        "s_waitcnt lgkmcnt(0)\n\t"
        "s_setprio 2\n\t"
        ".Ls_twa_%=:\n\t"
        "s_memtime s[92:93]\n\t"
        "s_memrealtime s[94:95]\n\t"
        "s_waitcnt lgkmcnt(0)\n\t"
        "s_sub_i32 s68, s99, s94\n\t"
        "s_sub_u32 s69, s68, 5\n\t"
        "s_cmp_lt_u32 s69, 400\n\t"
        "s_cbranch_scc1 .Ls_twa_%=\n\t"
        "s_cmp_lt_i32 s68, 1\n\t"
        "s_cbranch_scc1 .Ls_twb_%=\n\t"
        "s_nop 2\n\t"
        "s_cmp_lt_i32 s68, 2\n\t"
        "s_cbranch_scc1 .Ls_twb_%=\n\t"
        "s_nop 2\n\t"
        "s_cmp_lt_i32 s68, 3\n\t"
        "s_cbranch_scc1 .Ls_twb_%=\n\t"
        "s_nop 2\n\t"
        "s_cmp_lt_i32 s68, 4\n\t"
        "s_cbranch_scc1 .Ls_twb_%=\n\t"
        "s_nop 2\n\t"
        "s_branch .Ls_twb_%=\n\t"
        ".Ls_twb_%=:\n\t"
        "s_cmp_lt_u32 s45, 64\n\t"
        "s_cbranch_scc1 .Ls_qloop_%=\n\t"
        "s_branch .Ls_end_%=\n\t"
        ".Ls_qoth_%=:\n\t"
        "s_cmp_eq_u32 %[w], 3\n\t"
        "s_cbranch_scc1 .Ls_qarm_%=\n\t"
        "buffer_load_dwordx4 v[28:31], v0, %[rs], s49 offen offset:0 sc1\n\t"
        "buffer_load_dwordx4 v[32:35], v0, %[rs], s49 offen offset:1024 sc1\n\t"
        "buffer_load_dwordx4 v[36:39], v0, %[rs], s49 offen offset:2048 sc1\n\t"
        "buffer_load_dwordx4 v[40:43], v0, %[rs], s49 offen offset:3072 sc1\n\t"
        "s_sub_u32 s73, s45, %[w]\n\t"
        "s_add_u32 s71, s73, 4\n\t"
        "s_min_u32 s71, s71, 64\n\t"
        "s_add_u32 s70, s71, 3\n\t"
        "s_andn2_b32 s70, s70, 3\n\t"
        "s_cmp_le_u32 s71, 16\n\t"
        "s_cselect_b32 s70, s71, s70\n\t"
        "s_lshl_b32 s70, s70, 7\n\t"
        "v_mov_b32 v16, s70\n\t"
        "s_cmp_lt_u32 s51, s71\n\t"
        "s_cselect_b32 s70, 0, 0x70000000\n\t"
        "buffer_load_dwordx2 v[16:17], v16, %[rf], s70 offen sc1\n\t"
        ".Ls_qoth2_%=:\n\t"
        "s_setprio 0\n\t"
        "s_waitcnt lgkmcnt(0)\n\t"
        "s_barrier\n\t"
        "v_bfe_u32 v18, v0, 8, 1\n\t"
        "v_lshrrev_b32 v19, 9, v0\n\t"
        "v_lshlrev_b32 v19, 8, v19\n\t"
        "v_lshl_add_u32 v19, v18, 3, v19\n\t"
        "v_bfe_u32 v18, v0, 4, 1\n\t"
        "v_lshl_add_u32 v19, v18, 7, v19\n\t"
        "v_bfe_u32 v18, v0, 6, 2\n\t"
        "v_lshl_add_u32 v19, v18, 5, v19\n\t"
        "v_bfe_u32 v18, v0, 5, 1\n\t"
        "v_lshl_add_u32 v19, v18, 4, v19\n\t"
        "s_and_b32 s70, s45, 1\n\t"
        "s_lshl_b32 s70, s70, 13\n\t"
        "s_add_u32 s70, s70, 131072\n\t"
        "v_add_u32 v19, s70, v19\n\t"
        "s_cmp_eq_u32 %[w], 1\n\t"
        "s_cbranch_scc0 .Ls_qx1_%=\n\t"
        "ds_read_b64_tr_b16 v[4:5], v53 offset:6144\n\t"
        "ds_read_b64_tr_b16 v[6:7], v53 offset:6272\n\t"
        "ds_read_b64_tr_b16 v[8:9], v53 offset:7680\n\t"
        "ds_read_b64_tr_b16 v[10:11], v53 offset:7808\n\t"
        "ds_read2_b32 v[44:45], v19 offset0:0 offset1:128\n\t"
        "ds_read2_b32 v[46:47], v19 offset0:1 offset1:129\n\t"
        "v_add_u32 v19, 1024, v19\n\t"
        "ds_read2_b32 v[48:49], v19 offset0:0 offset1:128\n\t"
        "ds_read2_b32 v[50:51], v19 offset0:1 offset1:129\n\t"
        "v_add_u32 v19, 1024, v19\n\t"
        "ds_read2_b32 v[24:25], v19 offset0:0 offset1:128\n\t"
        "ds_read2_b32 v[26:27], v19 offset0:1 offset1:129\n\t"
        "v_add_u32 v19, 1024, v19\n\t"
        "ds_read2_b32 v[52:53], v19 offset0:0 offset1:128\n\t"
        "ds_read2_b32 v[54:55], v19 offset0:1 offset1:129\n\t"
        "v_add_u32 v19, 1024, v19\n\t"
        "s_waitcnt vmcnt(0)\n\t"
        "v_readfirstlane_b32 s68, v16\n\t"
        "v_readfirstlane_b32 s69, v17\n\t"
        "s_xor_b32 s68, s68, s54\n\t"
        "s_xor_b32 s69, s69, s55\n\t"
        "s_lshr_b32 s70, s68, 8\n\t"
        "s_or_b32 s70, s70, s69\n\t"
        "s_cmp_eq_u32 s70, 0\n\t"
        "s_cselect_b32 s68, s68, 0\n\t"
        "s_and_b32 s69, s68, 127\n\t"
        "s_max_u32 s51, s51, s69\n\t"
        "s_waitcnt lgkmcnt(10)\n\t"
        "v_smfmac_f32_16x16x64_bf16 v[20:23], v[4:7], v[28:35], v1\n\t"
        "s_waitcnt lgkmcnt(8)\n\t"
        "v_smfmac_f32_16x16x64_bf16 v[20:23], v[8:11], v[36:43], v1\n\t"
        "s_waitcnt lgkmcnt(6)\n\t"
        "v_mfma_f32_16x16x32_bf16 v[12:15], v[44:47], v[28:31], 0\n\t"
        "s_waitcnt lgkmcnt(4)\n\t"
        "v_mfma_f32_16x16x32_bf16 v[12:15], v[48:51], v[32:35], v[12:15]\n\t"
        "s_waitcnt lgkmcnt(2)\n\t"
        "v_mfma_f32_16x16x32_bf16 v[12:15], v[24:27], v[36:39], v[12:15]\n\t"
        "s_waitcnt lgkmcnt(0)\n\t"
        "v_mfma_f32_16x16x32_bf16 v[12:15], v[52:55], v[40:43], v[12:15]\n\t"
        "v_and_b32 v16, 0xf0, v0\n\t"
        "v_lshrrev_b32 v17, 8, v0\n\t"
        "v_lshl_add_u32 v16, v17, 2, v16\n\t"
        "s_nop 0\n\t"
        "v_add_f32 v20, v20, v21\n\t"
        "v_add_f32 v21, v22, v23\n\t"
        "v_mov_b32 v22, 0xff7fffff\n\t"
        "s_nop 1\n\t"
        "v_cndmask_b32_e64 v12, v22, v12, s[58:59]\n\t"
        "v_cndmask_b32_e64 v13, v22, v13, s[60:61]\n\t"
        "v_cndmask_b32_e64 v14, v22, v14, s[62:63]\n\t"
        "v_cndmask_b32_e64 v15, v22, v15, s[64:65]\n\t"
        "v_max3_f32 v8, v20, v21, v12\n\t"
        "v_max3_f32 v8, v8, v13, v14\n\t"
        "v_max_f32 v8, v8, v15\n\t"
        "v_mov_b32 v9, v8\n\t"
        "s_nop 1\n\t"
        "v_permlane16_swap_b32 v8, v9\n\t"
        "v_max_f32 v10, v8, v9\n\t"
        "v_max_f32 v11, v8, v9\n\t"
        "s_nop 1\n\t"
        "v_permlane32_swap_b32 v10, v11\n\t"
        "v_max_f32 v8, v10, v11\n\t"
        "v_mul_f32 v8, s66, v8\n\t"
        "v_fma_f32 v20, v20, s66, -v8\n\t"
        "v_fma_f32 v21, v21, s66, -v8\n\t"
        "v_fma_f32 v12, v12, s66, -v8\n\t"
        "v_fma_f32 v13, v13, s66, -v8\n\t"
        "v_fma_f32 v14, v14, s66, -v8\n\t"
        "v_fma_f32 v15, v15, s66, -v8\n\t"
        "v_exp_f32 v20, v20\n\t"
        "v_exp_f32 v21, v21\n\t"
        "v_exp_f32 v12, v12\n\t"
        "v_exp_f32 v13, v13\n\t"
        "v_exp_f32 v14, v14\n\t"
        "v_exp_f32 v15, v15\n\t"
        "v_add_f32 v8, v20, v21\n\t"
        "v_add_f32 v9, v12, v13\n\t"
        "v_add_f32 v8, v8, v14\n\t"
        "v_add_f32 v9, v9, v15\n\t"
        "v_add_f32 v10, v8, v9\n\t"
        "v_add_f32 v11, v8, v9\n\t"
        "s_nop 1\n\t"
        "v_permlane16_swap_b32 v10, v11\n\t"
        "v_add_f32 v8, v10, v11\n\t"
        "v_add_f32 v9, v10, v11\n\t"
        "s_nop 1\n\t"
        "v_permlane32_swap_b32 v8, v9\n\t"
        "v_add_f32 v8, v8, v9\n\t"
        "v_rcp_f32 v8, v8\n\t"
        "s_add_u32 s70, s50, s77\n\t"
        "s_add_u32 s71, s50, s78\n\t"
        "v_mul_f32 v20, v20, v8\n\t"
        "v_mul_f32 v21, v21, v8\n\t"
        "v_mul_f32 v12, v12, v8\n\t"
        "v_mul_f32 v13, v13, v8\n\t"
        "v_mul_f32 v14, v14, v8\n\t"
        "v_mul_f32 v15, v15, v8\n\t"
        "v_cvt_pk_bf16_f32 v20, v20, v21\n\t"
        "v_cvt_pk_bf16_f32 v24, v12, v13\n\t"
        "v_cvt_pk_bf16_f32 v25, v14, v15\n\t"
        "buffer_store_dwordx2 v[24:25], v0, %[rs], s70 offen sc1\n\t"
        "buffer_store_dword v20, v16, %[rs], s71 offen offset:0 sc1\n\t"
        "s_xor_b32 s70, s70, 0x2000\n\t"
        "s_xor_b32 s71, s71, 0x2000\n\t"
        "buffer_store_dwordx2 v[24:25], v0, %[rs], s70 offen sc1\n\t"
        "buffer_store_dword v20, v16, %[rs], s71 offen offset:0 sc1\n\t"
        "s_add_u32 s71, s45, 3\n\t"
        "s_min_u32 s71, s71, 64\n\t"
        "s_cmp_lt_u32 s45, 3\n\t"
        "s_cselect_b32 s71, 0, s71\n\t"
        "s_cmp_ge_u32 s51, s71\n\t"
        "s_cbranch_scc0 .Ls_fq0p_%=\n\t"
        ".Ls_fq0k_%=:\n\t"
        "s_branch .Ls_qend_%=\n\t"
        ".Ls_qx1_%=:\n\t"
        "ds_read_b64_tr_b16 v[4:5], v53 offset:6144\n\t"
        "ds_read_b64_tr_b16 v[6:7], v53 offset:6272\n\t"
        "ds_read_b64_tr_b16 v[8:9], v53 offset:7680\n\t"
        "ds_read_b64_tr_b16 v[10:11], v53 offset:7808\n\t"
        "ds_read2_b32 v[44:45], v19 offset0:0 offset1:128\n\t"
        "ds_read2_b32 v[46:47], v19 offset0:1 offset1:129\n\t"
        "v_add_u32 v19, 1024, v19\n\t"
        "ds_read2_b32 v[48:49], v19 offset0:0 offset1:128\n\t"
        "ds_read2_b32 v[50:51], v19 offset0:1 offset1:129\n\t"
        "v_add_u32 v19, 1024, v19\n\t"
        "ds_read2_b32 v[24:25], v19 offset0:0 offset1:128\n\t"
        "ds_read2_b32 v[26:27], v19 offset0:1 offset1:129\n\t"
        "v_add_u32 v19, 1024, v19\n\t"
        "ds_read2_b32 v[52:53], v19 offset0:0 offset1:128\n\t"
        "ds_read2_b32 v[54:55], v19 offset0:1 offset1:129\n\t"
        "v_add_u32 v19, 1024, v19\n\t"
        "s_waitcnt vmcnt(0)\n\t"
        "v_readfirstlane_b32 s68, v16\n\t"
        "v_readfirstlane_b32 s69, v17\n\t"
        "s_xor_b32 s68, s68, s54\n\t"
        "s_xor_b32 s69, s69, s55\n\t"
        "s_lshr_b32 s70, s68, 8\n\t"
        "s_or_b32 s70, s70, s69\n\t"
        "s_cmp_eq_u32 s70, 0\n\t"
        "s_cselect_b32 s68, s68, 0\n\t"
        "s_and_b32 s69, s68, 127\n\t"
        "s_max_u32 s51, s51, s69\n\t"
        "s_waitcnt lgkmcnt(10)\n\t"
        "v_smfmac_f32_16x16x64_bf16 v[20:23], v[4:7], v[28:35], v1\n\t"
        "s_waitcnt lgkmcnt(8)\n\t"
        "v_smfmac_f32_16x16x64_bf16 v[20:23], v[8:11], v[36:43], v1\n\t"
        "s_waitcnt lgkmcnt(6)\n\t"
        "v_mfma_f32_16x16x32_bf16 v[12:15], v[44:47], v[28:31], 0\n\t"
        "ds_read2_b32 v[44:45], v19 offset0:0 offset1:128\n\t"
        "ds_read2_b32 v[46:47], v19 offset0:1 offset1:129\n\t"
        "v_add_u32 v19, 1024, v19\n\t"
        "s_waitcnt lgkmcnt(6)\n\t"
        "v_mfma_f32_16x16x32_bf16 v[12:15], v[48:51], v[32:35], v[12:15]\n\t"
        "ds_read2_b32 v[48:49], v19 offset0:0 offset1:128\n\t"
        "ds_read2_b32 v[50:51], v19 offset0:1 offset1:129\n\t"
        "v_add_u32 v19, 1024, v19\n\t"
        "s_waitcnt lgkmcnt(6)\n\t"
        "v_mfma_f32_16x16x32_bf16 v[12:15], v[24:27], v[36:39], v[12:15]\n\t"
        "ds_read2_b32 v[24:25], v19 offset0:0 offset1:128\n\t"
        "ds_read2_b32 v[26:27], v19 offset0:1 offset1:129\n\t"
        "v_add_u32 v19, 1024, v19\n\t"
        "s_waitcnt lgkmcnt(6)\n\t"
        "v_mfma_f32_16x16x32_bf16 v[12:15], v[52:55], v[40:43], v[12:15]\n\t"
        "ds_read2_b32 v[52:53], v19 offset0:0 offset1:128\n\t"
        "ds_read2_b32 v[54:55], v19 offset0:1 offset1:129\n\t"
        "v_add_u32 v19, 1024, v19\n\t"
        "s_waitcnt lgkmcnt(6)\n\t"
        "v_mfma_f32_16x16x32_bf16 v[4:7], v[44:47], v[28:31], 0\n\t"
        "s_waitcnt lgkmcnt(4)\n\t"
        "v_mfma_f32_16x16x32_bf16 v[4:7], v[48:51], v[32:35], v[4:7]\n\t"
        "s_waitcnt lgkmcnt(2)\n\t"
        "v_mfma_f32_16x16x32_bf16 v[4:7], v[24:27], v[36:39], v[4:7]\n\t"
        "s_waitcnt lgkmcnt(0)\n\t"
        "v_mfma_f32_16x16x32_bf16 v[4:7], v[52:55], v[40:43], v[4:7]\n\t"
        "v_and_b32 v16, 0xf0, v0\n\t"
        "v_lshrrev_b32 v17, 8, v0\n\t"
        "v_lshl_add_u32 v16, v17, 2, v16\n\t"
        "s_nop 0\n\t"
        "v_add_f32 v20, v20, v21\n\t"
        "v_add_f32 v21, v22, v23\n\t"
        "v_mov_b32 v22, 0xff7fffff\n\t"
        "s_nop 1\n\t"
        "v_cndmask_b32_e64 v4, v22, v4, s[58:59]\n\t"
        "v_cndmask_b32_e64 v5, v22, v5, s[60:61]\n\t"
        "v_cndmask_b32_e64 v6, v22, v6, s[62:63]\n\t"
        "v_cndmask_b32_e64 v7, v22, v7, s[64:65]\n\t"
        "v_max3_f32 v8, v20, v21, v12\n\t"
        "v_max3_f32 v8, v8, v13, v14\n\t"
        "v_max_f32 v8, v8, v15\n\t"
        "v_max3_f32 v8, v8, v4, v5\n\t"
        "v_max3_f32 v8, v8, v6, v7\n\t"
        "v_mov_b32 v9, v8\n\t"
        "s_nop 1\n\t"
        "v_permlane16_swap_b32 v8, v9\n\t"
        "v_max_f32 v10, v8, v9\n\t"
        "v_max_f32 v11, v8, v9\n\t"
        "s_nop 1\n\t"
        "v_permlane32_swap_b32 v10, v11\n\t"
        "v_max_f32 v8, v10, v11\n\t"
        "v_mul_f32 v8, s66, v8\n\t"
        "v_fma_f32 v20, v20, s66, -v8\n\t"
        "v_fma_f32 v21, v21, s66, -v8\n\t"
        "v_fma_f32 v12, v12, s66, -v8\n\t"
        "v_fma_f32 v13, v13, s66, -v8\n\t"
        "v_fma_f32 v14, v14, s66, -v8\n\t"
        "v_fma_f32 v15, v15, s66, -v8\n\t"
        "v_fma_f32 v4, v4, s66, -v8\n\t"
        "v_fma_f32 v5, v5, s66, -v8\n\t"
        "v_fma_f32 v6, v6, s66, -v8\n\t"
        "v_fma_f32 v7, v7, s66, -v8\n\t"
        "v_exp_f32 v20, v20\n\t"
        "v_exp_f32 v21, v21\n\t"
        "v_exp_f32 v12, v12\n\t"
        "v_exp_f32 v13, v13\n\t"
        "v_exp_f32 v14, v14\n\t"
        "v_exp_f32 v15, v15\n\t"
        "v_exp_f32 v4, v4\n\t"
        "v_exp_f32 v5, v5\n\t"
        "v_exp_f32 v6, v6\n\t"
        "v_exp_f32 v7, v7\n\t"
        "v_add_f32 v8, v20, v21\n\t"
        "v_add_f32 v9, v12, v13\n\t"
        "v_add_f32 v8, v8, v14\n\t"
        "v_add_f32 v9, v9, v15\n\t"
        "v_add_f32 v8, v8, v4\n\t"
        "v_add_f32 v9, v9, v5\n\t"
        "v_add_f32 v8, v8, v6\n\t"
        "v_add_f32 v9, v9, v7\n\t"
        "v_add_f32 v10, v8, v9\n\t"
        "v_add_f32 v11, v8, v9\n\t"
        "s_nop 1\n\t"
        "v_permlane16_swap_b32 v10, v11\n\t"
        "v_add_f32 v8, v10, v11\n\t"
        "v_add_f32 v9, v10, v11\n\t"
        "s_nop 1\n\t"
        "v_permlane32_swap_b32 v8, v9\n\t"
        "v_add_f32 v8, v8, v9\n\t"
        "v_rcp_f32 v8, v8\n\t"
        "s_add_u32 s70, s50, s76\n\t"
        "s_add_u32 s71, s50, s78\n\t"
        "v_mul_f32 v20, v20, v8\n\t"
        "v_mul_f32 v21, v21, v8\n\t"
        "v_mul_f32 v12, v12, v8\n\t"
        "v_mul_f32 v13, v13, v8\n\t"
        "v_mul_f32 v14, v14, v8\n\t"
        "v_mul_f32 v15, v15, v8\n\t"
        "v_mul_f32 v4, v4, v8\n\t"
        "v_mul_f32 v5, v5, v8\n\t"
        "v_mul_f32 v6, v6, v8\n\t"
        "v_mul_f32 v7, v7, v8\n\t"
        "v_cvt_pk_bf16_f32 v20, v20, v21\n\t"
        "v_cvt_pk_bf16_f32 v24, v12, v13\n\t"
        "v_cvt_pk_bf16_f32 v25, v14, v15\n\t"
        "v_cvt_pk_bf16_f32 v26, v4, v5\n\t"
        "v_cvt_pk_bf16_f32 v27, v6, v7\n\t"
        "buffer_store_dwordx4 v[24:27], v0, %[rs], s70 offen sc1\n\t"
        "buffer_store_dword v20, v16, %[rs], s71 offen offset:2048 sc1\n\t"
        "s_xor_b32 s70, s70, 0x2000\n\t"
        "s_xor_b32 s71, s71, 0x2000\n\t"
        "buffer_store_dwordx4 v[24:27], v0, %[rs], s70 offen sc1\n\t"
        "buffer_store_dword v20, v16, %[rs], s71 offen offset:2048 sc1\n\t"
        "s_add_u32 s71, s45, 2\n\t"
        "s_min_u32 s71, s71, 64\n\t"
        "s_cmp_ge_u32 s51, s71\n\t"
        "s_cbranch_scc0 .Ls_fq1p_%=\n\t"
        ".Ls_fq1k_%=:\n\t"
        "s_branch .Ls_qend_%=\n\t"
        ".Ls_qlast_%=:\n\t"
        "ds_read_b96 v[52:54], v55\n\t"
        "s_waitcnt lgkmcnt(0)\n\t"
        "v_mov_b64 v[20:21], 0\n\t"
        "v_mov_b64 v[22:23], 0\n\t"
        "s_sub_u32 s70, %[w], 1\n\t"
        "s_cmp_lt_u32 s70, 2\n\t"
        "s_cbranch_scc0 .Ls_qlast2_%=\n\t"
        "buffer_load_dwordx4 v[28:31], v0, %[rs], s49 offen offset:0 sc1\n\t"
        "buffer_load_dwordx4 v[32:35], v0, %[rs], s49 offen offset:1024 sc1\n\t"
        "buffer_load_dwordx4 v[36:39], v0, %[rs], s49 offen offset:2048 sc1\n\t"
        "buffer_load_dwordx4 v[40:43], v0, %[rs], s49 offen offset:3072 sc1\n\t"
        "s_barrier\n\t"
        "s_branch .Ls_qoth2_%=\n\t"
        ".Ls_qlast2_%=:\n\t"
        "s_barrier\n\t"
        "s_barrier\n\t"
        "s_branch .Ls_end_%=\n\t"
        ".Ls_qarm_%=:\n\t"
        "s_setprio 0\n\t"
        "s_waitcnt lgkmcnt(0)\n\t"
        "s_barrier\n\t"
        "s_nop 15\n\t"
        "s_nop 15\n\t"
        "s_nop 15\n\t"
        "s_nop 15\n\t"
        "s_nop 15\n\t"
        "s_nop 15\n\t"
        "s_nop 3\n\t"
        "s_add_u32 s70, s50, 4194304\n\t"
        "s_and_b32 s70, s70, 0xffffff\n\t"
        "s_cmp_lt_u32 s45, 62\n\t"
        "s_cselect_b32 s70, s70, 0x7f000000\n\t"
        "v_mov_b32 v24, 0xffbfffbf\n\t"
        "v_mov_b32 v25, v24\n\t"
        "v_mov_b32 v26, v24\n\t"
        "v_mov_b32 v27, v24\n\t"
        "s_bitcmp0_b32 %[h], 0\n\t"
        "s_cselect_b32 s71, s70, 0x7f000000\n\t"
        "v_add_u32 v20, s74, v0\n\t"
        "v_and_b32 v21, 0xf0, v0\n\t"
        "v_bfe_u32 v22, v0, 8, 1\n\t"
        "v_lshl_add_u32 v21, v22, 11, v21\n\t"
        "v_add_u32 v21, s78, v21\n\t"
        "v_cmp_gt_u32 vcc, 512, v0\n\t"
        "v_lshrrev_b32 v22, 8, v0\n\t"
        "v_lshl_add_u32 v22, v22, 29, v0\n\t"
        "v_cndmask_b32 v20, v21, v20, vcc\n\t"
        "s_add_u32 s72, s70, s76\n\t"
        "s_add_u32 s73, s71, s77\n\t"
        "buffer_store_dwordx4 v[24:27], v0, %[rs], s72 offen sc1\n\t"
        "buffer_store_dwordx4 v[24:27], v0, %[rs], s73 offen sc1\n\t"
        "s_add_u32 s72, s71, s75\n\t"
        "buffer_store_dwordx4 v[24:27], v20, %[rs], s70 offen sc1\n\t"
        "buffer_store_dwordx4 v[24:27], v22, %[rs], s72 offen sc1\n\t"
        "s_xor_b32 s70, s70, 0x2000\n\t"
        "s_xor_b32 s71, s71, 0x2000\n\t"
        "s_add_u32 s72, s70, s76\n\t"
        "s_add_u32 s73, s71, s77\n\t"
        "buffer_store_dwordx4 v[24:27], v0, %[rs], s72 offen sc1\n\t"
        "buffer_store_dwordx4 v[24:27], v0, %[rs], s73 offen sc1\n\t"
        "s_add_u32 s72, s71, s75\n\t"
        "buffer_store_dwordx4 v[24:27], v20, %[rs], s70 offen sc1\n\t"
        "buffer_store_dwordx4 v[24:27], v22, %[rs], s72 offen sc1\n\t"
        ".Ls_qend_%=:\n\t"
        "v_add_u32 v54, 65536, v2\n\t"
        "ds_read_b128 v[28:31], v54 offset:0\n\t"
        "ds_read_b128 v[32:35], v54 offset:1024\n\t"
        "ds_read_b128 v[36:39], v54 offset:16384\n\t"
        "ds_read_b128 v[40:43], v54 offset:17408\n\t"
        "ds_read_b128 v[44:47], v54 offset:32768\n\t"
        "ds_read_b128 v[48:51], v54 offset:33792\n\t"
        "v_xor_b32 v52, 0x800, v2\n\t"
        "v_xor_b32 v53, 0x1000, v2\n\t"
        "v_xor_b32 v3, 0x1800, v2\n\t"
        "s_add_u32 s45, s45, 1\n\t"
        "s_add_u32 s47, s47, 2097152\n\t"
        "s_and_b32 s47, s47, 0xffffff\n\t"
        "s_xor_b32 s47, s47, 0x2000\n\t"
        "s_add_u32 s50, s50, 2097152\n\t"
        "s_and_b32 s50, s50, 0xffffff\n\t"
        "s_xor_b32 s50, s50, 0x2000\n\t"
        "s_add_u32 s49, s49, 0x80000\n\t"
        "s_add_u32 s70, s45, 0\n\t"
        "s_and_b32 s70, s70, 3\n\t"
        "s_cselect_b32 s70, 0, 0x2000\n\t"
        "s_xor_b32 s49, s49, s70\n\t"
        "s_and_b32 s70, s45, 1\n\t"
        "s_lshl_b32 s70, s70, 13\n\t"
        "s_add_u32 s71, s70, %[kx]\n\t"
        "v_bfe_u32 v12, v0, 4, 1\n\t"
        "v_bfe_u32 v13, v0, 8, 1\n\t"
        "v_lshrrev_b32 v14, 9, v0\n\t"
        "v_and_b32 v15, 0xe0, v0\n\t"
        "v_and_b32 v16, 0xf0, v0\n\t"
        "v_bfe_u32 v17, v0, 6, 2\n\t"
        "v_lshrrev_b32 v18, 8, v0\n\t"
        "v_lshrrev_b32 v4, 1, v15\n\t"
        "v_lshl_add_u32 v4, v12, 9, v4\n\t"
        "v_lshl_add_u32 v4, v13, 8, v4\n\t"
        "v_lshl_add_u32 v4, v14, 10, v4\n\t"
        "v_add_u32 v4, s71, v4\n\t"
        "v_bfe_u32 v5, v0, 4, 4\n\t"
        "v_xor_b32 v5, v5, v18\n\t"
        "v_lshlrev_b32 v5, 3, v5\n\t"
        "v_mul_u32_u24 v6, 384, v18\n\t"
        "v_add3_u32 v5, v5, v6, s52\n\t"
        "s_lshl_b32 s71, %[w], 2\n\t"
        "s_add_u32 s71, s71, 156672\n\t"
        "v_add_u32 v6, s71, v0\n\t"
        "v_lshl_add_u32 v7, v12, 2, v18\n\t"
        "v_xor_b32 v7, v7, v17\n\t"
        "v_lshlrev_b32 v7, 3, v7\n\t"
        "v_lshl_add_u32 v7, v15, 2, v7\n\t"
        "v_lshl_add_u32 v7, v17, 7, v7\n\t"
        "s_mul_i32 s71, %[w], 1536\n\t"
        "s_add_u32 s71, s71, s52\n\t"
        "v_add_u32 v7, s71, v7\n\t"
        "v_lshl_add_u32 v8, v13, 3, v16\n\t"
        "v_lshl_add_u32 v8, v14, 8, v8\n\t"
        "v_lshl_add_u32 v9, v13, 4, v15\n\t"
        "v_lshl_add_u32 v9, v14, 2, v9\n\t"
        "v_lshlrev_b32 v10, 13, v12\n\t"
        "s_and_b32 s71, s50, 0x2000\n\t"
        "v_xor_b32 v10, s71, v10\n\t"
        "v_add_u32 v9, v9, v10\n\t"
        "s_lshl_b32 s71, %[w], 10\n\t"
        "s_add_u32 s71, s71, 158720\n\t"
        "v_add_u32 v55, s71, v0\n\t"
        "v_add_u32 v14, 162816, v0\n\t"
        "v_and_b32 v10, 0x1f0, v0\n\t"
        "v_lshlrev_b32 v10, 1, v10\n\t"
        "ds_write_b128 v55, v[4:7]\n\t"
        "ds_write_b128 v14, v[8:11]\n\t"
        "s_xor_b32 s48, s50, %[tw]\n\t"
        "v_mov_b32 v12, 0xffbfffbf\n\t"
        "v_mov_b32 v13, v12\n\t"
        "ds_write_b64 v7, v[12:13]\n\t"
        "ds_write_b64 v7, v[12:13] offset:64\n\t"
        "s_add_u32 s72, s50, s74\n\t"
        "s_xor_b32 s73, s72, 0x2000\n\t"
        "s_andn2_b32 s67, s50, 0x2000\n\t"
        "s_add_u32 s67, s67, s75\n\t"
        "s_nop 0\n\t"
        "v_mov_b64 v[4:5], 0\n\t"
        "v_mov_b64 v[6:7], 0\n\t"
        "v_mov_b64 v[8:9], 0\n\t"
        "v_mov_b64 v[10:11], 0\n\t"
        "s_waitcnt lgkmcnt(0)\n\t"
        "s_setprio 2\n\t"
        "s_cmp_lt_u32 s45, 64\n\t"
        "s_cbranch_scc1 .Ls_qloop_%=\n\t"
        "s_branch .Ls_end_%=\n\t"
        ".Ls_kpro_%=:\n\t"
        "v_and_b32 v55, 3, v52\n\t"
        "v_lshrrev_b32 v53, 4, v52\n\t"
        "v_lshl_add_u32 v55, v55, 2, v53\n\t"
        "v_bfe_u32 v53, v52, 3, 1\n\t"
        "v_xor_b32 v55, v55, v53\n\t"
        "s_and_b32 s70, %[w], 1\n\t"
        "s_lshl_b32 s70, s70, 1\n\t"
        "v_xor_b32 v55, s70, v55\n\t"
        "v_lshlrev_b32 v55, 3, v55\n\t"
        "v_bfe_u32 v54, v52, 2, 2\n\t"
        "v_add_u32 v53, v53, v54\n\t"
        "v_lshl_add_u32 v55, v53, 7, v55\n\t"
        "s_mul_i32 s70, %[w], 768\n\t"
        "s_add_u32 s70, s70, 150528\n\t"
        "v_add_u32 v55, s70, v55\n\t"
        "s_add_u32 s49, s49, 50331648\n\t"
        "s_lshl_b32 s70, %[w], 11\n\t"
        "s_sub_u32 s70, s70, 8192\n\t"
        "s_add_u32 s49, s49, s70\n\t"
        "s_add_u32 s52, s70, 131072\n\t"
        "s_mov_b32 m0, s52\n\t"
        "s_add_u32 s67, s49, 33554432\n\t"
        "buffer_load_dwordx4 v0, %[rs], s49 offen sc1 lds\n\t"
        "buffer_load_dwordx4 v0, %[rs], s49 offen offset:1024 sc1 lds\n\t"
        "s_add_u32 s49, s49, 0x80000\n\t"
        "v_xor_b32 v52, 0x800, v2\n\t"
        "v_xor_b32 v53, 0x1000, v2\n\t"
        "s_setprio 1\n\t"
        "s_waitcnt lgkmcnt(0)\n\t"
        "s_branch .Ls_kprep_%=\n\t"
        ".p2align 6\n\t"
        ".Ls_kloop_%=:\n\t"
        "s_barrier\n\t"
        "buffer_load_dwordx4 v[12:15], v0, %[rs], s47 offen sc1\n\t"
        "s_nop 4\n\t"
        "buffer_load_dwordx4 v[16:19], v0, %[rs], s47 offen offset:1024 sc1\n\t"
        ".Ls_k0r_%=:\n\t"
        "s_waitcnt vmcnt(1)\n\t"
        "ds_write_b128 v2, v[12:15] offset:8192\n\t"
        "v_smfmac_f32_16x16x64_bf16 v[4:7], v[12:15], v[56:63], v1\n\t"
        "v_max_u32 v54, v12, v13\n\t"
        "v_smfmac_f32_16x16x64_bf16 v[8:11], v[12:15], v[64:71], v1\n\t"
        "v_max3_u32 v54, v54, v14, v15\n\t"
        "s_waitcnt vmcnt(0)\n\t"
        "ds_write_b128 v2, v[16:19] offset:9216\n\t"
        "v_max3_u32 v54, v54, v16, v17\n\t"
        "v_max3_u32 v54, v54, v18, v19\n\t"
        "v_smfmac_f32_16x16x64_bf16 v[4:7], v[16:19], v[72:79], v1\n\t"
        "v_cmp_eq_u32 vcc, 0xffbfffbf, v54\n\t"
        "v_smfmac_f32_16x16x64_bf16 v[8:11], v[16:19], v[80:87], v1\n\t"
        "s_waitcnt lgkmcnt(0)\n\t"
        "s_cbranch_vccnz .Ls_k0f_%=\n\t"
        ".Ls_k0k_%=:\n\t"
        "s_barrier\n\t"
        "s_memtime s[92:93]\n\t"
        "s_memrealtime s[94:95]\n\t"
        "ds_read_b128 v[20:23], v52 offset:8192\n\t"
        "ds_read_b128 v[24:27], v52 offset:9216\n\t"
        "ds_read_b128 v[12:15], v53 offset:8192\n\t"
        "ds_read_b128 v[16:19], v53 offset:9216\n\t"
        "s_setprio 3\n\t"
        "s_mov_b64 exec, 1\n\t"
        "s_waitcnt lgkmcnt(0)\n\t"
        "s_add_u32 s68, s94, s99\n\t"
        "s_or_b32 s68, s68, 1\n\t"
        "v_mov_b32 v54, s68\n\t"
        "buffer_store_dword v54, v0, %[rs], s53 offen sc1\n\t"
        "s_mov_b64 exec, -1\n\t"
        "s_setprio 1\n\t"
        "v_smfmac_f32_16x16x64_bf16 v[4:7], v[20:23], v[88:95], v1\n\t"
        "v_smfmac_f32_16x16x64_bf16 v[8:11], v[20:23], v[28:35], v1\n\t"
        "ds_read_b128 v[20:23], v3 offset:8192\n\t"
        "v_smfmac_f32_16x16x64_bf16 v[4:7], v[24:27], v[96:103], v1\n\t"
        "v_smfmac_f32_16x16x64_bf16 v[8:11], v[24:27], v[104:111], v1\n\t"
        "ds_read_b128 v[24:27], v3 offset:9216\n\t"
        "ds_read_b128 v[28:31], v2 offset:24576\n\t"
        "ds_read_b128 v[32:35], v2 offset:25600\n\t"
        "v_smfmac_f32_16x16x64_bf16 v[4:7], v[12:15], v[112:119], v1\n\t"
        "v_smfmac_f32_16x16x64_bf16 v[8:11], v[12:15], v[36:43], v1\n\t"
        "ds_read_b128 v[12:15], v2 offset:0\n\t"
        "v_smfmac_f32_16x16x64_bf16 v[4:7], v[16:19], v[120:127], v1\n\t"
        "v_smfmac_f32_16x16x64_bf16 v[8:11], v[16:19], v[128:135], v1\n\t"
        "ds_read_b128 v[16:19], v2 offset:1024\n\t"
        "ds_read_b128 v[36:39], v2 offset:40960\n\t"
        "ds_read_b128 v[40:43], v2 offset:41984\n\t"
        "s_waitcnt lgkmcnt(7)\n\t"
        "v_smfmac_f32_16x16x64_bf16 v[4:7], v[20:23], v[136:143], v1\n\t"
        "v_smfmac_f32_16x16x64_bf16 v[8:11], v[20:23], v[44:51], v1\n\t"
        "ds_read_b128 v[20:23], v52 offset:0\n\t"
        "ds_read_b128 v[44:47], v2 offset:57344\n\t"
        "ds_read_b128 v[48:51], v2 offset:58368\n\t"
        "s_waitcnt lgkmcnt(9)\n\t"
        "v_smfmac_f32_16x16x64_bf16 v[4:7], v[24:27], v[144:151], v1\n\t"
        "s_waitcnt lgkmcnt(7)\n\t"
        "v_smfmac_f32_16x16x64_bf16 v[8:11], v[24:27], v[28:35], v1\n\t"
        "v_add_u32 v54, 122880, v2\n\t"
        "ds_read_b128 v[24:27], v52 offset:1024\n\t"
        "ds_read_b128 v[28:31], v54 offset:0\n\t"
        "ds_read_b128 v[32:35], v54 offset:1024\n\t"
        "s_waitcnt lgkmcnt(9)\n\t"
        "v_smfmac_f32_16x16x64_bf16 v[4:7], v[12:15], v[152:159], v1\n\t"
        "s_waitcnt lgkmcnt(6)\n\t"
        "v_smfmac_f32_16x16x64_bf16 v[8:11], v[12:15], v[36:43], v1\n\t"
        "ds_read_b128 v[12:15], v53 offset:0\n\t"
        "ds_read_b128 v[36:39], v53 offset:1024\n\t"
        "ds_read_b128 v[40:43], v3 offset:0\n\t"
        "v_smfmac_f32_16x16x64_bf16 v[4:7], v[16:19], v[160:167], v1\n\t"
        "s_waitcnt lgkmcnt(6)\n\t"
        "v_smfmac_f32_16x16x64_bf16 v[8:11], v[16:19], v[44:51], v1\n\t"
        "ds_read_b128 v[16:19], v3 offset:1024\n\t"
        "v_smfmac_f32_16x16x64_bf16 v[4:7], v[20:23], v[168:175], v1\n\t"
        "s_waitcnt lgkmcnt(4)\n\t"
        "v_smfmac_f32_16x16x64_bf16 v[8:11], v[20:23], v[28:35], v1\n\t"
        "s_barrier\n\t"
        "v_smfmac_f32_16x16x64_bf16 v[4:7], v[24:27], v[176:183], v1\n\t"
        "v_smfmac_f32_16x16x64_bf16 v[8:11], v[24:27], v[184:191], v1\n\t"
        "buffer_load_dwordx4 v0, %[rs], s72 offen sc1 lds\n\t"
        "buffer_load_dwordx4 v0, %[rs], s72 offen offset:1024 sc1 lds\n\t"
        "s_waitcnt lgkmcnt(3)\n\t"
        "v_smfmac_f32_16x16x64_bf16 v[4:7], v[12:15], v[192:199], v1\n\t"
        "v_smfmac_f32_16x16x64_bf16 v[8:11], v[12:15], v[200:207], v1\n\t"
        "s_waitcnt lgkmcnt(2)\n\t"
        "v_smfmac_f32_16x16x64_bf16 v[4:7], v[36:39], v[208:215], v1\n\t"
        "v_smfmac_f32_16x16x64_bf16 v[8:11], v[36:39], v[216:223], v1\n\t"
        "s_waitcnt lgkmcnt(1)\n\t"
        "v_smfmac_f32_16x16x64_bf16 v[4:7], v[40:43], v[224:231], v1\n\t"
        "v_smfmac_f32_16x16x64_bf16 v[8:11], v[40:43], v[232:239], v1\n\t"
        "s_waitcnt lgkmcnt(0)\n\t"
        "v_smfmac_f32_16x16x64_bf16 v[4:7], v[16:19], v[240:247], v1\n\t"
        "v_smfmac_f32_16x16x64_bf16 v[8:11], v[16:19], v[248:255], v1\n\t"
        "s_nop 6\n\t"
        "v_add_f32 v4, v4, v5\n\t"
        "v_add_f32 v6, v6, v7\n\t"
        "v_add_f32 v8, v8, v9\n\t"
        "v_add_f32 v10, v10, v11\n\t"
        "v_cvt_pk_bf16_f32 v4, v4, v8\n\t"
        "v_cvt_pk_bf16_f32 v5, v6, v10\n\t"
        "ds_write_b64 v55, v[4:5]\n\t"
        "s_waitcnt lgkmcnt(0)\n\t"
        "s_barrier\n\t"
        "s_setprio 0\n\t"
        "s_nop 15\n\t"
        "s_nop 15\n\t"
        "s_nop 15\n\t"
        "s_nop 15\n\t"
        "s_nop 15\n\t"
        "s_nop 15\n\t"
        "s_nop 3\n\t"
        "s_cmp_lt_u32 s45, 4\n\t"
        "s_cbranch_scc1 .Ls_kearly_%=\n\t"
        ".Ls_klate_%=:\n\t"
        "v_mov_b32 v21, 0\n\t"
        "s_add_u32 s53, s53, 4194304\n\t"
        "s_and_b32 s53, s53, 0x70ffffff\n\t"
        "s_cmp_lt_u32 s45, 62\n\t"
        "s_cselect_b32 s53, s53, 0x7f000000\n\t"
        "s_mov_b64 exec, 1\n\t"
        "buffer_store_dword v21, v0, %[rs], s53 offen sc1\n\t"
        "s_mov_b64 exec, -1\n\t"
        "s_sub_u32 s68, s92, s96\n\t"
        "s_sub_u32 s69, s94, s97\n\t"
        "s_mov_b32 s96, s92\n\t"
        "s_mov_b32 s97, s94\n\t"
        "v_cvt_f32_u32 v17, s68\n\t"
        "v_cvt_f32_u32 v16, s69\n\t"
        "v_rcp_f32 v17, v17\n\t"
        "s_nop 0\n\t"
        "v_mul_f32 v17, v17, v16\n\t"
        "v_max_f32 v17, 0x3d2aaaab, v17\n\t"
        "v_min_f32 v17, 0x3d638e39, v17\n\t"
        "v_subrev_f32 v17, s98, v17\n\t"
        "v_mov_b32 v16, s98\n\t"
        "v_fmac_f32 v16, 0x3ec00000, v17\n\t"
        "v_mul_f32 v17, 0x3f800000, v16\n\t"
        "v_cvt_u32_f32 v17, v17\n\t"
        "v_readfirstlane_b32 s98, v16\n\t"
        "v_mul_f32 v16, 0x44d48000, v16\n\t"
        "v_cvt_u32_f32 v16, v16\n\t"
        "s_nop 0\n\t"
        "v_readfirstlane_b32 s99, v16\n\t"
        "s_add_u32 s67, s49, 33554432\n\t"
        "v_add_u32 v54, 73728, v2\n\t"
        "ds_read_b128 v[28:31], v54 offset:0\n\t"
        "ds_read_b128 v[32:35], v54 offset:1024\n\t"
        "ds_read_b128 v[36:39], v54 offset:16384\n\t"
        "ds_read_b128 v[40:43], v54 offset:17408\n\t"
        "ds_read_b128 v[44:47], v54 offset:32768\n\t"
        "ds_read_b128 v[48:51], v54 offset:33792\n\t"
        "v_xor_b32 v52, 0x800, v2\n\t"
        "v_xor_b32 v53, 0x1000, v2\n\t"
        "v_xor_b32 v3, 0x1800, v2\n\t"
        "s_add_u32 s45, s45, 1\n\t"
        "s_add_u32 s47, s47, 2097152\n\t"
        "s_and_b32 s47, s47, 0xffffff\n\t"
        "s_xor_b32 s47, s47, 0x2000\n\t"
        "s_add_u32 s50, s50, 2097152\n\t"
        "s_and_b32 s50, s50, 0xffffff\n\t"
        "s_xor_b32 s50, s50, 0x2000\n\t"
        "s_add_u32 s49, s49, 0x80000\n\t"
        "s_add_u32 s70, s45, 1\n\t"
        "s_and_b32 s70, s70, 3\n\t"
        "s_cselect_b32 s70, 0, 0x2000\n\t"
        "s_xor_b32 s49, s49, s70\n\t"
        "s_nop 0\n\t"
        "v_mov_b64 v[4:5], 0\n\t"
        "v_mov_b64 v[6:7], 0\n\t"
        "v_mov_b64 v[8:9], 0\n\t"
        "v_mov_b64 v[10:11], 0\n\t"
        "s_waitcnt lgkmcnt(0)\n\t"
        "s_and_b32 s70, %[w], 1\n\t"
        "s_lshl_b32 s70, s70, 13\n\t"
        "s_xor_b32 s53, s50, s70\n\t"
        "s_lshl_b32 s70, %[h], 5\n\t"
        "s_add_u32 s53, s53, s70\n\t"
        "s_add_u32 s53, s53, 327680\n\t"
        "s_cmp_lt_u32 %[w], 6\n\t"
        "s_cselect_b32 s53, s53, 0x70000000\n\t"
        "s_add_u32 s70, s45, 1\n\t"
        "s_and_b32 s70, s70, 1\n\t"
        "s_lshl_b32 s70, s70, 13\n\t"
        "s_add_u32 m0, s70, s52\n\t"
        "s_cmp_lt_u32 s45, 63\n\t"
        "s_cselect_b32 s72, s49, 0x7f000000\n\t"
        "s_add_u32 s73, s45, 2\n\t"
        "s_min_u32 s73, s73, 64\n\t"
        "s_cmp_lt_u32 s45, 4\n\t"
        "s_cselect_b32 s72, 0x7f000000, s72\n\t"
        "s_cselect_b32 m0, 163840, m0\n\t"
        "s_cselect_b32 s73, 0, s73\n\t"
        "s_cselect_b32 s67, 0x7f000000, s67\n\t"
        "s_setprio 1\n\t"
        "s_cmp_lt_u32 s45, 64\n\t"
        "s_cbranch_scc1 .Ls_kloop_%=\n\t"
        "s_branch .Ls_end_%=\n\t"
        ".Ls_kprep_%=:\n\t"
        "s_mov_b32 s99, 80\n\t"
        "s_and_b32 s70, %[w], 1\n\t"
        "s_lshl_b32 s70, s70, 13\n\t"
        "s_xor_b32 s53, s50, s70\n\t"
        "s_lshl_b32 s70, %[h], 5\n\t"
        "s_add_u32 s53, s53, s70\n\t"
        "s_add_u32 s53, s53, 327680\n\t"
        "s_cmp_lt_u32 %[w], 6\n\t"
        "s_cselect_b32 s53, s53, 0x70000000\n\t"
        "v_mov_b32 v21, 0\n\t"
        "s_add_u32 s53, s53, 2097152\n\t"
        "s_mov_b64 exec, 1\n\t"
        "buffer_store_dword v21, v0, %[rs], s53 offen sc1\n\t"
        "s_mov_b64 exec, -1\n\t"
        "s_and_b32 s70, %[w], 1\n\t"
        "s_lshl_b32 s70, s70, 13\n\t"
        "s_xor_b32 s53, s50, s70\n\t"
        "s_lshl_b32 s70, %[h], 5\n\t"
        "s_add_u32 s53, s53, s70\n\t"
        "s_add_u32 s53, s53, 327680\n\t"
        "s_cmp_lt_u32 %[w], 6\n\t"
        "s_cselect_b32 s53, s53, 0x70000000\n\t"
        "s_mov_b32 s72, 0x7f000000\n\t"
        "s_mov_b32 s67, s72\n\t"
        "s_mov_b32 s73, 0\n\t"
        "v_mov_b64 v[24:25], 0\n\t"
        "s_mov_b32 m0, 163840\n\t"
        "s_branch .Ls_k0r_%=\n\t"
        ".Ls_kearly_%=:\n\t"
        "s_add_u32 s71, s45, 2\n\t"
        "s_cmp_ge_u32 s51, s71\n\t"
        "s_cbranch_scc0 .Ls_fep_%=\n\t"
        ".Ls_fek_%=:\n\t"
        "s_add_u32 s70, s45, 1\n\t"
        "s_and_b32 s70, s70, 1\n\t"
        "s_lshl_b32 s70, s70, 13\n\t"
        "s_add_u32 m0, s70, s52\n\t"
        "s_cmp_lt_u32 s45, 63\n\t"
        "s_cselect_b32 s72, s49, 0x7f000000\n\t"
        "s_add_u32 s73, s45, 2\n\t"
        "s_min_u32 s73, s73, 64\n\t"
        "buffer_load_dwordx4 v0, %[rs], s72 offen sc1 lds\n\t"
        "buffer_load_dwordx4 v0, %[rs], s72 offen offset:1024 sc1 lds\n\t"
        "s_branch .Ls_klate_%=\n\t"
        ".Ls_f0p_%=:\n\t"
        "s_add_u32 s70, 1, 3\n\t"
        "s_andn2_b32 s70, s70, 3\n\t"
        "s_cmp_le_u32 1, 16\n\t"
        "s_cselect_b32 s70, 1, s70\n\t"
        "s_lshl_b32 s70, s70, 7\n\t"
        "v_mov_b32 v52, 0\n\t"
        "buffer_load_dwordx2 v[52:53], v52, %[rf], s70 offen sc1\n\t"
        "s_waitcnt vmcnt(0)\n\t"
        "v_readfirstlane_b32 s68, v52\n\t"
        "v_readfirstlane_b32 s69, v53\n\t"
        "s_xor_b32 s68, s68, s54\n\t"
        "s_xor_b32 s69, s69, s55\n\t"
        "s_lshr_b32 s70, s68, 8\n\t"
        "s_or_b32 s70, s70, s69\n\t"
        "s_cmp_eq_u32 s70, 0\n\t"
        "s_cselect_b32 s68, s68, 0\n\t"
        "s_and_b32 s69, s68, 127\n\t"
        "s_max_u32 s51, s51, s69\n\t"
        "s_cmp_ge_u32 s51, 1\n\t"
        "s_cbranch_scc1 .Ls_f0k_%=\n\t"
        "s_sleep 4\n\t"
        "s_sub_u32 s46, s46, 1\n\t"
        "s_cbranch_scc0 .Ls_f0p_%=\n\t"
        "s_mov_b32 s46, 0\n\t"
        "s_mov_b32 s46, 0\n\t"
        "v_mov_b32 v52, 0\n\t"
        "s_add_u32 s68, s45, 3000\n\t"
        "v_mov_b32 v53, s68\n\t"
        "global_store_dword v52, v53, %[tm] sc1\n\t"
        "s_branch .Ls_f0k_%=\n\t"
        ".Ls_q0f_%=:\n\t"
        "s_cmp_eq_u32 s45, 0\n\t"
        "s_cbranch_scc1 .Ls_q0k_%=\n\t"
        "s_add_u32 s79, s79, 1\n\t"
        "buffer_load_dwordx4 v[12:15], v0, %[rs], s47 offen sc1\n\t"
        "buffer_load_dwordx4 v[16:19], v0, %[rs], s47 offen offset:1024 sc1\n\t"
        "s_nop 7\n\t"
        "v_mov_b64 v[4:5], 0\n\t"
        "v_mov_b64 v[6:7], 0\n\t"
        "v_mov_b64 v[8:9], 0\n\t"
        "v_mov_b64 v[10:11], 0\n\t"
        "s_waitcnt vmcnt(0)\n\t"
        "s_sub_u32 s46, s46, 1\n\t"
        "s_cbranch_scc0 .Ls_q0r_%=\n\t"
        "s_mov_b32 s46, 0\n\t"
        "s_branch .Ls_q0k_%=\n\t"
        ".Ls_xf_%=:\n\t"
        "s_add_u32 s79, s79, 256\n\t"
        "v_max_u32 v4, v28, v29\n\t"
        "v_max3_u32 v4, v4, v30, v31\n\t"
        "v_max3_u32 v4, v4, v32, v33\n\t"
        "v_max3_u32 v4, v4, v34, v35\n\t"
        "v_max3_u32 v4, v4, v44, v45\n\t"
        "v_max3_u32 v4, v4, v46, v47\n\t"
        "v_max3_u32 v4, v4, v48, v49\n\t"
        "v_max3_u32 v4, v4, v50, v51\n\t"
        "v_cmp_eq_u32 vcc, 0xffbfffbf, v4\n\t"
        "s_cmp_eq_u32 %[w], 0\n\t"
        "s_cbranch_vccz .Ls_xk_%=\n\t"
        "v_mov_b64 v[12:13], 0\n\t"
        "v_mov_b64 v[14:15], 0\n\t"
        "s_branch .Ls_xs_%=\n\t"
        ".Ls_pmax_%=:\n\t"
        "s_cmp_eq_u32 s44, s45\n\t"
        "s_cbranch_scc1 .Ls_pok_%=\n\t"
        "s_mov_b32 s44, s45\n\t"
        "ds_read_b128 v[24:27], v54 offset:1024\n\t"
        "s_waitcnt lgkmcnt(0)\n\t"
        "v_max3_f32 v8, v24, v25, v26\n\t"
        "v_max3_f32 v8, v8, v27, v4\n\t"
        "v_max_f32 v8, v8, v6\n\t"
        "s_nop 1\n\t"
        "v_max_f32_dpp v9, v8, v8 quad_perm:[1,0,3,2] row_mask:0xf bank_mask:0xf\n\t"
        "v_max_f32_dpp v10, v8, v8 quad_perm:[1,0,3,2] row_mask:0xf bank_mask:0xf\n\t"
        "s_nop 1\n\t"
        "v_permlane16_swap_b32 v9, v10\n\t"
        "v_max_f32 v8, v9, v10\n\t"
        "v_max_f32 v11, v9, v10\n\t"
        "s_nop 1\n\t"
        "v_permlane32_swap_b32 v8, v11\n\t"
        "v_max_f32 v8, v8, v11\n\t"
        "v_sub_f32 v24, v24, v8\n\t"
        "v_sub_f32 v25, v25, v8\n\t"
        "v_sub_f32 v26, v26, v8\n\t"
        "v_sub_f32 v27, v27, v8\n\t"
        "v_sub_f32 v5, v4, v8\n\t"
        "v_sub_f32 v7, v6, v8\n\t"
        "v_exp_f32 v24, v24\n\t"
        "v_exp_f32 v25, v25\n\t"
        "v_exp_f32 v26, v26\n\t"
        "v_exp_f32 v27, v27\n\t"
        "v_exp_f32 v5, v5\n\t"
        "v_exp_f32 v7, v7\n\t"
        "v_add_f32 v8, v24, v25\n\t"
        "v_add_f32 v9, v26, v27\n\t"
        "v_add_f32 v8, v8, v9\n\t"
        "s_nop 1\n\t"
        "v_add_f32_dpp v8, v8, v8 quad_perm:[1,0,3,2] row_mask:0xf bank_mask:0xf\n\t"
        "s_branch .Ls_psum_%=\n\t"
        ".Ls_twx_%=:\n\t"
        "s_add_u32 s79, s79, 0x1000000\n\t"
        "s_sleep 4\n\t"
        "buffer_load_dword v36, v18, %[rs], s48 offen sc1\n\t"
        "s_waitcnt vmcnt(0)\n\t"
        "s_sub_u32 s46, s46, 1\n\t"
        "s_cbranch_scc0 .Ls_twm_%=\n\t"
        "s_mov_b32 s46, 0\n\t"
        "s_branch .Ls_twy_%=\n\t"
        ".Ls_fq0p_%=:\n\t"
        "s_add_u32 s70, s71, 3\n\t"
        "s_andn2_b32 s70, s70, 3\n\t"
        "s_cmp_le_u32 s71, 16\n\t"
        "s_cselect_b32 s70, s71, s70\n\t"
        "s_lshl_b32 s70, s70, 7\n\t"
        "v_mov_b32 v12, 0\n\t"
        "buffer_load_dwordx2 v[12:13], v12, %[rf], s70 offen sc1\n\t"
        "s_waitcnt vmcnt(0)\n\t"
        "v_readfirstlane_b32 s68, v12\n\t"
        "v_readfirstlane_b32 s69, v13\n\t"
        "s_xor_b32 s68, s68, s54\n\t"
        "s_xor_b32 s69, s69, s55\n\t"
        "s_lshr_b32 s70, s68, 8\n\t"
        "s_or_b32 s70, s70, s69\n\t"
        "s_cmp_eq_u32 s70, 0\n\t"
        "s_cselect_b32 s68, s68, 0\n\t"
        "s_and_b32 s69, s68, 127\n\t"
        "s_max_u32 s51, s51, s69\n\t"
        "s_cmp_ge_u32 s51, s71\n\t"
        "s_cbranch_scc1 .Ls_fq0k_%=\n\t"
        "s_sleep 4\n\t"
        "s_sub_u32 s46, s46, 1\n\t"
        "s_cbranch_scc0 .Ls_fq0p_%=\n\t"
        "s_mov_b32 s46, 0\n\t"
        "s_mov_b32 s46, 0\n\t"
        "v_mov_b32 v12, 0\n\t"
        "s_add_u32 s68, s45, 3000\n\t"
        "v_mov_b32 v13, s68\n\t"
        "global_store_dword v12, v13, %[tm] sc1\n\t"
        "s_branch .Ls_fq0k_%=\n\t"
        ".Ls_fq1p_%=:\n\t"
        "s_add_u32 s70, s71, 3\n\t"
        "s_andn2_b32 s70, s70, 3\n\t"
        "s_cmp_le_u32 s71, 16\n\t"
        "s_cselect_b32 s70, s71, s70\n\t"
        "s_lshl_b32 s70, s70, 7\n\t"
        "v_mov_b32 v12, 0\n\t"
        "buffer_load_dwordx2 v[12:13], v12, %[rf], s70 offen sc1\n\t"
        "s_waitcnt vmcnt(0)\n\t"
        "v_readfirstlane_b32 s68, v12\n\t"
        "v_readfirstlane_b32 s69, v13\n\t"
        "s_xor_b32 s68, s68, s54\n\t"
        "s_xor_b32 s69, s69, s55\n\t"
        "s_lshr_b32 s70, s68, 8\n\t"
        "s_or_b32 s70, s70, s69\n\t"
        "s_cmp_eq_u32 s70, 0\n\t"
        "s_cselect_b32 s68, s68, 0\n\t"
        "s_and_b32 s69, s68, 127\n\t"
        "s_max_u32 s51, s51, s69\n\t"
        "s_cmp_ge_u32 s51, s71\n\t"
        "s_cbranch_scc1 .Ls_fq1k_%=\n\t"
        "s_sleep 4\n\t"
        "s_sub_u32 s46, s46, 1\n\t"
        "s_cbranch_scc0 .Ls_fq1p_%=\n\t"
        "s_mov_b32 s46, 0\n\t"
        "s_mov_b32 s46, 0\n\t"
        "v_mov_b32 v12, 0\n\t"
        "s_add_u32 s68, s45, 3000\n\t"
        "v_mov_b32 v13, s68\n\t"
        "global_store_dword v12, v13, %[tm] sc1\n\t"
        "s_branch .Ls_fq1k_%=\n\t"
        ".Ls_k0f_%=:\n\t"
        "s_cmp_eq_u32 s45, 0\n\t"
        "s_cbranch_scc1 .Ls_k0k_%=\n\t"
        "s_add_u32 s79, s79, 1\n\t"
        "buffer_load_dwordx4 v[12:15], v0, %[rs], s47 offen sc1\n\t"
        "buffer_load_dwordx4 v[16:19], v0, %[rs], s47 offen offset:1024 sc1\n\t"
        "s_nop 7\n\t"
        "v_mov_b64 v[4:5], 0\n\t"
        "v_mov_b64 v[6:7], 0\n\t"
        "v_mov_b64 v[8:9], 0\n\t"
        "v_mov_b64 v[10:11], 0\n\t"
        "s_waitcnt vmcnt(0)\n\t"
        "s_sub_u32 s46, s46, 1\n\t"
        "s_cbranch_scc0 .Ls_k0r_%=\n\t"
        "s_mov_b32 s46, 0\n\t"
        "s_branch .Ls_k0k_%=\n\t"
        ".Ls_fep_%=:\n\t"
        "s_add_u32 s70, s71, 3\n\t"
        "s_andn2_b32 s70, s70, 3\n\t"
        "s_cmp_le_u32 s71, 16\n\t"
        "s_cselect_b32 s70, s71, s70\n\t"
        "s_lshl_b32 s70, s70, 7\n\t"
        "v_mov_b32 v24, 0\n\t"
        "buffer_load_dwordx2 v[24:25], v24, %[rf], s70 offen sc1\n\t"
        "s_waitcnt vmcnt(0)\n\t"
        "v_readfirstlane_b32 s68, v24\n\t"
        "v_readfirstlane_b32 s69, v25\n\t"
        "s_xor_b32 s68, s68, s54\n\t"
        "s_xor_b32 s69, s69, s55\n\t"
        "s_lshr_b32 s70, s68, 8\n\t"
        "s_or_b32 s70, s70, s69\n\t"
        "s_cmp_eq_u32 s70, 0\n\t"
        "s_cselect_b32 s68, s68, 0\n\t"
        "s_and_b32 s69, s68, 127\n\t"
        "s_max_u32 s51, s51, s69\n\t"
        "s_cmp_ge_u32 s51, s71\n\t"
        "s_cbranch_scc1 .Ls_fek_%=\n\t"
        "s_sleep 4\n\t"
        "s_sub_u32 s46, s46, 1\n\t"
        "s_cbranch_scc0 .Ls_fep_%=\n\t"
        "s_mov_b32 s46, 0\n\t"
        "s_mov_b32 s46, 0\n\t"
        "v_mov_b32 v24, 0\n\t"
        "s_add_u32 s68, s45, 3000\n\t"
        "v_mov_b32 v25, s68\n\t"
        "global_store_dword v24, v25, %[tm] sc1\n\t"
        "s_branch .Ls_fek_%=\n\t"
        ".Lcode_s1:\n\t"
        ".Ls_end_%=:\n\t"
        "s_endpgm\n\t"
        :: [rs] "s"(RSRC(arena(a.slots), ARENA_BYTES)), [rw] "s"(RSRC((w < 4 ? a.Wq : a.Wk) + co, (DM * HD - co) * 2)), [rm] "s"(RSRC(a.mem0, 8 * DM * 2)), [ep] "s"(a.boot + 144),
           [rf] "s"(PROG((w == 2 ? 32 : 0) + h)), [tm] "s"(a.sync + 40 * 32), [w] "s"(w), [h] "s"(h), [cs] "s"(cls_s(h)), [ra] "s"((4 + 2 * rep) * 16384),
           [kx] "s"(131072 + (w >> 1) * 4096 + (w & 1) * 128), [tw] "s"(TW(cls_s(h))) STIN(w == 0 ? h : w == KW ? 192 + h : 251)
        : "memory", "vcc", "scc", ALLV, ALLS);
    __builtin_unreachable();
}
__device__ __forceinline__ void value_wg(const int w, const int h, const int half, const int rep) {
    const Args a = get_args();
    const int co = hcol(h) + 64 * (w >> 2), j0 = 32 * half + 4 * w, oo = hcol(h) * DM + 16 * j0;
    asm volatile(
        ".Lcode_v0:\n\t"
        "v_mbcnt_lo_u32_b32 v52, -1, 0\n\t"
        "v_mbcnt_hi_u32_b32 v52, -1, v52\n\t"
        "s_load_dwordx2 s[54:55], %[ep], 0\n\t"
        "s_mov_b32 s79, 0\n\t"
        "s_mov_b32 s45, 0\n\t"
        "s_mov_b32 s46, 200000\n\t"
        "s_mov_b32 s51, 0\n\t"
        "v_lshlrev_b32 v0, 4, v52\n\t"
        "v_lshrrev_b32 v53, 3, v52\n\t"
        "v_and_b32 v54, 7, v52\n\t"
        "v_and_b32 v8, 2, v53\n\t"
        "v_xor_b32 v54, v54, v8\n\t"
        "v_lshlrev_b32 v54, 4, v54\n\t"
        "v_lshl_add_u32 v8, v53, 13, v54\n\t"
        "v_xor_b32 v9, 64, v8\n\t"
        "s_lshl_b32 s69, %[w], 12\n\t"
        "v_lshrrev_b32 v12, 2, v53\n\t"
        "v_lshlrev_b32 v12, 1, v12\n\t"
        "v_and_b32 v13, 1, v53\n\t"
        "v_add_u32 v12, v12, v13\n\t"
        "v_and_b32 v13, 2, v53\n\t"
        "v_lshl_add_u32 v12, v13, 3, v12\n\t"
        "v_lshl_add_u32 v12, v12, 11, v54\n\t"
        "v_xor_b32 v13, 64, v12\n\t"
        "v_lshrrev_b32 v11, 4, v52\n\t"
        "s_lshl_b32 s68, %[wa], 20\n\t"
        "s_add_u32 m0, s69, 0\n\t"
        "s_xor_b32 s70, s68, 0x0\n\t"
        "buffer_load_dwordx4 v8, %[rw], s70 offen offset:0 lds\n\t"
        "s_add_u32 s70, s70, 64512\n\t"
        "buffer_load_dwordx4 v9, %[rw], s70 offen offset:1024 lds\n\t"
        "s_add_u32 s70, s70, 64512\n\t"
        "buffer_load_dwordx4 v8, %[rw], s70 offen offset:2048 lds\n\t"
        "s_add_u32 s70, s70, 64512\n\t"
        "buffer_load_dwordx4 v9, %[rw], s70 offen offset:3072 lds\n\t"
        "v_bfe_u32 v60, v0, 5, 3\n\t"
        "v_lshlrev_b32 v60, 11, v60\n\t"
        "v_lshrrev_b32 v61, 8, v0\n\t"
        "v_lshl_add_u32 v60, v61, 5, v60\n\t"
        "v_bfe_u32 v61, v0, 4, 1\n\t"
        "v_lshl_add_u32 v60, v61, 2, v60\n\t"
        "s_lshl_b32 s71, %[wa], 8\n\t"
        "buffer_load_dword v20, v60, %[rm], s71 offen offset:0\n\t"
        "buffer_load_dword v21, v60, %[rm], s71 offen offset:8\n\t"
        "buffer_load_dword v22, v60, %[rm], s71 offen offset:16\n\t"
        "buffer_load_dword v23, v60, %[rm], s71 offen offset:24\n\t"
        "buffer_load_dword v24, v60, %[rm], s71 offen offset:128\n\t"
        "buffer_load_dword v25, v60, %[rm], s71 offen offset:136\n\t"
        "buffer_load_dword v26, v60, %[rm], s71 offen offset:144\n\t"
        "buffer_load_dword v27, v60, %[rm], s71 offen offset:152\n\t"
        "v_mov_b32 v28, 0\n\t"
        "s_mov_b32 s72, 52428544\n\t"
        "s_mov_b32 s73, 18874112\n\t"
        "s_mov_b32 s74, 376832\n\t"
        "s_add_u32 s75, s73, 2097152\n\t"
        "buffer_load_dword v29, v28, %[rf], 0 offen sc1\n\t"
        "buffer_load_dword v29, v28, %[rs], s72 offen sc1\n\t"
        "buffer_load_dword v29, v28, %[rs], s73 offen sc1\n\t"
        "buffer_load_dword v29, v28, %[rs], s74 offen sc1\n\t"
        "buffer_load_dword v29, v28, %[rs], s75 offen sc1\n\t"
        "s_lshl_b32 s68, %[wa], 20\n\t"
        "s_add_u32 m0, s69, 32768\n\t"
        "s_xor_b32 s70, s68, 0x40000\n\t"
        "buffer_load_dwordx4 v8, %[rw], s70 offen offset:0 lds\n\t"
        "s_add_u32 s70, s70, 64512\n\t"
        "buffer_load_dwordx4 v9, %[rw], s70 offen offset:1024 lds\n\t"
        "s_add_u32 s70, s70, 64512\n\t"
        "buffer_load_dwordx4 v8, %[rw], s70 offen offset:2048 lds\n\t"
        "s_add_u32 s70, s70, 64512\n\t"
        "buffer_load_dwordx4 v9, %[rw], s70 offen offset:3072 lds\n\t"
        "v_lshrrev_b32 v4, 2, v52\n\t"
        "v_lshl_add_u32 v4, v11, 2, v4\n\t"
        "v_lshlrev_b32 v4, 7, v4\n\t"
        "v_and_b32 v5, 3, v52\n\t"
        "v_lshl_add_u32 v4, v5, 3, v4\n\t"
        "v_add_u32 v4, s69, v4\n\t"
        "v_bfe_u32 v5, v52, 3, 2\n\t"
        "v_lshl_add_u32 v14, v5, 5, v4\n\t"
        "v_xor_b32 v15, 32, v14\n\t"
        "v_xor_b32 v16, 64, v14\n\t"
        "v_xor_b32 v17, 96, v14\n\t"
        "s_and_b32 s70, %[w], 3\n\t"
        "s_lshl_b32 s70, s70, 5\n\t"
        "v_xor_b32 v4, s70, v14\n\t"
        "v_xor_b32 v5, 0x1000, v4\n\t"
        "v_xor_b32 v6, 0x2000, v4\n\t"
        "v_xor_b32 v7, 0x3000, v4\n\t"
        "v_mov_b32 v53, 0x4444\n\t"
        "v_mov_b32 v54, 0xeeee\n\t"
        "v_and_b32 v1, 1, v52\n\t"
        "v_cmp_eq_u32 vcc, 0, v1\n\t"
        "s_lshl_b32 s70, %[wa], 11\n\t"
        "v_add_u32 v2, s70, v0\n\t"
        "v_cndmask_b32 v1, v54, v53, vcc\n\t"
        "s_waitcnt lgkmcnt(0)\n\t"
        "s_add_u32 s54, s54, 1\n\t"
        "s_addc_u32 s55, s55, 0\n\t"
        "s_lshl_b64 s[54:55], s[54:55], 8\n\t"
        "s_waitcnt vmcnt(4)\n\t"
        "s_barrier\n\t"
        "s_lshl_b32 s68, %[wa], 20\n\t"
        "s_add_u32 m0, s69, 65536\n\t"
        "s_xor_b32 s70, s68, 0x80000\n\t"
        "buffer_load_dwordx4 v8, %[rw], s70 offen offset:0 lds\n\t"
        "s_add_u32 s70, s70, 64512\n\t"
        "buffer_load_dwordx4 v9, %[rw], s70 offen offset:1024 lds\n\t"
        "s_add_u32 s70, s70, 64512\n\t"
        "buffer_load_dwordx4 v8, %[rw], s70 offen offset:2048 lds\n\t"
        "s_add_u32 s70, s70, 64512\n\t"
        "buffer_load_dwordx4 v9, %[rw], s70 offen offset:3072 lds\n\t"
        "ds_read_b64_tr_b16 v[64:65], v4\n\t"
        "ds_read_b64_tr_b16 v[66:67], v4 offset:512\n\t"
        "ds_read_b64_tr_b16 v[80:81], v5\n\t"
        "ds_read_b64_tr_b16 v[82:83], v5 offset:512\n\t"
        "ds_read_b64_tr_b16 v[96:97], v6\n\t"
        "ds_read_b64_tr_b16 v[98:99], v6 offset:512\n\t"
        "ds_read_b64_tr_b16 v[112:113], v7\n\t"
        "ds_read_b64_tr_b16 v[114:115], v7 offset:512\n\t"
        "s_waitcnt lgkmcnt(0)\n\t"
        "v_add_u32 v4, 32768, v4\n\t"
        "v_add_u32 v5, 32768, v5\n\t"
        "v_add_u32 v6, 32768, v6\n\t"
        "v_add_u32 v7, 32768, v7\n\t"
        "v_add_u32 v14, 32768, v14\n\t"
        "v_add_u32 v15, 32768, v15\n\t"
        "v_add_u32 v16, 32768, v16\n\t"
        "v_add_u32 v17, 32768, v17\n\t"
        "s_waitcnt vmcnt(4)\n\t"
        "s_barrier\n\t"
        "s_lshl_b32 s68, %[wa], 20\n\t"
        "s_add_u32 m0, s69, 0\n\t"
        "s_xor_b32 s70, s68, 0xc0000\n\t"
        "buffer_load_dwordx4 v8, %[rw], s70 offen offset:0 lds\n\t"
        "s_add_u32 s70, s70, 64512\n\t"
        "buffer_load_dwordx4 v9, %[rw], s70 offen offset:1024 lds\n\t"
        "s_add_u32 s70, s70, 64512\n\t"
        "buffer_load_dwordx4 v8, %[rw], s70 offen offset:2048 lds\n\t"
        "s_add_u32 s70, s70, 64512\n\t"
        "buffer_load_dwordx4 v9, %[rw], s70 offen offset:3072 lds\n\t"
        "ds_read_b64_tr_b16 v[68:69], v4\n\t"
        "ds_read_b64_tr_b16 v[70:71], v4 offset:512\n\t"
        "ds_read_b64_tr_b16 v[84:85], v5\n\t"
        "ds_read_b64_tr_b16 v[86:87], v5 offset:512\n\t"
        "ds_read_b64_tr_b16 v[100:101], v6\n\t"
        "ds_read_b64_tr_b16 v[102:103], v6 offset:512\n\t"
        "ds_read_b64_tr_b16 v[116:117], v7\n\t"
        "ds_read_b64_tr_b16 v[118:119], v7 offset:512\n\t"
        "s_waitcnt lgkmcnt(0)\n\t"
        "v_add_u32 v4, 32768, v4\n\t"
        "v_add_u32 v5, 32768, v5\n\t"
        "v_add_u32 v6, 32768, v6\n\t"
        "v_add_u32 v7, 32768, v7\n\t"
        "v_add_u32 v14, 32768, v14\n\t"
        "v_add_u32 v15, 32768, v15\n\t"
        "v_add_u32 v16, 32768, v16\n\t"
        "v_add_u32 v17, 32768, v17\n\t"
        "s_waitcnt vmcnt(4)\n\t"
        "s_barrier\n\t"
        "s_lshl_b32 s68, %[wa], 20\n\t"
        "s_add_u32 m0, s69, 32768\n\t"
        "s_xor_b32 s70, s68, 0x400000\n\t"
        "buffer_load_dwordx4 v8, %[rw], s70 offen offset:0 lds\n\t"
        "s_add_u32 s70, s70, 64512\n\t"
        "buffer_load_dwordx4 v9, %[rw], s70 offen offset:1024 lds\n\t"
        "s_add_u32 s70, s70, 64512\n\t"
        "buffer_load_dwordx4 v8, %[rw], s70 offen offset:2048 lds\n\t"
        "s_add_u32 s70, s70, 64512\n\t"
        "buffer_load_dwordx4 v9, %[rw], s70 offen offset:3072 lds\n\t"
        "ds_read_b64_tr_b16 v[72:73], v4\n\t"
        "ds_read_b64_tr_b16 v[74:75], v4 offset:512\n\t"
        "ds_read_b64_tr_b16 v[88:89], v5\n\t"
        "ds_read_b64_tr_b16 v[90:91], v5 offset:512\n\t"
        "ds_read_b64_tr_b16 v[104:105], v6\n\t"
        "ds_read_b64_tr_b16 v[106:107], v6 offset:512\n\t"
        "ds_read_b64_tr_b16 v[120:121], v7\n\t"
        "ds_read_b64_tr_b16 v[122:123], v7 offset:512\n\t"
        "s_waitcnt lgkmcnt(0)\n\t"
        "v_add_u32 v4, -65536, v4\n\t"
        "v_add_u32 v5, -65536, v5\n\t"
        "v_add_u32 v6, -65536, v6\n\t"
        "v_add_u32 v7, -65536, v7\n\t"
        "v_add_u32 v14, -65536, v14\n\t"
        "v_add_u32 v15, -65536, v15\n\t"
        "v_add_u32 v16, -65536, v16\n\t"
        "v_add_u32 v17, -65536, v17\n\t"
        "s_waitcnt vmcnt(4)\n\t"
        "s_barrier\n\t"
        "s_lshl_b32 s68, %[wa], 20\n\t"
        "s_add_u32 m0, s69, 65536\n\t"
        "s_xor_b32 s70, s68, 0x440000\n\t"
        "buffer_load_dwordx4 v8, %[rw], s70 offen offset:0 lds\n\t"
        "s_add_u32 s70, s70, 64512\n\t"
        "buffer_load_dwordx4 v9, %[rw], s70 offen offset:1024 lds\n\t"
        "s_add_u32 s70, s70, 64512\n\t"
        "buffer_load_dwordx4 v8, %[rw], s70 offen offset:2048 lds\n\t"
        "s_add_u32 s70, s70, 64512\n\t"
        "buffer_load_dwordx4 v9, %[rw], s70 offen offset:3072 lds\n\t"
        "ds_read_b64_tr_b16 v[76:77], v4\n\t"
        "ds_read_b64_tr_b16 v[78:79], v4 offset:512\n\t"
        "ds_read_b64_tr_b16 v[92:93], v5\n\t"
        "ds_read_b64_tr_b16 v[94:95], v5 offset:512\n\t"
        "ds_read_b64_tr_b16 v[108:109], v6\n\t"
        "ds_read_b64_tr_b16 v[110:111], v6 offset:512\n\t"
        "ds_read_b64_tr_b16 v[124:125], v7\n\t"
        "ds_read_b64_tr_b16 v[126:127], v7 offset:512\n\t"
        "s_waitcnt lgkmcnt(0)\n\t"
        "v_add_u32 v4, 32768, v4\n\t"
        "v_add_u32 v5, 32768, v5\n\t"
        "v_add_u32 v6, 32768, v6\n\t"
        "v_add_u32 v7, 32768, v7\n\t"
        "v_add_u32 v14, 32768, v14\n\t"
        "v_add_u32 v15, 32768, v15\n\t"
        "v_add_u32 v16, 32768, v16\n\t"
        "v_add_u32 v17, 32768, v17\n\t"
        "s_waitcnt vmcnt(4)\n\t"
        "s_barrier\n\t"
        "s_lshl_b32 s68, %[wa], 20\n\t"
        "s_add_u32 m0, s69, 0\n\t"
        "s_xor_b32 s70, s68, 0x480000\n\t"
        "buffer_load_dwordx4 v8, %[rw], s70 offen offset:0 lds\n\t"
        "s_add_u32 s70, s70, 64512\n\t"
        "buffer_load_dwordx4 v9, %[rw], s70 offen offset:1024 lds\n\t"
        "s_add_u32 s70, s70, 64512\n\t"
        "buffer_load_dwordx4 v8, %[rw], s70 offen offset:2048 lds\n\t"
        "s_add_u32 s70, s70, 64512\n\t"
        "buffer_load_dwordx4 v9, %[rw], s70 offen offset:3072 lds\n\t"
        "ds_read_b64_tr_b16 v[128:129], v4\n\t"
        "ds_read_b64_tr_b16 v[130:131], v4 offset:512\n\t"
        "ds_read_b64_tr_b16 v[144:145], v5\n\t"
        "ds_read_b64_tr_b16 v[146:147], v5 offset:512\n\t"
        "ds_read_b64_tr_b16 v[160:161], v6\n\t"
        "ds_read_b64_tr_b16 v[162:163], v6 offset:512\n\t"
        "ds_read_b64_tr_b16 v[176:177], v7\n\t"
        "ds_read_b64_tr_b16 v[178:179], v7 offset:512\n\t"
        "s_waitcnt lgkmcnt(0)\n\t"
        "v_add_u32 v4, 32768, v4\n\t"
        "v_add_u32 v5, 32768, v5\n\t"
        "v_add_u32 v6, 32768, v6\n\t"
        "v_add_u32 v7, 32768, v7\n\t"
        "v_add_u32 v14, 32768, v14\n\t"
        "v_add_u32 v15, 32768, v15\n\t"
        "v_add_u32 v16, 32768, v16\n\t"
        "v_add_u32 v17, 32768, v17\n\t"
        "s_waitcnt vmcnt(4)\n\t"
        "s_barrier\n\t"
        "s_lshl_b32 s68, %[wa], 20\n\t"
        "s_add_u32 m0, s69, 32768\n\t"
        "s_xor_b32 s70, s68, 0x4c0000\n\t"
        "buffer_load_dwordx4 v8, %[rw], s70 offen offset:0 lds\n\t"
        "s_add_u32 s70, s70, 64512\n\t"
        "buffer_load_dwordx4 v9, %[rw], s70 offen offset:1024 lds\n\t"
        "s_add_u32 s70, s70, 64512\n\t"
        "buffer_load_dwordx4 v8, %[rw], s70 offen offset:2048 lds\n\t"
        "s_add_u32 s70, s70, 64512\n\t"
        "buffer_load_dwordx4 v9, %[rw], s70 offen offset:3072 lds\n\t"
        "ds_read_b64_tr_b16 v[132:133], v4\n\t"
        "ds_read_b64_tr_b16 v[134:135], v4 offset:512\n\t"
        "ds_read_b64_tr_b16 v[148:149], v5\n\t"
        "ds_read_b64_tr_b16 v[150:151], v5 offset:512\n\t"
        "ds_read_b64_tr_b16 v[164:165], v6\n\t"
        "ds_read_b64_tr_b16 v[166:167], v6 offset:512\n\t"
        "ds_read_b64_tr_b16 v[180:181], v7\n\t"
        "ds_read_b64_tr_b16 v[182:183], v7 offset:512\n\t"
        "s_waitcnt lgkmcnt(0)\n\t"
        "v_add_u32 v4, -65536, v4\n\t"
        "v_add_u32 v5, -65536, v5\n\t"
        "v_add_u32 v6, -65536, v6\n\t"
        "v_add_u32 v7, -65536, v7\n\t"
        "v_add_u32 v14, -65536, v14\n\t"
        "v_add_u32 v15, -65536, v15\n\t"
        "v_add_u32 v16, -65536, v16\n\t"
        "v_add_u32 v17, -65536, v17\n\t"
        "s_waitcnt vmcnt(4)\n\t"
        "s_barrier\n\t"
        "s_mov_b32 s68, 0\n\t"
        "s_add_u32 m0, s69, 65536\n\t"
        "s_xor_b32 s70, s68, 0x0\n\t"
        "buffer_load_dwordx4 v12, %[rx], s70 offen offset:0 lds\n\t"
        "s_add_u32 s70, s70, 7168\n\t"
        "buffer_load_dwordx4 v13, %[rx], s70 offen offset:1024 lds\n\t"
        "s_add_u32 s70, s70, 7168\n\t"
        "buffer_load_dwordx4 v12, %[rx], s70 offen offset:2048 lds\n\t"
        "s_add_u32 s70, s70, 7168\n\t"
        "buffer_load_dwordx4 v13, %[rx], s70 offen offset:3072 lds\n\t"
        "ds_read_b64_tr_b16 v[136:137], v4\n\t"
        "ds_read_b64_tr_b16 v[138:139], v4 offset:512\n\t"
        "ds_read_b64_tr_b16 v[152:153], v5\n\t"
        "ds_read_b64_tr_b16 v[154:155], v5 offset:512\n\t"
        "ds_read_b64_tr_b16 v[168:169], v6\n\t"
        "ds_read_b64_tr_b16 v[170:171], v6 offset:512\n\t"
        "ds_read_b64_tr_b16 v[184:185], v7\n\t"
        "ds_read_b64_tr_b16 v[186:187], v7 offset:512\n\t"
        "s_waitcnt lgkmcnt(0)\n\t"
        "v_add_u32 v4, 32768, v4\n\t"
        "v_add_u32 v5, 32768, v5\n\t"
        "v_add_u32 v6, 32768, v6\n\t"
        "v_add_u32 v7, 32768, v7\n\t"
        "v_add_u32 v14, 32768, v14\n\t"
        "v_add_u32 v15, 32768, v15\n\t"
        "v_add_u32 v16, 32768, v16\n\t"
        "v_add_u32 v17, 32768, v17\n\t"
        "s_waitcnt vmcnt(4)\n\t"
        "s_barrier\n\t"
        "s_mov_b32 s68, 0\n\t"
        "s_add_u32 m0, s69, 0\n\t"
        "s_xor_b32 s70, s68, 0x10000\n\t"
        "buffer_load_dwordx4 v12, %[rx], s70 offen offset:0 lds\n\t"
        "s_add_u32 s70, s70, 7168\n\t"
        "buffer_load_dwordx4 v13, %[rx], s70 offen offset:1024 lds\n\t"
        "s_add_u32 s70, s70, 7168\n\t"
        "buffer_load_dwordx4 v12, %[rx], s70 offen offset:2048 lds\n\t"
        "s_add_u32 s70, s70, 7168\n\t"
        "buffer_load_dwordx4 v13, %[rx], s70 offen offset:3072 lds\n\t"
        "ds_read_b64_tr_b16 v[140:141], v4\n\t"
        "ds_read_b64_tr_b16 v[142:143], v4 offset:512\n\t"
        "ds_read_b64_tr_b16 v[156:157], v5\n\t"
        "ds_read_b64_tr_b16 v[158:159], v5 offset:512\n\t"
        "ds_read_b64_tr_b16 v[172:173], v6\n\t"
        "ds_read_b64_tr_b16 v[174:175], v6 offset:512\n\t"
        "ds_read_b64_tr_b16 v[188:189], v7\n\t"
        "ds_read_b64_tr_b16 v[190:191], v7 offset:512\n\t"
        "s_waitcnt lgkmcnt(0)\n\t"
        "v_add_u32 v4, 32768, v4\n\t"
        "v_add_u32 v5, 32768, v5\n\t"
        "v_add_u32 v6, 32768, v6\n\t"
        "v_add_u32 v7, 32768, v7\n\t"
        "v_add_u32 v14, 32768, v14\n\t"
        "v_add_u32 v15, 32768, v15\n\t"
        "v_add_u32 v16, 32768, v16\n\t"
        "v_add_u32 v17, 32768, v17\n\t"
        "s_waitcnt vmcnt(4)\n\t"
        "s_barrier\n\t"
        "s_mov_b32 s68, 0\n\t"
        "s_add_u32 m0, s69, 32768\n\t"
        "s_xor_b32 s70, s68, 0x20000\n\t"
        "buffer_load_dwordx4 v12, %[rx], s70 offen offset:0 lds\n\t"
        "s_add_u32 s70, s70, 7168\n\t"
        "buffer_load_dwordx4 v13, %[rx], s70 offen offset:1024 lds\n\t"
        "s_add_u32 s70, s70, 7168\n\t"
        "buffer_load_dwordx4 v12, %[rx], s70 offen offset:2048 lds\n\t"
        "s_add_u32 s70, s70, 7168\n\t"
        "buffer_load_dwordx4 v13, %[rx], s70 offen offset:3072 lds\n\t"
        "ds_read_b64_tr_b16 v[192:193], v14\n\t"
        "ds_read_b64_tr_b16 v[194:195], v14 offset:512\n\t"
        "ds_read_b64_tr_b16 v[208:209], v15\n\t"
        "ds_read_b64_tr_b16 v[210:211], v15 offset:512\n\t"
        "ds_read_b64_tr_b16 v[224:225], v16\n\t"
        "ds_read_b64_tr_b16 v[226:227], v16 offset:512\n\t"
        "ds_read_b64_tr_b16 v[240:241], v17\n\t"
        "ds_read_b64_tr_b16 v[242:243], v17 offset:512\n\t"
        "s_waitcnt lgkmcnt(0)\n\t"
        "v_add_u32 v4, -65536, v4\n\t"
        "v_add_u32 v5, -65536, v5\n\t"
        "v_add_u32 v6, -65536, v6\n\t"
        "v_add_u32 v7, -65536, v7\n\t"
        "v_add_u32 v14, -65536, v14\n\t"
        "v_add_u32 v15, -65536, v15\n\t"
        "v_add_u32 v16, -65536, v16\n\t"
        "v_add_u32 v17, -65536, v17\n\t"
        "s_waitcnt vmcnt(4)\n\t"
        "s_barrier\n\t"
        "s_mov_b32 s68, 0\n\t"
        "s_add_u32 m0, s69, 65536\n\t"
        "s_xor_b32 s70, s68, 0x30000\n\t"
        "buffer_load_dwordx4 v12, %[rx], s70 offen offset:0 lds\n\t"
        "s_add_u32 s70, s70, 7168\n\t"
        "buffer_load_dwordx4 v13, %[rx], s70 offen offset:1024 lds\n\t"
        "s_add_u32 s70, s70, 7168\n\t"
        "buffer_load_dwordx4 v12, %[rx], s70 offen offset:2048 lds\n\t"
        "s_add_u32 s70, s70, 7168\n\t"
        "buffer_load_dwordx4 v13, %[rx], s70 offen offset:3072 lds\n\t"
        "ds_read_b64_tr_b16 v[196:197], v14\n\t"
        "ds_read_b64_tr_b16 v[198:199], v14 offset:512\n\t"
        "ds_read_b64_tr_b16 v[212:213], v15\n\t"
        "ds_read_b64_tr_b16 v[214:215], v15 offset:512\n\t"
        "ds_read_b64_tr_b16 v[228:229], v16\n\t"
        "ds_read_b64_tr_b16 v[230:231], v16 offset:512\n\t"
        "ds_read_b64_tr_b16 v[244:245], v17\n\t"
        "ds_read_b64_tr_b16 v[246:247], v17 offset:512\n\t"
        "s_waitcnt lgkmcnt(0)\n\t"
        "v_add_u32 v4, 32768, v4\n\t"
        "v_add_u32 v5, 32768, v5\n\t"
        "v_add_u32 v6, 32768, v6\n\t"
        "v_add_u32 v7, 32768, v7\n\t"
        "v_add_u32 v14, 32768, v14\n\t"
        "v_add_u32 v15, 32768, v15\n\t"
        "v_add_u32 v16, 32768, v16\n\t"
        "v_add_u32 v17, 32768, v17\n\t"
        "s_waitcnt vmcnt(4)\n\t"
        "s_barrier\n\t"
        "ds_read_b64_tr_b16 v[200:201], v14\n\t"
        "ds_read_b64_tr_b16 v[202:203], v14 offset:512\n\t"
        "ds_read_b64_tr_b16 v[216:217], v15\n\t"
        "ds_read_b64_tr_b16 v[218:219], v15 offset:512\n\t"
        "ds_read_b64_tr_b16 v[232:233], v16\n\t"
        "ds_read_b64_tr_b16 v[234:235], v16 offset:512\n\t"
        "ds_read_b64_tr_b16 v[248:249], v17\n\t"
        "ds_read_b64_tr_b16 v[250:251], v17 offset:512\n\t"
        "s_waitcnt lgkmcnt(0)\n\t"
        "v_add_u32 v4, 32768, v4\n\t"
        "v_add_u32 v5, 32768, v5\n\t"
        "v_add_u32 v6, 32768, v6\n\t"
        "v_add_u32 v7, 32768, v7\n\t"
        "v_add_u32 v14, 32768, v14\n\t"
        "v_add_u32 v15, 32768, v15\n\t"
        "v_add_u32 v16, 32768, v16\n\t"
        "v_add_u32 v17, 32768, v17\n\t"
        "s_waitcnt vmcnt(0)\n\t"
        "s_barrier\n\t"
        "ds_read_b64_tr_b16 v[204:205], v14\n\t"
        "ds_read_b64_tr_b16 v[206:207], v14 offset:512\n\t"
        "ds_read_b64_tr_b16 v[220:221], v15\n\t"
        "ds_read_b64_tr_b16 v[222:223], v15 offset:512\n\t"
        "ds_read_b64_tr_b16 v[236:237], v16\n\t"
        "ds_read_b64_tr_b16 v[238:239], v16 offset:512\n\t"
        "ds_read_b64_tr_b16 v[252:253], v17\n\t"
        "ds_read_b64_tr_b16 v[254:255], v17 offset:512\n\t"
        "s_waitcnt lgkmcnt(0)\n\t"
        "v_add_u32 v4, -65536, v4\n\t"
        "v_add_u32 v5, -65536, v5\n\t"
        "v_add_u32 v6, -65536, v6\n\t"
        "v_add_u32 v7, -65536, v7\n\t"
        "v_add_u32 v14, -65536, v14\n\t"
        "v_add_u32 v15, -65536, v15\n\t"
        "v_add_u32 v16, -65536, v16\n\t"
        "v_add_u32 v17, -65536, v17\n\t"
        "v_mbcnt_lo_u32_b32 v52, -1, 0\n\t"
        "v_mbcnt_hi_u32_b32 v52, -1, v52\n\t"
        "s_lshr_b32 s72, %[w], 1\n\t"
        "v_and_b32 v55, 3, v52\n\t"
        "v_lshrrev_b32 v53, 4, v52\n\t"
        "v_lshl_add_u32 v55, v55, 2, v53\n\t"
        "v_bfe_u32 v53, v52, 3, 1\n\t"
        "v_xor_b32 v55, v55, v53\n\t"
        "s_and_b32 s70, s72, 1\n\t"
        "s_lshl_b32 s70, s70, 1\n\t"
        "v_xor_b32 v55, s70, v55\n\t"
        "v_lshlrev_b32 v55, 3, v55\n\t"
        "v_bfe_u32 v54, v52, 2, 2\n\t"
        "v_add_u32 v53, v53, v54\n\t"
        "v_lshl_add_u32 v55, v53, 7, v55\n\t"
        "v_and_b32 v53, 15, v52\n\t"
        "v_lshrrev_b32 v54, 4, v52\n\t"
        "v_xor_b32 v3, v53, v54\n\t"
        "v_lshlrev_b32 v3, 3, v3\n\t"
        "v_mul_u32_u24 v56, 384, v54\n\t"
        "v_add_u32 v3, v3, v56\n\t"
        "v_add_u32 v3, 131072, v3\n\t"
        "s_mul_i32 s70, s72, 768\n\t"
        "s_and_b32 s71, %[w], 1\n\t"
        "s_lshl_b32 s71, s71, 1\n\t"
        "s_add_u32 s70, s70, s71\n\t"
        "s_add_u32 s70, s70, 131072\n\t"
        "v_add_u32 v55, s70, v55\n\t"
        "v_bfe_u32 v56, v0, 4, 1\n\t"
        "v_lshlrev_b32 v56, 7, v56\n\t"
        "v_bfe_u32 v57, v0, 6, 2\n\t"
        "v_lshl_add_u32 v56, v57, 5, v56\n\t"
        "v_bfe_u32 v57, v0, 5, 1\n\t"
        "v_lshl_add_u32 v56, v57, 4, v56\n\t"
        "v_lshl_add_u32 v56, v54, 8, v56\n\t"
        "v_lshlrev_b32 v57, 4, v53\n\t"
        "v_lshl_add_u32 v58, v54, 14, v57\n\t"
        "v_lshl_add_u32 v57, v54, 2, v57\n\t"
        "s_barrier\n\t"
        "v_mov_b64 v[4:5], 0\n\t"
        "v_mov_b64 v[6:7], 0\n\t"
        "v_xor_b32 v8, 0x800, v2\n\t"
        "v_xor_b32 v9, 0x1000, v2\n\t"
        "v_xor_b32 v10, 0x1800, v2\n\t"
        "v_xor_b32 v11, 0x2000, v2\n\t"
        "v_xor_b32 v12, 0x2800, v2\n\t"
        "v_xor_b32 v13, 0x3000, v2\n\t"
        "v_xor_b32 v14, 0x3800, v2\n\t"
        "v_mov_b32 v36, 128\n\t"
        "buffer_load_dwordx2 v[36:37], v36, %[rf], 0 offen sc1\n\t"
        ".Lv_u0r_%=:\n\t"
        "s_waitcnt vmcnt(1)\n\t"
        "ds_write_b128 v2, v[20:23] offset:0\n\t"
        "v_smfmac_f32_16x16x64_bf16 v[4:7], v[20:23], v[64:71], v1\n\t"
        "v_max_u32 v54, v20, v21\n\t"
        "v_max3_u32 v54, v54, v22, v23\n\t"
        "s_waitcnt vmcnt(0)\n\t"
        "ds_write_b128 v2, v[24:27] offset:1024\n\t"
        "v_max3_u32 v54, v54, v24, v25\n\t"
        "v_max3_u32 v54, v54, v26, v27\n\t"
        "v_smfmac_f32_16x16x64_bf16 v[4:7], v[24:27], v[72:79], v1\n\t"
        "v_cmp_eq_u32 vcc, 0xffbfffbf, v54\n\t"
        "s_waitcnt lgkmcnt(0)\n\t"
        "s_cbranch_vccnz .Lv_u0f_%=\n\t"
        ".Lv_u0k_%=:\n\t"
        "s_barrier\n\t"
        "ds_read_b128 v[28:31], v8 offset:0\n\t"
        "ds_read_b128 v[32:35], v8 offset:1024\n\t"
        "ds_read_b128 v[20:23], v9 offset:0\n\t"
        "ds_read_b128 v[24:27], v9 offset:1024\n\t"
        "s_waitcnt lgkmcnt(3)\n\t"
        "v_smfmac_f32_16x16x64_bf16 v[4:7], v[28:31], v[80:87], v1\n\t"
        "ds_read_b128 v[28:31], v10 offset:0\n\t"
        "s_waitcnt lgkmcnt(3)\n\t"
        "v_smfmac_f32_16x16x64_bf16 v[4:7], v[32:35], v[88:95], v1\n\t"
        "ds_read_b128 v[32:35], v10 offset:1024\n\t"
        "s_waitcnt lgkmcnt(3)\n\t"
        "v_smfmac_f32_16x16x64_bf16 v[4:7], v[20:23], v[96:103], v1\n\t"
        "ds_read_b128 v[20:23], v11 offset:0\n\t"
        "s_waitcnt lgkmcnt(3)\n\t"
        "v_smfmac_f32_16x16x64_bf16 v[4:7], v[24:27], v[104:111], v1\n\t"
        "ds_read_b128 v[24:27], v11 offset:1024\n\t"
        "s_waitcnt lgkmcnt(3)\n\t"
        "v_smfmac_f32_16x16x64_bf16 v[4:7], v[28:31], v[112:119], v1\n\t"
        "ds_read_b128 v[28:31], v12 offset:0\n\t"
        "s_waitcnt lgkmcnt(3)\n\t"
        "v_smfmac_f32_16x16x64_bf16 v[4:7], v[32:35], v[120:127], v1\n\t"
        "ds_read_b128 v[32:35], v12 offset:1024\n\t"
        "s_waitcnt lgkmcnt(3)\n\t"
        "v_smfmac_f32_16x16x64_bf16 v[4:7], v[20:23], v[128:135], v1\n\t"
        "ds_read_b128 v[20:23], v13 offset:0\n\t"
        "s_waitcnt lgkmcnt(3)\n\t"
        "v_smfmac_f32_16x16x64_bf16 v[4:7], v[24:27], v[136:143], v1\n\t"
        "ds_read_b128 v[24:27], v13 offset:1024\n\t"
        "s_waitcnt lgkmcnt(3)\n\t"
        "v_smfmac_f32_16x16x64_bf16 v[4:7], v[28:31], v[144:151], v1\n\t"
        "ds_read_b128 v[28:31], v14 offset:0\n\t"
        "s_waitcnt lgkmcnt(3)\n\t"
        "v_smfmac_f32_16x16x64_bf16 v[4:7], v[32:35], v[152:159], v1\n\t"
        "ds_read_b128 v[32:35], v14 offset:1024\n\t"
        "s_waitcnt lgkmcnt(3)\n\t"
        "v_smfmac_f32_16x16x64_bf16 v[4:7], v[20:23], v[160:167], v1\n\t"
        "s_waitcnt lgkmcnt(2)\n\t"
        "v_smfmac_f32_16x16x64_bf16 v[4:7], v[24:27], v[168:175], v1\n\t"
        "s_waitcnt lgkmcnt(1)\n\t"
        "v_smfmac_f32_16x16x64_bf16 v[4:7], v[28:31], v[176:183], v1\n\t"
        "s_waitcnt lgkmcnt(0)\n\t"
        "v_smfmac_f32_16x16x64_bf16 v[4:7], v[32:35], v[184:191], v1\n\t"
        "v_mov_b64 v[8:9], 0\n\t"
        "v_mov_b64 v[10:11], 0\n\t"
        "v_mov_b64 v[12:13], 0\n\t"
        "v_mov_b64 v[14:15], 0\n\t"
        "v_mov_b64 v[16:17], 0\n\t"
        "v_mov_b64 v[18:19], 0\n\t"
        "s_nop 1\n\t"
        "v_add_f32 v4, v4, v5\n\t"
        "v_add_f32 v6, v6, v7\n\t"
        "v_cvt_pk_bf16_f32 v52, v4, v6\n\t"
        "ds_write_b16 v55, v52\n\t"
        "ds_write_b16_d16_hi v55, v52 offset:4\n\t"
        "v_mov_b64 v[4:5], 0\n\t"
        "v_mov_b64 v[6:7], 0\n\t"
        "s_waitcnt lgkmcnt(0)\n\t"
        "s_barrier\n\t"
        "ds_read_b64_tr_b16 v[20:21], v3 offset:0\n\t"
        "ds_read_b64_tr_b16 v[22:23], v3 offset:128\n\t"
        "ds_read_b64_tr_b16 v[24:25], v3 offset:1536\n\t"
        "ds_read_b64_tr_b16 v[26:27], v3 offset:1664\n\t"
        "s_waitcnt lgkmcnt(2)\n\t"
        "v_smfmac_f32_16x16x64_bf16 v[4:7], v[20:23], v[192:199], v1\n\t"
        "s_waitcnt lgkmcnt(0)\n\t"
        "v_smfmac_f32_16x16x64_bf16 v[4:7], v[24:27], v[200:207], v1\n\t"
        "v_smfmac_f32_16x16x64_bf16 v[12:15], v[20:23], v[224:231], v1\n\t"
        "v_smfmac_f32_16x16x64_bf16 v[12:15], v[24:27], v[232:239], v1\n\t"
        "v_smfmac_f32_16x16x64_bf16 v[8:11], v[20:23], v[208:215], v1\n\t"
        "v_smfmac_f32_16x16x64_bf16 v[8:11], v[24:27], v[216:223], v1\n\t"
        "v_smfmac_f32_16x16x64_bf16 v[16:19], v[20:23], v[240:247], v1\n\t"
        "v_smfmac_f32_16x16x64_bf16 v[16:19], v[24:27], v[248:255], v1\n\t"
        "s_nop 1\n\t"
        "v_add_f32 v4, v4, v5\n\t"
        "v_add_f32 v6, v6, v7\n\t"
        "v_cvt_pk_bf16_f32 v60, v4, v6\n\t"
        "v_add_f32 v12, v12, v13\n\t"
        "v_add_f32 v14, v14, v15\n\t"
        "v_cvt_pk_bf16_f32 v62, v12, v14\n\t"
        "s_nop 1\n\t"
        "v_add_f32 v8, v8, v9\n\t"
        "v_add_f32 v10, v10, v11\n\t"
        "v_cvt_pk_bf16_f32 v61, v8, v10\n\t"
        "v_permlane32_swap_b32 v60, v62\n\t"
        "v_add_f32 v16, v16, v17\n\t"
        "v_add_f32 v18, v18, v19\n\t"
        "v_cvt_pk_bf16_f32 v63, v16, v18\n\t"
        "s_nop 1\n\t"
        "v_permlane32_swap_b32 v61, v63\n\t"
        "s_nop 1\n\t"
        "v_permlane16_swap_b32 v60, v61\n\t"
        "v_permlane16_swap_b32 v62, v63\n\t"
        "s_memtime s[92:93]\n\t"
        "s_memrealtime s[94:95]\n\t"
        "s_mov_b32 s98, 0x3d380000\n\t"
        "s_waitcnt lgkmcnt(0)\n\t"
        "s_sub_u32 s96, s92, 4500\n\t"
        "s_sub_u32 s97, s94, 200\n\t"
        "v_readfirstlane_b32 s68, v36\n\t"
        "v_readfirstlane_b32 s69, v37\n\t"
        "s_xor_b32 s68, s68, s54\n\t"
        "s_xor_b32 s69, s69, s55\n\t"
        "s_lshr_b32 s70, s68, 8\n\t"
        "s_or_b32 s70, s70, s69\n\t"
        "s_cmp_eq_u32 s70, 0\n\t"
        "s_cselect_b32 s68, s68, 0\n\t"
        "s_and_b32 s69, s68, 127\n\t"
        "s_max_u32 s51, s51, s69\n\t"
        "s_cmp_ge_u32 s51, 1\n\t"
        "s_cbranch_scc0 .Lv_f0p_%=\n\t"
        ".Lv_f0k_%=:\n\t"
        "s_lshr_b32 s48, s68, 7\n\t"
        "s_lshl_b32 s48, s48, 13\n\t"
        "s_lshl_b32 s70, %[cv], 13\n\t"
        "s_xor_b32 s70, s70, s48\n\t"
        "s_lshr_b32 s71, %[wa], 2\n\t"
        "s_lshl_b32 s71, s71, 14\n\t"
        "s_and_b32 s72, %[wa], 3\n\t"
        "s_lshl_b32 s73, s72, 11\n\t"
        "s_add_u32 s71, s71, s73\n\t"
        "s_add_u32 s47, s71, %[ra]\n\t"
        "s_add_u32 s47, s47, s70\n\t"
        "s_xor_b32 s47, s47, 0x2000\n\t"
        "s_sub_u32 s47, s47, 2097152\n\t"
        "s_xor_b32 s49, %[ov], s48\n\t"
        "s_xor_b32 s50, %[om], s48\n\t"
        "s_xor_b32 s52, %[oy], s48\n\t"
        "s_mov_b32 s53, 0\n\t"
        "s_lshl_b32 s67, %[w], 10\n\t"
        "s_add_u32 s67, s67, 98304\n\t"
        "s_mov_b32 s60, 0\n\t"
        "s_mov_b32 s61, %[of]\n\t"
        "v_lshlrev_b32 v57, 3, v0\n\t"
        "s_mov_b32 s62, 0x7f000000\n\t"
        "s_mov_b32 s63, 0x7f000000\n\t"
        "s_mov_b32 s64, 0x7f000000\n\t"
        "s_mov_b32 s65, 0x7f000000\n\t"
        "s_getpc_b64 s[56:57]\n\t"
        ".Lv_pc_%=:\n\t"
        "s_add_u32 s56, s56, .Lv_yx_%=-.Lv_pc_%=\n\t"
        "s_addc_u32 s57, s57, 0\n\t"
        "s_add_u32 s71, s60, 1\n\t"
        "s_min_u32 s71, s71, 64\n\t"
        "s_cmp_ge_u32 s51, s71\n\t"
        "s_cbranch_scc0 .Lv_g1p_%=\n\t"
        ".Lv_g1k_%=:\n\t"
        "s_and_b32 s70, s60, 3\n\t"
        "s_lshl_b32 s70, s70, 13\n\t"
        "s_add_u32 m0, s70, s67\n\t"
        "s_cmp_lt_u32 s60, 64\n\t"
        "s_cselect_b32 s70, s49, 0x7f000000\n\t"
        "buffer_load_dwordx4 v0, %[rs], s70 offen sc1 lds\n\t"
        "s_add_u32 s60, s60, 1\n\t"
        "s_add_u32 s49, s49, 0x80000\n\t"
        "s_and_b32 s70, s60, 3\n\t"
        "s_cselect_b32 s70, 0, 0x2000\n\t"
        "s_xor_b32 s49, s49, s70\n\t"
        "s_branch .Lv_ym_%=\n\t"
        ".p2align 6\n\t"
        ".Lv_loop_%=:\n\t"
        "s_barrier\n\t"
        "buffer_load_dwordx4 v[20:23], v0, %[rs], s47 offen sc1\n\t"
        "s_nop 4\n\t"
        "buffer_load_dwordx4 v[24:27], v0, %[rs], s47 offen offset:1024 sc1\n\t"
        ".Lv_v0r_%=:\n\t"
        "s_waitcnt vmcnt(1)\n\t"
        "ds_write_b128 v2, v[20:23] offset:0\n\t"
        "v_smfmac_f32_16x16x64_bf16 v[4:7], v[20:23], v[64:71], v1\n\t"
        "v_max_u32 v54, v20, v21\n\t"
        "v_max3_u32 v54, v54, v22, v23\n\t"
        "s_waitcnt vmcnt(0)\n\t"
        "ds_write_b128 v2, v[24:27] offset:1024\n\t"
        "v_max3_u32 v54, v54, v24, v25\n\t"
        "v_max3_u32 v54, v54, v26, v27\n\t"
        "v_smfmac_f32_16x16x64_bf16 v[4:7], v[24:27], v[72:79], v1\n\t"
        "v_cmp_eq_u32 vcc, 0xffbfffbf, v54\n\t"
        "s_waitcnt lgkmcnt(0)\n\t"
        "s_cbranch_vccnz .Lv_v0f_%=\n\t"
        ".Lv_v0k_%=:\n\t"
        "s_barrier\n\t"
        "ds_read_b128 v[28:31], v8 offset:0\n\t"
        "ds_read_b128 v[32:35], v8 offset:1024\n\t"
        "ds_read_b128 v[20:23], v9 offset:0\n\t"
        "ds_read_b128 v[24:27], v9 offset:1024\n\t"
        "s_waitcnt lgkmcnt(3)\n\t"
        "v_smfmac_f32_16x16x64_bf16 v[4:7], v[28:31], v[80:87], v1\n\t"
        "ds_read_b128 v[28:31], v10 offset:0\n\t"
        "s_waitcnt lgkmcnt(3)\n\t"
        "v_smfmac_f32_16x16x64_bf16 v[4:7], v[32:35], v[88:95], v1\n\t"
        "ds_read_b128 v[32:35], v10 offset:1024\n\t"
        "s_waitcnt lgkmcnt(3)\n\t"
        "v_smfmac_f32_16x16x64_bf16 v[4:7], v[20:23], v[96:103], v1\n\t"
        "ds_read_b128 v[20:23], v11 offset:0\n\t"
        "s_waitcnt lgkmcnt(3)\n\t"
        "v_smfmac_f32_16x16x64_bf16 v[4:7], v[24:27], v[104:111], v1\n\t"
        "ds_read_b128 v[24:27], v11 offset:1024\n\t"
        "s_waitcnt lgkmcnt(3)\n\t"
        "v_smfmac_f32_16x16x64_bf16 v[4:7], v[28:31], v[112:119], v1\n\t"
        "ds_read_b128 v[28:31], v12 offset:0\n\t"
        "s_waitcnt lgkmcnt(3)\n\t"
        "v_smfmac_f32_16x16x64_bf16 v[4:7], v[32:35], v[120:127], v1\n\t"
        "ds_read_b128 v[32:35], v12 offset:1024\n\t"
        "s_waitcnt lgkmcnt(3)\n\t"
        "v_smfmac_f32_16x16x64_bf16 v[4:7], v[20:23], v[128:135], v1\n\t"
        "ds_read_b128 v[20:23], v13 offset:0\n\t"
        "s_waitcnt lgkmcnt(3)\n\t"
        "v_smfmac_f32_16x16x64_bf16 v[4:7], v[24:27], v[136:143], v1\n\t"
        "ds_read_b128 v[24:27], v13 offset:1024\n\t"
        "s_waitcnt lgkmcnt(3)\n\t"
        "v_smfmac_f32_16x16x64_bf16 v[4:7], v[28:31], v[144:151], v1\n\t"
        "ds_read_b128 v[28:31], v14 offset:0\n\t"
        "s_waitcnt lgkmcnt(3)\n\t"
        "v_smfmac_f32_16x16x64_bf16 v[4:7], v[32:35], v[152:159], v1\n\t"
        "ds_read_b128 v[32:35], v14 offset:1024\n\t"
        "s_waitcnt lgkmcnt(3)\n\t"
        "v_smfmac_f32_16x16x64_bf16 v[4:7], v[20:23], v[160:167], v1\n\t"
        "s_waitcnt lgkmcnt(2)\n\t"
        "v_smfmac_f32_16x16x64_bf16 v[4:7], v[24:27], v[168:175], v1\n\t"
        "s_waitcnt lgkmcnt(1)\n\t"
        "v_smfmac_f32_16x16x64_bf16 v[4:7], v[28:31], v[176:183], v1\n\t"
        "s_waitcnt lgkmcnt(0)\n\t"
        "v_smfmac_f32_16x16x64_bf16 v[4:7], v[32:35], v[184:191], v1\n\t"
        "v_mov_b64 v[8:9], 0\n\t"
        "v_mov_b64 v[10:11], 0\n\t"
        "v_mov_b64 v[12:13], 0\n\t"
        "v_mov_b64 v[14:15], 0\n\t"
        "v_mov_b64 v[16:17], 0\n\t"
        "v_mov_b64 v[18:19], 0\n\t"
        "s_nop 1\n\t"
        "v_add_f32 v4, v4, v5\n\t"
        "v_add_f32 v6, v6, v7\n\t"
        "v_cvt_pk_bf16_f32 v52, v4, v6\n\t"
        "ds_write_b16 v55, v52\n\t"
        "ds_write_b16_d16_hi v55, v52 offset:4\n\t"
        "v_mov_b64 v[4:5], 0\n\t"
        "v_mov_b64 v[6:7], 0\n\t"
        "s_waitcnt lgkmcnt(0)\n\t"
        "s_barrier\n\t"
        "ds_read_b64_tr_b16 v[20:21], v3 offset:0\n\t"
        "ds_read_b64_tr_b16 v[22:23], v3 offset:128\n\t"
        "ds_read_b64_tr_b16 v[24:25], v3 offset:1536\n\t"
        "ds_read_b64_tr_b16 v[26:27], v3 offset:1664\n\t"
        "s_waitcnt lgkmcnt(2)\n\t"
        "v_smfmac_f32_16x16x64_bf16 v[4:7], v[20:23], v[192:199], v1\n\t"
        "s_waitcnt lgkmcnt(0)\n\t"
        "v_smfmac_f32_16x16x64_bf16 v[4:7], v[24:27], v[200:207], v1\n\t"
        "v_smfmac_f32_16x16x64_bf16 v[12:15], v[20:23], v[224:231], v1\n\t"
        "v_smfmac_f32_16x16x64_bf16 v[12:15], v[24:27], v[232:239], v1\n\t"
        "v_smfmac_f32_16x16x64_bf16 v[8:11], v[20:23], v[208:215], v1\n\t"
        "v_smfmac_f32_16x16x64_bf16 v[8:11], v[24:27], v[216:223], v1\n\t"
        "v_smfmac_f32_16x16x64_bf16 v[16:19], v[20:23], v[240:247], v1\n\t"
        "v_smfmac_f32_16x16x64_bf16 v[16:19], v[24:27], v[248:255], v1\n\t"
        "s_nop 1\n\t"
        "v_add_f32 v4, v4, v5\n\t"
        "v_add_f32 v6, v6, v7\n\t"
        "v_cvt_pk_bf16_f32 v60, v4, v6\n\t"
        "v_add_f32 v12, v12, v13\n\t"
        "v_add_f32 v14, v14, v15\n\t"
        "v_cvt_pk_bf16_f32 v62, v12, v14\n\t"
        "s_nop 1\n\t"
        "v_add_f32 v8, v8, v9\n\t"
        "v_add_f32 v10, v10, v11\n\t"
        "v_cvt_pk_bf16_f32 v61, v8, v10\n\t"
        "v_permlane32_swap_b32 v60, v62\n\t"
        "v_add_f32 v16, v16, v17\n\t"
        "v_add_f32 v18, v18, v19\n\t"
        "v_cvt_pk_bf16_f32 v63, v16, v18\n\t"
        "s_nop 1\n\t"
        "v_permlane32_swap_b32 v61, v63\n\t"
        "s_nop 1\n\t"
        "v_permlane16_swap_b32 v60, v61\n\t"
        "v_permlane16_swap_b32 v62, v63\n\t"
        ".Lv_ym_%=:\n\t"
        "buffer_store_dwordx4 v[60:63], v58, %[rs], s50 offen sc1\n\t"
        "s_lshr_b32 s71, s50, 21\n\t"
        "s_lshl_b32 s71, s71, 21\n\t"
        "s_and_b32 s70, s45, 1\n\t"
        "s_lshl_b32 s70, s70, 13\n\t"
        "s_xor_b32 s70, s70, s48\n\t"
        "s_add_u32 s71, s71, s70\n\t"
        "s_cmp_eq_u32 %[w], 0\n\t"
        "s_cselect_b32 s77, s71, 0x70000000\n\t"
        "s_cmp_eq_u32 s45, 0\n\t"
        "s_cbranch_scc1 .Lv_early_%=\n\t"
        "s_nop 15\n\t"
        "s_nop 15\n\t"
        "s_nop 15\n\t"
        "s_nop 1\n\t"
        "s_xor_b32 s76, s77, %[tw]\n\t"
        "v_and_b32 v52, 0x1f0, v0\n\t"
        "v_lshlrev_b32 v52, 1, v52\n\t"
        "buffer_load_dword v54, v52, %[rs], s76 offen sc1\n\t"
        "s_cmp_eq_u32 s45, 1\n\t"
        "s_cbranch_scc1 .Lv_second_%=\n\t"
        ".Lv_third_%=:\n\t"
        "s_add_u32 s71, s60, 1\n\t"
        "s_min_u32 s71, s71, 64\n\t"
        "s_cmp_ge_u32 s51, s71\n\t"
        "s_cbranch_scc0 .Lv_g2p_%=\n\t"
        ".Lv_g2k_%=:\n\t"
        "s_and_b32 s70, s60, 3\n\t"
        "s_lshl_b32 s70, s70, 13\n\t"
        "s_add_u32 m0, s70, s67\n\t"
        "s_cmp_lt_u32 s60, 64\n\t"
        "s_cselect_b32 s70, s49, 0x7f000000\n\t"
        "buffer_load_dwordx4 v0, %[rs], s70 offen sc1 lds\n\t"
        "s_add_u32 s60, s60, 1\n\t"
        "s_add_u32 s49, s49, 0x80000\n\t"
        "s_and_b32 s70, s60, 3\n\t"
        "s_cselect_b32 s70, 0, 0x2000\n\t"
        "s_xor_b32 s49, s49, s70\n\t"
        "s_add_u32 s71, s60, 1\n\t"
        "s_min_u32 s71, s71, 64\n\t"
        "s_add_u32 s70, s71, 3\n\t"
        "s_andn2_b32 s70, s70, 3\n\t"
        "s_cmp_le_u32 s71, 16\n\t"
        "s_cselect_b32 s70, s71, s70\n\t"
        "s_lshl_b32 s70, s70, 7\n\t"
        "v_mov_b32 v62, s70\n\t"
        "s_cmp_lt_u32 s51, s71\n\t"
        "s_cselect_b32 s70, 0, 0x70000000\n\t"
        "buffer_load_dwordx2 v[62:63], v62, %[rf], s70 offen sc1\n\t"
        "s_swappc_b64 s[58:59], s[56:57]\n\t"
        "s_waitcnt vmcnt(4)\n\t"
        "v_readfirstlane_b32 s68, v62\n\t"
        "v_readfirstlane_b32 s69, v63\n\t"
        "s_xor_b32 s68, s68, s54\n\t"
        "s_xor_b32 s69, s69, s55\n\t"
        "s_lshr_b32 s70, s68, 8\n\t"
        "s_or_b32 s70, s70, s69\n\t"
        "s_cmp_eq_u32 s70, 0\n\t"
        "s_cselect_b32 s68, s68, 0\n\t"
        "s_and_b32 s69, s68, 127\n\t"
        "s_max_u32 s51, s51, s69\n\t"
        ".Lv_eg_%=:\n\t"
        "v_xor_b32 v8, 0x800, v2\n\t"
        "v_xor_b32 v9, 0x1000, v2\n\t"
        "v_xor_b32 v10, 0x1800, v2\n\t"
        "v_xor_b32 v11, 0x2000, v2\n\t"
        "v_xor_b32 v12, 0x2800, v2\n\t"
        "v_xor_b32 v13, 0x3000, v2\n\t"
        "v_xor_b32 v14, 0x3800, v2\n\t"
        "v_mov_b64 v[4:5], 0\n\t"
        "v_mov_b64 v[6:7], 0\n\t"
        "s_add_u32 s45, s45, 1\n\t"
        "s_add_u32 s47, s47, 2097152\n\t"
        "s_and_b32 s47, s47, 0xffffff\n\t"
        "s_xor_b32 s47, s47, 0x2000\n\t"
        "s_add_u32 s50, s50, 2097152\n\t"
        "s_and_b32 s50, s50, 0xffffff\n\t"
        "s_xor_b32 s50, s50, 0x2000\n\t"
        "s_cmp_eq_u32 %[w], 0\n\t"
        "s_cbranch_scc0 .Lv_nw_%=\n\t"
        "s_sub_u32 s68, s92, s96\n\t"
        "s_sub_u32 s69, s94, s97\n\t"
        "s_mov_b32 s96, s92\n\t"
        "s_mov_b32 s97, s94\n\t"
        "v_cvt_f32_u32 v61, s68\n\t"
        "v_cvt_f32_u32 v60, s69\n\t"
        "v_rcp_f32 v61, v61\n\t"
        "s_nop 0\n\t"
        "v_mul_f32 v61, v61, v60\n\t"
        "v_max_f32 v61, 0x3d2aaaab, v61\n\t"
        "v_min_f32 v61, 0x3d638e39, v61\n\t"
        "v_subrev_f32 v61, s98, v61\n\t"
        "v_mov_b32 v60, s98\n\t"
        "v_fmac_f32 v60, 0x3f000000, v61\n\t"
        "v_mul_f32 v61, 0x44200000, v60\n\t"
        "v_cvt_u32_f32 v61, v61\n\t"
        "v_readfirstlane_b32 s98, v60\n\t"
        ".Lv_twm_%=:\n\t"
        "s_waitcnt vmcnt(8)\n\t"
        "v_cmp_eq_u32 s[68:69], 0, v54\n\t"
        "v_max_u32_dpp v54, v54, v54 row_shr:1 row_mask:0xf bank_mask:0xf\n\t"
        "s_nop 1\n\t"
        "v_max_u32_dpp v54, v54, v54 row_shr:2 row_mask:0xf bank_mask:0xf\n\t"
        "s_nop 1\n\t"
        "v_max_u32_dpp v54, v54, v54 row_shr:4 row_mask:0xf bank_mask:0xf\n\t"
        "s_nop 1\n\t"
        "v_max_u32_dpp v54, v54, v54 row_shr:8 row_mask:0xf bank_mask:0xf\n\t"
        "s_nop 1\n\t"
        "v_max_u32_dpp v54, v54, v54 row_bcast:15 row_mask:0xf bank_mask:0xf\n\t"
        "s_nop 1\n\t"
        "v_readlane_b32 s70, v54, 63\n\t"
        "s_cmp_lg_u64 s[68:69], 0\n\t"
        "v_readfirstlane_b32 s68, v61\n\t"
        "s_cbranch_scc1 .Lv_twx_%=\n\t"
        ".Lv_twy_%=:\n\t"
        "s_add_i32 s70, s70, 51\n\t"
        "s_add_u32 s76, s70, s68\n\t"
        ".Lv_twa_%=:\n\t"
        "s_memtime s[92:93]\n\t"
        "s_memrealtime s[94:95]\n\t"
        "s_waitcnt lgkmcnt(0)\n\t"
        "s_sub_i32 s68, s76, s94\n\t"
        "s_sub_u32 s69, s68, 5\n\t"
        "s_cmp_lt_u32 s69, 400\n\t"
        "s_cbranch_scc1 .Lv_twa_%=\n\t"
        "s_cmp_lt_i32 s68, 1\n\t"
        "s_cbranch_scc1 .Lv_twb_%=\n\t"
        "s_nop 2\n\t"
        "s_cmp_lt_i32 s68, 2\n\t"
        "s_cbranch_scc1 .Lv_twb_%=\n\t"
        "s_nop 2\n\t"
        "s_cmp_lt_i32 s68, 3\n\t"
        "s_cbranch_scc1 .Lv_twb_%=\n\t"
        "s_nop 2\n\t"
        "s_cmp_lt_i32 s68, 4\n\t"
        "s_cbranch_scc1 .Lv_twb_%=\n\t"
        "s_nop 2\n\t"
        "s_branch .Lv_twb_%=\n\t"
        ".Lv_twb_%=:\n\t"
        ".Lv_nw_%=:\n\t"
        "s_cmp_lt_u32 s45, 64\n\t"
        "s_cbranch_scc1 .Lv_loop_%=\n\t"
        "s_branch .Lv_end_%=\n\t"
        ".Lv_early_%=:\n\t"
        "s_add_u32 s71, s60, 1\n\t"
        "s_min_u32 s71, s71, 64\n\t"
        "s_add_u32 s70, s71, 3\n\t"
        "s_andn2_b32 s70, s70, 3\n\t"
        "s_cmp_le_u32 s71, 16\n\t"
        "s_cselect_b32 s70, s71, s70\n\t"
        "s_lshl_b32 s70, s70, 7\n\t"
        "v_mov_b32 v62, s70\n\t"
        "s_cmp_lt_u32 s51, s71\n\t"
        "s_cselect_b32 s70, 0, 0x70000000\n\t"
        "buffer_load_dwordx2 v[62:63], v62, %[rf], s70 offen sc1\n\t"
        "s_waitcnt vmcnt(2)\n\t"
        "s_barrier\n\t"
        "s_swappc_b64 s[58:59], s[56:57]\n\t"
        "s_waitcnt vmcnt(4)\n\t"
        "v_readfirstlane_b32 s68, v62\n\t"
        "v_readfirstlane_b32 s69, v63\n\t"
        "s_xor_b32 s68, s68, s54\n\t"
        "s_xor_b32 s69, s69, s55\n\t"
        "s_lshr_b32 s70, s68, 8\n\t"
        "s_or_b32 s70, s70, s69\n\t"
        "s_cmp_eq_u32 s70, 0\n\t"
        "s_cselect_b32 s68, s68, 0\n\t"
        "s_and_b32 s69, s68, 127\n\t"
        "s_max_u32 s51, s51, s69\n\t"
        "s_add_u32 s71, s60, 1\n\t"
        "s_min_u32 s71, s71, 64\n\t"
        "s_cmp_ge_u32 s51, s71\n\t"
        "s_cbranch_scc0 .Lv_g3p_%=\n\t"
        ".Lv_g3k_%=:\n\t"
        "s_and_b32 s70, s60, 3\n\t"
        "s_lshl_b32 s70, s70, 13\n\t"
        "s_add_u32 m0, s70, s67\n\t"
        "s_cmp_lt_u32 s60, 64\n\t"
        "s_cselect_b32 s70, s49, 0x7f000000\n\t"
        "buffer_load_dwordx4 v0, %[rs], s70 offen sc1 lds\n\t"
        "s_add_u32 s60, s60, 1\n\t"
        "s_add_u32 s49, s49, 0x80000\n\t"
        "s_and_b32 s70, s60, 3\n\t"
        "s_cselect_b32 s70, 0, 0x2000\n\t"
        "s_xor_b32 s49, s49, s70\n\t"
        "s_add_u32 s71, s60, 1\n\t"
        "s_min_u32 s71, s71, 64\n\t"
        "s_add_u32 s70, s71, 3\n\t"
        "s_andn2_b32 s70, s70, 3\n\t"
        "s_cmp_le_u32 s71, 16\n\t"
        "s_cselect_b32 s70, s71, s70\n\t"
        "s_lshl_b32 s70, s70, 7\n\t"
        "v_mov_b32 v62, s70\n\t"
        "s_cmp_lt_u32 s51, s71\n\t"
        "s_cselect_b32 s70, 0, 0x70000000\n\t"
        "buffer_load_dwordx2 v[62:63], v62, %[rf], s70 offen sc1\n\t"
        "buffer_store_dwordx4 v[36:39], v0, %[rs], s62 offen sc1\n\t"
        "buffer_store_dwordx4 v[40:43], v0, %[rs], s63 offen sc1\n\t"
        "buffer_store_dwordx4 v[44:47], v0, %[rs], s64 offen sc1\n\t"
        "buffer_store_dwordx4 v[48:51], v0, %[rs], s65 offen sc1\n\t"
        "s_mov_b32 s62, 0x7f000000\n\t"
        "s_mov_b32 s63, 0x7f000000\n\t"
        "s_mov_b32 s64, 0x7f000000\n\t"
        "s_mov_b32 s65, 0x7f000000\n\t"
        "s_mov_b64 exec, 15\n\t"
        "s_waitcnt vmcnt(0)\n\t"
        "buffer_store_dword v1, v57, %[rs], s61 offen sc1\n\t"
        "s_mov_b64 exec, -1\n\t"
        "s_add_u32 s61, s61, 8192\n\t"
        "s_waitcnt vmcnt(1)\n\t"
        "s_barrier\n\t"
        "s_swappc_b64 s[58:59], s[56:57]\n\t"
        "v_readfirstlane_b32 s68, v62\n\t"
        "v_readfirstlane_b32 s69, v63\n\t"
        "s_xor_b32 s68, s68, s54\n\t"
        "s_xor_b32 s69, s69, s55\n\t"
        "s_lshr_b32 s70, s68, 8\n\t"
        "s_or_b32 s70, s70, s69\n\t"
        "s_cmp_eq_u32 s70, 0\n\t"
        "s_cselect_b32 s68, s68, 0\n\t"
        "s_and_b32 s69, s68, 127\n\t"
        "s_max_u32 s51, s51, s69\n\t"
        "s_add_u32 s71, s60, 1\n\t"
        "s_min_u32 s71, s71, 64\n\t"
        "s_cmp_ge_u32 s51, s71\n\t"
        "s_cbranch_scc0 .Lv_g4p_%=\n\t"
        ".Lv_g4k_%=:\n\t"
        "s_and_b32 s70, s60, 3\n\t"
        "s_lshl_b32 s70, s70, 13\n\t"
        "s_add_u32 m0, s70, s67\n\t"
        "s_cmp_lt_u32 s60, 64\n\t"
        "s_cselect_b32 s70, s49, 0x7f000000\n\t"
        "buffer_load_dwordx4 v0, %[rs], s70 offen sc1 lds\n\t"
        "s_add_u32 s60, s60, 1\n\t"
        "s_add_u32 s49, s49, 0x80000\n\t"
        "s_and_b32 s70, s60, 3\n\t"
        "s_cselect_b32 s70, 0, 0x2000\n\t"
        "s_xor_b32 s49, s49, s70\n\t"
        "s_add_u32 s71, s60, 1\n\t"
        "s_min_u32 s71, s71, 64\n\t"
        "s_add_u32 s70, s71, 3\n\t"
        "s_andn2_b32 s70, s70, 3\n\t"
        "s_cmp_le_u32 s71, 16\n\t"
        "s_cselect_b32 s70, s71, s70\n\t"
        "s_lshl_b32 s70, s70, 7\n\t"
        "v_mov_b32 v62, s70\n\t"
        "s_cmp_lt_u32 s51, s71\n\t"
        "s_cselect_b32 s70, 0, 0x70000000\n\t"
        "buffer_load_dwordx2 v[62:63], v62, %[rf], s70 offen sc1\n\t"
        "buffer_store_dwordx4 v[36:39], v0, %[rs], s62 offen sc1\n\t"
        "buffer_store_dwordx4 v[40:43], v0, %[rs], s63 offen sc1\n\t"
        "buffer_store_dwordx4 v[44:47], v0, %[rs], s64 offen sc1\n\t"
        "buffer_store_dwordx4 v[48:51], v0, %[rs], s65 offen sc1\n\t"
        "s_mov_b32 s62, 0x7f000000\n\t"
        "s_mov_b32 s63, 0x7f000000\n\t"
        "s_mov_b32 s64, 0x7f000000\n\t"
        "s_mov_b32 s65, 0x7f000000\n\t"
        "s_waitcnt vmcnt(5)\n\t"
        "s_barrier\n\t"
        "s_swappc_b64 s[58:59], s[56:57]\n\t"
        "s_waitcnt vmcnt(8)\n\t"
        "v_readfirstlane_b32 s68, v62\n\t"
        "v_readfirstlane_b32 s69, v63\n\t"
        "s_xor_b32 s68, s68, s54\n\t"
        "s_xor_b32 s69, s69, s55\n\t"
        "s_lshr_b32 s70, s68, 8\n\t"
        "s_or_b32 s70, s70, s69\n\t"
        "s_cmp_eq_u32 s70, 0\n\t"
        "s_cselect_b32 s68, s68, 0\n\t"
        "s_and_b32 s69, s68, 127\n\t"
        "s_max_u32 s51, s51, s69\n\t"
        "s_add_u32 s71, s60, 1\n\t"
        "s_min_u32 s71, s71, 64\n\t"
        "s_cmp_ge_u32 s51, s71\n\t"
        "s_cbranch_scc0 .Lv_g5p_%=\n\t"
        ".Lv_g5k_%=:\n\t"
        "s_and_b32 s70, s60, 3\n\t"
        "s_lshl_b32 s70, s70, 13\n\t"
        "s_add_u32 m0, s70, s67\n\t"
        "s_cmp_lt_u32 s60, 64\n\t"
        "s_cselect_b32 s70, s49, 0x7f000000\n\t"
        "buffer_load_dwordx4 v0, %[rs], s70 offen sc1 lds\n\t"
        "s_add_u32 s60, s60, 1\n\t"
        "s_add_u32 s49, s49, 0x80000\n\t"
        "s_and_b32 s70, s60, 3\n\t"
        "s_cselect_b32 s70, 0, 0x2000\n\t"
        "s_xor_b32 s49, s49, s70\n\t"
        "s_add_u32 s71, s60, 1\n\t"
        "s_min_u32 s71, s71, 64\n\t"
        "s_add_u32 s70, s71, 3\n\t"
        "s_andn2_b32 s70, s70, 3\n\t"
        "s_cmp_le_u32 s71, 16\n\t"
        "s_cselect_b32 s70, s71, s70\n\t"
        "s_lshl_b32 s70, s70, 7\n\t"
        "v_mov_b32 v62, s70\n\t"
        "s_cmp_lt_u32 s51, s71\n\t"
        "s_cselect_b32 s70, 0, 0x70000000\n\t"
        "buffer_load_dwordx2 v[62:63], v62, %[rf], s70 offen sc1\n\t"
        "buffer_store_dwordx4 v[36:39], v0, %[rs], s62 offen sc1\n\t"
        "buffer_store_dwordx4 v[40:43], v0, %[rs], s63 offen sc1\n\t"
        "buffer_store_dwordx4 v[44:47], v0, %[rs], s64 offen sc1\n\t"
        "buffer_store_dwordx4 v[48:51], v0, %[rs], s65 offen sc1\n\t"
        "s_mov_b32 s62, 0x7f000000\n\t"
        "s_mov_b32 s63, 0x7f000000\n\t"
        "s_mov_b32 s64, 0x7f000000\n\t"
        "s_mov_b32 s65, 0x7f000000\n\t"
        "s_mov_b64 exec, 15\n\t"
        "s_waitcnt vmcnt(10)\n\t"
        "buffer_store_dword v1, v57, %[rs], s61 offen sc1\n\t"
        "s_mov_b64 exec, -1\n\t"
        "s_add_u32 s61, s61, 8192\n\t"
        "s_waitcnt vmcnt(6)\n\t"
        "s_barrier\n\t"
        "s_swappc_b64 s[58:59], s[56:57]\n\t"
        "s_waitcnt vmcnt(9)\n\t"
        "v_readfirstlane_b32 s68, v62\n\t"
        "v_readfirstlane_b32 s69, v63\n\t"
        "s_xor_b32 s68, s68, s54\n\t"
        "s_xor_b32 s69, s69, s55\n\t"
        "s_lshr_b32 s70, s68, 8\n\t"
        "s_or_b32 s70, s70, s69\n\t"
        "s_cmp_eq_u32 s70, 0\n\t"
        "s_cselect_b32 s68, s68, 0\n\t"
        "s_and_b32 s69, s68, 127\n\t"
        "s_max_u32 s51, s51, s69\n\t"
        "s_add_u32 s71, s60, 1\n\t"
        "s_min_u32 s71, s71, 64\n\t"
        "s_cmp_ge_u32 s51, s71\n\t"
        "s_cbranch_scc0 .Lv_g6p_%=\n\t"
        ".Lv_g6k_%=:\n\t"
        "s_and_b32 s70, s60, 3\n\t"
        "s_lshl_b32 s70, s70, 13\n\t"
        "s_add_u32 m0, s70, s67\n\t"
        "s_cmp_lt_u32 s60, 64\n\t"
        "s_cselect_b32 s70, s49, 0x7f000000\n\t"
        "buffer_load_dwordx4 v0, %[rs], s70 offen sc1 lds\n\t"
        "s_add_u32 s60, s60, 1\n\t"
        "s_add_u32 s49, s49, 0x80000\n\t"
        "s_and_b32 s70, s60, 3\n\t"
        "s_cselect_b32 s70, 0, 0x2000\n\t"
        "s_xor_b32 s49, s49, s70\n\t"
        "buffer_store_dwordx4 v[36:39], v0, %[rs], s62 offen sc1\n\t"
        "buffer_store_dwordx4 v[40:43], v0, %[rs], s63 offen sc1\n\t"
        "buffer_store_dwordx4 v[44:47], v0, %[rs], s64 offen sc1\n\t"
        "buffer_store_dwordx4 v[48:51], v0, %[rs], s65 offen sc1\n\t"
        "s_mov_b32 s62, 0x7f000000\n\t"
        "s_mov_b32 s63, 0x7f000000\n\t"
        "s_mov_b32 s64, 0x7f000000\n\t"
        "s_mov_b32 s65, 0x7f000000\n\t"
        "s_mov_b64 exec, 15\n\t"
        "s_waitcnt vmcnt(10)\n\t"
        "buffer_store_dword v1, v57, %[rs], s61 offen sc1\n\t"
        "s_mov_b64 exec, -1\n\t"
        "s_add_u32 s61, s61, 8192\n\t"
        "s_waitcnt vmcnt(5)\n\t"
        "s_barrier\n\t"
        "s_swappc_b64 s[58:59], s[56:57]\n\t"
        "s_xor_b32 s76, s77, %[tw]\n\t"
        "v_and_b32 v52, 0x1f0, v0\n\t"
        "v_lshlrev_b32 v52, 1, v52\n\t"
        "buffer_load_dword v54, v52, %[rs], s76 offen sc1\n\t"
        "s_waitcnt vmcnt(0)\n\t"
        "s_branch .Lv_eg_%=\n\t"
        ".Lv_second_%=:\n\t"
        "s_add_u32 s71, s60, 1\n\t"
        "s_min_u32 s71, s71, 64\n\t"
        "s_cmp_ge_u32 s51, s71\n\t"
        "s_cbranch_scc0 .Lv_g7p_%=\n\t"
        ".Lv_g7k_%=:\n\t"
        "s_and_b32 s70, s60, 3\n\t"
        "s_lshl_b32 s70, s70, 13\n\t"
        "s_add_u32 m0, s70, s67\n\t"
        "s_cmp_lt_u32 s60, 64\n\t"
        "s_cselect_b32 s70, s49, 0x7f000000\n\t"
        "buffer_load_dwordx4 v0, %[rs], s70 offen sc1 lds\n\t"
        "s_add_u32 s60, s60, 1\n\t"
        "s_add_u32 s49, s49, 0x80000\n\t"
        "s_and_b32 s70, s60, 3\n\t"
        "s_cselect_b32 s70, 0, 0x2000\n\t"
        "s_xor_b32 s49, s49, s70\n\t"
        "s_waitcnt vmcnt(0)\n\t"
        "s_barrier\n\t"
        "s_branch .Lv_third_%=\n\t"
        ".Lv_yx_%=:\n\t"
        "s_cmp_lt_u32 s53, 64\n\t"
        "s_cbranch_scc0 .Lv_yxl_%=\n\t"
        "s_and_b32 s70, s53, 3\n\t"
        "s_lshl_b32 s70, s70, 13\n\t"
        "s_add_u32 s70, s70, 98304\n\t"
        "v_add_u32 v59, s70, v56\n\t"
        "ds_read_b128 v[20:23], v59 offset:0\n\t"
        "ds_read_b128 v[24:27], v59 offset:1024\n\t"
        "ds_read_b128 v[28:31], v59 offset:2048\n\t"
        "ds_read_b128 v[32:35], v59 offset:3072\n\t"
        "buffer_store_dwordx4 v[36:39], v0, %[rs], s62 offen sc1\n\t"
        "s_waitcnt lgkmcnt(3)\n\t"
        "v_mfma_f32_16x16x32_bf16 v[4:7], v[20:23], v[192:195], 0\n\t"
        "v_mfma_f32_16x16x32_bf16 v[12:15], v[20:23], v[208:211], 0\n\t"
        "ds_read_b128 v[20:23], v59 offset:4096\n\t"
        "s_waitcnt lgkmcnt(3)\n\t"
        "v_mfma_f32_16x16x32_bf16 v[4:7], v[24:27], v[196:199], v[4:7]\n\t"
        "v_mfma_f32_16x16x32_bf16 v[12:15], v[24:27], v[212:215], v[12:15]\n\t"
        "ds_read_b128 v[24:27], v59 offset:5120\n\t"
        "s_waitcnt lgkmcnt(3)\n\t"
        "v_mfma_f32_16x16x32_bf16 v[4:7], v[28:31], v[200:203], v[4:7]\n\t"
        "v_mfma_f32_16x16x32_bf16 v[12:15], v[28:31], v[216:219], v[12:15]\n\t"
        "ds_read_b128 v[28:31], v59 offset:6144\n\t"
        "buffer_store_dwordx4 v[40:43], v0, %[rs], s63 offen sc1\n\t"
        "s_waitcnt lgkmcnt(3)\n\t"
        "v_mfma_f32_16x16x32_bf16 v[4:7], v[32:35], v[204:207], v[4:7]\n\t"
        "v_mfma_f32_16x16x32_bf16 v[12:15], v[32:35], v[220:223], v[12:15]\n\t"
        "ds_read_b128 v[32:35], v59 offset:7168\n\t"
        "s_waitcnt lgkmcnt(3)\n\t"
        "v_mfma_f32_16x16x32_bf16 v[8:11], v[20:23], v[192:195], 0\n\t"
        "v_mfma_f32_16x16x32_bf16 v[16:19], v[20:23], v[208:211], 0\n\t"
        "ds_read_b128 v[20:23], v59 offset:0\n\t"
        "s_waitcnt lgkmcnt(3)\n\t"
        "v_mfma_f32_16x16x32_bf16 v[8:11], v[24:27], v[196:199], v[8:11]\n\t"
        "v_mfma_f32_16x16x32_bf16 v[16:19], v[24:27], v[212:215], v[16:19]\n\t"
        "ds_read_b128 v[24:27], v59 offset:1024\n\t"
        "s_waitcnt lgkmcnt(3)\n\t"
        "v_mfma_f32_16x16x32_bf16 v[8:11], v[28:31], v[200:203], v[8:11]\n\t"
        "v_mfma_f32_16x16x32_bf16 v[16:19], v[28:31], v[216:219], v[16:19]\n\t"
        "ds_read_b128 v[28:31], v59 offset:2048\n\t"
        "buffer_store_dwordx4 v[44:47], v0, %[rs], s64 offen sc1\n\t"
        "s_waitcnt lgkmcnt(3)\n\t"
        "v_mfma_f32_16x16x32_bf16 v[8:11], v[32:35], v[204:207], v[8:11]\n\t"
        "v_mfma_f32_16x16x32_bf16 v[16:19], v[32:35], v[220:223], v[16:19]\n\t"
        "ds_read_b128 v[32:35], v59 offset:3072\n\t"
        "s_nop 5\n\t"
        "v_cvt_pk_bf16_f32 v36, v4, v5\n\t"
        "v_cvt_pk_bf16_f32 v37, v6, v7\n\t"
        "v_cvt_pk_bf16_f32 v40, v12, v13\n\t"
        "v_cvt_pk_bf16_f32 v41, v14, v15\n\t"
        "v_cvt_pk_bf16_f32 v38, v8, v9\n\t"
        "v_cvt_pk_bf16_f32 v39, v10, v11\n\t"
        "v_cvt_pk_bf16_f32 v42, v16, v17\n\t"
        "v_cvt_pk_bf16_f32 v43, v18, v19\n\t"
        "s_waitcnt lgkmcnt(3)\n\t"
        "v_mfma_f32_16x16x32_bf16 v[4:7], v[20:23], v[224:227], 0\n\t"
        "v_mfma_f32_16x16x32_bf16 v[12:15], v[20:23], v[240:243], 0\n\t"
        "ds_read_b128 v[20:23], v59 offset:4096\n\t"
        "s_waitcnt lgkmcnt(3)\n\t"
        "v_mfma_f32_16x16x32_bf16 v[4:7], v[24:27], v[228:231], v[4:7]\n\t"
        "v_mfma_f32_16x16x32_bf16 v[12:15], v[24:27], v[244:247], v[12:15]\n\t"
        "ds_read_b128 v[24:27], v59 offset:5120\n\t"
        "s_waitcnt lgkmcnt(3)\n\t"
        "v_mfma_f32_16x16x32_bf16 v[4:7], v[28:31], v[232:235], v[4:7]\n\t"
        "v_mfma_f32_16x16x32_bf16 v[12:15], v[28:31], v[248:251], v[12:15]\n\t"
        "ds_read_b128 v[28:31], v59 offset:6144\n\t"
        "buffer_store_dwordx4 v[48:51], v0, %[rs], s65 offen sc1\n\t"
        "s_waitcnt lgkmcnt(3)\n\t"
        "v_mfma_f32_16x16x32_bf16 v[4:7], v[32:35], v[236:239], v[4:7]\n\t"
        "v_mfma_f32_16x16x32_bf16 v[12:15], v[32:35], v[252:255], v[12:15]\n\t"
        "ds_read_b128 v[32:35], v59 offset:7168\n\t"
        "s_waitcnt lgkmcnt(3)\n\t"
        "v_mfma_f32_16x16x32_bf16 v[8:11], v[20:23], v[224:227], 0\n\t"
        "v_mfma_f32_16x16x32_bf16 v[16:19], v[20:23], v[240:243], 0\n\t"
        "s_waitcnt lgkmcnt(2)\n\t"
        "v_mfma_f32_16x16x32_bf16 v[8:11], v[24:27], v[228:231], v[8:11]\n\t"
        "v_mfma_f32_16x16x32_bf16 v[16:19], v[24:27], v[244:247], v[16:19]\n\t"
        "s_waitcnt lgkmcnt(1)\n\t"
        "v_mfma_f32_16x16x32_bf16 v[8:11], v[28:31], v[232:235], v[8:11]\n\t"
        "v_mfma_f32_16x16x32_bf16 v[16:19], v[28:31], v[248:251], v[16:19]\n\t"
        "s_waitcnt lgkmcnt(0)\n\t"
        "v_mfma_f32_16x16x32_bf16 v[8:11], v[32:35], v[236:239], v[8:11]\n\t"
        "v_mfma_f32_16x16x32_bf16 v[16:19], v[32:35], v[252:255], v[16:19]\n\t"
        "s_nop 5\n\t"
        "v_cvt_pk_bf16_f32 v44, v4, v5\n\t"
        "v_cvt_pk_bf16_f32 v45, v6, v7\n\t"
        "v_cvt_pk_bf16_f32 v48, v12, v13\n\t"
        "v_cvt_pk_bf16_f32 v49, v14, v15\n\t"
        "v_cvt_pk_bf16_f32 v46, v8, v9\n\t"
        "v_cvt_pk_bf16_f32 v47, v10, v11\n\t"
        "v_cvt_pk_bf16_f32 v50, v16, v17\n\t"
        "v_cvt_pk_bf16_f32 v51, v18, v19\n\t"
        "s_mov_b32 s62, s52\n\t"
        "s_add_u32 s63, s52, 65536\n\t"
        "s_add_u32 s64, s52, 131072\n\t"
        "s_add_u32 s65, s52, 196608\n\t"
        "s_add_u32 s53, s53, 1\n\t"
        "s_add_u32 s52, s52, 4194304\n\t"
        "s_and_b32 s70, s53, 7\n\t"
        "s_cselect_b32 s70, 0, 33554432\n\t"
        "s_sub_u32 s52, s52, s70\n\t"
        "s_setpc_b64 s[58:59]\n\t"
        ".Lv_yxl_%=:\n\t"
        "buffer_store_dwordx4 v[36:39], v0, %[rs], s62 offen sc1\n\t"
        "buffer_store_dwordx4 v[40:43], v0, %[rs], s63 offen sc1\n\t"
        "buffer_store_dwordx4 v[44:47], v0, %[rs], s64 offen sc1\n\t"
        "buffer_store_dwordx4 v[48:51], v0, %[rs], s65 offen sc1\n\t"
        "s_mov_b32 s62, 0x7f000000\n\t"
        "s_mov_b32 s63, 0x7f000000\n\t"
        "s_mov_b32 s64, 0x7f000000\n\t"
        "s_mov_b32 s65, 0x7f000000\n\t"
        "s_setpc_b64 s[58:59]\n\t"
        ".Lv_u0f_%=:\n\t"
        "s_cmp_eq_u32 s45, 0\n\t"
        "s_cbranch_scc1 .Lv_u0k_%=\n\t"
        "s_add_u32 s79, s79, 1\n\t"
        "buffer_load_dwordx4 v[20:23], v0, %[rs], s47 offen sc1\n\t"
        "buffer_load_dwordx4 v[24:27], v0, %[rs], s47 offen offset:1024 sc1\n\t"
        "s_nop 7\n\t"
        "v_mov_b64 v[4:5], 0\n\t"
        "v_mov_b64 v[6:7], 0\n\t"
        "s_waitcnt vmcnt(0)\n\t"
        "s_sub_u32 s46, s46, 1\n\t"
        "s_cbranch_scc0 .Lv_u0r_%=\n\t"
        "s_mov_b32 s46, 0\n\t"
        "s_mov_b32 s46, 0\n\t"
        "v_mov_b32 v52, 0\n\t"
        "s_add_u32 s68, s45, 1000\n\t"
        "v_mov_b32 v53, s68\n\t"
        "global_store_dword v52, v53, %[tm] sc1\n\t"
        "s_branch .Lv_u0k_%=\n\t"
        ".Lv_f0p_%=:\n\t"
        "s_add_u32 s70, 1, 3\n\t"
        "s_andn2_b32 s70, s70, 3\n\t"
        "s_cmp_le_u32 1, 16\n\t"
        "s_cselect_b32 s70, 1, s70\n\t"
        "s_lshl_b32 s70, s70, 7\n\t"
        "v_mov_b32 v20, 0\n\t"
        "buffer_load_dwordx2 v[20:21], v20, %[rf], s70 offen sc1\n\t"
        "s_waitcnt vmcnt(0)\n\t"
        "v_readfirstlane_b32 s68, v20\n\t"
        "v_readfirstlane_b32 s69, v21\n\t"
        "s_xor_b32 s68, s68, s54\n\t"
        "s_xor_b32 s69, s69, s55\n\t"
        "s_lshr_b32 s70, s68, 8\n\t"
        "s_or_b32 s70, s70, s69\n\t"
        "s_cmp_eq_u32 s70, 0\n\t"
        "s_cselect_b32 s68, s68, 0\n\t"
        "s_and_b32 s69, s68, 127\n\t"
        "s_max_u32 s51, s51, s69\n\t"
        "s_cmp_ge_u32 s51, 1\n\t"
        "s_cbranch_scc1 .Lv_f0k_%=\n\t"
        "s_sleep 4\n\t"
        "s_sub_u32 s46, s46, 1\n\t"
        "s_cbranch_scc0 .Lv_f0p_%=\n\t"
        "s_mov_b32 s46, 0\n\t"
        "s_mov_b32 s46, 0\n\t"
        "v_mov_b32 v20, 0\n\t"
        "s_add_u32 s68, s45, 3000\n\t"
        "v_mov_b32 v21, s68\n\t"
        "global_store_dword v20, v21, %[tm] sc1\n\t"
        "s_branch .Lv_f0k_%=\n\t"
        ".Lv_g1p_%=:\n\t"
        "s_add_u32 s70, s71, 3\n\t"
        "s_andn2_b32 s70, s70, 3\n\t"
        "s_cmp_le_u32 s71, 16\n\t"
        "s_cselect_b32 s70, s71, s70\n\t"
        "s_lshl_b32 s70, s70, 7\n\t"
        "v_mov_b32 v20, 0\n\t"
        "buffer_load_dwordx2 v[20:21], v20, %[rf], s70 offen sc1\n\t"
        "s_waitcnt vmcnt(0)\n\t"
        "v_readfirstlane_b32 s68, v20\n\t"
        "v_readfirstlane_b32 s69, v21\n\t"
        "s_xor_b32 s68, s68, s54\n\t"
        "s_xor_b32 s69, s69, s55\n\t"
        "s_lshr_b32 s70, s68, 8\n\t"
        "s_or_b32 s70, s70, s69\n\t"
        "s_cmp_eq_u32 s70, 0\n\t"
        "s_cselect_b32 s68, s68, 0\n\t"
        "s_and_b32 s69, s68, 127\n\t"
        "s_max_u32 s51, s51, s69\n\t"
        "s_cmp_ge_u32 s51, s71\n\t"
        "s_cbranch_scc1 .Lv_g1k_%=\n\t"
        "s_sleep 4\n\t"
        "s_sub_u32 s46, s46, 1\n\t"
        "s_cbranch_scc0 .Lv_g1p_%=\n\t"
        "s_mov_b32 s46, 0\n\t"
        "s_mov_b32 s46, 0\n\t"
        "v_mov_b32 v20, 0\n\t"
        "s_add_u32 s68, s45, 3000\n\t"
        "v_mov_b32 v21, s68\n\t"
        "global_store_dword v20, v21, %[tm] sc1\n\t"
        "s_branch .Lv_g1k_%=\n\t"
        ".Lv_v0f_%=:\n\t"
        "s_cmp_eq_u32 s45, 0\n\t"
        "s_cbranch_scc1 .Lv_v0k_%=\n\t"
        "s_add_u32 s79, s79, 1\n\t"
        "buffer_load_dwordx4 v[20:23], v0, %[rs], s47 offen sc1\n\t"
        "buffer_load_dwordx4 v[24:27], v0, %[rs], s47 offen offset:1024 sc1\n\t"
        "s_nop 7\n\t"
        "v_mov_b64 v[4:5], 0\n\t"
        "v_mov_b64 v[6:7], 0\n\t"
        "s_waitcnt vmcnt(0)\n\t"
        "s_sub_u32 s46, s46, 1\n\t"
        "s_cbranch_scc0 .Lv_v0r_%=\n\t"
        "s_mov_b32 s46, 0\n\t"
        "s_mov_b32 s46, 0\n\t"
        "v_mov_b32 v52, 0\n\t"
        "s_add_u32 s68, s45, 1000\n\t"
        "v_mov_b32 v53, s68\n\t"
        "global_store_dword v52, v53, %[tm] sc1\n\t"
        "s_branch .Lv_v0k_%=\n\t"
        ".Lv_g2p_%=:\n\t"
        "s_add_u32 s70, s71, 3\n\t"
        "s_andn2_b32 s70, s70, 3\n\t"
        "s_cmp_le_u32 s71, 16\n\t"
        "s_cselect_b32 s70, s71, s70\n\t"
        "s_lshl_b32 s70, s70, 7\n\t"
        "v_mov_b32 v20, 0\n\t"
        "buffer_load_dwordx2 v[20:21], v20, %[rf], s70 offen sc1\n\t"
        "s_waitcnt vmcnt(0)\n\t"
        "v_readfirstlane_b32 s68, v20\n\t"
        "v_readfirstlane_b32 s69, v21\n\t"
        "s_xor_b32 s68, s68, s54\n\t"
        "s_xor_b32 s69, s69, s55\n\t"
        "s_lshr_b32 s70, s68, 8\n\t"
        "s_or_b32 s70, s70, s69\n\t"
        "s_cmp_eq_u32 s70, 0\n\t"
        "s_cselect_b32 s68, s68, 0\n\t"
        "s_and_b32 s69, s68, 127\n\t"
        "s_max_u32 s51, s51, s69\n\t"
        "s_cmp_ge_u32 s51, s71\n\t"
        "s_cbranch_scc1 .Lv_g2k_%=\n\t"
        "s_sleep 4\n\t"
        "s_sub_u32 s46, s46, 1\n\t"
        "s_cbranch_scc0 .Lv_g2p_%=\n\t"
        "s_mov_b32 s46, 0\n\t"
        "s_mov_b32 s46, 0\n\t"
        "v_mov_b32 v20, 0\n\t"
        "s_add_u32 s68, s45, 3000\n\t"
        "v_mov_b32 v21, s68\n\t"
        "global_store_dword v20, v21, %[tm] sc1\n\t"
        "s_branch .Lv_g2k_%=\n\t"
        ".Lv_twx_%=:\n\t"
        "s_add_u32 s79, s79, 0x1000000\n\t"
        "s_sleep 4\n\t"
        "buffer_load_dword v54, v52, %[rs], s76 offen sc1\n\t"
        "s_waitcnt vmcnt(0)\n\t"
        "s_sub_u32 s46, s46, 1\n\t"
        "s_cbranch_scc0 .Lv_twm_%=\n\t"
        "s_mov_b32 s46, 0\n\t"
        "s_branch .Lv_twy_%=\n\t"
        ".Lv_g3p_%=:\n\t"
        "s_add_u32 s70, s71, 3\n\t"
        "s_andn2_b32 s70, s70, 3\n\t"
        "s_cmp_le_u32 s71, 16\n\t"
        "s_cselect_b32 s70, s71, s70\n\t"
        "s_lshl_b32 s70, s70, 7\n\t"
        "v_mov_b32 v20, 0\n\t"
        "buffer_load_dwordx2 v[20:21], v20, %[rf], s70 offen sc1\n\t"
        "s_waitcnt vmcnt(0)\n\t"
        "v_readfirstlane_b32 s68, v20\n\t"
        "v_readfirstlane_b32 s69, v21\n\t"
        "s_xor_b32 s68, s68, s54\n\t"
        "s_xor_b32 s69, s69, s55\n\t"
        "s_lshr_b32 s70, s68, 8\n\t"
        "s_or_b32 s70, s70, s69\n\t"
        "s_cmp_eq_u32 s70, 0\n\t"
        "s_cselect_b32 s68, s68, 0\n\t"
        "s_and_b32 s69, s68, 127\n\t"
        "s_max_u32 s51, s51, s69\n\t"
        "s_cmp_ge_u32 s51, s71\n\t"
        "s_cbranch_scc1 .Lv_g3k_%=\n\t"
        "s_sleep 4\n\t"
        "s_sub_u32 s46, s46, 1\n\t"
        "s_cbranch_scc0 .Lv_g3p_%=\n\t"
        "s_mov_b32 s46, 0\n\t"
        "s_mov_b32 s46, 0\n\t"
        "v_mov_b32 v20, 0\n\t"
        "s_add_u32 s68, s45, 3000\n\t"
        "v_mov_b32 v21, s68\n\t"
        "global_store_dword v20, v21, %[tm] sc1\n\t"
        "s_branch .Lv_g3k_%=\n\t"
        ".Lv_g4p_%=:\n\t"
        "s_add_u32 s70, s71, 3\n\t"
        "s_andn2_b32 s70, s70, 3\n\t"
        "s_cmp_le_u32 s71, 16\n\t"
        "s_cselect_b32 s70, s71, s70\n\t"
        "s_lshl_b32 s70, s70, 7\n\t"
        "v_mov_b32 v20, 0\n\t"
        "buffer_load_dwordx2 v[20:21], v20, %[rf], s70 offen sc1\n\t"
        "s_waitcnt vmcnt(0)\n\t"
        "v_readfirstlane_b32 s68, v20\n\t"
        "v_readfirstlane_b32 s69, v21\n\t"
        "s_xor_b32 s68, s68, s54\n\t"
        "s_xor_b32 s69, s69, s55\n\t"
        "s_lshr_b32 s70, s68, 8\n\t"
        "s_or_b32 s70, s70, s69\n\t"
        "s_cmp_eq_u32 s70, 0\n\t"
        "s_cselect_b32 s68, s68, 0\n\t"
        "s_and_b32 s69, s68, 127\n\t"
        "s_max_u32 s51, s51, s69\n\t"
        "s_cmp_ge_u32 s51, s71\n\t"
        "s_cbranch_scc1 .Lv_g4k_%=\n\t"
        "s_sleep 4\n\t"
        "s_sub_u32 s46, s46, 1\n\t"
        "s_cbranch_scc0 .Lv_g4p_%=\n\t"
        "s_mov_b32 s46, 0\n\t"
        "s_mov_b32 s46, 0\n\t"
        "v_mov_b32 v20, 0\n\t"
        "s_add_u32 s68, s45, 3000\n\t"
        "v_mov_b32 v21, s68\n\t"
        "global_store_dword v20, v21, %[tm] sc1\n\t"
        "s_branch .Lv_g4k_%=\n\t"
        ".Lv_g5p_%=:\n\t"
        "s_add_u32 s70, s71, 3\n\t"
        "s_andn2_b32 s70, s70, 3\n\t"
        "s_cmp_le_u32 s71, 16\n\t"
        "s_cselect_b32 s70, s71, s70\n\t"
        "s_lshl_b32 s70, s70, 7\n\t"
        "v_mov_b32 v20, 0\n\t"
        "buffer_load_dwordx2 v[20:21], v20, %[rf], s70 offen sc1\n\t"
        "s_waitcnt vmcnt(0)\n\t"
        "v_readfirstlane_b32 s68, v20\n\t"
        "v_readfirstlane_b32 s69, v21\n\t"
        "s_xor_b32 s68, s68, s54\n\t"
        "s_xor_b32 s69, s69, s55\n\t"
        "s_lshr_b32 s70, s68, 8\n\t"
        "s_or_b32 s70, s70, s69\n\t"
        "s_cmp_eq_u32 s70, 0\n\t"
        "s_cselect_b32 s68, s68, 0\n\t"
        "s_and_b32 s69, s68, 127\n\t"
        "s_max_u32 s51, s51, s69\n\t"
        "s_cmp_ge_u32 s51, s71\n\t"
        "s_cbranch_scc1 .Lv_g5k_%=\n\t"
        "s_sleep 4\n\t"
        "s_sub_u32 s46, s46, 1\n\t"
        "s_cbranch_scc0 .Lv_g5p_%=\n\t"
        "s_mov_b32 s46, 0\n\t"
        "s_mov_b32 s46, 0\n\t"
        "v_mov_b32 v20, 0\n\t"
        "s_add_u32 s68, s45, 3000\n\t"
        "v_mov_b32 v21, s68\n\t"
        "global_store_dword v20, v21, %[tm] sc1\n\t"
        "s_branch .Lv_g5k_%=\n\t"
        ".Lv_g6p_%=:\n\t"
        "s_add_u32 s70, s71, 3\n\t"
        "s_andn2_b32 s70, s70, 3\n\t"
        "s_cmp_le_u32 s71, 16\n\t"
        "s_cselect_b32 s70, s71, s70\n\t"
        "s_lshl_b32 s70, s70, 7\n\t"
        "v_mov_b32 v20, 0\n\t"
        "buffer_load_dwordx2 v[20:21], v20, %[rf], s70 offen sc1\n\t"
        "s_waitcnt vmcnt(0)\n\t"
        "v_readfirstlane_b32 s68, v20\n\t"
        "v_readfirstlane_b32 s69, v21\n\t"
        "s_xor_b32 s68, s68, s54\n\t"
        "s_xor_b32 s69, s69, s55\n\t"
        "s_lshr_b32 s70, s68, 8\n\t"
        "s_or_b32 s70, s70, s69\n\t"
        "s_cmp_eq_u32 s70, 0\n\t"
        "s_cselect_b32 s68, s68, 0\n\t"
        "s_and_b32 s69, s68, 127\n\t"
        "s_max_u32 s51, s51, s69\n\t"
        "s_cmp_ge_u32 s51, s71\n\t"
        "s_cbranch_scc1 .Lv_g6k_%=\n\t"
        "s_sleep 4\n\t"
        "s_sub_u32 s46, s46, 1\n\t"
        "s_cbranch_scc0 .Lv_g6p_%=\n\t"
        "s_mov_b32 s46, 0\n\t"
        "s_mov_b32 s46, 0\n\t"
        "v_mov_b32 v20, 0\n\t"
        "s_add_u32 s68, s45, 3000\n\t"
        "v_mov_b32 v21, s68\n\t"
        "global_store_dword v20, v21, %[tm] sc1\n\t"
        "s_branch .Lv_g6k_%=\n\t"
        ".Lv_g7p_%=:\n\t"
        "s_add_u32 s70, s71, 3\n\t"
        "s_andn2_b32 s70, s70, 3\n\t"
        "s_cmp_le_u32 s71, 16\n\t"
        "s_cselect_b32 s70, s71, s70\n\t"
        "s_lshl_b32 s70, s70, 7\n\t"
        "v_mov_b32 v20, 0\n\t"
        "buffer_load_dwordx2 v[20:21], v20, %[rf], s70 offen sc1\n\t"
        "s_waitcnt vmcnt(0)\n\t"
        "v_readfirstlane_b32 s68, v20\n\t"
        "v_readfirstlane_b32 s69, v21\n\t"
        "s_xor_b32 s68, s68, s54\n\t"
        "s_xor_b32 s69, s69, s55\n\t"
        "s_lshr_b32 s70, s68, 8\n\t"
        "s_or_b32 s70, s70, s69\n\t"
        "s_cmp_eq_u32 s70, 0\n\t"
        "s_cselect_b32 s68, s68, 0\n\t"
        "s_and_b32 s69, s68, 127\n\t"
        "s_max_u32 s51, s51, s69\n\t"
        "s_cmp_ge_u32 s51, s71\n\t"
        "s_cbranch_scc1 .Lv_g7k_%=\n\t"
        "s_sleep 4\n\t"
        "s_sub_u32 s46, s46, 1\n\t"
        "s_cbranch_scc0 .Lv_g7p_%=\n\t"
        "s_mov_b32 s46, 0\n\t"
        "s_mov_b32 s46, 0\n\t"
        "v_mov_b32 v20, 0\n\t"
        "s_add_u32 s68, s45, 3000\n\t"
        "v_mov_b32 v21, s68\n\t"
        "global_store_dword v20, v21, %[tm] sc1\n\t"
        "s_branch .Lv_g7k_%=\n\t"
        ".Lcode_v1:\n\t"
        ".Lv_end_%=:\n\t"
        "s_endpgm\n\t"
        :: [rs] "s"(RSRC(arena(a.slots), ARENA_BYTES)), [rw] "s"(RSRC(a.Wv + co, (DM * HD - co) * 2)), [rx] "s"(RSRC(a.Wo + oo, (DM * HD - oo) * 2)), [rm] "s"(RSRC(a.mem0, 8 * DM * 2)),
           [ep] "s"(a.boot + 144), [rf] "s"(PROG(64 + h)), [tm] "s"(a.sync + 40 * 32), [w] "s"(w), [wa] "s"(w ^ (4 * half)), [cv] "s"(cls_v(h, half)), [ra] "s"((4 + 2 * rep) * 16384),
           [ov] "s"(KV_OFF + h * 16384 + (cls_s(h) ^ 1) * 8192 + w * 1024), [om] "s"((64 + j0) * 16384 + cls_r(j0) * 8192 + h * 256),
           [oy] "s"(YX_OFF + (4 * j0 + (h >> 3)) * 16384 + (cls_r(j0) ^ half ^ YXC) * 8192 + (h & 7) * 1024), [tw] "s"(TW(cls_v(h, half))), [of] "s"(21 * 16384 + j0 * 128 + h * 4) STIN(w ? 251 : 32 + 2 * h + half)
        : "memory", "vcc", "scc", ALLV, ALLS);
    __builtin_unreachable();
}
__device__ __forceinline__ void column_wg(const int w, const int j, const unsigned t0) {
    const Args a = get_args();
    asm volatile(
        ".Lcode_r0:\n\t"
        "v_mbcnt_lo_u32_b32 v5, -1, 0\n\t"
        "v_mbcnt_hi_u32_b32 v5, -1, v5\n\t"
        "s_mov_b32 s79, 0\n\t"
        "s_mov_b32 s45, 0\n\t"
        "s_mov_b32 s46, 200000\n\t"
        "v_lshlrev_b32 v0, 4, v5\n\t"
        "s_mov_b32 s66, 0xffbfffbf\n\t"
        "s_lshl_b32 s70, %[cr], 13\n\t"
        "s_xor_b32 s48, s70, %[t0]\n\t"
        "s_mov_b32 s49, %[t0]\n\t"
        "v_lshrrev_b32 v6, 4, v5\n\t"
        "s_mov_b32 s44, 0\n\t"
        "s_cmp_gt_u32 %[w], 2\n\t"
        "s_cbranch_scc1 .Lr_dma_%=\n\t"
        "s_cmp_gt_u32 %[w], 0\n\t"
        "s_cbranch_scc1 .Lr_ox_%=\n\t"
        "v_mov_b32 v8, 0x4444\n\t"
        "v_mov_b32 v7, 0xeeee\n\t"
        "v_and_b32 v1, 1, v5\n\t"
        "v_cmp_eq_u32 vcc, 0, v1\n\t"
        "s_mov_b32 s64, 0x55555555\n\t"
        "s_mov_b32 s65, 0x55555555\n\t"
        "v_cndmask_b32 v1, v7, v8, vcc\n\t"
        "v_lshlrev_b32 v2, 6, v6\n\t"
        "v_and_b32 v7, 1, v5\n\t"
        "v_lshl_add_u32 v2, v7, 5, v2\n\t"
        "v_bfe_u32 v7, v5, 1, 1\n\t"
        "v_lshl_add_u32 v2, v7, 4, v2\n\t"
        "v_bfe_u32 v7, v5, 2, 2\n\t"
        "v_lshl_add_u32 v2, v7, 2, v2\n\t"
        "s_setprio 3\n\t"
        "s_memtime s[62:63]\n\t"
        "s_add_u32 s52, %[al], s48\n\t"
        "s_mov_b32 s53, %[ah]\n\t"
        "s_mov_b32 s54, 2097152\n\t"
        "s_mov_b32 s55, 0x20000\n\t"
        "s_xor_b32 s56, s52, 0x2000\n\t"
        "s_mov_b32 s57, s53\n\t"
        "s_mov_b32 s58, 2097152\n\t"
        "s_mov_b32 s59, 0x20000\n\t"
        "s_mov_b32 s50, 4096\n\t"
        "s_mov_b32 s51, 16384\n\t"
        "s_mov_b32 s60, 20480\n\t"
        "s_mov_b32 s61, 32768\n\t"
        "s_add_u32 s47, %[om], 4096\n\t"
        "v_add_u32 v3, 98304, v0\n\t"
        "s_waitcnt lgkmcnt(0)\n\t"
        "s_add_u32 s72, %[oa], 65536\n\t"
        "s_add_u32 s73, %[oa], 98304\n\t"
        "s_add_u32 s74, %[oa], 131072\n\t"
        "s_add_u32 s75, %[oa], 163840\n\t"
        "v_mov_b64 v[8:9], 0\n\t"
        "v_mov_b64 v[10:11], 0\n\t"
        "s_mov_b64 vcc, s[64:65]\n\t"
        "v_mov_b32 v21, 0\n\t"
        "s_branch .Lr_st_%=\n\t"
        ".p2align 6\n\t"
        ".Lr_loop_%=:\n\t"
        ".Lr_twa_%=:\n\t"
        "s_memrealtime s[68:69]\n\t"
        "s_waitcnt lgkmcnt(0)\n\t"
        "s_sub_i32 s68, s67, s68\n\t"
        "s_sub_u32 s69, s68, 5\n\t"
        "s_cmp_lt_u32 s69, 400\n\t"
        "s_cbranch_scc1 .Lr_twa_%=\n\t"
        "s_cmp_lt_i32 s68, 1\n\t"
        "s_cbranch_scc1 .Lr_twb_%=\n\t"
        "s_nop 2\n\t"
        "s_cmp_lt_i32 s68, 2\n\t"
        "s_cbranch_scc1 .Lr_twb_%=\n\t"
        "s_nop 2\n\t"
        "s_cmp_lt_i32 s68, 3\n\t"
        "s_cbranch_scc1 .Lr_twb_%=\n\t"
        "s_nop 2\n\t"
        "s_cmp_lt_i32 s68, 4\n\t"
        "s_cbranch_scc1 .Lr_twb_%=\n\t"
        "s_nop 2\n\t"
        "s_branch .Lr_twb_%=\n\t"
        ".Lr_twb_%=:\n\t"
        ".Lr_pp_%=:\n\t"
        "buffer_load_dwordx4 v[224:227], v0, s[52:55], %[om] offen offset:0 sc1\n\t"
        "buffer_load_dwordx4 v[228:231], v0, s[52:55], %[om] offen offset:1024 sc1\n\t"
        "buffer_load_dwordx4 v[232:235], v0, s[52:55], %[om] offen offset:2048 sc1\n\t"
        "buffer_load_dwordx4 v[236:239], v0, s[52:55], %[om] offen offset:3072 sc1\n\t"
        "buffer_load_dwordx4 v[240:243], v0, s[52:55], s47 offen offset:0 sc1\n\t"
        "buffer_load_dwordx4 v[244:247], v0, s[52:55], s47 offen offset:1024 sc1\n\t"
        "buffer_load_dwordx4 v[248:251], v0, s[52:55], s47 offen offset:2048 sc1\n\t"
        "buffer_load_dwordx4 v[252:255], v0, s[52:55], s47 offen offset:3072 sc1\n\t"
        "s_nop 9\n\t"
        "buffer_load_dwordx4 v[16:19], v0, s[52:55], 0 offen offset:0 sc1\n\t"
        "buffer_load_dwordx4 v[20:23], v0, s[52:55], 0 offen offset:1024 sc1\n\t"
        "buffer_load_dwordx4 v[24:27], v0, s[52:55], 0 offen offset:2048 sc1\n\t"
        "buffer_load_dwordx4 v[28:31], v0, s[52:55], 0 offen offset:3072 sc1\n\t"
        "buffer_load_dwordx4 v[32:35], v0, s[52:55], s50 offen offset:0 sc1\n\t"
        "buffer_load_dwordx4 v[36:39], v0, s[52:55], s50 offen offset:1024 sc1\n\t"
        "buffer_load_dwordx4 v[40:43], v0, s[52:55], s50 offen offset:2048 sc1\n\t"
        "buffer_load_dwordx4 v[44:47], v0, s[52:55], s50 offen offset:3072 sc1\n\t"
        "buffer_load_dwordx4 v[48:51], v0, s[52:55], s51 offen offset:0 sc1\n\t"
        "buffer_load_dwordx4 v[52:55], v0, s[52:55], s51 offen offset:1024 sc1\n\t"
        "buffer_load_dwordx4 v[56:59], v0, s[52:55], s51 offen offset:2048 sc1\n\t"
        "buffer_load_dwordx4 v[60:63], v0, s[52:55], s51 offen offset:3072 sc1\n\t"
        "buffer_load_dwordx4 v[64:67], v0, s[52:55], s60 offen offset:0 sc1\n\t"
        "buffer_load_dwordx4 v[68:71], v0, s[52:55], s60 offen offset:1024 sc1\n\t"
        "buffer_load_dwordx4 v[72:75], v0, s[52:55], s60 offen offset:2048 sc1\n\t"
        "buffer_load_dwordx4 v[76:79], v0, s[52:55], s60 offen offset:3072 sc1\n\t"
        "buffer_load_dwordx4 v[80:83], v0, s[52:55], s61 offen offset:0 sc1\n\t"
        "buffer_load_dwordx4 v[84:87], v0, s[52:55], s61 offen offset:1024 sc1\n\t"
        "buffer_load_dwordx4 v[88:91], v0, s[52:55], s61 offen offset:2048 sc1\n\t"
        "buffer_load_dwordx4 v[92:95], v0, s[52:55], s61 offen offset:3072 sc1\n\t"
        "s_waitcnt vmcnt(18)\n\t"
        "v_smfmac_f32_16x16x64_bf16 v[8:11], v[16:19], v[96:103], v1\n\t"
        "v_smfmac_f32_16x16x64_bf16 v[8:11], v[20:23], v[104:111], v1\n\t"
        "s_waitcnt vmcnt(16)\n\t"
        "v_smfmac_f32_16x16x64_bf16 v[8:11], v[24:27], v[112:119], v1\n\t"
        "v_smfmac_f32_16x16x64_bf16 v[8:11], v[28:31], v[120:127], v1\n\t"
        "s_waitcnt vmcnt(14)\n\t"
        "v_smfmac_f32_16x16x64_bf16 v[8:11], v[32:35], v[128:135], v1\n\t"
        "v_smfmac_f32_16x16x64_bf16 v[8:11], v[36:39], v[136:143], v1\n\t"
        "s_waitcnt vmcnt(12)\n\t"
        "v_smfmac_f32_16x16x64_bf16 v[8:11], v[40:43], v[144:151], v1\n\t"
        "v_smfmac_f32_16x16x64_bf16 v[8:11], v[44:47], v[152:159], v1\n\t"
        "s_waitcnt vmcnt(10)\n\t"
        "v_smfmac_f32_16x16x64_bf16 v[8:11], v[48:51], v[160:167], v1\n\t"
        "v_smfmac_f32_16x16x64_bf16 v[8:11], v[52:55], v[168:175], v1\n\t"
        "s_waitcnt vmcnt(8)\n\t"
        "v_smfmac_f32_16x16x64_bf16 v[8:11], v[56:59], v[176:183], v1\n\t"
        "v_smfmac_f32_16x16x64_bf16 v[8:11], v[60:63], v[184:191], v1\n\t"
        "s_waitcnt vmcnt(6)\n\t"
        "v_smfmac_f32_16x16x64_bf16 v[8:11], v[64:67], v[192:199], v1\n\t"
        "v_smfmac_f32_16x16x64_bf16 v[8:11], v[68:71], v[200:207], v1\n\t"
        "s_waitcnt vmcnt(4)\n\t"
        "v_smfmac_f32_16x16x64_bf16 v[8:11], v[72:75], v[208:215], v1\n\t"
        "v_smfmac_f32_16x16x64_bf16 v[8:11], v[76:79], v[216:223], v1\n\t"
        "s_waitcnt vmcnt(3)\n\t"
        "v_smfmac_f32_16x16x64_bf16 v[8:11], v[80:83], v[224:231], v1\n\t"
        "s_waitcnt vmcnt(2)\n\t"
        "v_smfmac_f32_16x16x64_bf16 v[8:11], v[84:87], v[232:239], v1\n\t"
        "s_waitcnt vmcnt(1)\n\t"
        "v_smfmac_f32_16x16x64_bf16 v[8:11], v[88:91], v[240:247], v1\n\t"
        "s_waitcnt vmcnt(0)\n\t"
        "v_smfmac_f32_16x16x64_bf16 v[8:11], v[92:95], v[248:255], v1\n\t"
        "s_nop 7\n\t"
        "v_add_f32 v5, v8, v9\n\t"
        "v_add_f32 v6, v10, v11\n\t"
        "v_cmp_u_f32 s[68:69], v5, v6\n\t"
        "s_nop 0\n\t"
        "v_cndmask_b32_dpp v7, v6, v5, vcc quad_perm:[0,0,2,2] row_mask:0xf bank_mask:0xf\n\t"
        "s_not_b64 vcc, vcc\n\t"
        "v_cndmask_b32_dpp v12, v5, v6, vcc quad_perm:[1,1,3,3] row_mask:0xf bank_mask:0xf\n\t"
        "s_cmp_lg_u64 s[68:69], 0\n\t"
        "v_cvt_pk_bf16_f32 v7, v7, v12\n\t"
        "s_cbranch_scc1 .Lr_pf_%=\n\t"
        ".Lr_pk_%=:\n\t"
        "buffer_store_dword v7, v2, s[56:59], s72 offen sc1\n\t"
        "buffer_store_dword v7, v2, s[52:55], s72 offen sc1\n\t"
        "buffer_store_dword v7, v2, s[56:59], s73 offen sc1\n\t"
        "buffer_store_dword v7, v2, s[52:55], s73 offen sc1\n\t"
        "buffer_store_dword v7, v2, s[56:59], s74 offen sc1\n\t"
        "buffer_store_dword v7, v2, s[52:55], s74 offen sc1\n\t"
        "buffer_store_dword v7, v2, s[56:59], s75 offen sc1\n\t"
        "buffer_store_dword v7, v2, s[52:55], s75 offen sc1\n\t"
        "s_memtime s[62:63]\n\t"
        "s_barrier\n\t"
        ".Lr_pl_%=:\n\t"
        "ds_write_b128 v3, v[224:227] offset:0\n\t"
        "ds_write_b128 v3, v[228:231] offset:1024\n\t"
        "ds_write_b128 v3, v[232:235] offset:2048\n\t"
        "ds_write_b128 v3, v[236:239] offset:3072\n\t"
        "ds_write_b128 v3, v[240:243] offset:4096\n\t"
        "ds_write_b128 v3, v[244:247] offset:5120\n\t"
        "ds_write_b128 v3, v[248:251] offset:6144\n\t"
        "ds_write_b128 v3, v[252:255] offset:7168\n\t"
        "s_add_u32 s70, s45, 1\n\t"
        "v_mov_b32 v5, 114720\n\t"
        "v_mov_b32 v6, s70\n\t"
        "ds_write_b32 v5, v6\n\t"
        "v_xor_b32 v3, 0x2000, v3\n\t"
        "s_add_u32 s48, s48, 2097152\n\t"
        "s_and_b32 s48, s48, 0xffffff\n\t"
        "s_xor_b32 s48, s48, 0x2000\n\t"
        "s_add_u32 s52, %[al], s48\n\t"
        "s_addc_u32 s53, %[ah], 0\n\t"
        "s_xor_b32 s56, s52, 0x2000\n\t"
        "s_mov_b32 s57, s53\n\t"
        "s_add_u32 s49, s49, 2097152\n\t"
        "s_and_b32 s49, s49, 0xffffff\n\t"
        "s_xor_b32 s49, s49, 0x2000\n\t"
        "v_mov_b64 v[8:9], 0\n\t"
        "v_mov_b64 v[10:11], 0\n\t"
        "s_mov_b64 vcc, s[64:65]\n\t"
        "s_add_u32 s44, s44, 32768\n\t"
        "s_cmp_lt_u32 s44, 98304\n\t"
        "s_cselect_b32 s44, s44, 0\n\t"
        "s_add_u32 s45, s45, 1\n\t"
        "s_cmp_lt_u32 s45, 64\n\t"
        "s_cbranch_scc0 .Lr_end_%=\n\t"
        "v_mov_b32 v21, 0\n\t"
        "s_cmp_eq_u32 s45, 63\n\t"
        "s_cbranch_scc1 .Lr_lastw_%=\n\t"
        "v_mov_b32 v5, 114704\n\t"
        "v_add_u32 v12, s44, v0\n\t"
        ".Lr_fl0_%=:\n\t"
        "ds_read_b32 v13, v5 offset:0\n\t"
        "s_waitcnt lgkmcnt(0)\n\t"
        "v_cmp_lt_u32 s[68:69], s45, v13\n\t"
        "s_nop 3\n\t"
        "s_cmp_lg_u64 s[68:69], 0\n\t"
        "s_cbranch_scc0 .Lr_fl0_%=\n\t"
        "ds_read_b128 v[96:99], v12 offset:0\n\t"
        "ds_read_b128 v[100:103], v12 offset:1024\n\t"
        "ds_read_b128 v[104:107], v12 offset:2048\n\t"
        "ds_read_b128 v[108:111], v12 offset:3072\n\t"
        "ds_read_b128 v[112:115], v12 offset:4096\n\t"
        "ds_read_b128 v[116:119], v12 offset:5120\n\t"
        "ds_read_b128 v[120:123], v12 offset:6144\n\t"
        "ds_read_b128 v[124:127], v12 offset:7168\n\t"
        ".Lr_fl1_%=:\n\t"
        "ds_read_b32 v13, v5 offset:4\n\t"
        "s_waitcnt lgkmcnt(0)\n\t"
        "v_cmp_lt_u32 s[68:69], s45, v13\n\t"
        "s_nop 3\n\t"
        "s_cmp_lg_u64 s[68:69], 0\n\t"
        "s_cbranch_scc0 .Lr_fl1_%=\n\t"
        "ds_read_b128 v[128:131], v12 offset:8192\n\t"
        "ds_read_b128 v[132:135], v12 offset:9216\n\t"
        "ds_read_b128 v[136:139], v12 offset:10240\n\t"
        "ds_read_b128 v[140:143], v12 offset:11264\n\t"
        "ds_read_b128 v[144:147], v12 offset:12288\n\t"
        "ds_read_b128 v[148:151], v12 offset:13312\n\t"
        "ds_read_b128 v[152:155], v12 offset:14336\n\t"
        "ds_read_b128 v[156:159], v12 offset:15360\n\t"
        ".Lr_fl2_%=:\n\t"
        "ds_read_b32 v13, v5 offset:8\n\t"
        "s_waitcnt lgkmcnt(0)\n\t"
        "v_cmp_lt_u32 s[68:69], s45, v13\n\t"
        "s_nop 3\n\t"
        "s_cmp_lg_u64 s[68:69], 0\n\t"
        "s_cbranch_scc0 .Lr_fl2_%=\n\t"
        "ds_read_b128 v[160:163], v12 offset:16384\n\t"
        "ds_read_b128 v[164:167], v12 offset:17408\n\t"
        "ds_read_b128 v[168:171], v12 offset:18432\n\t"
        "ds_read_b128 v[172:175], v12 offset:19456\n\t"
        "ds_read_b128 v[176:179], v12 offset:20480\n\t"
        "ds_read_b128 v[180:183], v12 offset:21504\n\t"
        "ds_read_b128 v[184:187], v12 offset:22528\n\t"
        "ds_read_b128 v[188:191], v12 offset:23552\n\t"
        ".Lr_fl3_%=:\n\t"
        "ds_read_b32 v13, v5 offset:12\n\t"
        "s_waitcnt lgkmcnt(0)\n\t"
        "v_cmp_lt_u32 s[68:69], s45, v13\n\t"
        "s_nop 3\n\t"
        "s_cmp_lg_u64 s[68:69], 0\n\t"
        "s_cbranch_scc0 .Lr_fl3_%=\n\t"
        "ds_read_b128 v[192:195], v12 offset:24576\n\t"
        "ds_read_b128 v[196:199], v12 offset:25600\n\t"
        "ds_read_b128 v[200:203], v12 offset:26624\n\t"
        "ds_read_b128 v[204:207], v12 offset:27648\n\t"
        "ds_read_b128 v[208:211], v12 offset:28672\n\t"
        "ds_read_b128 v[212:215], v12 offset:29696\n\t"
        "ds_read_b128 v[216:219], v12 offset:30720\n\t"
        "ds_read_b128 v[220:223], v12 offset:31744\n\t"
        "s_waitcnt lgkmcnt(0)\n\t"
        "s_memtime s[68:69]\n\t"
        "s_add_u32 s70, s62, 1900\n\t"
        "s_waitcnt lgkmcnt(0)\n\t"
        "s_sub_i32 s70, s70, s68\n\t"
        "s_max_i32 s70, s70, 0\n\t"
        "s_min_i32 s70, s70, 8000\n\t"
        ".Lr_tr_%=:\n\t"
        "s_sub_u32 s70, s70, 33\n\t"
        "s_cbranch_scc0 .Lr_tr_%=\n\t"
        "s_xor_b32 s67, s49, %[tw]\n\t"
        "v_and_b32 v16, 0x1f0, v0\n\t"
        "v_lshlrev_b32 v16, 1, v16\n\t"
        "buffer_load_dword v18, v16, %[rs], s67 offen sc1\n\t"
        ".Lr_s1_%=:\n\t"
        ".Lr_twm_%=:\n\t"
        "s_waitcnt vmcnt(0)\n\t"
        "v_cmp_eq_u32 s[68:69], 0, v18\n\t"
        "v_max_u32_dpp v18, v18, v18 row_shr:1 row_mask:0xf bank_mask:0xf\n\t"
        "s_nop 1\n\t"
        "v_max_u32_dpp v18, v18, v18 row_shr:2 row_mask:0xf bank_mask:0xf\n\t"
        "s_nop 1\n\t"
        "v_max_u32_dpp v18, v18, v18 row_shr:4 row_mask:0xf bank_mask:0xf\n\t"
        "s_nop 1\n\t"
        "v_max_u32_dpp v18, v18, v18 row_shr:8 row_mask:0xf bank_mask:0xf\n\t"
        "s_nop 1\n\t"
        "v_max_u32_dpp v18, v18, v18 row_bcast:15 row_mask:0xf bank_mask:0xf\n\t"
        "s_nop 1\n\t"
        "v_readlane_b32 s70, v18, 63\n\t"
        "s_cmp_lg_u64 s[68:69], 0\n\t"
        "v_readfirstlane_b32 s68, v21\n\t"
        "s_cbranch_scc1 .Lr_twx_%=\n\t"
        ".Lr_twy_%=:\n\t"
        "s_add_i32 s70, s70, 5\n\t"
        "s_add_u32 s67, s70, s68\n\t"
        "s_branch .Lr_loop_%=\n\t"
        ".Lr_lastw_%=:\n\t"
        "s_memtime s[68:69]\n\t"
        "s_add_u32 s70, s62, 1700\n\t"
        "s_waitcnt lgkmcnt(0)\n\t"
        "s_sub_i32 s70, s70, s68\n\t"
        "s_max_i32 s70, s70, 0\n\t"
        "s_min_i32 s70, s70, 8000\n\t"
        ".Lr_tq_%=:\n\t"
        "s_sub_u32 s70, s70, 33\n\t"
        "s_cbranch_scc0 .Lr_tq_%=\n\t"
        "s_xor_b32 s67, s49, %[tw]\n\t"
        "v_and_b32 v16, 0x1f0, v0\n\t"
        "v_lshlrev_b32 v16, 1, v16\n\t"
        "buffer_load_dword v18, v16, %[rs], s67 offen sc1\n\t"
        ".Lr_tlm_%=:\n\t"
        "s_waitcnt vmcnt(0)\n\t"
        "v_cmp_eq_u32 s[68:69], 0, v18\n\t"
        "v_max_u32_dpp v18, v18, v18 row_shr:1 row_mask:0xf bank_mask:0xf\n\t"
        "s_nop 1\n\t"
        "v_max_u32_dpp v18, v18, v18 row_shr:2 row_mask:0xf bank_mask:0xf\n\t"
        "s_nop 1\n\t"
        "v_max_u32_dpp v18, v18, v18 row_shr:4 row_mask:0xf bank_mask:0xf\n\t"
        "s_nop 1\n\t"
        "v_max_u32_dpp v18, v18, v18 row_shr:8 row_mask:0xf bank_mask:0xf\n\t"
        "s_nop 1\n\t"
        "v_max_u32_dpp v18, v18, v18 row_bcast:15 row_mask:0xf bank_mask:0xf\n\t"
        "s_nop 1\n\t"
        "v_readlane_b32 s70, v18, 63\n\t"
        "s_cmp_lg_u64 s[68:69], 0\n\t"
        "v_readfirstlane_b32 s68, v21\n\t"
        "s_cbranch_scc1 .Lr_tlx_%=\n\t"
        ".Lr_tly_%=:\n\t"
        "s_add_i32 s70, s70, -25\n\t"
        "s_add_u32 s67, s70, s68\n\t"
        "v_mov_b32 v5, 114704\n\t"
        "v_add_u32 v12, s44, v0\n\t"
        ".Lr_fl30_%=:\n\t"
        "ds_read_b32 v13, v5 offset:0\n\t"
        "s_waitcnt lgkmcnt(0)\n\t"
        "v_cmp_lt_u32 s[68:69], s45, v13\n\t"
        "s_nop 3\n\t"
        "s_cmp_lg_u64 s[68:69], 0\n\t"
        "s_cbranch_scc0 .Lr_fl30_%=\n\t"
        ".Lr_fl31_%=:\n\t"
        "ds_read_b32 v13, v5 offset:4\n\t"
        "s_waitcnt lgkmcnt(0)\n\t"
        "v_cmp_lt_u32 s[68:69], s45, v13\n\t"
        "s_nop 3\n\t"
        "s_cmp_lg_u64 s[68:69], 0\n\t"
        "s_cbranch_scc0 .Lr_fl31_%=\n\t"
        ".Lr_fl32_%=:\n\t"
        "ds_read_b32 v13, v5 offset:8\n\t"
        "s_waitcnt lgkmcnt(0)\n\t"
        "v_cmp_lt_u32 s[68:69], s45, v13\n\t"
        "s_nop 3\n\t"
        "s_cmp_lg_u64 s[68:69], 0\n\t"
        "s_cbranch_scc0 .Lr_fl32_%=\n\t"
        ".Lr_fl33_%=:\n\t"
        "ds_read_b32 v13, v5 offset:12\n\t"
        "s_waitcnt lgkmcnt(0)\n\t"
        "v_cmp_lt_u32 s[68:69], s45, v13\n\t"
        "s_nop 3\n\t"
        "s_cmp_lg_u64 s[68:69], 0\n\t"
        "s_cbranch_scc0 .Lr_fl33_%=\n\t"
        "s_waitcnt lgkmcnt(0)\n\t"
        ".Lr_tla_%=:\n\t"
        "s_memrealtime s[68:69]\n\t"
        "s_waitcnt lgkmcnt(0)\n\t"
        "s_sub_i32 s68, s67, s68\n\t"
        "s_sub_u32 s69, s68, 5\n\t"
        "s_cmp_lt_u32 s69, 400\n\t"
        "s_cbranch_scc1 .Lr_tla_%=\n\t"
        "s_cmp_lt_i32 s68, 1\n\t"
        "s_cbranch_scc1 .Lr_tlb_%=\n\t"
        "s_nop 2\n\t"
        "s_cmp_lt_i32 s68, 2\n\t"
        "s_cbranch_scc1 .Lr_tlb_%=\n\t"
        "s_nop 2\n\t"
        "s_cmp_lt_i32 s68, 3\n\t"
        "s_cbranch_scc1 .Lr_tlb_%=\n\t"
        "s_nop 2\n\t"
        "s_cmp_lt_i32 s68, 4\n\t"
        "s_cbranch_scc1 .Lr_tlb_%=\n\t"
        "s_nop 2\n\t"
        "s_branch .Lr_tlb_%=\n\t"
        ".Lr_tlb_%=:\n\t"
        "buffer_load_dwordx4 v[224:227], v0, s[52:55], %[om] offen offset:0 sc1\n\t"
        "buffer_load_dwordx4 v[228:231], v0, s[52:55], %[om] offen offset:1024 sc1\n\t"
        "buffer_load_dwordx4 v[232:235], v0, s[52:55], %[om] offen offset:2048 sc1\n\t"
        "buffer_load_dwordx4 v[236:239], v0, s[52:55], %[om] offen offset:3072 sc1\n\t"
        "buffer_load_dwordx4 v[240:243], v0, s[52:55], s47 offen offset:0 sc1\n\t"
        "buffer_load_dwordx4 v[244:247], v0, s[52:55], s47 offen offset:1024 sc1\n\t"
        "buffer_load_dwordx4 v[248:251], v0, s[52:55], s47 offen offset:2048 sc1\n\t"
        "buffer_load_dwordx4 v[252:255], v0, s[52:55], s47 offen offset:3072 sc1\n\t"
        "s_barrier\n\t"
        "s_branch .Lr_last2_%=\n\t"
        ".Lr_last_%=:\n\t"
        "buffer_load_dwordx4 v[224:227], v0, s[52:55], %[om] offen offset:0 sc1\n\t"
        "buffer_load_dwordx4 v[228:231], v0, s[52:55], %[om] offen offset:1024 sc1\n\t"
        "buffer_load_dwordx4 v[232:235], v0, s[52:55], %[om] offen offset:2048 sc1\n\t"
        "buffer_load_dwordx4 v[236:239], v0, s[52:55], %[om] offen offset:3072 sc1\n\t"
        "buffer_load_dwordx4 v[240:243], v0, s[52:55], s47 offen offset:0 sc1\n\t"
        "buffer_load_dwordx4 v[244:247], v0, s[52:55], s47 offen offset:1024 sc1\n\t"
        "buffer_load_dwordx4 v[248:251], v0, s[52:55], s47 offen offset:2048 sc1\n\t"
        "buffer_load_dwordx4 v[252:255], v0, s[52:55], s47 offen offset:3072 sc1\n\t"
        ".Lr_last2_%=:\n\t"
        "s_waitcnt vmcnt(0)\n\t"
        "v_max_u32 v4, v224, v225\n\t"
        "v_max3_u32 v4, v4, v226, v227\n\t"
        "v_max3_u32 v4, v4, v228, v229\n\t"
        "v_max3_u32 v4, v4, v230, v231\n\t"
        "v_max3_u32 v4, v4, v232, v233\n\t"
        "v_max3_u32 v4, v4, v234, v235\n\t"
        "v_max3_u32 v4, v4, v236, v237\n\t"
        "v_max3_u32 v4, v4, v238, v239\n\t"
        "v_max3_u32 v4, v4, v240, v241\n\t"
        "v_max3_u32 v4, v4, v242, v243\n\t"
        "v_max3_u32 v4, v4, v244, v245\n\t"
        "v_max3_u32 v4, v4, v246, v247\n\t"
        "v_max3_u32 v4, v4, v248, v249\n\t"
        "v_max3_u32 v4, v4, v250, v251\n\t"
        "v_max3_u32 v4, v4, v252, v253\n\t"
        "v_max3_u32 v4, v4, v254, v255\n\t"
        "v_cmp_eq_u32 s[68:69], s66, v4\n\t"
        "s_nop 3\n\t"
        "s_cmp_lg_u64 s[68:69], 0\n\t"
        "s_cbranch_scc0 .Lr_pl_%=\n\t"
        "s_sub_u32 s46, s46, 1\n\t"
        "s_cbranch_scc0 .Lr_last_%=\n\t"
        "s_mov_b32 s46, 0\n\t"
        "s_branch .Lr_pl_%=\n\t"
        ".Lr_dma_%=:\n\t"
        "s_sub_u32 s52, %[w], 4\n\t"
        "s_max_i32 s52, s52, 0\n\t"
        "s_xor_b32 s50, %[oy], %[t0]\n\t"
        "s_lshl_b32 s70, s52, 14\n\t"
        "s_add_u32 s50, s50, s70\n\t"
        "s_lshl_b32 s70, s52, 2\n\t"
        "s_add_u32 s70, s70, 114704\n\t"
        "v_mov_b32 v10, s70\n\t"
        "s_lshl_b32 s52, s52, 13\n\t"
        "v_add_u32 v11, s52, v0\n\t"
        "s_setprio 2\n\t"
        "v_mov_b32 v12, s66\n\t"
        "v_mov_b32 v13, s66\n\t"
        "v_mov_b32 v14, s66\n\t"
        "v_mov_b32 v15, s66\n\t"
        "v_and_b32 v5, 0xf0, v0\n\t"
        "s_lshl_b32 s57, %[oo], 2\n\t"
        "s_add_u32 s57, s57, 344064\n\t"
        "s_mov_b32 s58, 0\n\t"
        "v_bfe_u32 v48, v0, 2, 7\n\t"
        "s_add_u32 s64, s49, 2097152\n\t"
        "s_and_b32 s64, s64, 0xffffff\n\t"
        "s_add_u32 s71, s64, %[oa]\n\t"
        "s_add_u32 s71, s71, 65536\n\t"
        "s_add_u32 s72, s64, %[oa]\n\t"
        "s_add_u32 s72, s72, 65536\n\t"
        "s_xor_b32 s72, s72, 0x2000\n\t"
        "s_add_u32 s73, s64, %[oa]\n\t"
        "s_add_u32 s73, s73, 98304\n\t"
        "s_add_u32 s74, s64, %[oa]\n\t"
        "s_add_u32 s74, s74, 98304\n\t"
        "s_xor_b32 s74, s74, 0x2000\n\t"
        "s_add_u32 s75, s64, %[oa]\n\t"
        "s_add_u32 s75, s75, 131072\n\t"
        "s_add_u32 s76, s64, %[oa]\n\t"
        "s_add_u32 s76, s76, 131072\n\t"
        "s_xor_b32 s76, s76, 0x2000\n\t"
        "s_add_u32 s77, s64, %[oa]\n\t"
        "s_add_u32 s77, s77, 163840\n\t"
        "s_add_u32 s78, s64, %[oa]\n\t"
        "s_add_u32 s78, s78, 163840\n\t"
        "s_xor_b32 s78, s78, 0x2000\n\t"
        "s_add_u32 s67, s48, 2097152\n\t"
        "s_and_b32 s67, s67, 0xffffff\n\t"
        "s_cmp_lt_u32 s45, 61\n\t"
        "s_cselect_b32 s65, 0, 0x7f000000\n\t"
        "s_xor_b32 s67, s67, 8192\n\t"
        "s_add_u32 s67, s67, %[om]\n\t"
        "s_lshr_b32 s68, s52, 2\n\t"
        "s_add_u32 s67, s67, s68\n\t"
        "s_add_u32 s67, s67, s65\n\t"
        "s_cmp_eq_u32 %[w], 3\n\t"
        "s_cselect_b32 s65, s65, 0x7f000000\n\t"
        "v_mov_b32 v8, 0x7f000000\n\t"
        "s_xor_b32 s70, s71, 8192\n\t"
        "v_cmp_eq_u32 vcc, 0, v6\n\t"
        "v_mov_b32 v4, s70\n\t"
        "s_nop 0\n\t"
        "v_cndmask_b32 v8, v8, v4, vcc\n\t"
        "s_xor_b32 s70, s72, 8192\n\t"
        "v_cmp_eq_u32 vcc, 1, v6\n\t"
        "v_mov_b32 v4, s70\n\t"
        "s_nop 0\n\t"
        "v_cndmask_b32 v8, v8, v4, vcc\n\t"
        "s_xor_b32 s70, s73, 8192\n\t"
        "v_cmp_eq_u32 vcc, 2, v6\n\t"
        "v_mov_b32 v4, s70\n\t"
        "s_nop 0\n\t"
        "v_cndmask_b32 v8, v8, v4, vcc\n\t"
        "s_xor_b32 s70, s74, 8192\n\t"
        "v_cmp_eq_u32 vcc, 3, v6\n\t"
        "v_mov_b32 v4, s70\n\t"
        "s_nop 0\n\t"
        "v_cndmask_b32 v8, v8, v4, vcc\n\t"
        "v_add_u32 v8, v8, v5\n\t"
        "v_mov_b32 v9, 0x7f000000\n\t"
        "s_xor_b32 s70, s75, 8192\n\t"
        "v_cmp_eq_u32 vcc, 0, v6\n\t"
        "v_mov_b32 v4, s70\n\t"
        "s_nop 0\n\t"
        "v_cndmask_b32 v9, v9, v4, vcc\n\t"
        "s_xor_b32 s70, s76, 8192\n\t"
        "v_cmp_eq_u32 vcc, 1, v6\n\t"
        "v_mov_b32 v4, s70\n\t"
        "s_nop 0\n\t"
        "v_cndmask_b32 v9, v9, v4, vcc\n\t"
        "s_xor_b32 s70, s77, 8192\n\t"
        "v_cmp_eq_u32 vcc, 2, v6\n\t"
        "v_mov_b32 v4, s70\n\t"
        "s_nop 0\n\t"
        "v_cndmask_b32 v9, v9, v4, vcc\n\t"
        "s_xor_b32 s70, s78, 8192\n\t"
        "v_cmp_eq_u32 vcc, 3, v6\n\t"
        "v_mov_b32 v4, s70\n\t"
        "s_nop 0\n\t"
        "v_cndmask_b32 v9, v9, v4, vcc\n\t"
        "v_add_u32 v9, v9, v5\n\t"
        "buffer_store_dwordx4 v[12:15], v8, %[rs], s65 offen sc1\n\t"
        "buffer_store_dwordx4 v[12:15], v9, %[rs], s65 offen sc1\n\t"
        "buffer_store_dwordx4 v[12:15], v0, %[rs], s67 offen sc1\n\t"
        "buffer_store_dwordx4 v[12:15], v0, %[rs], s67 offen offset:1024 sc1\n\t"
        "s_add_u32 s64, s49, 4194304\n\t"
        "s_and_b32 s64, s64, 0xffffff\n\t"
        "s_add_u32 s71, s64, %[oa]\n\t"
        "s_add_u32 s71, s71, 65536\n\t"
        "s_add_u32 s72, s64, %[oa]\n\t"
        "s_add_u32 s72, s72, 65536\n\t"
        "s_xor_b32 s72, s72, 0x2000\n\t"
        "s_add_u32 s73, s64, %[oa]\n\t"
        "s_add_u32 s73, s73, 98304\n\t"
        "s_add_u32 s74, s64, %[oa]\n\t"
        "s_add_u32 s74, s74, 98304\n\t"
        "s_xor_b32 s74, s74, 0x2000\n\t"
        "s_add_u32 s75, s64, %[oa]\n\t"
        "s_add_u32 s75, s75, 131072\n\t"
        "s_add_u32 s76, s64, %[oa]\n\t"
        "s_add_u32 s76, s76, 131072\n\t"
        "s_xor_b32 s76, s76, 0x2000\n\t"
        "s_add_u32 s77, s64, %[oa]\n\t"
        "s_add_u32 s77, s77, 163840\n\t"
        "s_add_u32 s78, s64, %[oa]\n\t"
        "s_add_u32 s78, s78, 163840\n\t"
        "s_xor_b32 s78, s78, 0x2000\n\t"
        "s_add_u32 s67, s48, 4194304\n\t"
        "s_and_b32 s67, s67, 0xffffff\n\t"
        "s_cmp_lt_u32 s45, 61\n\t"
        "s_cselect_b32 s65, 0, 0x7f000000\n\t"
        "s_xor_b32 s67, s67, 0\n\t"
        "s_add_u32 s67, s67, %[om]\n\t"
        "s_lshr_b32 s68, s52, 2\n\t"
        "s_add_u32 s67, s67, s68\n\t"
        "s_add_u32 s67, s67, s65\n\t"
        "s_cmp_eq_u32 %[w], 3\n\t"
        "s_cselect_b32 s65, s65, 0x7f000000\n\t"
        "v_mov_b32 v8, 0x7f000000\n\t"
        "s_xor_b32 s70, s71, 0\n\t"
        "v_cmp_eq_u32 vcc, 0, v6\n\t"
        "v_mov_b32 v4, s70\n\t"
        "s_nop 0\n\t"
        "v_cndmask_b32 v8, v8, v4, vcc\n\t"
        "s_xor_b32 s70, s72, 0\n\t"
        "v_cmp_eq_u32 vcc, 1, v6\n\t"
        "v_mov_b32 v4, s70\n\t"
        "s_nop 0\n\t"
        "v_cndmask_b32 v8, v8, v4, vcc\n\t"
        "s_xor_b32 s70, s73, 0\n\t"
        "v_cmp_eq_u32 vcc, 2, v6\n\t"
        "v_mov_b32 v4, s70\n\t"
        "s_nop 0\n\t"
        "v_cndmask_b32 v8, v8, v4, vcc\n\t"
        "s_xor_b32 s70, s74, 0\n\t"
        "v_cmp_eq_u32 vcc, 3, v6\n\t"
        "v_mov_b32 v4, s70\n\t"
        "s_nop 0\n\t"
        "v_cndmask_b32 v8, v8, v4, vcc\n\t"
        "v_add_u32 v8, v8, v5\n\t"
        "v_mov_b32 v9, 0x7f000000\n\t"
        "s_xor_b32 s70, s75, 0\n\t"
        "v_cmp_eq_u32 vcc, 0, v6\n\t"
        "v_mov_b32 v4, s70\n\t"
        "s_nop 0\n\t"
        "v_cndmask_b32 v9, v9, v4, vcc\n\t"
        "s_xor_b32 s70, s76, 0\n\t"
        "v_cmp_eq_u32 vcc, 1, v6\n\t"
        "v_mov_b32 v4, s70\n\t"
        "s_nop 0\n\t"
        "v_cndmask_b32 v9, v9, v4, vcc\n\t"
        "s_xor_b32 s70, s77, 0\n\t"
        "v_cmp_eq_u32 vcc, 2, v6\n\t"
        "v_mov_b32 v4, s70\n\t"
        "s_nop 0\n\t"
        "v_cndmask_b32 v9, v9, v4, vcc\n\t"
        "s_xor_b32 s70, s78, 0\n\t"
        "v_cmp_eq_u32 vcc, 3, v6\n\t"
        "v_mov_b32 v4, s70\n\t"
        "s_nop 0\n\t"
        "v_cndmask_b32 v9, v9, v4, vcc\n\t"
        "v_add_u32 v9, v9, v5\n\t"
        "buffer_store_dwordx4 v[12:15], v8, %[rs], s65 offen sc1\n\t"
        "buffer_store_dwordx4 v[12:15], v9, %[rs], s65 offen sc1\n\t"
        "buffer_store_dwordx4 v[12:15], v0, %[rs], s67 offen sc1\n\t"
        "buffer_store_dwordx4 v[12:15], v0, %[rs], s67 offen offset:1024 sc1\n\t"
        ".Lr_an0_%=:\n\t"
        "buffer_load_dword v49, v48, %[rs], s57 offen sc1\n\t"
        "s_waitcnt vmcnt(0)\n\t"
        "v_cmp_eq_u32 vcc, s66, v49\n\t"
        "s_cbranch_vccnz .Lr_an0f_%=\n\t"
        ".Lr_an0k_%=:\n\t"
        "s_add_u32 s57, s57, 8192\n\t"
        "s_add_u32 s69, s50, 4096\n\t"
        "buffer_load_dwordx4 v[16:19], v0, %[rs], s50 offen offset:0 sc1\n\t"
        "buffer_load_dwordx4 v[20:23], v0, %[rs], s50 offen offset:1024 sc1\n\t"
        "buffer_load_dwordx4 v[24:27], v0, %[rs], s50 offen offset:2048 sc1\n\t"
        "buffer_load_dwordx4 v[28:31], v0, %[rs], s50 offen offset:3072 sc1\n\t"
        "buffer_load_dwordx4 v[32:35], v0, %[rs], s69 offen offset:0 sc1\n\t"
        "buffer_load_dwordx4 v[36:39], v0, %[rs], s69 offen offset:1024 sc1\n\t"
        "buffer_load_dwordx4 v[40:43], v0, %[rs], s69 offen offset:2048 sc1\n\t"
        "buffer_load_dwordx4 v[44:47], v0, %[rs], s69 offen offset:3072 sc1\n\t"
        "v_add_u32 v7, s44, v11\n\t"
        "s_add_u32 s58, s58, 1\n\t"
        "v_mov_b32 v4, s58\n\t"
        "s_add_u32 s50, s50, 4194304\n\t"
        "s_and_b32 s70, s58, 7\n\t"
        "s_cselect_b32 s70, 0, 33554432\n\t"
        "s_sub_u32 s50, s50, s70\n\t"
        "s_waitcnt vmcnt(7)\n\t"
        "ds_write_b128 v7, v[16:19] offset:0\n\t"
        "s_waitcnt vmcnt(6)\n\t"
        "ds_write_b128 v7, v[20:23] offset:1024\n\t"
        "s_waitcnt vmcnt(5)\n\t"
        "ds_write_b128 v7, v[24:27] offset:2048\n\t"
        "s_waitcnt vmcnt(4)\n\t"
        "ds_write_b128 v7, v[28:31] offset:3072\n\t"
        "s_waitcnt vmcnt(3)\n\t"
        "ds_write_b128 v7, v[32:35] offset:4096\n\t"
        "s_waitcnt vmcnt(2)\n\t"
        "ds_write_b128 v7, v[36:39] offset:5120\n\t"
        "s_waitcnt vmcnt(1)\n\t"
        "ds_write_b128 v7, v[40:43] offset:6144\n\t"
        "s_waitcnt vmcnt(0)\n\t"
        "ds_write_b128 v7, v[44:47] offset:7168\n\t"
        "ds_write_b32 v10, v4\n\t"
        "s_mov_b32 s44, 32768\n\t"
        ".Lr_an1_%=:\n\t"
        "buffer_load_dword v49, v48, %[rs], s57 offen sc1\n\t"
        "s_waitcnt vmcnt(0)\n\t"
        "v_cmp_eq_u32 vcc, s66, v49\n\t"
        "s_cbranch_vccnz .Lr_an1f_%=\n\t"
        ".Lr_an1k_%=:\n\t"
        "s_add_u32 s57, s57, 8192\n\t"
        "s_add_u32 s69, s50, 4096\n\t"
        "buffer_load_dwordx4 v[16:19], v0, %[rs], s50 offen offset:0 sc1\n\t"
        "buffer_load_dwordx4 v[20:23], v0, %[rs], s50 offen offset:1024 sc1\n\t"
        "buffer_load_dwordx4 v[24:27], v0, %[rs], s50 offen offset:2048 sc1\n\t"
        "buffer_load_dwordx4 v[28:31], v0, %[rs], s50 offen offset:3072 sc1\n\t"
        "buffer_load_dwordx4 v[32:35], v0, %[rs], s69 offen offset:0 sc1\n\t"
        "buffer_load_dwordx4 v[36:39], v0, %[rs], s69 offen offset:1024 sc1\n\t"
        "buffer_load_dwordx4 v[40:43], v0, %[rs], s69 offen offset:2048 sc1\n\t"
        "buffer_load_dwordx4 v[44:47], v0, %[rs], s69 offen offset:3072 sc1\n\t"
        "v_add_u32 v7, s44, v11\n\t"
        "s_add_u32 s58, s58, 1\n\t"
        "v_mov_b32 v4, s58\n\t"
        "s_add_u32 s50, s50, 4194304\n\t"
        "s_and_b32 s70, s58, 7\n\t"
        "s_cselect_b32 s70, 0, 33554432\n\t"
        "s_sub_u32 s50, s50, s70\n\t"
        "s_waitcnt vmcnt(7)\n\t"
        "ds_write_b128 v7, v[16:19] offset:0\n\t"
        "s_waitcnt vmcnt(6)\n\t"
        "ds_write_b128 v7, v[20:23] offset:1024\n\t"
        "s_waitcnt vmcnt(5)\n\t"
        "ds_write_b128 v7, v[24:27] offset:2048\n\t"
        "s_waitcnt vmcnt(4)\n\t"
        "ds_write_b128 v7, v[28:31] offset:3072\n\t"
        "s_waitcnt vmcnt(3)\n\t"
        "ds_write_b128 v7, v[32:35] offset:4096\n\t"
        "s_waitcnt vmcnt(2)\n\t"
        "ds_write_b128 v7, v[36:39] offset:5120\n\t"
        "s_waitcnt vmcnt(1)\n\t"
        "ds_write_b128 v7, v[40:43] offset:6144\n\t"
        "s_waitcnt vmcnt(0)\n\t"
        "ds_write_b128 v7, v[44:47] offset:7168\n\t"
        "ds_write_b32 v10, v4\n\t"
        "s_mov_b32 s44, 65536\n\t"
        ".Lr_dloop_%=:\n\t"
        "s_add_u32 s64, s49, 6291456\n\t"
        "s_and_b32 s64, s64, 0xffffff\n\t"
        "s_add_u32 s71, s64, %[oa]\n\t"
        "s_add_u32 s71, s71, 65536\n\t"
        "s_add_u32 s72, s64, %[oa]\n\t"
        "s_add_u32 s72, s72, 65536\n\t"
        "s_xor_b32 s72, s72, 0x2000\n\t"
        "s_add_u32 s73, s64, %[oa]\n\t"
        "s_add_u32 s73, s73, 98304\n\t"
        "s_add_u32 s74, s64, %[oa]\n\t"
        "s_add_u32 s74, s74, 98304\n\t"
        "s_xor_b32 s74, s74, 0x2000\n\t"
        "s_add_u32 s75, s64, %[oa]\n\t"
        "s_add_u32 s75, s75, 131072\n\t"
        "s_add_u32 s76, s64, %[oa]\n\t"
        "s_add_u32 s76, s76, 131072\n\t"
        "s_xor_b32 s76, s76, 0x2000\n\t"
        "s_add_u32 s77, s64, %[oa]\n\t"
        "s_add_u32 s77, s77, 163840\n\t"
        "s_add_u32 s78, s64, %[oa]\n\t"
        "s_add_u32 s78, s78, 163840\n\t"
        "s_xor_b32 s78, s78, 0x2000\n\t"
        "s_add_u32 s67, s48, 6291456\n\t"
        "s_and_b32 s67, s67, 0xffffff\n\t"
        "s_cmp_lt_u32 s45, 61\n\t"
        "s_cselect_b32 s65, 0, 0x7f000000\n\t"
        "s_xor_b32 s67, s67, 8192\n\t"
        "s_add_u32 s67, s67, %[om]\n\t"
        "s_lshr_b32 s68, s52, 2\n\t"
        "s_add_u32 s67, s67, s68\n\t"
        "s_add_u32 s67, s67, s65\n\t"
        "s_cmp_eq_u32 %[w], 3\n\t"
        "s_cselect_b32 s65, s65, 0x7f000000\n\t"
        "v_mov_b32 v8, 0x7f000000\n\t"
        "s_xor_b32 s70, s71, 8192\n\t"
        "v_cmp_eq_u32 vcc, 0, v6\n\t"
        "v_mov_b32 v4, s70\n\t"
        "s_nop 0\n\t"
        "v_cndmask_b32 v8, v8, v4, vcc\n\t"
        "s_xor_b32 s70, s72, 8192\n\t"
        "v_cmp_eq_u32 vcc, 1, v6\n\t"
        "v_mov_b32 v4, s70\n\t"
        "s_nop 0\n\t"
        "v_cndmask_b32 v8, v8, v4, vcc\n\t"
        "s_xor_b32 s70, s73, 8192\n\t"
        "v_cmp_eq_u32 vcc, 2, v6\n\t"
        "v_mov_b32 v4, s70\n\t"
        "s_nop 0\n\t"
        "v_cndmask_b32 v8, v8, v4, vcc\n\t"
        "s_xor_b32 s70, s74, 8192\n\t"
        "v_cmp_eq_u32 vcc, 3, v6\n\t"
        "v_mov_b32 v4, s70\n\t"
        "s_nop 0\n\t"
        "v_cndmask_b32 v8, v8, v4, vcc\n\t"
        "v_add_u32 v8, v8, v5\n\t"
        "v_mov_b32 v9, 0x7f000000\n\t"
        "s_xor_b32 s70, s75, 8192\n\t"
        "v_cmp_eq_u32 vcc, 0, v6\n\t"
        "v_mov_b32 v4, s70\n\t"
        "s_nop 0\n\t"
        "v_cndmask_b32 v9, v9, v4, vcc\n\t"
        "s_xor_b32 s70, s76, 8192\n\t"
        "v_cmp_eq_u32 vcc, 1, v6\n\t"
        "v_mov_b32 v4, s70\n\t"
        "s_nop 0\n\t"
        "v_cndmask_b32 v9, v9, v4, vcc\n\t"
        "s_xor_b32 s70, s77, 8192\n\t"
        "v_cmp_eq_u32 vcc, 2, v6\n\t"
        "v_mov_b32 v4, s70\n\t"
        "s_nop 0\n\t"
        "v_cndmask_b32 v9, v9, v4, vcc\n\t"
        "s_xor_b32 s70, s78, 8192\n\t"
        "v_cmp_eq_u32 vcc, 3, v6\n\t"
        "v_mov_b32 v4, s70\n\t"
        "s_nop 0\n\t"
        "v_cndmask_b32 v9, v9, v4, vcc\n\t"
        "v_add_u32 v9, v9, v5\n\t"
        "s_cmp_lt_u32 s45, 62\n\t"
        "s_barrier\n\t"
        "s_cbranch_scc0 .Lr_d2_%=\n\t"
        "s_nop 15\n\t"
        "s_nop 15\n\t"
        "s_nop 15\n\t"
        "s_nop 15\n\t"
        "s_nop 15\n\t"
        "s_nop 15\n\t"
        "s_nop 15\n\t"
        "s_nop 15\n\t"
        "s_nop 15\n\t"
        "s_nop 15\n\t"
        "s_nop 15\n\t"
        "s_nop 15\n\t"
        "s_nop 15\n\t"
        "s_nop 15\n\t"
        "s_nop 15\n\t"
        "s_nop 15\n\t"
        "s_nop 15\n\t"
        "s_nop 15\n\t"
        "s_nop 15\n\t"
        "s_nop 15\n\t"
        "s_nop 15\n\t"
        "s_nop 13\n\t"
        "s_cmp_eq_u32 s45, 0\n\t"
        "s_cbranch_scc0 .Lr_d3_%=\n\t"
        ".Lr_an2_%=:\n\t"
        "buffer_load_dword v49, v48, %[rs], s57 offen sc1\n\t"
        "s_waitcnt vmcnt(0)\n\t"
        "v_cmp_eq_u32 vcc, s66, v49\n\t"
        "s_cbranch_vccnz .Lr_an2f_%=\n\t"
        ".Lr_an2k_%=:\n\t"
        "s_add_u32 s57, s57, 8192\n\t"
        ".Lr_d3_%=:\n\t"
        "s_add_u32 s69, s50, 4096\n\t"
        "buffer_load_dwordx4 v[16:19], v0, %[rs], s50 offen offset:0 sc1\n\t"
        "buffer_load_dwordx4 v[20:23], v0, %[rs], s50 offen offset:1024 sc1\n\t"
        "buffer_load_dwordx4 v[24:27], v0, %[rs], s50 offen offset:2048 sc1\n\t"
        "buffer_load_dwordx4 v[28:31], v0, %[rs], s50 offen offset:3072 sc1\n\t"
        "buffer_load_dwordx4 v[32:35], v0, %[rs], s69 offen offset:0 sc1\n\t"
        "buffer_load_dwordx4 v[36:39], v0, %[rs], s69 offen offset:1024 sc1\n\t"
        "buffer_load_dwordx4 v[40:43], v0, %[rs], s69 offen offset:2048 sc1\n\t"
        "buffer_load_dwordx4 v[44:47], v0, %[rs], s69 offen offset:3072 sc1\n\t"
        "v_add_u32 v7, s44, v11\n\t"
        "s_add_u32 s58, s58, 1\n\t"
        "v_mov_b32 v4, s58\n\t"
        "s_add_u32 s50, s50, 4194304\n\t"
        "s_and_b32 s70, s58, 7\n\t"
        "s_cselect_b32 s70, 0, 33554432\n\t"
        "s_sub_u32 s50, s50, s70\n\t"
        "s_waitcnt vmcnt(7)\n\t"
        "ds_write_b128 v7, v[16:19] offset:0\n\t"
        "s_waitcnt vmcnt(6)\n\t"
        "ds_write_b128 v7, v[20:23] offset:1024\n\t"
        "s_waitcnt vmcnt(5)\n\t"
        "ds_write_b128 v7, v[24:27] offset:2048\n\t"
        "s_waitcnt vmcnt(4)\n\t"
        "ds_write_b128 v7, v[28:31] offset:3072\n\t"
        "s_waitcnt vmcnt(3)\n\t"
        "ds_write_b128 v7, v[32:35] offset:4096\n\t"
        "s_waitcnt vmcnt(2)\n\t"
        "ds_write_b128 v7, v[36:39] offset:5120\n\t"
        "s_waitcnt vmcnt(1)\n\t"
        "ds_write_b128 v7, v[40:43] offset:6144\n\t"
        "s_waitcnt vmcnt(0)\n\t"
        "ds_write_b128 v7, v[44:47] offset:7168\n\t"
        "ds_write_b32 v10, v4\n\t"
        ".Lr_d2_%=:\n\t"
        "buffer_store_dwordx4 v[12:15], v8, %[rs], s65 offen sc1\n\t"
        "buffer_store_dwordx4 v[12:15], v9, %[rs], s65 offen sc1\n\t"
        "buffer_store_dwordx4 v[12:15], v0, %[rs], s67 offen sc1\n\t"
        "buffer_store_dwordx4 v[12:15], v0, %[rs], s67 offen offset:1024 sc1\n\t"
        "s_waitcnt vmcnt(0)\n\t"
        "s_add_u32 s44, s44, 32768\n\t"
        "s_cmp_lt_u32 s44, 98304\n\t"
        "s_cselect_b32 s44, s44, 0\n\t"
        "s_add_u32 s48, s48, 2097152\n\t"
        "s_and_b32 s48, s48, 0xffffff\n\t"
        "s_xor_b32 s48, s48, 0x2000\n\t"
        "s_add_u32 s49, s49, 2097152\n\t"
        "s_and_b32 s49, s49, 0xffffff\n\t"
        "s_xor_b32 s49, s49, 0x2000\n\t"
        "s_add_u32 s45, s45, 1\n\t"
        "s_cmp_lt_u32 s45, 64\n\t"
        "s_cbranch_scc1 .Lr_dloop_%=\n\t"
        "s_branch .Lr_end_%=\n\t"
        ".Lr_ox_%=:\n\t"
        "v_and_b32 v7, 15, v5\n\t"
        "v_lshlrev_b32 v3, 12, v7\n\t"
        "v_lshl_add_u32 v3, v6, 3, v3\n\t"
        "s_mov_b32 s52, %[oo]\n\t"
        "s_cmp_eq_u32 %[w], 2\n\t"
        "s_cbranch_scc1 .Lr_oloop0_%=\n\t"
        "s_add_u32 s52, s52, 65536\n\t"
        ".Lr_oloop1_%=:\n\t"
        "s_and_b32 s70, s45, 1\n\t"
        "v_add_u32 v1, s44, v0\n\t"
        "s_lshl_b32 s70, s70, 13\n\t"
        "s_add_u32 s70, s70, 98304\n\t"
        "v_add_u32 v2, s70, v0\n\t"
        "v_mov_b32 v5, 114720\n\t"
        "s_cmp_eq_u32 s45, 63\n\t"
        "s_barrier\n\t"
        "s_cbranch_scc1 .Lr_ol1_%=\n\t"
        "s_nop 15\n\t"
        "s_nop 15\n\t"
        "s_nop 15\n\t"
        "s_nop 15\n\t"
        "s_nop 10\n\t"
        ".Lr_om1_%=:\n\t"
        "s_add_u32 s70, s48, 196608\n\t"
        "buffer_load_dwordx4 v[96:99], v0, %[rs], s70 offen offset:0 sc1\n\t"
        "buffer_load_dwordx4 v[100:103], v0, %[rs], s70 offen offset:1024 sc1\n\t"
        "buffer_load_dwordx4 v[104:107], v0, %[rs], s70 offen offset:2048 sc1\n\t"
        "buffer_load_dwordx4 v[108:111], v0, %[rs], s70 offen offset:3072 sc1\n\t"
        "s_add_u32 s70, s48, 200704\n\t"
        "buffer_load_dwordx4 v[112:115], v0, %[rs], s70 offen offset:0 sc1\n\t"
        "buffer_load_dwordx4 v[116:119], v0, %[rs], s70 offen offset:1024 sc1\n\t"
        "buffer_load_dwordx4 v[120:123], v0, %[rs], s70 offen offset:2048 sc1\n\t"
        "buffer_load_dwordx4 v[124:127], v0, %[rs], s70 offen offset:3072 sc1\n\t"
        "s_add_u32 s70, s48, 229376\n\t"
        "buffer_load_dwordx4 v[128:131], v0, %[rs], s70 offen offset:0 sc1\n\t"
        "buffer_load_dwordx4 v[132:135], v0, %[rs], s70 offen offset:1024 sc1\n\t"
        "buffer_load_dwordx4 v[136:139], v0, %[rs], s70 offen offset:2048 sc1\n\t"
        "buffer_load_dwordx4 v[140:143], v0, %[rs], s70 offen offset:3072 sc1\n\t"
        "s_add_u32 s70, s48, 233472\n\t"
        "buffer_load_dwordx4 v[144:147], v0, %[rs], s70 offen offset:0 sc1\n\t"
        "buffer_load_dwordx4 v[148:151], v0, %[rs], s70 offen offset:1024 sc1\n\t"
        "buffer_load_dwordx4 v[152:155], v0, %[rs], s70 offen offset:2048 sc1\n\t"
        "buffer_load_dwordx4 v[156:159], v0, %[rs], s70 offen offset:3072 sc1\n\t"
        "s_add_u32 s70, s48, 262144\n\t"
        "buffer_load_dwordx4 v[160:163], v0, %[rs], s70 offen offset:0 sc1\n\t"
        "buffer_load_dwordx4 v[164:167], v0, %[rs], s70 offen offset:1024 sc1\n\t"
        "buffer_load_dwordx4 v[168:171], v0, %[rs], s70 offen offset:2048 sc1\n\t"
        "buffer_load_dwordx4 v[172:175], v0, %[rs], s70 offen offset:3072 sc1\n\t"
        "s_add_u32 s70, s48, 266240\n\t"
        "buffer_load_dwordx4 v[176:179], v0, %[rs], s70 offen offset:0 sc1\n\t"
        "buffer_load_dwordx4 v[180:183], v0, %[rs], s70 offen offset:1024 sc1\n\t"
        "buffer_load_dwordx4 v[184:187], v0, %[rs], s70 offen offset:2048 sc1\n\t"
        "buffer_load_dwordx4 v[188:191], v0, %[rs], s70 offen offset:3072 sc1\n\t"
        "ds_read_b128 v[16:19], v1 offset:0\n\t"
        "ds_read_b128 v[20:23], v1 offset:1024\n\t"
        "ds_read_b128 v[24:27], v1 offset:2048\n\t"
        "ds_read_b128 v[28:31], v1 offset:3072\n\t"
        "ds_read_b128 v[32:35], v1 offset:4096\n\t"
        "ds_read_b128 v[36:39], v1 offset:5120\n\t"
        "ds_read_b128 v[40:43], v1 offset:6144\n\t"
        "ds_read_b128 v[44:47], v1 offset:7168\n\t"
        "ds_read_b128 v[48:51], v1 offset:8192\n\t"
        "ds_read_b128 v[52:55], v1 offset:9216\n\t"
        "ds_read_b128 v[56:59], v1 offset:10240\n\t"
        "ds_read_b128 v[60:63], v1 offset:11264\n\t"
        "s_waitcnt vmcnt(23)\n\t"
        "s_waitcnt lgkmcnt(10)\n\t"
        "v_mfma_f32_16x16x32_bf16 v[72:75], v[16:19], v[96:99], 0\n\t"
        "s_add_u32 s70, s48, 294912\n\t"
        "buffer_load_dwordx4 v[192:195], v0, %[rs], s70 offen offset:0 sc1\n\t"
        "ds_read_b128 v[16:19], v1 offset:12288\n\t"
        "s_waitcnt vmcnt(23)\n\t"
        "v_mfma_f32_16x16x32_bf16 v[8:11], v[20:23], v[100:103], 0\n\t"
        "buffer_load_dwordx4 v[196:199], v0, %[rs], s70 offen offset:1024 sc1\n\t"
        "ds_read_b128 v[20:23], v1 offset:13312\n\t"
        "s_waitcnt vmcnt(23)\n\t"
        "s_waitcnt lgkmcnt(10)\n\t"
        "v_mfma_f32_16x16x32_bf16 v[72:75], v[24:27], v[104:107], v[72:75]\n\t"
        "buffer_load_dwordx4 v[200:203], v0, %[rs], s70 offen offset:2048 sc1\n\t"
        "ds_read_b128 v[24:27], v1 offset:14336\n\t"
        "s_waitcnt vmcnt(23)\n\t"
        "v_mfma_f32_16x16x32_bf16 v[8:11], v[28:31], v[108:111], v[8:11]\n\t"
        "buffer_load_dwordx4 v[204:207], v0, %[rs], s70 offen offset:3072 sc1\n\t"
        "ds_read_b128 v[28:31], v1 offset:15360\n\t"
        "s_waitcnt vmcnt(23)\n\t"
        "s_waitcnt lgkmcnt(10)\n\t"
        "v_mfma_f32_16x16x32_bf16 v[72:75], v[32:35], v[112:115], v[72:75]\n\t"
        "s_add_u32 s70, s48, 299008\n\t"
        "buffer_load_dwordx4 v[208:211], v0, %[rs], s70 offen offset:0 sc1\n\t"
        "ds_read_b128 v[32:35], v1 offset:16384\n\t"
        "s_waitcnt vmcnt(23)\n\t"
        "v_mfma_f32_16x16x32_bf16 v[8:11], v[36:39], v[116:119], v[8:11]\n\t"
        "buffer_load_dwordx4 v[212:215], v0, %[rs], s70 offen offset:1024 sc1\n\t"
        "ds_read_b128 v[36:39], v1 offset:17408\n\t"
        "s_waitcnt vmcnt(23)\n\t"
        "s_waitcnt lgkmcnt(10)\n\t"
        "v_mfma_f32_16x16x32_bf16 v[72:75], v[40:43], v[120:123], v[72:75]\n\t"
        "buffer_load_dwordx4 v[216:219], v0, %[rs], s70 offen offset:2048 sc1\n\t"
        "ds_read_b128 v[40:43], v1 offset:18432\n\t"
        "s_waitcnt vmcnt(23)\n\t"
        "v_mfma_f32_16x16x32_bf16 v[8:11], v[44:47], v[124:127], v[8:11]\n\t"
        "buffer_load_dwordx4 v[220:223], v0, %[rs], s70 offen offset:3072 sc1\n\t"
        "ds_read_b128 v[44:47], v1 offset:19456\n\t"
        "s_waitcnt vmcnt(23)\n\t"
        "s_waitcnt lgkmcnt(10)\n\t"
        "v_mfma_f32_16x16x32_bf16 v[72:75], v[48:51], v[128:131], v[72:75]\n\t"
        "s_add_u32 s70, s48, 217088\n\t"
        "buffer_load_dwordx4 v[224:227], v0, %[rs], s70 offen offset:2048 sc1\n\t"
        "ds_read_b128 v[48:51], v1 offset:20480\n\t"
        "s_waitcnt vmcnt(23)\n\t"
        "v_mfma_f32_16x16x32_bf16 v[8:11], v[52:55], v[132:135], v[8:11]\n\t"
        "buffer_load_dwordx4 v[228:231], v0, %[rs], s70 offen offset:3072 sc1\n\t"
        "ds_read_b128 v[52:55], v1 offset:21504\n\t"
        "s_waitcnt vmcnt(23)\n\t"
        "s_waitcnt lgkmcnt(10)\n\t"
        "v_mfma_f32_16x16x32_bf16 v[72:75], v[56:59], v[136:139], v[72:75]\n\t"
        "s_add_u32 s70, s48, 249856\n\t"
        "buffer_load_dwordx4 v[232:235], v0, %[rs], s70 offen offset:2048 sc1\n\t"
        "ds_read_b128 v[56:59], v1 offset:22528\n\t"
        "s_waitcnt vmcnt(23)\n\t"
        "v_mfma_f32_16x16x32_bf16 v[8:11], v[60:63], v[140:143], v[8:11]\n\t"
        "buffer_load_dwordx4 v[236:239], v0, %[rs], s70 offen offset:3072 sc1\n\t"
        "ds_read_b128 v[60:63], v1 offset:23552\n\t"
        "s_waitcnt vmcnt(23)\n\t"
        "s_waitcnt lgkmcnt(10)\n\t"
        "v_mfma_f32_16x16x32_bf16 v[72:75], v[16:19], v[144:147], v[72:75]\n\t"
        "s_add_u32 s70, s48, 282624\n\t"
        "buffer_load_dwordx4 v[240:243], v0, %[rs], s70 offen offset:2048 sc1\n\t"
        "ds_read_b128 v[16:19], v1 offset:24576\n\t"
        "s_waitcnt vmcnt(23)\n\t"
        "v_mfma_f32_16x16x32_bf16 v[8:11], v[20:23], v[148:151], v[8:11]\n\t"
        "buffer_load_dwordx4 v[244:247], v0, %[rs], s70 offen offset:3072 sc1\n\t"
        "ds_read_b128 v[20:23], v1 offset:25600\n\t"
        "s_waitcnt vmcnt(23)\n\t"
        "s_waitcnt lgkmcnt(10)\n\t"
        "v_mfma_f32_16x16x32_bf16 v[72:75], v[24:27], v[152:155], v[72:75]\n\t"
        "s_add_u32 s70, s48, 315392\n\t"
        "buffer_load_dwordx4 v[248:251], v0, %[rs], s70 offen offset:2048 sc1\n\t"
        "ds_read_b128 v[24:27], v1 offset:26624\n\t"
        "s_waitcnt vmcnt(23)\n\t"
        "v_mfma_f32_16x16x32_bf16 v[8:11], v[28:31], v[156:159], v[8:11]\n\t"
        "buffer_load_dwordx4 v[252:255], v0, %[rs], s70 offen offset:3072 sc1\n\t"
        "ds_read_b128 v[28:31], v1 offset:27648\n\t"
        "s_waitcnt vmcnt(23)\n\t"
        "s_waitcnt lgkmcnt(10)\n\t"
        "v_mfma_f32_16x16x32_bf16 v[72:75], v[32:35], v[160:163], v[72:75]\n\t"
        "ds_read_b128 v[32:35], v1 offset:28672\n\t"
        "s_waitcnt vmcnt(22)\n\t"
        "v_mfma_f32_16x16x32_bf16 v[8:11], v[36:39], v[164:167], v[8:11]\n\t"
        "ds_read_b128 v[36:39], v1 offset:29696\n\t"
        "s_waitcnt vmcnt(21)\n\t"
        "s_waitcnt lgkmcnt(10)\n\t"
        "v_mfma_f32_16x16x32_bf16 v[72:75], v[40:43], v[168:171], v[72:75]\n\t"
        "ds_read_b128 v[40:43], v1 offset:30720\n\t"
        "s_waitcnt vmcnt(20)\n\t"
        "v_mfma_f32_16x16x32_bf16 v[8:11], v[44:47], v[172:175], v[8:11]\n\t"
        "ds_read_b128 v[44:47], v1 offset:31744\n\t"
        "s_waitcnt vmcnt(19)\n\t"
        "s_waitcnt lgkmcnt(10)\n\t"
        "v_mfma_f32_16x16x32_bf16 v[72:75], v[48:51], v[176:179], v[72:75]\n\t"
        ".Lr_o31_%=:\n\t"
        "ds_read_b32 v4, v5\n\t"
        "s_waitcnt lgkmcnt(0)\n\t"
        "v_cmp_lt_u32 vcc, s45, v4\n\t"
        "s_cbranch_vccz .Lr_o31_%=\n\t"
        "ds_read_b128 v[48:51], v2 offset:0\n\t"
        "s_waitcnt vmcnt(18)\n\t"
        "v_mfma_f32_16x16x32_bf16 v[8:11], v[52:55], v[180:183], v[8:11]\n\t"
        "ds_read_b128 v[52:55], v2 offset:1024\n\t"
        "s_waitcnt vmcnt(17)\n\t"
        "v_mfma_f32_16x16x32_bf16 v[72:75], v[56:59], v[184:187], v[72:75]\n\t"
        "ds_read_b128 v[56:59], v2 offset:2048\n\t"
        "s_waitcnt vmcnt(16)\n\t"
        "v_mfma_f32_16x16x32_bf16 v[8:11], v[60:63], v[188:191], v[8:11]\n\t"
        "ds_read_b128 v[60:63], v2 offset:3072\n\t"
        "s_waitcnt vmcnt(15)\n\t"
        "v_mfma_f32_16x16x32_bf16 v[72:75], v[16:19], v[192:195], v[72:75]\n\t"
        "ds_read_b128 v[16:19], v2 offset:4096\n\t"
        "s_waitcnt vmcnt(14)\n\t"
        "v_mfma_f32_16x16x32_bf16 v[8:11], v[20:23], v[196:199], v[8:11]\n\t"
        "ds_read_b128 v[20:23], v2 offset:5120\n\t"
        "s_waitcnt vmcnt(13)\n\t"
        "v_mfma_f32_16x16x32_bf16 v[72:75], v[24:27], v[200:203], v[72:75]\n\t"
        "ds_read_b128 v[24:27], v2 offset:6144\n\t"
        "s_waitcnt vmcnt(12)\n\t"
        "v_mfma_f32_16x16x32_bf16 v[8:11], v[28:31], v[204:207], v[8:11]\n\t"
        "ds_read_b128 v[28:31], v2 offset:7168\n\t"
        "s_waitcnt vmcnt(11)\n\t"
        "v_mfma_f32_16x16x32_bf16 v[72:75], v[32:35], v[208:211], v[72:75]\n\t"
        "s_waitcnt vmcnt(10)\n\t"
        "v_mfma_f32_16x16x32_bf16 v[8:11], v[36:39], v[212:215], v[8:11]\n\t"
        "s_waitcnt vmcnt(9)\n\t"
        "v_mfma_f32_16x16x32_bf16 v[72:75], v[40:43], v[216:219], v[72:75]\n\t"
        "s_waitcnt vmcnt(8)\n\t"
        "v_mfma_f32_16x16x32_bf16 v[8:11], v[44:47], v[220:223], v[8:11]\n\t"
        "s_waitcnt vmcnt(7)\n\t"
        "s_waitcnt lgkmcnt(6)\n\t"
        "v_mfma_f32_16x16x32_bf16 v[72:75], v[48:51], v[224:227], v[72:75]\n\t"
        "s_waitcnt vmcnt(6)\n\t"
        "v_mfma_f32_16x16x32_bf16 v[8:11], v[52:55], v[228:231], v[8:11]\n\t"
        "s_waitcnt vmcnt(5)\n\t"
        "s_waitcnt lgkmcnt(4)\n\t"
        "v_mfma_f32_16x16x32_bf16 v[72:75], v[56:59], v[232:235], v[72:75]\n\t"
        "s_waitcnt vmcnt(4)\n\t"
        "v_mfma_f32_16x16x32_bf16 v[8:11], v[60:63], v[236:239], v[8:11]\n\t"
        "s_waitcnt vmcnt(3)\n\t"
        "s_waitcnt lgkmcnt(2)\n\t"
        "v_mfma_f32_16x16x32_bf16 v[72:75], v[16:19], v[240:243], v[72:75]\n\t"
        "s_waitcnt vmcnt(2)\n\t"
        "v_mfma_f32_16x16x32_bf16 v[8:11], v[20:23], v[244:247], v[8:11]\n\t"
        "s_waitcnt vmcnt(1)\n\t"
        "s_waitcnt lgkmcnt(0)\n\t"
        "v_mfma_f32_16x16x32_bf16 v[72:75], v[24:27], v[248:251], v[72:75]\n\t"
        "s_waitcnt vmcnt(0)\n\t"
        "v_mfma_f32_16x16x32_bf16 v[8:11], v[28:31], v[252:255], v[8:11]\n\t"
        "s_nop 7\n\t"
        "v_add_f32 v8, v8, v72\n\t"
        "v_add_f32 v9, v9, v73\n\t"
        "v_add_f32 v10, v10, v74\n\t"
        "v_add_f32 v11, v11, v75\n\t"
        ".Lr_o21_%=:\n\t"
        "v_cmp_u_f32 vcc, v8, v9\n\t"
        "v_cvt_pk_bf16_f32 v4, v8, v9\n\t"
        "v_cvt_pk_bf16_f32 v5, v10, v11\n\t"
        "s_cbranch_vccnz .Lr_of1_%=\n\t"
        ".Lr_ok1_%=:\n\t"
        "buffer_store_dwordx2 v[4:5], v3, %[ro], s52 offen\n\t"
        "s_add_u32 s52, s52, 131072\n\t"
        "s_add_u32 s44, s44, 32768\n\t"
        "s_cmp_lt_u32 s44, 98304\n\t"
        "s_cselect_b32 s44, s44, 0\n\t"
        "s_add_u32 s48, s48, 2097152\n\t"
        "s_and_b32 s48, s48, 0xffffff\n\t"
        "s_xor_b32 s48, s48, 0x2000\n\t"
        "s_add_u32 s49, s49, 2097152\n\t"
        "s_and_b32 s49, s49, 0xffffff\n\t"
        "s_xor_b32 s49, s49, 0x2000\n\t"
        "s_add_u32 s45, s45, 1\n\t"
        "s_cmp_lt_u32 s45, 64\n\t"
        "s_cbranch_scc1 .Lr_oloop1_%=\n\t"
        "s_branch .Lr_end_%=\n\t"
        ".Lr_ol1_%=:\n\t"
        "s_branch .Lr_om1_%=\n\t"
        ".Lr_of1_%=:\n\t"
        "v_max_u32 v6, v96, v97\n\t"
        "v_max3_u32 v6, v6, v98, v99\n\t"
        "v_max3_u32 v6, v6, v100, v101\n\t"
        "v_max3_u32 v6, v6, v102, v103\n\t"
        "v_max3_u32 v6, v6, v104, v105\n\t"
        "v_max3_u32 v6, v6, v106, v107\n\t"
        "v_max3_u32 v6, v6, v108, v109\n\t"
        "v_max3_u32 v6, v6, v110, v111\n\t"
        "v_max3_u32 v6, v6, v112, v113\n\t"
        "v_max3_u32 v6, v6, v114, v115\n\t"
        "v_max3_u32 v6, v6, v116, v117\n\t"
        "v_max3_u32 v6, v6, v118, v119\n\t"
        "v_max3_u32 v6, v6, v120, v121\n\t"
        "v_max3_u32 v6, v6, v122, v123\n\t"
        "v_max3_u32 v6, v6, v124, v125\n\t"
        "v_max3_u32 v6, v6, v126, v127\n\t"
        "v_max3_u32 v6, v6, v128, v129\n\t"
        "v_max3_u32 v6, v6, v130, v131\n\t"
        "v_max3_u32 v6, v6, v132, v133\n\t"
        "v_max3_u32 v6, v6, v134, v135\n\t"
        "v_max3_u32 v6, v6, v136, v137\n\t"
        "v_max3_u32 v6, v6, v138, v139\n\t"
        "v_max3_u32 v6, v6, v140, v141\n\t"
        "v_max3_u32 v6, v6, v142, v143\n\t"
        "v_max3_u32 v6, v6, v144, v145\n\t"
        "v_max3_u32 v6, v6, v146, v147\n\t"
        "v_max3_u32 v6, v6, v148, v149\n\t"
        "v_max3_u32 v6, v6, v150, v151\n\t"
        "v_max3_u32 v6, v6, v152, v153\n\t"
        "v_max3_u32 v6, v6, v154, v155\n\t"
        "v_max3_u32 v6, v6, v156, v157\n\t"
        "v_max3_u32 v6, v6, v158, v159\n\t"
        "v_max3_u32 v6, v6, v160, v161\n\t"
        "v_max3_u32 v6, v6, v162, v163\n\t"
        "v_max3_u32 v6, v6, v164, v165\n\t"
        "v_max3_u32 v6, v6, v166, v167\n\t"
        "v_max3_u32 v6, v6, v168, v169\n\t"
        "v_max3_u32 v6, v6, v170, v171\n\t"
        "v_max3_u32 v6, v6, v172, v173\n\t"
        "v_max3_u32 v6, v6, v174, v175\n\t"
        "v_max3_u32 v6, v6, v176, v177\n\t"
        "v_max3_u32 v6, v6, v178, v179\n\t"
        "v_max3_u32 v6, v6, v180, v181\n\t"
        "v_max3_u32 v6, v6, v182, v183\n\t"
        "v_max3_u32 v6, v6, v184, v185\n\t"
        "v_max3_u32 v6, v6, v186, v187\n\t"
        "v_max3_u32 v6, v6, v188, v189\n\t"
        "v_max3_u32 v6, v6, v190, v191\n\t"
        "v_max3_u32 v6, v6, v192, v193\n\t"
        "v_max3_u32 v6, v6, v194, v195\n\t"
        "v_max3_u32 v6, v6, v196, v197\n\t"
        "v_max3_u32 v6, v6, v198, v199\n\t"
        "v_max3_u32 v6, v6, v200, v201\n\t"
        "v_max3_u32 v6, v6, v202, v203\n\t"
        "v_max3_u32 v6, v6, v204, v205\n\t"
        "v_max3_u32 v6, v6, v206, v207\n\t"
        "v_max3_u32 v6, v6, v208, v209\n\t"
        "v_max3_u32 v6, v6, v210, v211\n\t"
        "v_max3_u32 v6, v6, v212, v213\n\t"
        "v_max3_u32 v6, v6, v214, v215\n\t"
        "v_max3_u32 v6, v6, v216, v217\n\t"
        "v_max3_u32 v6, v6, v218, v219\n\t"
        "v_max3_u32 v6, v6, v220, v221\n\t"
        "v_max3_u32 v6, v6, v222, v223\n\t"
        "v_max3_u32 v6, v6, v224, v225\n\t"
        "v_max3_u32 v6, v6, v226, v227\n\t"
        "v_max3_u32 v6, v6, v228, v229\n\t"
        "v_max3_u32 v6, v6, v230, v231\n\t"
        "v_max3_u32 v6, v6, v232, v233\n\t"
        "v_max3_u32 v6, v6, v234, v235\n\t"
        "v_max3_u32 v6, v6, v236, v237\n\t"
        "v_max3_u32 v6, v6, v238, v239\n\t"
        "v_max3_u32 v6, v6, v240, v241\n\t"
        "v_max3_u32 v6, v6, v242, v243\n\t"
        "v_max3_u32 v6, v6, v244, v245\n\t"
        "v_max3_u32 v6, v6, v246, v247\n\t"
        "v_max3_u32 v6, v6, v248, v249\n\t"
        "v_max3_u32 v6, v6, v250, v251\n\t"
        "v_max3_u32 v6, v6, v252, v253\n\t"
        "v_max3_u32 v6, v6, v254, v255\n\t"
        "v_cmp_eq_u32 vcc, s66, v6\n\t"
        "s_cbranch_vccz .Lr_ok1_%=\n\t"
        "s_add_u32 s79, s79, 1\n\t"
        "s_sub_u32 s46, s46, 1\n\t"
        "s_cbranch_scc1 .Lr_og1_%=\n\t"
        "s_add_u32 s70, s48, 196608\n\t"
        "buffer_load_dwordx4 v[96:99], v0, %[rs], s70 offen offset:0 sc1\n\t"
        "buffer_load_dwordx4 v[100:103], v0, %[rs], s70 offen offset:1024 sc1\n\t"
        "buffer_load_dwordx4 v[104:107], v0, %[rs], s70 offen offset:2048 sc1\n\t"
        "buffer_load_dwordx4 v[108:111], v0, %[rs], s70 offen offset:3072 sc1\n\t"
        "s_add_u32 s70, s48, 200704\n\t"
        "buffer_load_dwordx4 v[112:115], v0, %[rs], s70 offen offset:0 sc1\n\t"
        "buffer_load_dwordx4 v[116:119], v0, %[rs], s70 offen offset:1024 sc1\n\t"
        "buffer_load_dwordx4 v[120:123], v0, %[rs], s70 offen offset:2048 sc1\n\t"
        "buffer_load_dwordx4 v[124:127], v0, %[rs], s70 offen offset:3072 sc1\n\t"
        "s_add_u32 s70, s48, 229376\n\t"
        "buffer_load_dwordx4 v[128:131], v0, %[rs], s70 offen offset:0 sc1\n\t"
        "buffer_load_dwordx4 v[132:135], v0, %[rs], s70 offen offset:1024 sc1\n\t"
        "buffer_load_dwordx4 v[136:139], v0, %[rs], s70 offen offset:2048 sc1\n\t"
        "buffer_load_dwordx4 v[140:143], v0, %[rs], s70 offen offset:3072 sc1\n\t"
        "s_add_u32 s70, s48, 233472\n\t"
        "buffer_load_dwordx4 v[144:147], v0, %[rs], s70 offen offset:0 sc1\n\t"
        "buffer_load_dwordx4 v[148:151], v0, %[rs], s70 offen offset:1024 sc1\n\t"
        "buffer_load_dwordx4 v[152:155], v0, %[rs], s70 offen offset:2048 sc1\n\t"
        "buffer_load_dwordx4 v[156:159], v0, %[rs], s70 offen offset:3072 sc1\n\t"
        "s_add_u32 s70, s48, 262144\n\t"
        "buffer_load_dwordx4 v[160:163], v0, %[rs], s70 offen offset:0 sc1\n\t"
        "buffer_load_dwordx4 v[164:167], v0, %[rs], s70 offen offset:1024 sc1\n\t"
        "buffer_load_dwordx4 v[168:171], v0, %[rs], s70 offen offset:2048 sc1\n\t"
        "buffer_load_dwordx4 v[172:175], v0, %[rs], s70 offen offset:3072 sc1\n\t"
        "s_add_u32 s70, s48, 266240\n\t"
        "buffer_load_dwordx4 v[176:179], v0, %[rs], s70 offen offset:0 sc1\n\t"
        "buffer_load_dwordx4 v[180:183], v0, %[rs], s70 offen offset:1024 sc1\n\t"
        "buffer_load_dwordx4 v[184:187], v0, %[rs], s70 offen offset:2048 sc1\n\t"
        "buffer_load_dwordx4 v[188:191], v0, %[rs], s70 offen offset:3072 sc1\n\t"
        "s_add_u32 s70, s48, 294912\n\t"
        "buffer_load_dwordx4 v[192:195], v0, %[rs], s70 offen offset:0 sc1\n\t"
        "buffer_load_dwordx4 v[196:199], v0, %[rs], s70 offen offset:1024 sc1\n\t"
        "buffer_load_dwordx4 v[200:203], v0, %[rs], s70 offen offset:2048 sc1\n\t"
        "buffer_load_dwordx4 v[204:207], v0, %[rs], s70 offen offset:3072 sc1\n\t"
        "s_add_u32 s70, s48, 299008\n\t"
        "buffer_load_dwordx4 v[208:211], v0, %[rs], s70 offen offset:0 sc1\n\t"
        "buffer_load_dwordx4 v[212:215], v0, %[rs], s70 offen offset:1024 sc1\n\t"
        "buffer_load_dwordx4 v[216:219], v0, %[rs], s70 offen offset:2048 sc1\n\t"
        "buffer_load_dwordx4 v[220:223], v0, %[rs], s70 offen offset:3072 sc1\n\t"
        "s_add_u32 s70, s48, 217088\n\t"
        "buffer_load_dwordx4 v[224:227], v0, %[rs], s70 offen offset:2048 sc1\n\t"
        "buffer_load_dwordx4 v[228:231], v0, %[rs], s70 offen offset:3072 sc1\n\t"
        "s_add_u32 s70, s48, 249856\n\t"
        "buffer_load_dwordx4 v[232:235], v0, %[rs], s70 offen offset:2048 sc1\n\t"
        "buffer_load_dwordx4 v[236:239], v0, %[rs], s70 offen offset:3072 sc1\n\t"
        "s_add_u32 s70, s48, 282624\n\t"
        "buffer_load_dwordx4 v[240:243], v0, %[rs], s70 offen offset:2048 sc1\n\t"
        "buffer_load_dwordx4 v[244:247], v0, %[rs], s70 offen offset:3072 sc1\n\t"
        "s_add_u32 s70, s48, 315392\n\t"
        "buffer_load_dwordx4 v[248:251], v0, %[rs], s70 offen offset:2048 sc1\n\t"
        "buffer_load_dwordx4 v[252:255], v0, %[rs], s70 offen offset:3072 sc1\n\t"
        "ds_read_b128 v[16:19], v1 offset:0\n\t"
        "ds_read_b128 v[20:23], v1 offset:1024\n\t"
        "ds_read_b128 v[24:27], v1 offset:2048\n\t"
        "ds_read_b128 v[28:31], v1 offset:3072\n\t"
        "ds_read_b128 v[32:35], v1 offset:4096\n\t"
        "ds_read_b128 v[36:39], v1 offset:5120\n\t"
        "ds_read_b128 v[40:43], v1 offset:6144\n\t"
        "ds_read_b128 v[44:47], v1 offset:7168\n\t"
        "ds_read_b128 v[48:51], v1 offset:8192\n\t"
        "ds_read_b128 v[52:55], v1 offset:9216\n\t"
        "ds_read_b128 v[56:59], v1 offset:10240\n\t"
        "ds_read_b128 v[60:63], v1 offset:11264\n\t"
        "s_waitcnt vmcnt(39)\n\t"
        "s_waitcnt lgkmcnt(10)\n\t"
        "v_mfma_f32_16x16x32_bf16 v[72:75], v[16:19], v[96:99], 0\n\t"
        "ds_read_b128 v[16:19], v1 offset:12288\n\t"
        "s_waitcnt vmcnt(38)\n\t"
        "v_mfma_f32_16x16x32_bf16 v[8:11], v[20:23], v[100:103], 0\n\t"
        "ds_read_b128 v[20:23], v1 offset:13312\n\t"
        "s_waitcnt vmcnt(37)\n\t"
        "s_waitcnt lgkmcnt(10)\n\t"
        "v_mfma_f32_16x16x32_bf16 v[72:75], v[24:27], v[104:107], v[72:75]\n\t"
        "ds_read_b128 v[24:27], v1 offset:14336\n\t"
        "s_waitcnt vmcnt(36)\n\t"
        "v_mfma_f32_16x16x32_bf16 v[8:11], v[28:31], v[108:111], v[8:11]\n\t"
        "ds_read_b128 v[28:31], v1 offset:15360\n\t"
        "s_waitcnt vmcnt(35)\n\t"
        "s_waitcnt lgkmcnt(10)\n\t"
        "v_mfma_f32_16x16x32_bf16 v[72:75], v[32:35], v[112:115], v[72:75]\n\t"
        "ds_read_b128 v[32:35], v1 offset:16384\n\t"
        "s_waitcnt vmcnt(34)\n\t"
        "v_mfma_f32_16x16x32_bf16 v[8:11], v[36:39], v[116:119], v[8:11]\n\t"
        "ds_read_b128 v[36:39], v1 offset:17408\n\t"
        "s_waitcnt vmcnt(33)\n\t"
        "s_waitcnt lgkmcnt(10)\n\t"
        "v_mfma_f32_16x16x32_bf16 v[72:75], v[40:43], v[120:123], v[72:75]\n\t"
        "ds_read_b128 v[40:43], v1 offset:18432\n\t"
        "s_waitcnt vmcnt(32)\n\t"
        "v_mfma_f32_16x16x32_bf16 v[8:11], v[44:47], v[124:127], v[8:11]\n\t"
        "ds_read_b128 v[44:47], v1 offset:19456\n\t"
        "s_waitcnt vmcnt(31)\n\t"
        "s_waitcnt lgkmcnt(10)\n\t"
        "v_mfma_f32_16x16x32_bf16 v[72:75], v[48:51], v[128:131], v[72:75]\n\t"
        "ds_read_b128 v[48:51], v1 offset:20480\n\t"
        "s_waitcnt vmcnt(30)\n\t"
        "v_mfma_f32_16x16x32_bf16 v[8:11], v[52:55], v[132:135], v[8:11]\n\t"
        "ds_read_b128 v[52:55], v1 offset:21504\n\t"
        "s_waitcnt vmcnt(29)\n\t"
        "s_waitcnt lgkmcnt(10)\n\t"
        "v_mfma_f32_16x16x32_bf16 v[72:75], v[56:59], v[136:139], v[72:75]\n\t"
        "ds_read_b128 v[56:59], v1 offset:22528\n\t"
        "s_waitcnt vmcnt(28)\n\t"
        "v_mfma_f32_16x16x32_bf16 v[8:11], v[60:63], v[140:143], v[8:11]\n\t"
        "ds_read_b128 v[60:63], v1 offset:23552\n\t"
        "s_waitcnt vmcnt(27)\n\t"
        "s_waitcnt lgkmcnt(10)\n\t"
        "v_mfma_f32_16x16x32_bf16 v[72:75], v[16:19], v[144:147], v[72:75]\n\t"
        "ds_read_b128 v[16:19], v1 offset:24576\n\t"
        "s_waitcnt vmcnt(26)\n\t"
        "v_mfma_f32_16x16x32_bf16 v[8:11], v[20:23], v[148:151], v[8:11]\n\t"
        "ds_read_b128 v[20:23], v1 offset:25600\n\t"
        "s_waitcnt vmcnt(25)\n\t"
        "s_waitcnt lgkmcnt(10)\n\t"
        "v_mfma_f32_16x16x32_bf16 v[72:75], v[24:27], v[152:155], v[72:75]\n\t"
        "ds_read_b128 v[24:27], v1 offset:26624\n\t"
        "s_waitcnt vmcnt(24)\n\t"
        "v_mfma_f32_16x16x32_bf16 v[8:11], v[28:31], v[156:159], v[8:11]\n\t"
        "ds_read_b128 v[28:31], v1 offset:27648\n\t"
        "s_waitcnt vmcnt(23)\n\t"
        "s_waitcnt lgkmcnt(10)\n\t"
        "v_mfma_f32_16x16x32_bf16 v[72:75], v[32:35], v[160:163], v[72:75]\n\t"
        "ds_read_b128 v[32:35], v1 offset:28672\n\t"
        "s_waitcnt vmcnt(22)\n\t"
        "v_mfma_f32_16x16x32_bf16 v[8:11], v[36:39], v[164:167], v[8:11]\n\t"
        "ds_read_b128 v[36:39], v1 offset:29696\n\t"
        "s_waitcnt vmcnt(21)\n\t"
        "s_waitcnt lgkmcnt(10)\n\t"
        "v_mfma_f32_16x16x32_bf16 v[72:75], v[40:43], v[168:171], v[72:75]\n\t"
        "ds_read_b128 v[40:43], v1 offset:30720\n\t"
        "s_waitcnt vmcnt(20)\n\t"
        "v_mfma_f32_16x16x32_bf16 v[8:11], v[44:47], v[172:175], v[8:11]\n\t"
        "ds_read_b128 v[44:47], v1 offset:31744\n\t"
        "s_waitcnt vmcnt(19)\n\t"
        "s_waitcnt lgkmcnt(10)\n\t"
        "v_mfma_f32_16x16x32_bf16 v[72:75], v[48:51], v[176:179], v[72:75]\n\t"
        "ds_read_b128 v[48:51], v2 offset:0\n\t"
        "s_waitcnt vmcnt(18)\n\t"
        "v_mfma_f32_16x16x32_bf16 v[8:11], v[52:55], v[180:183], v[8:11]\n\t"
        "ds_read_b128 v[52:55], v2 offset:1024\n\t"
        "s_waitcnt vmcnt(17)\n\t"
        "s_waitcnt lgkmcnt(10)\n\t"
        "v_mfma_f32_16x16x32_bf16 v[72:75], v[56:59], v[184:187], v[72:75]\n\t"
        "ds_read_b128 v[56:59], v2 offset:2048\n\t"
        "s_waitcnt vmcnt(16)\n\t"
        "v_mfma_f32_16x16x32_bf16 v[8:11], v[60:63], v[188:191], v[8:11]\n\t"
        "ds_read_b128 v[60:63], v2 offset:3072\n\t"
        "s_waitcnt vmcnt(15)\n\t"
        "s_waitcnt lgkmcnt(10)\n\t"
        "v_mfma_f32_16x16x32_bf16 v[72:75], v[16:19], v[192:195], v[72:75]\n\t"
        "ds_read_b128 v[16:19], v2 offset:4096\n\t"
        "s_waitcnt vmcnt(14)\n\t"
        "v_mfma_f32_16x16x32_bf16 v[8:11], v[20:23], v[196:199], v[8:11]\n\t"
        "ds_read_b128 v[20:23], v2 offset:5120\n\t"
        "s_waitcnt vmcnt(13)\n\t"
        "s_waitcnt lgkmcnt(10)\n\t"
        "v_mfma_f32_16x16x32_bf16 v[72:75], v[24:27], v[200:203], v[72:75]\n\t"
        "ds_read_b128 v[24:27], v2 offset:6144\n\t"
        "s_waitcnt vmcnt(12)\n\t"
        "v_mfma_f32_16x16x32_bf16 v[8:11], v[28:31], v[204:207], v[8:11]\n\t"
        "ds_read_b128 v[28:31], v2 offset:7168\n\t"
        "s_waitcnt vmcnt(11)\n\t"
        "s_waitcnt lgkmcnt(10)\n\t"
        "v_mfma_f32_16x16x32_bf16 v[72:75], v[32:35], v[208:211], v[72:75]\n\t"
        "s_waitcnt vmcnt(10)\n\t"
        "v_mfma_f32_16x16x32_bf16 v[8:11], v[36:39], v[212:215], v[8:11]\n\t"
        "s_waitcnt vmcnt(9)\n\t"
        "s_waitcnt lgkmcnt(8)\n\t"
        "v_mfma_f32_16x16x32_bf16 v[72:75], v[40:43], v[216:219], v[72:75]\n\t"
        "s_waitcnt vmcnt(8)\n\t"
        "v_mfma_f32_16x16x32_bf16 v[8:11], v[44:47], v[220:223], v[8:11]\n\t"
        "s_waitcnt vmcnt(7)\n\t"
        "s_waitcnt lgkmcnt(6)\n\t"
        "v_mfma_f32_16x16x32_bf16 v[72:75], v[48:51], v[224:227], v[72:75]\n\t"
        "s_waitcnt vmcnt(6)\n\t"
        "v_mfma_f32_16x16x32_bf16 v[8:11], v[52:55], v[228:231], v[8:11]\n\t"
        "s_waitcnt vmcnt(5)\n\t"
        "s_waitcnt lgkmcnt(4)\n\t"
        "v_mfma_f32_16x16x32_bf16 v[72:75], v[56:59], v[232:235], v[72:75]\n\t"
        "s_waitcnt vmcnt(4)\n\t"
        "v_mfma_f32_16x16x32_bf16 v[8:11], v[60:63], v[236:239], v[8:11]\n\t"
        "s_waitcnt vmcnt(3)\n\t"
        "s_waitcnt lgkmcnt(2)\n\t"
        "v_mfma_f32_16x16x32_bf16 v[72:75], v[16:19], v[240:243], v[72:75]\n\t"
        "s_waitcnt vmcnt(2)\n\t"
        "v_mfma_f32_16x16x32_bf16 v[8:11], v[20:23], v[244:247], v[8:11]\n\t"
        "s_waitcnt vmcnt(1)\n\t"
        "s_waitcnt lgkmcnt(0)\n\t"
        "v_mfma_f32_16x16x32_bf16 v[72:75], v[24:27], v[248:251], v[72:75]\n\t"
        "s_waitcnt vmcnt(0)\n\t"
        "v_mfma_f32_16x16x32_bf16 v[8:11], v[28:31], v[252:255], v[8:11]\n\t"
        "s_nop 7\n\t"
        "v_add_f32 v8, v8, v72\n\t"
        "v_add_f32 v9, v9, v73\n\t"
        "v_add_f32 v10, v10, v74\n\t"
        "v_add_f32 v11, v11, v75\n\t"
        "s_branch .Lr_o21_%=\n\t"
        ".Lr_og1_%=:\n\t"
        "s_mov_b32 s46, 0\n\t"
        "s_branch .Lr_ok1_%=\n\t"
        ".Lr_oloop0_%=:\n\t"
        "s_and_b32 s70, s45, 1\n\t"
        "v_add_u32 v1, s44, v0\n\t"
        "s_lshl_b32 s70, s70, 13\n\t"
        "s_add_u32 s70, s70, 98304\n\t"
        "v_add_u32 v2, s70, v0\n\t"
        "v_mov_b32 v5, 114720\n\t"
        "s_cmp_eq_u32 s45, 63\n\t"
        "s_barrier\n\t"
        "s_cbranch_scc1 .Lr_ol0_%=\n\t"
        "s_nop 15\n\t"
        "s_nop 15\n\t"
        "s_nop 15\n\t"
        "s_nop 15\n\t"
        "s_nop 10\n\t"
        ".Lr_om0_%=:\n\t"
        "s_add_u32 s70, s48, 212992\n\t"
        "buffer_load_dwordx4 v[96:99], v0, %[rs], s70 offen offset:0 sc1\n\t"
        "buffer_load_dwordx4 v[100:103], v0, %[rs], s70 offen offset:1024 sc1\n\t"
        "buffer_load_dwordx4 v[104:107], v0, %[rs], s70 offen offset:2048 sc1\n\t"
        "buffer_load_dwordx4 v[108:111], v0, %[rs], s70 offen offset:3072 sc1\n\t"
        "s_add_u32 s70, s48, 245760\n\t"
        "buffer_load_dwordx4 v[112:115], v0, %[rs], s70 offen offset:0 sc1\n\t"
        "buffer_load_dwordx4 v[116:119], v0, %[rs], s70 offen offset:1024 sc1\n\t"
        "buffer_load_dwordx4 v[120:123], v0, %[rs], s70 offen offset:2048 sc1\n\t"
        "buffer_load_dwordx4 v[124:127], v0, %[rs], s70 offen offset:3072 sc1\n\t"
        "s_add_u32 s70, s48, 278528\n\t"
        "buffer_load_dwordx4 v[128:131], v0, %[rs], s70 offen offset:0 sc1\n\t"
        "buffer_load_dwordx4 v[132:135], v0, %[rs], s70 offen offset:1024 sc1\n\t"
        "buffer_load_dwordx4 v[136:139], v0, %[rs], s70 offen offset:2048 sc1\n\t"
        "buffer_load_dwordx4 v[140:143], v0, %[rs], s70 offen offset:3072 sc1\n\t"
        "s_add_u32 s70, s48, 311296\n\t"
        "buffer_load_dwordx4 v[144:147], v0, %[rs], s70 offen offset:0 sc1\n\t"
        "buffer_load_dwordx4 v[148:151], v0, %[rs], s70 offen offset:1024 sc1\n\t"
        "buffer_load_dwordx4 v[152:155], v0, %[rs], s70 offen offset:2048 sc1\n\t"
        "buffer_load_dwordx4 v[156:159], v0, %[rs], s70 offen offset:3072 sc1\n\t"
        "s_add_u32 s70, s48, 217088\n\t"
        "buffer_load_dwordx4 v[160:163], v0, %[rs], s70 offen offset:0 sc1\n\t"
        "buffer_load_dwordx4 v[164:167], v0, %[rs], s70 offen offset:1024 sc1\n\t"
        "s_add_u32 s70, s48, 249856\n\t"
        "buffer_load_dwordx4 v[168:171], v0, %[rs], s70 offen offset:0 sc1\n\t"
        "buffer_load_dwordx4 v[172:175], v0, %[rs], s70 offen offset:1024 sc1\n\t"
        "s_add_u32 s70, s48, 282624\n\t"
        "buffer_load_dwordx4 v[176:179], v0, %[rs], s70 offen offset:0 sc1\n\t"
        "buffer_load_dwordx4 v[180:183], v0, %[rs], s70 offen offset:1024 sc1\n\t"
        "s_add_u32 s70, s48, 315392\n\t"
        "buffer_load_dwordx4 v[184:187], v0, %[rs], s70 offen offset:0 sc1\n\t"
        "buffer_load_dwordx4 v[188:191], v0, %[rs], s70 offen offset:1024 sc1\n\t"
        "ds_read_b64 v[16:17], v1 offset:0\n\t"
        "ds_read_b64 v[20:21], v1 offset:1024\n\t"
        "ds_read_b64 v[24:25], v1 offset:2048\n\t"
        "ds_read_b64 v[28:29], v1 offset:3072\n\t"
        "ds_read_b64 v[32:33], v1 offset:4096\n\t"
        "ds_read_b64 v[36:37], v1 offset:5120\n\t"
        "ds_read_b64 v[40:41], v1 offset:6144\n\t"
        "ds_read_b64 v[44:45], v1 offset:7168\n\t"
        "ds_read_b64 v[48:49], v1 offset:8192\n\t"
        "ds_read_b64 v[52:53], v1 offset:9216\n\t"
        "ds_read_b64 v[56:57], v1 offset:10240\n\t"
        "ds_read_b64 v[60:61], v1 offset:11264\n\t"
        "s_waitcnt vmcnt(23)\n\t"
        "s_waitcnt lgkmcnt(10)\n\t"
        "v_mfma_f32_16x16x16_bf16 v[76:79], v[16:17], v[96:97], 0\n\t"
        "ds_read_b64 v[16:17], v1 offset:12288\n\t"
        "v_mfma_f32_16x16x16_bf16 v[12:15], v[20:21], v[98:99], 0\n\t"
        "ds_read_b64 v[20:21], v1 offset:13312\n\t"
        "s_waitcnt vmcnt(22)\n\t"
        "s_waitcnt lgkmcnt(10)\n\t"
        "v_mfma_f32_16x16x16_bf16 v[76:79], v[24:25], v[100:101], v[76:79]\n\t"
        "ds_read_b64 v[24:25], v1 offset:14336\n\t"
        "v_mfma_f32_16x16x16_bf16 v[12:15], v[28:29], v[102:103], v[12:15]\n\t"
        "ds_read_b64 v[28:29], v1 offset:15360\n\t"
        "s_waitcnt vmcnt(21)\n\t"
        "s_waitcnt lgkmcnt(10)\n\t"
        "v_mfma_f32_16x16x16_bf16 v[76:79], v[32:33], v[104:105], v[76:79]\n\t"
        "ds_read_b64 v[32:33], v1 offset:16384\n\t"
        "v_mfma_f32_16x16x16_bf16 v[12:15], v[36:37], v[106:107], v[12:15]\n\t"
        "ds_read_b64 v[36:37], v1 offset:17408\n\t"
        "s_waitcnt vmcnt(20)\n\t"
        "s_waitcnt lgkmcnt(10)\n\t"
        "v_mfma_f32_16x16x16_bf16 v[76:79], v[40:41], v[108:109], v[76:79]\n\t"
        "ds_read_b64 v[40:41], v1 offset:18432\n\t"
        "v_mfma_f32_16x16x16_bf16 v[12:15], v[44:45], v[110:111], v[12:15]\n\t"
        "ds_read_b64 v[44:45], v1 offset:19456\n\t"
        "s_waitcnt vmcnt(19)\n\t"
        "s_waitcnt lgkmcnt(10)\n\t"
        "v_mfma_f32_16x16x16_bf16 v[76:79], v[48:49], v[112:113], v[76:79]\n\t"
        "ds_read_b64 v[48:49], v1 offset:20480\n\t"
        "v_mfma_f32_16x16x16_bf16 v[12:15], v[52:53], v[114:115], v[12:15]\n\t"
        "ds_read_b64 v[52:53], v1 offset:21504\n\t"
        "s_waitcnt vmcnt(18)\n\t"
        "s_waitcnt lgkmcnt(10)\n\t"
        "v_mfma_f32_16x16x16_bf16 v[76:79], v[56:57], v[116:117], v[76:79]\n\t"
        "ds_read_b64 v[56:57], v1 offset:22528\n\t"
        "v_mfma_f32_16x16x16_bf16 v[12:15], v[60:61], v[118:119], v[12:15]\n\t"
        "ds_read_b64 v[60:61], v1 offset:23552\n\t"
        "s_waitcnt vmcnt(17)\n\t"
        "s_waitcnt lgkmcnt(10)\n\t"
        "v_mfma_f32_16x16x16_bf16 v[76:79], v[16:17], v[120:121], v[76:79]\n\t"
        "ds_read_b64 v[16:17], v1 offset:24576\n\t"
        "v_mfma_f32_16x16x16_bf16 v[12:15], v[20:21], v[122:123], v[12:15]\n\t"
        "ds_read_b64 v[20:21], v1 offset:25600\n\t"
        "s_waitcnt vmcnt(16)\n\t"
        "s_waitcnt lgkmcnt(10)\n\t"
        "v_mfma_f32_16x16x16_bf16 v[76:79], v[24:25], v[124:125], v[76:79]\n\t"
        "ds_read_b64 v[24:25], v1 offset:26624\n\t"
        "v_mfma_f32_16x16x16_bf16 v[12:15], v[28:29], v[126:127], v[12:15]\n\t"
        "ds_read_b64 v[28:29], v1 offset:27648\n\t"
        "s_waitcnt vmcnt(15)\n\t"
        "s_waitcnt lgkmcnt(10)\n\t"
        "v_mfma_f32_16x16x16_bf16 v[76:79], v[32:33], v[128:129], v[76:79]\n\t"
        "ds_read_b64 v[32:33], v1 offset:28672\n\t"
        "v_mfma_f32_16x16x16_bf16 v[12:15], v[36:37], v[130:131], v[12:15]\n\t"
        "ds_read_b64 v[36:37], v1 offset:29696\n\t"
        "s_waitcnt vmcnt(14)\n\t"
        "s_waitcnt lgkmcnt(10)\n\t"
        "v_mfma_f32_16x16x16_bf16 v[76:79], v[40:41], v[132:133], v[76:79]\n\t"
        "ds_read_b64 v[40:41], v1 offset:30720\n\t"
        "v_mfma_f32_16x16x16_bf16 v[12:15], v[44:45], v[134:135], v[12:15]\n\t"
        "ds_read_b64 v[44:45], v1 offset:31744\n\t"
        "s_waitcnt vmcnt(13)\n\t"
        "s_waitcnt lgkmcnt(10)\n\t"
        "v_mfma_f32_16x16x16_bf16 v[76:79], v[48:49], v[136:137], v[76:79]\n\t"
        ".Lr_o30_%=:\n\t"
        "ds_read_b32 v4, v5\n\t"
        "s_waitcnt lgkmcnt(0)\n\t"
        "v_cmp_lt_u32 vcc, s45, v4\n\t"
        "s_cbranch_vccz .Lr_o30_%=\n\t"
        "ds_read_b128 v[48:51], v2 offset:0\n\t"
        "v_mfma_f32_16x16x16_bf16 v[12:15], v[52:53], v[138:139], v[12:15]\n\t"
        "ds_read_b128 v[52:55], v2 offset:1024\n\t"
        "s_waitcnt vmcnt(12)\n\t"
        "v_mfma_f32_16x16x16_bf16 v[76:79], v[56:57], v[140:141], v[76:79]\n\t"
        "ds_read_b128 v[56:59], v2 offset:2048\n\t"
        "v_mfma_f32_16x16x16_bf16 v[12:15], v[60:61], v[142:143], v[12:15]\n\t"
        "ds_read_b128 v[60:63], v2 offset:3072\n\t"
        "s_waitcnt vmcnt(11)\n\t"
        "v_mfma_f32_16x16x16_bf16 v[76:79], v[16:17], v[144:145], v[76:79]\n\t"
        "ds_read_b128 v[16:19], v2 offset:4096\n\t"
        "v_mfma_f32_16x16x16_bf16 v[12:15], v[20:21], v[146:147], v[12:15]\n\t"
        "ds_read_b128 v[20:23], v2 offset:5120\n\t"
        "s_waitcnt vmcnt(10)\n\t"
        "v_mfma_f32_16x16x16_bf16 v[76:79], v[24:25], v[148:149], v[76:79]\n\t"
        "ds_read_b128 v[24:27], v2 offset:6144\n\t"
        "v_mfma_f32_16x16x16_bf16 v[12:15], v[28:29], v[150:151], v[12:15]\n\t"
        "ds_read_b128 v[28:31], v2 offset:7168\n\t"
        "s_waitcnt vmcnt(9)\n\t"
        "v_mfma_f32_16x16x16_bf16 v[76:79], v[32:33], v[152:153], v[76:79]\n\t"
        "v_mfma_f32_16x16x16_bf16 v[12:15], v[36:37], v[154:155], v[12:15]\n\t"
        "s_waitcnt vmcnt(8)\n\t"
        "v_mfma_f32_16x16x16_bf16 v[76:79], v[40:41], v[156:157], v[76:79]\n\t"
        "v_mfma_f32_16x16x16_bf16 v[12:15], v[44:45], v[158:159], v[12:15]\n\t"
        "s_waitcnt vmcnt(7)\n\t"
        "s_waitcnt lgkmcnt(6)\n\t"
        "v_mfma_f32_16x16x32_bf16 v[72:75], v[48:51], v[160:163], 0\n\t"
        "s_waitcnt vmcnt(6)\n\t"
        "v_mfma_f32_16x16x32_bf16 v[8:11], v[52:55], v[164:167], 0\n\t"
        "s_waitcnt vmcnt(5)\n\t"
        "s_waitcnt lgkmcnt(4)\n\t"
        "v_mfma_f32_16x16x32_bf16 v[72:75], v[56:59], v[168:171], v[72:75]\n\t"
        "s_waitcnt vmcnt(4)\n\t"
        "v_mfma_f32_16x16x32_bf16 v[8:11], v[60:63], v[172:175], v[8:11]\n\t"
        "s_waitcnt vmcnt(3)\n\t"
        "s_waitcnt lgkmcnt(2)\n\t"
        "v_mfma_f32_16x16x32_bf16 v[72:75], v[16:19], v[176:179], v[72:75]\n\t"
        "s_waitcnt vmcnt(2)\n\t"
        "v_mfma_f32_16x16x32_bf16 v[8:11], v[20:23], v[180:183], v[8:11]\n\t"
        "s_waitcnt vmcnt(1)\n\t"
        "s_waitcnt lgkmcnt(0)\n\t"
        "v_mfma_f32_16x16x32_bf16 v[72:75], v[24:27], v[184:187], v[72:75]\n\t"
        "s_waitcnt vmcnt(0)\n\t"
        "v_mfma_f32_16x16x32_bf16 v[8:11], v[28:31], v[188:191], v[8:11]\n\t"
        "s_nop 7\n\t"
        "v_add_f32 v8, v8, v72\n\t"
        "v_add_f32 v9, v9, v73\n\t"
        "v_add_f32 v10, v10, v74\n\t"
        "v_add_f32 v11, v11, v75\n\t"
        "v_add_f32 v12, v12, v76\n\t"
        "v_add_f32 v13, v13, v77\n\t"
        "v_add_f32 v14, v14, v78\n\t"
        "v_add_f32 v15, v15, v79\n\t"
        "v_add_f32 v8, v8, v12\n\t"
        "v_add_f32 v9, v9, v13\n\t"
        "v_add_f32 v10, v10, v14\n\t"
        "v_add_f32 v11, v11, v15\n\t"
        ".Lr_o20_%=:\n\t"
        "v_cmp_u_f32 vcc, v8, v9\n\t"
        "v_cvt_pk_bf16_f32 v4, v8, v9\n\t"
        "v_cvt_pk_bf16_f32 v5, v10, v11\n\t"
        "s_cbranch_vccnz .Lr_of0_%=\n\t"
        ".Lr_ok0_%=:\n\t"
        "buffer_store_dwordx2 v[4:5], v3, %[ro], s52 offen\n\t"
        "s_add_u32 s52, s52, 131072\n\t"
        "s_add_u32 s44, s44, 32768\n\t"
        "s_cmp_lt_u32 s44, 98304\n\t"
        "s_cselect_b32 s44, s44, 0\n\t"
        "s_add_u32 s48, s48, 2097152\n\t"
        "s_and_b32 s48, s48, 0xffffff\n\t"
        "s_xor_b32 s48, s48, 0x2000\n\t"
        "s_add_u32 s49, s49, 2097152\n\t"
        "s_and_b32 s49, s49, 0xffffff\n\t"
        "s_xor_b32 s49, s49, 0x2000\n\t"
        "s_add_u32 s45, s45, 1\n\t"
        "s_cmp_lt_u32 s45, 64\n\t"
        "s_cbranch_scc1 .Lr_oloop0_%=\n\t"
        "s_branch .Lr_end_%=\n\t"
        ".Lr_ol0_%=:\n\t"
        "s_branch .Lr_om0_%=\n\t"
        ".Lr_of0_%=:\n\t"
        "v_max_u32 v6, v96, v97\n\t"
        "v_max3_u32 v6, v6, v98, v99\n\t"
        "v_max3_u32 v6, v6, v100, v101\n\t"
        "v_max3_u32 v6, v6, v102, v103\n\t"
        "v_max3_u32 v6, v6, v104, v105\n\t"
        "v_max3_u32 v6, v6, v106, v107\n\t"
        "v_max3_u32 v6, v6, v108, v109\n\t"
        "v_max3_u32 v6, v6, v110, v111\n\t"
        "v_max3_u32 v6, v6, v112, v113\n\t"
        "v_max3_u32 v6, v6, v114, v115\n\t"
        "v_max3_u32 v6, v6, v116, v117\n\t"
        "v_max3_u32 v6, v6, v118, v119\n\t"
        "v_max3_u32 v6, v6, v120, v121\n\t"
        "v_max3_u32 v6, v6, v122, v123\n\t"
        "v_max3_u32 v6, v6, v124, v125\n\t"
        "v_max3_u32 v6, v6, v126, v127\n\t"
        "v_max3_u32 v6, v6, v128, v129\n\t"
        "v_max3_u32 v6, v6, v130, v131\n\t"
        "v_max3_u32 v6, v6, v132, v133\n\t"
        "v_max3_u32 v6, v6, v134, v135\n\t"
        "v_max3_u32 v6, v6, v136, v137\n\t"
        "v_max3_u32 v6, v6, v138, v139\n\t"
        "v_max3_u32 v6, v6, v140, v141\n\t"
        "v_max3_u32 v6, v6, v142, v143\n\t"
        "v_max3_u32 v6, v6, v144, v145\n\t"
        "v_max3_u32 v6, v6, v146, v147\n\t"
        "v_max3_u32 v6, v6, v148, v149\n\t"
        "v_max3_u32 v6, v6, v150, v151\n\t"
        "v_max3_u32 v6, v6, v152, v153\n\t"
        "v_max3_u32 v6, v6, v154, v155\n\t"
        "v_max3_u32 v6, v6, v156, v157\n\t"
        "v_max3_u32 v6, v6, v158, v159\n\t"
        "v_max3_u32 v6, v6, v160, v161\n\t"
        "v_max3_u32 v6, v6, v162, v163\n\t"
        "v_max3_u32 v6, v6, v164, v165\n\t"
        "v_max3_u32 v6, v6, v166, v167\n\t"
        "v_max3_u32 v6, v6, v168, v169\n\t"
        "v_max3_u32 v6, v6, v170, v171\n\t"
        "v_max3_u32 v6, v6, v172, v173\n\t"
        "v_max3_u32 v6, v6, v174, v175\n\t"
        "v_max3_u32 v6, v6, v176, v177\n\t"
        "v_max3_u32 v6, v6, v178, v179\n\t"
        "v_max3_u32 v6, v6, v180, v181\n\t"
        "v_max3_u32 v6, v6, v182, v183\n\t"
        "v_max3_u32 v6, v6, v184, v185\n\t"
        "v_max3_u32 v6, v6, v186, v187\n\t"
        "v_max3_u32 v6, v6, v188, v189\n\t"
        "v_max3_u32 v6, v6, v190, v191\n\t"
        "v_cmp_eq_u32 vcc, s66, v6\n\t"
        "s_cbranch_vccz .Lr_ok0_%=\n\t"
        "s_add_u32 s79, s79, 1\n\t"
        "s_sub_u32 s46, s46, 1\n\t"
        "s_cbranch_scc1 .Lr_og0_%=\n\t"
        "s_add_u32 s70, s48, 212992\n\t"
        "buffer_load_dwordx4 v[96:99], v0, %[rs], s70 offen offset:0 sc1\n\t"
        "buffer_load_dwordx4 v[100:103], v0, %[rs], s70 offen offset:1024 sc1\n\t"
        "buffer_load_dwordx4 v[104:107], v0, %[rs], s70 offen offset:2048 sc1\n\t"
        "buffer_load_dwordx4 v[108:111], v0, %[rs], s70 offen offset:3072 sc1\n\t"
        "s_add_u32 s70, s48, 245760\n\t"
        "buffer_load_dwordx4 v[112:115], v0, %[rs], s70 offen offset:0 sc1\n\t"
        "buffer_load_dwordx4 v[116:119], v0, %[rs], s70 offen offset:1024 sc1\n\t"
        "buffer_load_dwordx4 v[120:123], v0, %[rs], s70 offen offset:2048 sc1\n\t"
        "buffer_load_dwordx4 v[124:127], v0, %[rs], s70 offen offset:3072 sc1\n\t"
        "s_add_u32 s70, s48, 278528\n\t"
        "buffer_load_dwordx4 v[128:131], v0, %[rs], s70 offen offset:0 sc1\n\t"
        "buffer_load_dwordx4 v[132:135], v0, %[rs], s70 offen offset:1024 sc1\n\t"
        "buffer_load_dwordx4 v[136:139], v0, %[rs], s70 offen offset:2048 sc1\n\t"
        "buffer_load_dwordx4 v[140:143], v0, %[rs], s70 offen offset:3072 sc1\n\t"
        "s_add_u32 s70, s48, 311296\n\t"
        "buffer_load_dwordx4 v[144:147], v0, %[rs], s70 offen offset:0 sc1\n\t"
        "buffer_load_dwordx4 v[148:151], v0, %[rs], s70 offen offset:1024 sc1\n\t"
        "buffer_load_dwordx4 v[152:155], v0, %[rs], s70 offen offset:2048 sc1\n\t"
        "buffer_load_dwordx4 v[156:159], v0, %[rs], s70 offen offset:3072 sc1\n\t"
        "s_add_u32 s70, s48, 217088\n\t"
        "buffer_load_dwordx4 v[160:163], v0, %[rs], s70 offen offset:0 sc1\n\t"
        "buffer_load_dwordx4 v[164:167], v0, %[rs], s70 offen offset:1024 sc1\n\t"
        "s_add_u32 s70, s48, 249856\n\t"
        "buffer_load_dwordx4 v[168:171], v0, %[rs], s70 offen offset:0 sc1\n\t"
        "buffer_load_dwordx4 v[172:175], v0, %[rs], s70 offen offset:1024 sc1\n\t"
        "s_add_u32 s70, s48, 282624\n\t"
        "buffer_load_dwordx4 v[176:179], v0, %[rs], s70 offen offset:0 sc1\n\t"
        "buffer_load_dwordx4 v[180:183], v0, %[rs], s70 offen offset:1024 sc1\n\t"
        "s_add_u32 s70, s48, 315392\n\t"
        "buffer_load_dwordx4 v[184:187], v0, %[rs], s70 offen offset:0 sc1\n\t"
        "buffer_load_dwordx4 v[188:191], v0, %[rs], s70 offen offset:1024 sc1\n\t"
        "ds_read_b64 v[16:17], v1 offset:0\n\t"
        "ds_read_b64 v[20:21], v1 offset:1024\n\t"
        "ds_read_b64 v[24:25], v1 offset:2048\n\t"
        "ds_read_b64 v[28:29], v1 offset:3072\n\t"
        "ds_read_b64 v[32:33], v1 offset:4096\n\t"
        "ds_read_b64 v[36:37], v1 offset:5120\n\t"
        "ds_read_b64 v[40:41], v1 offset:6144\n\t"
        "ds_read_b64 v[44:45], v1 offset:7168\n\t"
        "ds_read_b64 v[48:49], v1 offset:8192\n\t"
        "ds_read_b64 v[52:53], v1 offset:9216\n\t"
        "ds_read_b64 v[56:57], v1 offset:10240\n\t"
        "ds_read_b64 v[60:61], v1 offset:11264\n\t"
        "s_waitcnt vmcnt(23)\n\t"
        "s_waitcnt lgkmcnt(10)\n\t"
        "v_mfma_f32_16x16x16_bf16 v[76:79], v[16:17], v[96:97], 0\n\t"
        "ds_read_b64 v[16:17], v1 offset:12288\n\t"
        "v_mfma_f32_16x16x16_bf16 v[12:15], v[20:21], v[98:99], 0\n\t"
        "ds_read_b64 v[20:21], v1 offset:13312\n\t"
        "s_waitcnt vmcnt(22)\n\t"
        "s_waitcnt lgkmcnt(10)\n\t"
        "v_mfma_f32_16x16x16_bf16 v[76:79], v[24:25], v[100:101], v[76:79]\n\t"
        "ds_read_b64 v[24:25], v1 offset:14336\n\t"
        "v_mfma_f32_16x16x16_bf16 v[12:15], v[28:29], v[102:103], v[12:15]\n\t"
        "ds_read_b64 v[28:29], v1 offset:15360\n\t"
        "s_waitcnt vmcnt(21)\n\t"
        "s_waitcnt lgkmcnt(10)\n\t"
        "v_mfma_f32_16x16x16_bf16 v[76:79], v[32:33], v[104:105], v[76:79]\n\t"
        "ds_read_b64 v[32:33], v1 offset:16384\n\t"
        "v_mfma_f32_16x16x16_bf16 v[12:15], v[36:37], v[106:107], v[12:15]\n\t"
        "ds_read_b64 v[36:37], v1 offset:17408\n\t"
        "s_waitcnt vmcnt(20)\n\t"
        "s_waitcnt lgkmcnt(10)\n\t"
        "v_mfma_f32_16x16x16_bf16 v[76:79], v[40:41], v[108:109], v[76:79]\n\t"
        "ds_read_b64 v[40:41], v1 offset:18432\n\t"
        "v_mfma_f32_16x16x16_bf16 v[12:15], v[44:45], v[110:111], v[12:15]\n\t"
        "ds_read_b64 v[44:45], v1 offset:19456\n\t"
        "s_waitcnt vmcnt(19)\n\t"
        "s_waitcnt lgkmcnt(10)\n\t"
        "v_mfma_f32_16x16x16_bf16 v[76:79], v[48:49], v[112:113], v[76:79]\n\t"
        "ds_read_b64 v[48:49], v1 offset:20480\n\t"
        "v_mfma_f32_16x16x16_bf16 v[12:15], v[52:53], v[114:115], v[12:15]\n\t"
        "ds_read_b64 v[52:53], v1 offset:21504\n\t"
        "s_waitcnt vmcnt(18)\n\t"
        "s_waitcnt lgkmcnt(10)\n\t"
        "v_mfma_f32_16x16x16_bf16 v[76:79], v[56:57], v[116:117], v[76:79]\n\t"
        "ds_read_b64 v[56:57], v1 offset:22528\n\t"
        "v_mfma_f32_16x16x16_bf16 v[12:15], v[60:61], v[118:119], v[12:15]\n\t"
        "ds_read_b64 v[60:61], v1 offset:23552\n\t"
        "s_waitcnt vmcnt(17)\n\t"
        "s_waitcnt lgkmcnt(10)\n\t"
        "v_mfma_f32_16x16x16_bf16 v[76:79], v[16:17], v[120:121], v[76:79]\n\t"
        "ds_read_b64 v[16:17], v1 offset:24576\n\t"
        "v_mfma_f32_16x16x16_bf16 v[12:15], v[20:21], v[122:123], v[12:15]\n\t"
        "ds_read_b64 v[20:21], v1 offset:25600\n\t"
        "s_waitcnt vmcnt(16)\n\t"
        "s_waitcnt lgkmcnt(10)\n\t"
        "v_mfma_f32_16x16x16_bf16 v[76:79], v[24:25], v[124:125], v[76:79]\n\t"
        "ds_read_b64 v[24:25], v1 offset:26624\n\t"
        "v_mfma_f32_16x16x16_bf16 v[12:15], v[28:29], v[126:127], v[12:15]\n\t"
        "ds_read_b64 v[28:29], v1 offset:27648\n\t"
        "s_waitcnt vmcnt(15)\n\t"
        "s_waitcnt lgkmcnt(10)\n\t"
        "v_mfma_f32_16x16x16_bf16 v[76:79], v[32:33], v[128:129], v[76:79]\n\t"
        "ds_read_b64 v[32:33], v1 offset:28672\n\t"
        "v_mfma_f32_16x16x16_bf16 v[12:15], v[36:37], v[130:131], v[12:15]\n\t"
        "ds_read_b64 v[36:37], v1 offset:29696\n\t"
        "s_waitcnt vmcnt(14)\n\t"
        "s_waitcnt lgkmcnt(10)\n\t"
        "v_mfma_f32_16x16x16_bf16 v[76:79], v[40:41], v[132:133], v[76:79]\n\t"
        "ds_read_b64 v[40:41], v1 offset:30720\n\t"
        "v_mfma_f32_16x16x16_bf16 v[12:15], v[44:45], v[134:135], v[12:15]\n\t"
        "ds_read_b64 v[44:45], v1 offset:31744\n\t"
        "s_waitcnt vmcnt(13)\n\t"
        "s_waitcnt lgkmcnt(10)\n\t"
        "v_mfma_f32_16x16x16_bf16 v[76:79], v[48:49], v[136:137], v[76:79]\n\t"
        "ds_read_b128 v[48:51], v2 offset:0\n\t"
        "v_mfma_f32_16x16x16_bf16 v[12:15], v[52:53], v[138:139], v[12:15]\n\t"
        "ds_read_b128 v[52:55], v2 offset:1024\n\t"
        "s_waitcnt vmcnt(12)\n\t"
        "s_waitcnt lgkmcnt(10)\n\t"
        "v_mfma_f32_16x16x16_bf16 v[76:79], v[56:57], v[140:141], v[76:79]\n\t"
        "ds_read_b128 v[56:59], v2 offset:2048\n\t"
        "v_mfma_f32_16x16x16_bf16 v[12:15], v[60:61], v[142:143], v[12:15]\n\t"
        "ds_read_b128 v[60:63], v2 offset:3072\n\t"
        "s_waitcnt vmcnt(11)\n\t"
        "s_waitcnt lgkmcnt(10)\n\t"
        "v_mfma_f32_16x16x16_bf16 v[76:79], v[16:17], v[144:145], v[76:79]\n\t"
        "ds_read_b128 v[16:19], v2 offset:4096\n\t"
        "v_mfma_f32_16x16x16_bf16 v[12:15], v[20:21], v[146:147], v[12:15]\n\t"
        "ds_read_b128 v[20:23], v2 offset:5120\n\t"
        "s_waitcnt vmcnt(10)\n\t"
        "s_waitcnt lgkmcnt(10)\n\t"
        "v_mfma_f32_16x16x16_bf16 v[76:79], v[24:25], v[148:149], v[76:79]\n\t"
        "ds_read_b128 v[24:27], v2 offset:6144\n\t"
        "v_mfma_f32_16x16x16_bf16 v[12:15], v[28:29], v[150:151], v[12:15]\n\t"
        "ds_read_b128 v[28:31], v2 offset:7168\n\t"
        "s_waitcnt vmcnt(9)\n\t"
        "s_waitcnt lgkmcnt(10)\n\t"
        "v_mfma_f32_16x16x16_bf16 v[76:79], v[32:33], v[152:153], v[76:79]\n\t"
        "v_mfma_f32_16x16x16_bf16 v[12:15], v[36:37], v[154:155], v[12:15]\n\t"
        "s_waitcnt vmcnt(8)\n\t"
        "s_waitcnt lgkmcnt(8)\n\t"
        "v_mfma_f32_16x16x16_bf16 v[76:79], v[40:41], v[156:157], v[76:79]\n\t"
        "v_mfma_f32_16x16x16_bf16 v[12:15], v[44:45], v[158:159], v[12:15]\n\t"
        "s_waitcnt vmcnt(7)\n\t"
        "s_waitcnt lgkmcnt(6)\n\t"
        "v_mfma_f32_16x16x32_bf16 v[72:75], v[48:51], v[160:163], 0\n\t"
        "s_waitcnt vmcnt(6)\n\t"
        "v_mfma_f32_16x16x32_bf16 v[8:11], v[52:55], v[164:167], 0\n\t"
        "s_waitcnt vmcnt(5)\n\t"
        "s_waitcnt lgkmcnt(4)\n\t"
        "v_mfma_f32_16x16x32_bf16 v[72:75], v[56:59], v[168:171], v[72:75]\n\t"
        "s_waitcnt vmcnt(4)\n\t"
        "v_mfma_f32_16x16x32_bf16 v[8:11], v[60:63], v[172:175], v[8:11]\n\t"
        "s_waitcnt vmcnt(3)\n\t"
        "s_waitcnt lgkmcnt(2)\n\t"
        "v_mfma_f32_16x16x32_bf16 v[72:75], v[16:19], v[176:179], v[72:75]\n\t"
        "s_waitcnt vmcnt(2)\n\t"
        "v_mfma_f32_16x16x32_bf16 v[8:11], v[20:23], v[180:183], v[8:11]\n\t"
        "s_waitcnt vmcnt(1)\n\t"
        "s_waitcnt lgkmcnt(0)\n\t"
        "v_mfma_f32_16x16x32_bf16 v[72:75], v[24:27], v[184:187], v[72:75]\n\t"
        "s_waitcnt vmcnt(0)\n\t"
        "v_mfma_f32_16x16x32_bf16 v[8:11], v[28:31], v[188:191], v[8:11]\n\t"
        "s_nop 7\n\t"
        "v_add_f32 v8, v8, v72\n\t"
        "v_add_f32 v9, v9, v73\n\t"
        "v_add_f32 v10, v10, v74\n\t"
        "v_add_f32 v11, v11, v75\n\t"
        "v_add_f32 v12, v12, v76\n\t"
        "v_add_f32 v13, v13, v77\n\t"
        "v_add_f32 v14, v14, v78\n\t"
        "v_add_f32 v15, v15, v79\n\t"
        "v_add_f32 v8, v8, v12\n\t"
        "v_add_f32 v9, v9, v13\n\t"
        "v_add_f32 v10, v10, v14\n\t"
        "v_add_f32 v11, v11, v15\n\t"
        "s_branch .Lr_o20_%=\n\t"
        ".Lr_og0_%=:\n\t"
        "s_mov_b32 s46, 0\n\t"
        "s_branch .Lr_ok0_%=\n\t"
        ".Lr_twx_%=:\n\t"
        "s_add_u32 s79, s79, 0x1000000\n\t"
        "s_sleep 4\n\t"
        "buffer_load_dword v18, v16, %[rs], s67 offen sc1\n\t"
        "s_waitcnt vmcnt(0)\n\t"
        "s_sub_u32 s46, s46, 1\n\t"
        "s_cbranch_scc0 .Lr_twm_%=\n\t"
        "s_mov_b32 s46, 0\n\t"
        "s_branch .Lr_twy_%=\n\t"
        ".Lr_tlx_%=:\n\t"
        "s_add_u32 s79, s79, 0x1000000\n\t"
        "s_sleep 4\n\t"
        "buffer_load_dword v18, v16, %[rs], s67 offen sc1\n\t"
        "s_waitcnt vmcnt(0)\n\t"
        "s_sub_u32 s46, s46, 1\n\t"
        "s_cbranch_scc0 .Lr_tlm_%=\n\t"
        "s_mov_b32 s46, 0\n\t"
        "s_branch .Lr_tly_%=\n\t"
        ".Lr_pf_%=:\n\t"
        "v_max_u32 v4, v224, v225\n\t"
        "v_max3_u32 v4, v4, v226, v227\n\t"
        "v_max3_u32 v4, v4, v228, v229\n\t"
        "v_max3_u32 v4, v4, v230, v231\n\t"
        "v_max3_u32 v4, v4, v232, v233\n\t"
        "v_max3_u32 v4, v4, v234, v235\n\t"
        "v_max3_u32 v4, v4, v236, v237\n\t"
        "v_max3_u32 v4, v4, v238, v239\n\t"
        "v_max3_u32 v4, v4, v240, v241\n\t"
        "v_max3_u32 v4, v4, v242, v243\n\t"
        "v_max3_u32 v4, v4, v244, v245\n\t"
        "v_max3_u32 v4, v4, v246, v247\n\t"
        "v_max3_u32 v4, v4, v248, v249\n\t"
        "v_max3_u32 v4, v4, v250, v251\n\t"
        "v_max3_u32 v4, v4, v252, v253\n\t"
        "v_max3_u32 v4, v4, v254, v255\n\t"
        "v_max3_u32 v4, v4, v16, v17\n\t"
        "v_max3_u32 v4, v4, v18, v19\n\t"
        "v_max3_u32 v4, v4, v20, v21\n\t"
        "v_max3_u32 v4, v4, v22, v23\n\t"
        "v_max3_u32 v4, v4, v24, v25\n\t"
        "v_max3_u32 v4, v4, v26, v27\n\t"
        "v_max3_u32 v4, v4, v28, v29\n\t"
        "v_max3_u32 v4, v4, v30, v31\n\t"
        "v_max3_u32 v4, v4, v32, v33\n\t"
        "v_max3_u32 v4, v4, v34, v35\n\t"
        "v_max3_u32 v4, v4, v36, v37\n\t"
        "v_max3_u32 v4, v4, v38, v39\n\t"
        "v_max3_u32 v4, v4, v40, v41\n\t"
        "v_max3_u32 v4, v4, v42, v43\n\t"
        "v_max3_u32 v4, v4, v44, v45\n\t"
        "v_max3_u32 v4, v4, v46, v47\n\t"
        "v_max3_u32 v4, v4, v48, v49\n\t"
        "v_max3_u32 v4, v4, v50, v51\n\t"
        "v_max3_u32 v4, v4, v52, v53\n\t"
        "v_max3_u32 v4, v4, v54, v55\n\t"
        "v_max3_u32 v4, v4, v56, v57\n\t"
        "v_max3_u32 v4, v4, v58, v59\n\t"
        "v_max3_u32 v4, v4, v60, v61\n\t"
        "v_max3_u32 v4, v4, v62, v63\n\t"
        "v_max3_u32 v4, v4, v64, v65\n\t"
        "v_max3_u32 v4, v4, v66, v67\n\t"
        "v_max3_u32 v4, v4, v68, v69\n\t"
        "v_max3_u32 v4, v4, v70, v71\n\t"
        "v_max3_u32 v4, v4, v72, v73\n\t"
        "v_max3_u32 v4, v4, v74, v75\n\t"
        "v_max3_u32 v4, v4, v76, v77\n\t"
        "v_max3_u32 v4, v4, v78, v79\n\t"
        "v_max3_u32 v4, v4, v80, v81\n\t"
        "v_max3_u32 v4, v4, v82, v83\n\t"
        "v_max3_u32 v4, v4, v84, v85\n\t"
        "v_max3_u32 v4, v4, v86, v87\n\t"
        "v_max3_u32 v4, v4, v88, v89\n\t"
        "v_max3_u32 v4, v4, v90, v91\n\t"
        "v_max3_u32 v4, v4, v92, v93\n\t"
        "v_max3_u32 v4, v4, v94, v95\n\t"
        "v_cmp_eq_u32 s[68:69], s66, v4\n\t"
        "s_nop 3\n\t"
        "s_cmp_lg_u64 s[68:69], 0\n\t"
        "s_cbranch_scc0 .Lr_pk_%=\n\t"
        "v_mov_b64 v[8:9], 0\n\t"
        "v_mov_b64 v[10:11], 0\n\t"
        "s_mov_b64 vcc, s[64:65]\n\t"
        "s_sub_u32 s46, s46, 1\n\t"
        "s_cbranch_scc0 .Lr_pp_%=\n\t"
        "s_mov_b32 s46, 0\n\t"
        "v_mov_b32 v5, 0\n\t"
        "s_add_u32 s68, s45, 2100\n\t"
        "v_mov_b32 v6, s68\n\t"
        "global_store_dword v5, v6, %[tm] sc1\n\t"
        "s_branch .Lr_pk_%=\n\t"
        ".Lr_st_%=:\n\t"
        "v_mov_b32 v5, 114704\n\t"
        "v_add_u32 v12, s44, v0\n\t"
        ".Lr_fl20_%=:\n\t"
        "ds_read_b32 v13, v5 offset:0\n\t"
        "s_waitcnt lgkmcnt(0)\n\t"
        "v_cmp_lt_u32 s[68:69], s45, v13\n\t"
        "s_nop 3\n\t"
        "s_cmp_lg_u64 s[68:69], 0\n\t"
        "s_cbranch_scc0 .Lr_fl20_%=\n\t"
        "ds_read_b128 v[96:99], v12 offset:0\n\t"
        "ds_read_b128 v[100:103], v12 offset:1024\n\t"
        "ds_read_b128 v[104:107], v12 offset:2048\n\t"
        "ds_read_b128 v[108:111], v12 offset:3072\n\t"
        "ds_read_b128 v[112:115], v12 offset:4096\n\t"
        "ds_read_b128 v[116:119], v12 offset:5120\n\t"
        "ds_read_b128 v[120:123], v12 offset:6144\n\t"
        "ds_read_b128 v[124:127], v12 offset:7168\n\t"
        ".Lr_fl21_%=:\n\t"
        "ds_read_b32 v13, v5 offset:4\n\t"
        "s_waitcnt lgkmcnt(0)\n\t"
        "v_cmp_lt_u32 s[68:69], s45, v13\n\t"
        "s_nop 3\n\t"
        "s_cmp_lg_u64 s[68:69], 0\n\t"
        "s_cbranch_scc0 .Lr_fl21_%=\n\t"
        "ds_read_b128 v[128:131], v12 offset:8192\n\t"
        "ds_read_b128 v[132:135], v12 offset:9216\n\t"
        "ds_read_b128 v[136:139], v12 offset:10240\n\t"
        "ds_read_b128 v[140:143], v12 offset:11264\n\t"
        "ds_read_b128 v[144:147], v12 offset:12288\n\t"
        "ds_read_b128 v[148:151], v12 offset:13312\n\t"
        "ds_read_b128 v[152:155], v12 offset:14336\n\t"
        "ds_read_b128 v[156:159], v12 offset:15360\n\t"
        ".Lr_fl22_%=:\n\t"
        "ds_read_b32 v13, v5 offset:8\n\t"
        "s_waitcnt lgkmcnt(0)\n\t"
        "v_cmp_lt_u32 s[68:69], s45, v13\n\t"
        "s_nop 3\n\t"
        "s_cmp_lg_u64 s[68:69], 0\n\t"
        "s_cbranch_scc0 .Lr_fl22_%=\n\t"
        "ds_read_b128 v[160:163], v12 offset:16384\n\t"
        "ds_read_b128 v[164:167], v12 offset:17408\n\t"
        "ds_read_b128 v[168:171], v12 offset:18432\n\t"
        "ds_read_b128 v[172:175], v12 offset:19456\n\t"
        "ds_read_b128 v[176:179], v12 offset:20480\n\t"
        "ds_read_b128 v[180:183], v12 offset:21504\n\t"
        "ds_read_b128 v[184:187], v12 offset:22528\n\t"
        "ds_read_b128 v[188:191], v12 offset:23552\n\t"
        ".Lr_fl23_%=:\n\t"
        "ds_read_b32 v13, v5 offset:12\n\t"
        "s_waitcnt lgkmcnt(0)\n\t"
        "v_cmp_lt_u32 s[68:69], s45, v13\n\t"
        "s_nop 3\n\t"
        "s_cmp_lg_u64 s[68:69], 0\n\t"
        "s_cbranch_scc0 .Lr_fl23_%=\n\t"
        "ds_read_b128 v[192:195], v12 offset:24576\n\t"
        "ds_read_b128 v[196:199], v12 offset:25600\n\t"
        "ds_read_b128 v[200:203], v12 offset:26624\n\t"
        "ds_read_b128 v[204:207], v12 offset:27648\n\t"
        "ds_read_b128 v[208:211], v12 offset:28672\n\t"
        "ds_read_b128 v[212:215], v12 offset:29696\n\t"
        "ds_read_b128 v[216:219], v12 offset:30720\n\t"
        "ds_read_b128 v[220:223], v12 offset:31744\n\t"
        "s_waitcnt lgkmcnt(0)\n\t"
        "s_xor_b32 s67, s49, %[tw]\n\t"
        "v_and_b32 v16, 0x1f0, v0\n\t"
        "v_lshlrev_b32 v16, 1, v16\n\t"
        "buffer_load_dword v18, v16, %[rs], s67 offen sc1\n\t"
        "s_branch .Lr_s1_%=\n\t"
        ".Lr_an0f_%=:\n\t"
        "s_add_u32 s79, s79, 1\n\t"
        "s_sleep 4\n\t"
        "s_sub_u32 s46, s46, 1\n\t"
        "s_cbranch_scc0 .Lr_an0_%=\n\t"
        "s_mov_b32 s46, 0\n\t"
        "s_branch .Lr_an0k_%=\n\t"
        ".Lr_an1f_%=:\n\t"
        "s_add_u32 s79, s79, 1\n\t"
        "s_sleep 4\n\t"
        "s_sub_u32 s46, s46, 1\n\t"
        "s_cbranch_scc0 .Lr_an1_%=\n\t"
        "s_mov_b32 s46, 0\n\t"
        "s_branch .Lr_an1k_%=\n\t"
        ".Lr_an2f_%=:\n\t"
        "s_add_u32 s79, s79, 1\n\t"
        "s_sleep 4\n\t"
        "s_sub_u32 s46, s46, 1\n\t"
        "s_cbranch_scc0 .Lr_an2_%=\n\t"
        "s_mov_b32 s46, 0\n\t"
        "s_branch .Lr_an2k_%=\n\t"
        ".Lcode_r1:\n\t"
        ".Lr_end_%=:\n\t"
        "s_or_b32 s70, %[oo], %[w]\n\t"
        "s_cmp_eq_u32 s70, 0\n\t"
        "s_cbranch_scc0 .Lr_fin_%=\n\t"
        "s_load_dwordx2 s[54:55], %[ep], 0\n\t"
        "v_mov_b32 v4, 0\n\t"
        "s_waitcnt lgkmcnt(0)\n\t"
        "s_add_u32 s54, s54, 1\n\t"
        "s_addc_u32 s55, s55, 0\n\t"
        "v_mov_b32 v6, s54\n\t"
        "v_mov_b32 v7, s55\n\t"
        "s_mov_b64 exec, 1\n\t"
        "global_store_dwordx2 v4, v[6:7], %[ep] sc1\n\t"
        ".Lr_fin_%=:\n\t"
        "s_endpgm\n\t"
        :: [rs] "s"(RSRC(arena(a.slots), ARENA_BYTES)), [ro] "s"(RSRC(a.out, S_LEN * OUTW * 2)), [tm] "s"(a.sync + 40 * 32), [ep] "s"(a.boot + 144), [w] "s"(w), [cr] "s"(cls_r(j)), [t0] "s"(t0), [om] "s"((64 + j) * 16384),
           [oy] "s"(YX_OFF + 4 * j * 16384 + (cls_r(j) ^ (j >> 5) ^ YXC) * 8192), [oa] "s"((j >> 5) * 16384 + (j & 31) * 256), [oo] "s"(j * 32), [tw] "s"(TW(cls_r(j))),
           [al] "s"((unsigned)(uintptr_t)arena(a.slots)), [ah] "s"((unsigned)((uintptr_t)arena(a.slots) >> 32) & 0xffffu) STIN(w == 0 ? 96 + j : w == 1 ? 160 + (j & 31) : w == 3 ? 288 + (j & 31) : 251)
        : "memory", "vcc", "scc", ALLV, ALLS);
    __builtin_unreachable();
}

struct cpx { float x, y; };
__device__ __forceinline__ cpx cmul(cpx a, cpx b) { return {a.x * b.x - a.y * b.y, a.x * b.y + a.y * b.x}; }
__device__ __forceinline__ cpx cadd(cpx a, cpx b) { return {a.x + b.x, a.y + b.y}; }
__device__ __forceinline__ cpx csub(cpx a, cpx b) { return {a.x - b.x, a.y - b.y}; }
template <int DIR> __device__ __forceinline__ cpx mul_i(cpx a) {
    return (DIR < 0) ? cpx{a.y, -a.x} : cpx{-a.y, a.x};
}
template <int DIR> __device__ __forceinline__ void r4(cpx& a, cpx& b, cpx& c, cpx& d) {
    cpx t0 = cadd(a, c), t1 = csub(a, c), t2 = cadd(b, d), t3 = mul_i<DIR>(csub(b, d));
    a = cadd(t0, t2); b = cadd(t1, t3); c = csub(t0, t2); d = csub(t1, t3);
}
template <int DIR> __device__ __forceinline__ cpx twc(float c, float s) { return cpx{c, (float)DIR * s}; }
template <int DIR> __device__ __forceinline__ void dft16(cpx (&v)[16]) {
#pragma unroll
    for (int a = 0; a < 4; ++a) r4<DIR>(v[a], v[a + 4], v[a + 8], v[a + 12]);
    const float C1 = 0.92387953251128674f, S1 = 0.38268343236508977f, R2 = 0.70710678118654752f;
    v[1 + 4] = cmul(v[1 + 4], twc<DIR>(C1, S1));
    v[1 + 8] = cmul(v[1 + 8], twc<DIR>(R2, R2));
    v[1 + 12] = cmul(v[1 + 12], twc<DIR>(S1, C1));
    v[2 + 4] = cmul(v[2 + 4], twc<DIR>(R2, R2));
    v[2 + 8] = mul_i<DIR>(v[2 + 8]);
    v[2 + 12] = cmul(v[2 + 12], twc<DIR>(-R2, R2));
    v[3 + 4] = cmul(v[3 + 4], twc<DIR>(S1, C1));
    v[3 + 8] = cmul(v[3 + 8], twc<DIR>(-R2, R2));
    v[3 + 12] = cmul(v[3 + 12], twc<DIR>(-C1, -S1));
#pragma unroll
    for (int d = 0; d < 4; ++d) r4<DIR>(v[4 * d], v[4 * d + 1], v[4 * d + 2], v[4 * d + 3]);
}
__device__ __forceinline__ constexpr int x16(int q) { return 4 * (q & 3) + (q >> 2); }
template <int DIR> __device__ __forceinline__ void dft8(cpx (&v)[8]) {
    r4<DIR>(v[0], v[2], v[4], v[6]);
    r4<DIR>(v[1], v[3], v[5], v[7]);
    const float R2 = 0.70710678118654752f;
    v[3] = cmul(v[3], twc<DIR>(R2, R2));
    v[5] = mul_i<DIR>(v[5]);
    v[7] = cmul(v[7], twc<DIR>(-R2, R2));
#pragma unroll
    for (int d = 0; d < 4; ++d) { cpx p = v[2 * d], q = v[2 * d + 1]; v[2 * d] = cadd(p, q); v[2 * d + 1] = csub(p, q); }
}
__device__ __forceinline__ int pad(int i) { return i + (i >> 4); }
__device__ __forceinline__ cpx expi(float x) { float s, c; sincospif(x, &s, &c); return cpx{c, s}; }
template <int R> __device__ __forceinline__ void powers(cpx* w, cpx w1) {
    w[1] = w1;
#pragma unroll
    for (int r = 2; r < R; ++r) w[r] = (r & 1) ? cmul(w[r - 1], w1) : cmul(w[r / 2], w[r / 2]);
}
template <int R, int DIR> __device__ __forceinline__ void rotate(cpx* v, const cpx* w) {
#pragma unroll
    for (int r = 1; r < R; ++r) v[r] = DIR < 0 ? cmul(v[r], w[r]) : cpx{v[r].x * w[r].x + v[r].y * w[r].y, v[r].y * w[r].x - v[r].x * w[r].y};
}
struct TwBase { cpx b; cpx c[4]; };
template <int N> __device__ __forceinline__ TwBase make_tw(int t) {
    TwBase tb;
    tb.b = expi(-(float)(t & 15) * (1.0f / 128.0f));
#pragma unroll
    for (int q = 0; q < 4; ++q) tb.c[q] = expi(-(float)(t + 64 * q) * (2.0f / (float)N));
    return tb;
}
__device__ __forceinline__ void wave_sync() { __builtin_amdgcn_fence(__ATOMIC_ACQ_REL, "wavefront"); __builtin_amdgcn_wave_barrier(); }
template <int N, int DIR, bool RIN, bool ROUT>
__device__ __forceinline__ void fft_reg(cpx* b0, cpx* b1, const TwBase& tb, const int t, cpx (&v)[2][16]) {
    constexpr int T = N / 16;
    cpx* const bf[2] = {b0, b1};
    const int tt[2] = {t, N == 2048 ? t + 64 : t};
    if constexpr (!RIN) {
#pragma unroll
        for (int e = 0; e < 2; ++e)
#pragma unroll
            for (int r = 0; r < 16; ++r) v[e][r] = bf[e][pad(tt[e] + T * r)];
        wave_sync();
    }
#pragma unroll
    for (int e = 0; e < 2; ++e) {
        dft16<DIR>(v[e]);
#pragma unroll
        for (int q = 0; q < 16; ++q) bf[e][pad(16 * tt[e] + x16(q))] = v[e][q];
    }
    wave_sync();
    {
#pragma unroll
        for (int e = 0; e < 2; ++e)
#pragma unroll
            for (int r = 0; r < 16; ++r) v[e][r] = bf[e][pad(tt[e] + T * r)];
        wave_sync();
        cpx w[16];
        powers<16>(w, tb.b);
#pragma unroll
        for (int e = 0; e < 2; ++e) {
            rotate<16, DIR>(v[e], w);
            dft16<DIR>(v[e]);
            const int o = (tt[e] >> 4) * 256 + (t & 15);
#pragma unroll
            for (int q = 0; q < 16; ++q) bf[e][pad(o + 16 * x16(q))] = v[e][q];
        }
        wave_sync();
    }
    if constexpr (N == 1024) {
#pragma unroll
        for (int e = 0; e < 2; ++e)
#pragma unroll
            for (int q = 0; q < 4; ++q)
#pragma unroll
                for (int r = 0; r < 4; ++r) v[e][4 * q + r] = bf[e][pad(t + 64 * q + 256 * r)];
#pragma unroll
        for (int q = 0; q < 4; ++q) {
            cpx w[4];
            powers<4>(w, tb.c[q]);
#pragma unroll
            for (int e = 0; e < 2; ++e) {
                rotate<4, DIR>(&v[e][4 * q], w);
                r4<DIR>(v[e][4 * q], v[e][4 * q + 1], v[e][4 * q + 2], v[e][4 * q + 3]);
#pragma unroll
                for (int r = 0; r < 4; ++r) bf[e][pad(t + 64 * q + 256 * r)] = v[e][4 * q + r];
            }
        }
        wave_sync();
    } else {
        cpx u[4][8];
#pragma unroll
        for (int q = 0; q < 4; ++q)
#pragma unroll
            for (int r = 0; r < 8; ++r) u[q][r] = b0[pad(t + 64 * q + 256 * r)];
        if constexpr (ROUT) wave_sync();
#pragma unroll
        for (int q = 0; q < 4; ++q) {
            cpx w[8];
            powers<8>(w, tb.c[q]);
            rotate<8, DIR>(u[q], w);
            dft8<DIR>(u[q]);
#pragma unroll
            for (int d = 0; d < 4; ++d) {
                if constexpr (ROUT) { v[q & 1][(q >> 1) + 2 * d] = u[q][2 * d]; v[q & 1][(q >> 1) + 2 * d + 8] = u[q][2 * d + 1]; }
                else { b0[pad(t + 64 * q + 256 * d)] = u[q][2 * d]; b0[pad(t + 64 * q + 256 * (d + 4))] = u[q][2 * d + 1]; }
            }
        }
        if constexpr (!ROUT) wave_sync();
    }
}
constexpr int WP = 2048 + 96;
constexpr int FP = 514;
constexpr int PB1 = 1024 + 64, PB2 = 2048 + 128;
typedef float f32x4v __attribute__((ext_vector_type(4)));
typedef unsigned u32x4v __attribute__((ext_vector_type(4)));

__device__ __forceinline__ void fft_rows_fwd(const u16* __restrict__ x, rsrc_t rsF, const int g, char* smem, const int tid) {
    cpx (*buf)[PB1] = (cpx (*)[PB1])smem;
    const int f = tid >> 6, t = tid & 63;
    const u16* p0 = x + (size_t)(g * 32 + 4 * f) * 1024 + 16 * t;
    u32x4v a[4][2];
#pragma unroll
    for (int r = 0; r < 4; ++r) { a[r][0] = *(const u32x4v*)(p0 + r * 1024); a[r][1] = *(const u32x4v*)(p0 + r * 1024 + 8); }
    const TwBase tb = make_tw<1024>(t);
#pragma unroll
    for (int e = 0; e < 2; ++e) {
        cpx* d = &buf[2 * f + e][pad(16 * t)];
#pragma unroll
        for (int c = 0; c < 8; ++c) {
            const unsigned p = a[2 * e][c >> 2][c & 3], q = a[2 * e + 1][c >> 2][c & 3];
            d[2 * c] = cpx{__uint_as_float(p << 16), __uint_as_float(q << 16)};
            d[2 * c + 1] = cpx{__uint_as_float(p & 0xFFFF0000u), __uint_as_float(q & 0xFFFF0000u)};
        }
    }
    wave_sync();
    cpx z[2][16];
    fft_reg<1024, -1, false, false>(buf[2 * f], buf[2 * f + 1], tb, t, z);
    __syncthreads();
    const int bl = tid >> 7;
    for (int idx = tid & 127; idx < 513 * 4; idx += 128) {
        const int k = idx >> 2, ff = 4 * bl + (idx & 3);
        const cpx zk = buf[ff][pad(k)], zn = buf[ff][pad((1024 - k) & 1023)];
        const f32x4v o = {0.5f * (zk.x + zn.x), 0.5f * (zk.y - zn.y), 0.5f * (zk.y + zn.y), -0.5f * (zk.x - zn.x)};
        st_sc1(rsF, (4 * g + bl) * FP * 64 + idx * 16, __builtin_bit_cast(v4u, o));
    }
}
__device__ __forceinline__ void filt_transpose(const u16* __restrict__ wr, const u16* __restrict__ wi, rsrc_t rsW, const int tb, char* smem, const int tid) {
    const v4u* s0 = (const v4u*)(wr + (size_t)tb * 32 * 513);
    const v4u* s1 = (const v4u*)(wi + (size_t)tb * 32 * 513);
    v4u r0[5], r1[5];
#pragma unroll
    for (int e = 0; e < 5; ++e) {
        const int idx = tid + 512 * e, ic = idx < 2052 ? idx : 2051;
        r0[e] = s0[ic]; r1[e] = s1[ic];
    }
#pragma unroll
    for (int e = 0; e < 5; ++e) {
        const int idx = tid + 512 * e;
        if (idx < 2052) { *(v4u*)(smem + idx * 16) = r0[e]; *(v4u*)(smem + 32832 + idx * 16) = r1[e]; }
    }
    __syncthreads();
    const u16* a0 = (const u16*)smem;
    const u16* a1 = (const u16*)(smem + 32832);
    for (int idx = tid; idx < 513 * 8; idx += 512) {
        const int k = idx >> 3, n4 = (idx & 7) * 4;
        v4u v;
#pragma unroll
        for (int j = 0; j < 4; ++j) v[j] = (unsigned)a0[(n4 + j) * 513 + k] | ((unsigned)a1[(n4 + j) * 513 + k] << 16);
        st_sc1(rsW, (k * WP + tb * 32 + n4) * 4, v);
    }
}

__device__ __forceinline__ void fft_col(const cpx* F, const unsigned* Wt, const int k, char* smem, const int t) {
    cpx* buf = (cpx*)smem;
    cpx z[2][16];
    unsigned wv[2][16];
#pragma unroll
    for (int e = 0; e < 2; ++e)
#pragma unroll
        for (int r = 0; r < 16; ++r) z[e][r] = F[((size_t)((t >> 3) + 8 * e + 16 * r) * FP + k) * 8 + (t & 7)];
#pragma unroll
    for (int e = 0; e < 2; ++e)
#pragma unroll
        for (int r = 0; r < 16; ++r) wv[e][r] = Wt[(size_t)k * WP + t + 64 * e + 128 * r];
    FST(8)
    const TwBase tb = make_tw<2048>(t);
    FWT FST(9)
    fft_reg<2048, -1, true, true>(buf, buf, tb, t, z);
    FST(10)
#pragma unroll
    for (int e = 0; e < 2; ++e)
#pragma unroll
        for (int r = 0; r < 16; ++r) z[e][r] = cmul(z[e][r], cpx{__uint_as_float(wv[e][r] << 16), __uint_as_float(wv[e][r] & 0xFFFF0000u)});
    fft_reg<2048, 1, true, false>(buf, buf, tb, t, z);
    FST(11)
    FST(12)
}

__device__ __forceinline__ void fft_col_out(rsrc_t rsF, const int k, const char* smem, const int t) {
    const cpx* buf = (const cpx*)smem + ((t >> 2) & 1) * PB2;
    const float sc = 1.0f / 2048.0f;
#pragma unroll
    for (int m = 0; m < 16; ++m) {
        const int rb = (k & 1) * 128 + (t >> 3) + 8 * m;
        const cpx* d = &buf[pad(rb * 8 + 2 * (t & 3))];
        st_sc1(rsF, (rb * FP + (k & ~1)) * 64 + (t & 7) * 16, __builtin_bit_cast(v4u, f32x4v{d[0].x * sc, d[0].y * sc, d[1].x * sc, d[1].y * sc}));
    }
}

__device__ __forceinline__ void fft_rows_inv(const cpx* F, u16* __restrict__ out, const int g, char* smem, const int tid) {
    cpx (*buf)[PB1] = (cpx (*)[PB1])smem;
    const int f = tid >> 6, t = tid & 63, bl = tid >> 7, u = tid & 127;
    f32x4v v[17];
    const f32x4v* src = (const f32x4v*)(F + (size_t)(4 * g + bl) * FP * 8);
#pragma unroll
    for (int m = 0; m < 16; ++m) v[m] = src[u + 128 * m];
    v[16] = src[2048 + (u & 3)];
    const TwBase tb = make_tw<1024>(t);
    cpx* bf = buf[4 * bl + (u & 3)];
#pragma unroll
    for (int m = 0; m < 16; ++m) {
        const int k = (u >> 2) + 32 * m;
        if (m == 0 && k == 0) { bf[0] = cpx{v[0][0], v[0][2]}; bf[pad(512)] = cpx{v[16][0], v[16][2]}; }
        else { bf[pad(k)] = cpx{v[m][0] - v[m][3], v[m][1] + v[m][2]}; bf[pad(1024 - k)] = cpx{v[m][0] + v[m][3], v[m][2] - v[m][1]}; }
    }
    __syncthreads();
    cpx z[2][16];
    fft_reg<1024, 1, false, false>(buf[2 * f], buf[2 * f + 1], tb, t, z);
    const float sc = 1.0f / 1024.0f;
#pragma unroll
    for (int e = 0; e < 2; ++e) {
        u32x4v a[2], b[2];
#pragma unroll
        for (int h = 0; h < 2; ++h) {
            const cpx* d = &buf[2 * f + e][pad(512 * h + 8 * t)];
#pragma unroll
            for (int c = 0; c < 4; ++c) {
                cpx p = d[2 * c], q = d[2 * c + 1];
                a[h][c] = cvt_pk_bf16(p.x * sc, q.x * sc);
                b[h][c] = cvt_pk_bf16(p.y * sc, q.y * sc);
            }
        }
        u16* o0 = out + (size_t)(g * 32 + 4 * f + 2 * e) * OUTW + 1024 + 8 * t;
        *(u32x4v*)o0 = a[0]; *(u32x4v*)(o0 + 512) = a[1];
        *(u32x4v*)(o0 + OUTW) = b[0]; *(u32x4v*)(o0 + OUTW + 512) = b[1];
    }
}

#define K8_BAR do { asm volatile("" ::: "memory"); __builtin_amdgcn_s_barrier(); asm volatile("" ::: "memory"); } while (0)
#define K8_WV(n) asm volatile("s_waitcnt vmcnt(" #n ")" ::: "memory")
#define K8_WL(n) asm volatile("s_waitcnt lgkmcnt(" #n ")" ::: "memory")
#define K8_SCHED __builtin_amdgcn_sched_barrier(0)
__device__ __forceinline__ void xw_stream(const int w, const int idx) {
    const Args a = get_args();
    const int kind = idx >> 5, h = idx & 31;
    asm volatile(
        ".Lcode_x0:\n\t"
        "v_mbcnt_lo_u32_b32 v1, -1, 0\n\t"
        "v_mbcnt_hi_u32_b32 v1, -1, v1\n\t"
        "v_lshlrev_b32 v0, 4, v1\n\t"
        "v_lshrrev_b32 v2, 3, v1\n\t"
        "v_and_b32 v3, 7, v1\n\t"
        "v_xor_b32 v3, v3, v2\n\t"
        "v_lshlrev_b32 v3, 4, v3\n\t"
        "v_lshl_add_u32 v4, v2, 11, v3\n\t"
        "s_lshl_b32 s70, %[w], 8\n\t"
        "s_add_u32 s70, s70, 4096\n\t"
        "v_add_u32 v4, s70, v4\n\t"
        "v_add_u32 v5, 15360, v4\n\t"
        "v_add_u32 v6, -1920, v4\n\t"
        "v_add_u32 v7, 13440, v4\n\t"
        "v_lshrrev_b32 v2, 4, v1\n\t"
        "v_and_b32 v3, 15, v1\n\t"
        "s_and_b32 s70, %[w], 1\n\t"
        "s_lshl_b32 s70, s70, 2\n\t"
        "v_or_b32 v8, s70, v2\n\t"
        "v_lshlrev_b32 v8, 1, v8\n\t"
        "v_xor_b32 v8, v8, v3\n\t"
        "v_lshlrev_b32 v8, 4, v8\n\t"
        "v_lshl_add_u32 v8, v2, 13, v8\n\t"
        "s_lshr_b32 s44, %[w], 2\n\t"
        "s_and_b32 s45, %[w], 3\n\t"
        "s_lshl_b32 s46, s44, 22\n\t"
        "s_lshl_b32 s70, s45, 16\n\t"
        "s_add_u32 s46, s46, s70\n\t"
        "s_lshl_b32 s47, %[w], 12\n\t"
        "s_lshl_b32 s48, %[w], 11\n\t"
        "s_add_u32 s48, s48, 65536\n\t"
        "s_mov_b32 s49, 0\n\t"
        "s_add_u32 m0, s48, 0\n\t"
        "s_add_u32 s70, s46, 0\n\t"
        "s_add_u32 s71, s46, 31744\n\t"
        "buffer_load_dwordx4 v8, %[rw], s70 offen lds\n\t"
        "buffer_load_dwordx4 v8, %[rw], s71 offen offset:1024 lds\n\t"
        "s_add_u32 m0, s48, 16384\n\t"
        "s_add_u32 s70, s46, 262144\n\t"
        "s_add_u32 s71, s46, 293888\n\t"
        "buffer_load_dwordx4 v8, %[rw], s70 offen lds\n\t"
        "buffer_load_dwordx4 v8, %[rw], s71 offen offset:1024 lds\n\t"
        "s_add_u32 m0, s47, 0\n\t"
        "s_nop 0\n\t"
        "buffer_load_dwordx4 v4, %[rx], s49 offen offset:0 lds\n\t"
        "buffer_load_dwordx4 v5, %[rx], s49 offen offset:1024 lds\n\t"
        "buffer_load_dwordx4 v6, %[rx], s49 offen offset:2048 lds\n\t"
        "buffer_load_dwordx4 v7, %[rx], s49 offen offset:3072 lds\n\t"
        "s_add_u32 s49, s49, 32768\n\t"
        "s_add_u32 m0, s47, 32768\n\t"
        "s_nop 0\n\t"
        "buffer_load_dwordx4 v4, %[rx], s49 offen offset:0 lds\n\t"
        "buffer_load_dwordx4 v5, %[rx], s49 offen offset:1024 lds\n\t"
        "buffer_load_dwordx4 v6, %[rx], s49 offen offset:2048 lds\n\t"
        "buffer_load_dwordx4 v7, %[rx], s49 offen offset:3072 lds\n\t"
        "s_add_u32 s49, s49, 32768\n\t"
        "s_add_u32 m0, s48, 32768\n\t"
        "s_add_u32 s70, s46, 524288\n\t"
        "s_add_u32 s71, s46, 556032\n\t"
        "buffer_load_dwordx4 v8, %[rw], s70 offen lds\n\t"
        "buffer_load_dwordx4 v8, %[rw], s71 offen offset:1024 lds\n\t"
        "s_cmp_eq_u32 %[kn], 0\n\t"
        "s_cselect_b64 s[56:57], -1, 0\n\t"
        "v_and_b32 v72, 1, v2\n\t"
        "v_lshlrev_b32 v72, 1, v72\n\t"
        "v_lshrrev_b32 v73, 1, v2\n\t"
        "v_add_u32 v72, v72, v73\n\t"
        "v_lshlrev_b32 v72, 8, v72\n\t"
        "v_lshl_add_u32 v72, v3, 4, v72\n\t"
        "v_cndmask_b32_e64 v79, v0, v72, s[56:57]\n\t"
        "v_and_b32 v9, 7, v1\n\t"
        "v_xor_b32 v10, v2, v9\n\t"
        "v_lshlrev_b32 v10, 4, v10\n\t"
        "v_lshl_add_u32 v10, v3, 7, v10\n\t"
        "s_lshl_b32 s70, s44, 14\n\t"
        "v_add_u32 v10, s70, v10\n\t"
        "v_xor_b32 v11, 64, v10\n\t"
        "v_add_u32 v12, 65536, v10\n\t"
        "v_add_u32 v13, 65536, v11\n\t"
        "s_lshl_b32 s70, s45, 11\n\t"
        "s_add_u32 s70, s70, 131072\n\t"
        "v_add_u32 v14, s70, v0\n\t"
        "v_lshrrev_b32 v9, 2, v3\n\t"
        "v_lshl_add_u32 v15, v2, 3, v9\n\t"
        "v_lshlrev_b32 v15, 8, v15\n\t"
        "v_and_b32 v72, 1, v2\n\t"
        "v_lshl_add_u32 v9, v72, 2, v9\n\t"
        "s_lshl_b32 s70, s45, 1\n\t"
        "v_xor_b32 v9, s70, v9\n\t"
        "v_lshl_add_u32 v15, v9, 5, v15\n\t"
        "v_and_b32 v9, 3, v1\n\t"
        "v_lshl_add_u32 v15, v9, 3, v15\n\t"
        "s_lshl_b32 s70, s44, 13\n\t"
        "s_add_u32 s70, s70, 65536\n\t"
        "v_add_u32 v15, s70, v15\n\t"
        "v_xor_b32 v9, 32, v15\n\t"
        "s_waitcnt vmcnt(12)\n\t"
        "s_barrier\n\t"
        "s_add_u32 m0, s48, 49152\n\t"
        "s_add_u32 s70, s46, 786432\n\t"
        "s_add_u32 s71, s46, 818176\n\t"
        "buffer_load_dwordx4 v8, %[rw], s70 offen lds\n\t"
        "buffer_load_dwordx4 v8, %[rw], s71 offen offset:1024 lds\n\t"
        "ds_read_b64_tr_b16 v[128:129], v15 offset:0\n\t"
        "ds_read_b64_tr_b16 v[130:131], v15 offset:1024\n\t"
        "ds_read_b64_tr_b16 v[132:133], v9 offset:0\n\t"
        "ds_read_b64_tr_b16 v[134:135], v9 offset:1024\n\t"
        "s_waitcnt lgkmcnt(0)\n\t"
        "s_waitcnt vmcnt(4)\n\t"
        "s_barrier\n\t"
        "s_add_u32 m0, s48, 0\n\t"
        "s_add_u32 s70, s46, 1048576\n\t"
        "s_add_u32 s71, s46, 1080320\n\t"
        "buffer_load_dwordx4 v8, %[rw], s70 offen lds\n\t"
        "buffer_load_dwordx4 v8, %[rw], s71 offen offset:1024 lds\n\t"
        "ds_read_b64_tr_b16 v[136:137], v15 offset:16384\n\t"
        "ds_read_b64_tr_b16 v[138:139], v15 offset:17408\n\t"
        "ds_read_b64_tr_b16 v[140:141], v9 offset:16384\n\t"
        "ds_read_b64_tr_b16 v[142:143], v9 offset:17408\n\t"
        "ds_read_b128 v[16:19], v10 offset:0\n\t"
        "ds_read_b128 v[20:23], v10 offset:32768\n\t"
        "s_waitcnt lgkmcnt(0)\n\t"
        "v_mfma_f32_16x16x32_bf16 v[32:35], v[128:131], v[16:19], 0\n\t"
        "v_mfma_f32_16x16x32_bf16 v[36:39], v[132:135], v[16:19], 0\n\t"
        "v_mfma_f32_16x16x32_bf16 v[40:43], v[128:131], v[20:23], 0\n\t"
        "v_mfma_f32_16x16x32_bf16 v[44:47], v[132:135], v[20:23], 0\n\t"
        "s_waitcnt vmcnt(4)\n\t"
        "s_barrier\n\t"
        "s_add_u32 m0, s48, 16384\n\t"
        "s_add_u32 s70, s46, 1310720\n\t"
        "s_add_u32 s71, s46, 1342464\n\t"
        "buffer_load_dwordx4 v8, %[rw], s70 offen lds\n\t"
        "buffer_load_dwordx4 v8, %[rw], s71 offen offset:1024 lds\n\t"
        "ds_read_b64_tr_b16 v[144:145], v15 offset:32768\n\t"
        "ds_read_b64_tr_b16 v[146:147], v15 offset:33792\n\t"
        "ds_read_b64_tr_b16 v[148:149], v9 offset:32768\n\t"
        "ds_read_b64_tr_b16 v[150:151], v9 offset:33792\n\t"
        "ds_read_b128 v[16:19], v11 offset:0\n\t"
        "ds_read_b128 v[20:23], v11 offset:32768\n\t"
        "s_waitcnt lgkmcnt(0)\n\t"
        "v_mfma_f32_16x16x32_bf16 v[32:35], v[136:139], v[16:19], v[32:35]\n\t"
        "v_mfma_f32_16x16x32_bf16 v[36:39], v[140:143], v[16:19], v[36:39]\n\t"
        "v_mfma_f32_16x16x32_bf16 v[40:43], v[136:139], v[20:23], v[40:43]\n\t"
        "v_mfma_f32_16x16x32_bf16 v[44:47], v[140:143], v[20:23], v[44:47]\n\t"
        "s_waitcnt vmcnt(4)\n\t"
        "s_barrier\n\t"
        "s_add_u32 m0, s48, 32768\n\t"
        "s_add_u32 s70, s46, 1572864\n\t"
        "s_add_u32 s71, s46, 1604608\n\t"
        "buffer_load_dwordx4 v8, %[rw], s70 offen lds\n\t"
        "buffer_load_dwordx4 v8, %[rw], s71 offen offset:1024 lds\n\t"
        "ds_read_b64_tr_b16 v[152:153], v15 offset:49152\n\t"
        "ds_read_b64_tr_b16 v[154:155], v15 offset:50176\n\t"
        "ds_read_b64_tr_b16 v[156:157], v9 offset:49152\n\t"
        "ds_read_b64_tr_b16 v[158:159], v9 offset:50176\n\t"
        "ds_read_b128 v[16:19], v10 offset:2048\n\t"
        "ds_read_b128 v[20:23], v10 offset:34816\n\t"
        "s_waitcnt lgkmcnt(0)\n\t"
        "v_mfma_f32_16x16x32_bf16 v[32:35], v[144:147], v[16:19], v[32:35]\n\t"
        "v_mfma_f32_16x16x32_bf16 v[36:39], v[148:151], v[16:19], v[36:39]\n\t"
        "v_mfma_f32_16x16x32_bf16 v[40:43], v[144:147], v[20:23], v[40:43]\n\t"
        "v_mfma_f32_16x16x32_bf16 v[44:47], v[148:151], v[20:23], v[44:47]\n\t"
        "s_waitcnt vmcnt(4)\n\t"
        "s_barrier\n\t"
        "s_add_u32 m0, s48, 49152\n\t"
        "s_add_u32 s70, s46, 1835008\n\t"
        "s_add_u32 s71, s46, 1866752\n\t"
        "buffer_load_dwordx4 v8, %[rw], s70 offen lds\n\t"
        "buffer_load_dwordx4 v8, %[rw], s71 offen offset:1024 lds\n\t"
        "ds_read_b64_tr_b16 v[160:161], v15 offset:0\n\t"
        "ds_read_b64_tr_b16 v[162:163], v15 offset:1024\n\t"
        "ds_read_b64_tr_b16 v[164:165], v9 offset:0\n\t"
        "ds_read_b64_tr_b16 v[166:167], v9 offset:1024\n\t"
        "ds_read_b128 v[16:19], v11 offset:2048\n\t"
        "ds_read_b128 v[20:23], v11 offset:34816\n\t"
        "s_waitcnt lgkmcnt(0)\n\t"
        "v_mfma_f32_16x16x32_bf16 v[32:35], v[152:155], v[16:19], v[32:35]\n\t"
        "v_mfma_f32_16x16x32_bf16 v[36:39], v[156:159], v[16:19], v[36:39]\n\t"
        "v_mfma_f32_16x16x32_bf16 v[40:43], v[152:155], v[20:23], v[40:43]\n\t"
        "v_mfma_f32_16x16x32_bf16 v[44:47], v[156:159], v[20:23], v[44:47]\n\t"
        "s_waitcnt vmcnt(4)\n\t"
        "s_barrier\n\t"
        "s_add_u32 m0, s48, 0\n\t"
        "s_add_u32 s70, s46, 2097152\n\t"
        "s_add_u32 s71, s46, 2128896\n\t"
        "buffer_load_dwordx4 v8, %[rw], s70 offen lds\n\t"
        "buffer_load_dwordx4 v8, %[rw], s71 offen offset:1024 lds\n\t"
        "ds_read_b64_tr_b16 v[168:169], v15 offset:16384\n\t"
        "ds_read_b64_tr_b16 v[170:171], v15 offset:17408\n\t"
        "ds_read_b64_tr_b16 v[172:173], v9 offset:16384\n\t"
        "ds_read_b64_tr_b16 v[174:175], v9 offset:17408\n\t"
        "ds_read_b128 v[16:19], v10 offset:4096\n\t"
        "ds_read_b128 v[20:23], v10 offset:36864\n\t"
        "s_waitcnt lgkmcnt(0)\n\t"
        "v_mfma_f32_16x16x32_bf16 v[32:35], v[160:163], v[16:19], v[32:35]\n\t"
        "v_mfma_f32_16x16x32_bf16 v[36:39], v[164:167], v[16:19], v[36:39]\n\t"
        "v_mfma_f32_16x16x32_bf16 v[40:43], v[160:163], v[20:23], v[40:43]\n\t"
        "v_mfma_f32_16x16x32_bf16 v[44:47], v[164:167], v[20:23], v[44:47]\n\t"
        "s_waitcnt vmcnt(4)\n\t"
        "s_barrier\n\t"
        "s_add_u32 m0, s48, 16384\n\t"
        "s_add_u32 s70, s46, 2359296\n\t"
        "s_add_u32 s71, s46, 2391040\n\t"
        "buffer_load_dwordx4 v8, %[rw], s70 offen lds\n\t"
        "buffer_load_dwordx4 v8, %[rw], s71 offen offset:1024 lds\n\t"
        "ds_read_b64_tr_b16 v[176:177], v15 offset:32768\n\t"
        "ds_read_b64_tr_b16 v[178:179], v15 offset:33792\n\t"
        "ds_read_b64_tr_b16 v[180:181], v9 offset:32768\n\t"
        "ds_read_b64_tr_b16 v[182:183], v9 offset:33792\n\t"
        "ds_read_b128 v[16:19], v11 offset:4096\n\t"
        "ds_read_b128 v[20:23], v11 offset:36864\n\t"
        "s_waitcnt lgkmcnt(0)\n\t"
        "v_mfma_f32_16x16x32_bf16 v[32:35], v[168:171], v[16:19], v[32:35]\n\t"
        "v_mfma_f32_16x16x32_bf16 v[36:39], v[172:175], v[16:19], v[36:39]\n\t"
        "v_mfma_f32_16x16x32_bf16 v[40:43], v[168:171], v[20:23], v[40:43]\n\t"
        "v_mfma_f32_16x16x32_bf16 v[44:47], v[172:175], v[20:23], v[44:47]\n\t"
        "s_waitcnt vmcnt(4)\n\t"
        "s_barrier\n\t"
        "s_add_u32 m0, s48, 32768\n\t"
        "s_add_u32 s70, s46, 2621440\n\t"
        "s_add_u32 s71, s46, 2653184\n\t"
        "buffer_load_dwordx4 v8, %[rw], s70 offen lds\n\t"
        "buffer_load_dwordx4 v8, %[rw], s71 offen offset:1024 lds\n\t"
        "ds_read_b64_tr_b16 v[184:185], v15 offset:49152\n\t"
        "ds_read_b64_tr_b16 v[186:187], v15 offset:50176\n\t"
        "ds_read_b64_tr_b16 v[188:189], v9 offset:49152\n\t"
        "ds_read_b64_tr_b16 v[190:191], v9 offset:50176\n\t"
        "ds_read_b128 v[16:19], v10 offset:6144\n\t"
        "ds_read_b128 v[20:23], v10 offset:38912\n\t"
        "s_waitcnt lgkmcnt(0)\n\t"
        "v_mfma_f32_16x16x32_bf16 v[32:35], v[176:179], v[16:19], v[32:35]\n\t"
        "v_mfma_f32_16x16x32_bf16 v[36:39], v[180:183], v[16:19], v[36:39]\n\t"
        "v_mfma_f32_16x16x32_bf16 v[40:43], v[176:179], v[20:23], v[40:43]\n\t"
        "v_mfma_f32_16x16x32_bf16 v[44:47], v[180:183], v[20:23], v[44:47]\n\t"
        "s_waitcnt vmcnt(4)\n\t"
        "s_barrier\n\t"
        "s_add_u32 m0, s48, 49152\n\t"
        "s_add_u32 s70, s46, 2883584\n\t"
        "s_add_u32 s71, s46, 2915328\n\t"
        "buffer_load_dwordx4 v8, %[rw], s70 offen lds\n\t"
        "buffer_load_dwordx4 v8, %[rw], s71 offen offset:1024 lds\n\t"
        "ds_read_b64_tr_b16 v[192:193], v15 offset:0\n\t"
        "ds_read_b64_tr_b16 v[194:195], v15 offset:1024\n\t"
        "ds_read_b64_tr_b16 v[196:197], v9 offset:0\n\t"
        "ds_read_b64_tr_b16 v[198:199], v9 offset:1024\n\t"
        "ds_read_b128 v[16:19], v11 offset:6144\n\t"
        "ds_read_b128 v[20:23], v11 offset:38912\n\t"
        "s_waitcnt lgkmcnt(0)\n\t"
        "v_mfma_f32_16x16x32_bf16 v[32:35], v[184:187], v[16:19], v[32:35]\n\t"
        "v_mfma_f32_16x16x32_bf16 v[36:39], v[188:191], v[16:19], v[36:39]\n\t"
        "v_mfma_f32_16x16x32_bf16 v[40:43], v[184:187], v[20:23], v[40:43]\n\t"
        "v_mfma_f32_16x16x32_bf16 v[44:47], v[188:191], v[20:23], v[44:47]\n\t"
        "s_waitcnt vmcnt(4)\n\t"
        "s_barrier\n\t"
        "s_add_u32 m0, s48, 0\n\t"
        "s_add_u32 s70, s46, 3145728\n\t"
        "s_add_u32 s71, s46, 3177472\n\t"
        "buffer_load_dwordx4 v8, %[rw], s70 offen lds\n\t"
        "buffer_load_dwordx4 v8, %[rw], s71 offen offset:1024 lds\n\t"
        "ds_read_b64_tr_b16 v[200:201], v15 offset:16384\n\t"
        "ds_read_b64_tr_b16 v[202:203], v15 offset:17408\n\t"
        "ds_read_b64_tr_b16 v[204:205], v9 offset:16384\n\t"
        "ds_read_b64_tr_b16 v[206:207], v9 offset:17408\n\t"
        "ds_read_b128 v[16:19], v10 offset:8192\n\t"
        "ds_read_b128 v[20:23], v10 offset:40960\n\t"
        "s_waitcnt lgkmcnt(0)\n\t"
        "v_mfma_f32_16x16x32_bf16 v[32:35], v[192:195], v[16:19], v[32:35]\n\t"
        "v_mfma_f32_16x16x32_bf16 v[36:39], v[196:199], v[16:19], v[36:39]\n\t"
        "v_mfma_f32_16x16x32_bf16 v[40:43], v[192:195], v[20:23], v[40:43]\n\t"
        "v_mfma_f32_16x16x32_bf16 v[44:47], v[196:199], v[20:23], v[44:47]\n\t"
        "s_waitcnt vmcnt(4)\n\t"
        "s_barrier\n\t"
        "s_add_u32 m0, s48, 16384\n\t"
        "s_add_u32 s70, s46, 3407872\n\t"
        "s_add_u32 s71, s46, 3439616\n\t"
        "buffer_load_dwordx4 v8, %[rw], s70 offen lds\n\t"
        "buffer_load_dwordx4 v8, %[rw], s71 offen offset:1024 lds\n\t"
        "ds_read_b64_tr_b16 v[208:209], v15 offset:32768\n\t"
        "ds_read_b64_tr_b16 v[210:211], v15 offset:33792\n\t"
        "ds_read_b64_tr_b16 v[212:213], v9 offset:32768\n\t"
        "ds_read_b64_tr_b16 v[214:215], v9 offset:33792\n\t"
        "ds_read_b128 v[16:19], v11 offset:8192\n\t"
        "ds_read_b128 v[20:23], v11 offset:40960\n\t"
        "s_waitcnt lgkmcnt(0)\n\t"
        "v_mfma_f32_16x16x32_bf16 v[32:35], v[200:203], v[16:19], v[32:35]\n\t"
        "v_mfma_f32_16x16x32_bf16 v[36:39], v[204:207], v[16:19], v[36:39]\n\t"
        "v_mfma_f32_16x16x32_bf16 v[40:43], v[200:203], v[20:23], v[40:43]\n\t"
        "v_mfma_f32_16x16x32_bf16 v[44:47], v[204:207], v[20:23], v[44:47]\n\t"
        "s_waitcnt vmcnt(4)\n\t"
        "s_barrier\n\t"
        "s_add_u32 m0, s48, 32768\n\t"
        "s_add_u32 s70, s46, 3670016\n\t"
        "s_add_u32 s71, s46, 3701760\n\t"
        "buffer_load_dwordx4 v8, %[rw], s70 offen lds\n\t"
        "buffer_load_dwordx4 v8, %[rw], s71 offen offset:1024 lds\n\t"
        "ds_read_b64_tr_b16 v[216:217], v15 offset:49152\n\t"
        "ds_read_b64_tr_b16 v[218:219], v15 offset:50176\n\t"
        "ds_read_b64_tr_b16 v[220:221], v9 offset:49152\n\t"
        "ds_read_b64_tr_b16 v[222:223], v9 offset:50176\n\t"
        "ds_read_b128 v[16:19], v10 offset:10240\n\t"
        "ds_read_b128 v[20:23], v10 offset:43008\n\t"
        "s_waitcnt lgkmcnt(0)\n\t"
        "v_mfma_f32_16x16x32_bf16 v[32:35], v[208:211], v[16:19], v[32:35]\n\t"
        "v_mfma_f32_16x16x32_bf16 v[36:39], v[212:215], v[16:19], v[36:39]\n\t"
        "v_mfma_f32_16x16x32_bf16 v[40:43], v[208:211], v[20:23], v[40:43]\n\t"
        "v_mfma_f32_16x16x32_bf16 v[44:47], v[212:215], v[20:23], v[44:47]\n\t"
        "s_waitcnt vmcnt(4)\n\t"
        "s_barrier\n\t"
        "s_add_u32 m0, s48, 49152\n\t"
        "s_add_u32 s70, s46, 3932160\n\t"
        "s_add_u32 s71, s46, 3963904\n\t"
        "buffer_load_dwordx4 v8, %[rw], s70 offen lds\n\t"
        "buffer_load_dwordx4 v8, %[rw], s71 offen offset:1024 lds\n\t"
        "ds_read_b64_tr_b16 v[224:225], v15 offset:0\n\t"
        "ds_read_b64_tr_b16 v[226:227], v15 offset:1024\n\t"
        "ds_read_b64_tr_b16 v[228:229], v9 offset:0\n\t"
        "ds_read_b64_tr_b16 v[230:231], v9 offset:1024\n\t"
        "ds_read_b128 v[16:19], v11 offset:10240\n\t"
        "ds_read_b128 v[20:23], v11 offset:43008\n\t"
        "s_waitcnt lgkmcnt(0)\n\t"
        "v_mfma_f32_16x16x32_bf16 v[32:35], v[216:219], v[16:19], v[32:35]\n\t"
        "v_mfma_f32_16x16x32_bf16 v[36:39], v[220:223], v[16:19], v[36:39]\n\t"
        "v_mfma_f32_16x16x32_bf16 v[40:43], v[216:219], v[20:23], v[40:43]\n\t"
        "v_mfma_f32_16x16x32_bf16 v[44:47], v[220:223], v[20:23], v[44:47]\n\t"
        "s_waitcnt vmcnt(4)\n\t"
        "s_barrier\n\t"
        "ds_read_b64_tr_b16 v[232:233], v15 offset:16384\n\t"
        "ds_read_b64_tr_b16 v[234:235], v15 offset:17408\n\t"
        "ds_read_b64_tr_b16 v[236:237], v9 offset:16384\n\t"
        "ds_read_b64_tr_b16 v[238:239], v9 offset:17408\n\t"
        "ds_read_b128 v[16:19], v10 offset:12288\n\t"
        "ds_read_b128 v[20:23], v10 offset:45056\n\t"
        "s_waitcnt lgkmcnt(0)\n\t"
        "v_mfma_f32_16x16x32_bf16 v[32:35], v[224:227], v[16:19], v[32:35]\n\t"
        "v_mfma_f32_16x16x32_bf16 v[36:39], v[228:231], v[16:19], v[36:39]\n\t"
        "v_mfma_f32_16x16x32_bf16 v[40:43], v[224:227], v[20:23], v[40:43]\n\t"
        "v_mfma_f32_16x16x32_bf16 v[44:47], v[228:231], v[20:23], v[44:47]\n\t"
        "s_waitcnt vmcnt(2)\n\t"
        "s_barrier\n\t"
        "ds_read_b64_tr_b16 v[240:241], v15 offset:32768\n\t"
        "ds_read_b64_tr_b16 v[242:243], v15 offset:33792\n\t"
        "ds_read_b64_tr_b16 v[244:245], v9 offset:32768\n\t"
        "ds_read_b64_tr_b16 v[246:247], v9 offset:33792\n\t"
        "ds_read_b128 v[16:19], v11 offset:12288\n\t"
        "ds_read_b128 v[20:23], v11 offset:45056\n\t"
        "s_waitcnt lgkmcnt(0)\n\t"
        "v_mfma_f32_16x16x32_bf16 v[32:35], v[232:235], v[16:19], v[32:35]\n\t"
        "v_mfma_f32_16x16x32_bf16 v[36:39], v[236:239], v[16:19], v[36:39]\n\t"
        "v_mfma_f32_16x16x32_bf16 v[40:43], v[232:235], v[20:23], v[40:43]\n\t"
        "v_mfma_f32_16x16x32_bf16 v[44:47], v[236:239], v[20:23], v[44:47]\n\t"
        "s_waitcnt vmcnt(0)\n\t"
        "s_barrier\n\t"
        "ds_read_b64_tr_b16 v[248:249], v15 offset:49152\n\t"
        "ds_read_b64_tr_b16 v[250:251], v15 offset:50176\n\t"
        "ds_read_b64_tr_b16 v[252:253], v9 offset:49152\n\t"
        "ds_read_b64_tr_b16 v[254:255], v9 offset:50176\n\t"
        "ds_read_b128 v[16:19], v10 offset:14336\n\t"
        "ds_read_b128 v[20:23], v10 offset:47104\n\t"
        "s_waitcnt lgkmcnt(0)\n\t"
        "v_mfma_f32_16x16x32_bf16 v[32:35], v[240:243], v[16:19], v[32:35]\n\t"
        "v_mfma_f32_16x16x32_bf16 v[36:39], v[244:247], v[16:19], v[36:39]\n\t"
        "v_mfma_f32_16x16x32_bf16 v[40:43], v[240:243], v[20:23], v[40:43]\n\t"
        "v_mfma_f32_16x16x32_bf16 v[44:47], v[244:247], v[20:23], v[44:47]\n\t"
        "ds_read_b128 v[16:19], v11 offset:14336\n\t"
        "ds_read_b128 v[20:23], v11 offset:47104\n\t"
        "s_waitcnt lgkmcnt(0)\n\t"
        "v_mfma_f32_16x16x32_bf16 v[32:35], v[248:251], v[16:19], v[32:35]\n\t"
        "v_mfma_f32_16x16x32_bf16 v[36:39], v[252:255], v[16:19], v[36:39]\n\t"
        "v_mfma_f32_16x16x32_bf16 v[40:43], v[248:251], v[20:23], v[40:43]\n\t"
        "v_mfma_f32_16x16x32_bf16 v[44:47], v[252:255], v[20:23], v[44:47]\n\t"
        "s_cmp_eq_u32 %[w], 7\n\t"
        "s_cbranch_scc0 .Lx_nb_%=\n\t"
        "v_lshrrev_b32 v72, 1, v0\n\t"
        "v_mov_b32 v73, 0\n\t"
        "s_mov_b32 s70, 100000\n\t"
        ".Lx_bw_%=:\n\t"
        "global_load_dwordx2 v[74:75], v72, %[bt] sc1\n\t"
        "global_load_dwordx2 v[76:77], v73, %[bt] offset:1152 sc1\n\t"
        "s_waitcnt vmcnt(0)\n\t"
        "v_readfirstlane_b32 s54, v76\n\t"
        "v_readfirstlane_b32 s55, v77\n\t"
        "v_or_b32 v76, 1, v74\n\t"
        "s_add_u32 s54, s54, 1\n\t"
        "s_addc_u32 s55, s55, 0\n\t"
        "s_lshl_b64 s[72:73], s[54:55], 1\n\t"
        "s_or_b32 s72, s72, 1\n\t"
        "v_cmp_eq_u32 vcc, s72, v76\n\t"
        "v_cmp_eq_u32 s[68:69], s73, v75\n\t"
        "v_and_b32 v74, 1, v74\n\t"
        "s_and_b64 vcc, vcc, s[68:69]\n\t"
        "s_cmp_eq_u64 vcc, -1\n\t"
        "s_cbranch_scc1 .Lx_bk_%=\n\t"
        "s_sleep 16\n\t"
        "s_sub_u32 s70, s70, 1\n\t"
        "s_cbranch_scc0 .Lx_bw_%=\n\t"
        ".Lx_bk_%=:\n\t"
        "v_cmp_eq_u32 vcc, 1, v74\n\t"
        "s_lshl_b64 s[54:55], s[54:55], 8\n\t"
        "s_nop 1\n\t"
        "s_bcnt1_i32_b64 s70, vcc\n\t"
        "s_cmp_gt_u32 s70, 32\n\t"
        "s_cselect_b32 s70, 128, 0\n\t"
        "s_or_b32 s54, s54, s70\n\t"
        "v_mov_b32 v72, 147456\n\t"
        "v_mov_b32 v74, s54\n\t"
        "v_mov_b32 v75, s55\n\t"
        "ds_write_b64 v72, v[74:75]\n\t"
        ".Lx_nb_%=:\n\t"
        "s_cmp_eq_u32 s44, 0\n\t"
        "s_cbranch_scc0 .Lx_k1_%=\n\t"
        "s_nop 7\n\t"
        "ds_write_b128 v14, v[40:43] offset:8192\n\t"
        "ds_write_b128 v14, v[44:47] offset:9216\n\t"
        "s_waitcnt lgkmcnt(0)\n\t"
        "s_barrier\n\t"
        "v_mov_b32 v72, 147456\n\t"
        "ds_read_b64 v[76:77], v72\n\t"
        "ds_read_b128 v[64:67], v14 offset:0\n\t"
        "ds_read_b128 v[68:71], v14 offset:1024\n\t"
        "s_add_u32 m0, s47, 65536\n\t"
        "s_nop 0\n\t"
        "buffer_load_dwordx4 v4, %[rx], s49 offen offset:0 lds\n\t"
        "buffer_load_dwordx4 v5, %[rx], s49 offen offset:1024 lds\n\t"
        "buffer_load_dwordx4 v6, %[rx], s49 offen offset:2048 lds\n\t"
        "buffer_load_dwordx4 v7, %[rx], s49 offen offset:3072 lds\n\t"
        "s_add_u32 s49, s49, 32768\n\t"
        "s_add_u32 m0, s47, 98304\n\t"
        "s_nop 0\n\t"
        "buffer_load_dwordx4 v4, %[rx], s49 offen offset:0 lds\n\t"
        "buffer_load_dwordx4 v5, %[rx], s49 offen offset:1024 lds\n\t"
        "buffer_load_dwordx4 v6, %[rx], s49 offen offset:2048 lds\n\t"
        "buffer_load_dwordx4 v7, %[rx], s49 offen offset:3072 lds\n\t"
        "s_add_u32 s49, s49, 32768\n\t"
        "s_waitcnt lgkmcnt(0)\n\t"
        "v_readfirstlane_b32 s54, v76\n\t"
        "v_lshrrev_b32 v78, 4, v1\n\t"
        "v_lshlrev_b32 v78, 24, v78\n\t"
        "v_lshl_add_u32 v78, v3, 3, v78\n\t"
        "s_cmp_eq_u32 %[w], 0\n\t"
        "s_cselect_b32 s70, 0, 0x40000000\n\t"
        "v_add_u32 v78, s70, v78\n\t"
        "s_and_b32 s70, s54, 128\n\t"
        "s_lshl_b32 s70, s70, 6\n\t"
        "s_xor_b32 s50, %[co], s70\n\t"
        "s_lshl_b32 s70, %[w], 10\n\t"
        "s_add_u32 s50, s50, s70\n\t"
        "s_mov_b32 s51, 0\n\t"
        "v_add_f32 v64, v64, v32\n\t"
        "v_add_f32 v65, v65, v33\n\t"
        "v_add_f32 v66, v66, v34\n\t"
        "v_add_f32 v67, v67, v35\n\t"
        "v_add_f32 v68, v68, v36\n\t"
        "v_add_f32 v69, v69, v37\n\t"
        "v_add_f32 v70, v70, v38\n\t"
        "v_add_f32 v71, v71, v39\n\t"
        "v_cvt_pk_bf16_f32 v72, v64, v65\n\t"
        "v_cvt_pk_bf16_f32 v73, v68, v69\n\t"
        "v_cvt_pk_bf16_f32 v74, v66, v67\n\t"
        "v_cvt_pk_bf16_f32 v75, v70, v71\n\t"
        "s_mov_b64 exec, s[56:57]\n\t"
        "v_swap_b32 v73, v74\n\t"
        "s_nop 1\n\t"
        "v_permlane16_swap_b32 v72, v74\n\t"
        "v_permlane16_swap_b32 v73, v75\n\t"
        "s_mov_b64 exec, -1\n\t"
        "buffer_store_dwordx4 v[72:75], v79, %[rs], s50 offen sc1\n\t"
        "s_waitcnt vmcnt(0)\n\t"
        "s_barrier\n\t"
        "s_add_u32 s54, s54, 1\n\t"
        "v_mov_b32 v76, s54\n\t"
        "s_and_b32 s71, s54, 127\n\t"
        "s_lshl_b32 s71, s71, 7\n\t"
        "buffer_store_dwordx2 v[76:77], v78, %[rf], s71 offen sc1\n\t"
        "s_add_u32 m0, s47, 0\n\t"
        "s_nop 0\n\t"
        "buffer_load_dwordx4 v4, %[rx], s49 offen offset:0 lds\n\t"
        "buffer_load_dwordx4 v5, %[rx], s49 offen offset:1024 lds\n\t"
        "buffer_load_dwordx4 v6, %[rx], s49 offen offset:2048 lds\n\t"
        "buffer_load_dwordx4 v7, %[rx], s49 offen offset:3072 lds\n\t"
        "s_add_u32 s49, s49, 32768\n\t"
        "ds_read_b128 v[16:19], v12 offset:0\n\t"
        "ds_read_b128 v[20:23], v13 offset:0\n\t"
        "ds_read_b128 v[24:27], v12 offset:2048\n\t"
        "s_add_u32 m0, s47, 32768\n\t"
        "s_nop 0\n\t"
        "buffer_load_dwordx4 v4, %[rx], s49 offen offset:0 lds\n\t"
        "buffer_load_dwordx4 v5, %[rx], s49 offen offset:1024 lds\n\t"
        "buffer_load_dwordx4 v6, %[rx], s49 offen offset:2048 lds\n\t"
        "buffer_load_dwordx4 v7, %[rx], s49 offen offset:3072 lds\n\t"
        "s_add_u32 s49, s49, 32768\n\t"
        "ds_read_b128 v[28:31], v13 offset:2048\n\t"
        "s_waitcnt lgkmcnt(3)\n\t"
        "v_mfma_f32_16x16x32_bf16 v[48:51], v[128:131], v[16:19], 0\n\t"
        "v_mfma_f32_16x16x32_bf16 v[52:55], v[132:135], v[16:19], 0\n\t"
        "ds_read_b128 v[16:19], v12 offset:4096\n\t"
        "s_waitcnt lgkmcnt(3)\n\t"
        "v_mfma_f32_16x16x32_bf16 v[48:51], v[136:139], v[20:23], v[48:51]\n\t"
        "v_mfma_f32_16x16x32_bf16 v[52:55], v[140:143], v[20:23], v[52:55]\n\t"
        "ds_read_b128 v[20:23], v13 offset:4096\n\t"
        "s_waitcnt lgkmcnt(3)\n\t"
        "v_mfma_f32_16x16x32_bf16 v[48:51], v[144:147], v[24:27], v[48:51]\n\t"
        "v_mfma_f32_16x16x32_bf16 v[52:55], v[148:151], v[24:27], v[52:55]\n\t"
        "ds_read_b128 v[24:27], v12 offset:6144\n\t"
        "s_waitcnt lgkmcnt(3)\n\t"
        "v_mfma_f32_16x16x32_bf16 v[48:51], v[152:155], v[28:31], v[48:51]\n\t"
        "v_mfma_f32_16x16x32_bf16 v[52:55], v[156:159], v[28:31], v[52:55]\n\t"
        "ds_read_b128 v[28:31], v13 offset:6144\n\t"
        "s_waitcnt lgkmcnt(3)\n\t"
        "v_mfma_f32_16x16x32_bf16 v[48:51], v[160:163], v[16:19], v[48:51]\n\t"
        "v_mfma_f32_16x16x32_bf16 v[52:55], v[164:167], v[16:19], v[52:55]\n\t"
        "ds_read_b128 v[16:19], v12 offset:8192\n\t"
        "s_waitcnt lgkmcnt(3)\n\t"
        "v_mfma_f32_16x16x32_bf16 v[48:51], v[168:171], v[20:23], v[48:51]\n\t"
        "v_mfma_f32_16x16x32_bf16 v[52:55], v[172:175], v[20:23], v[52:55]\n\t"
        "ds_read_b128 v[20:23], v13 offset:8192\n\t"
        "s_waitcnt lgkmcnt(3)\n\t"
        "v_mfma_f32_16x16x32_bf16 v[48:51], v[176:179], v[24:27], v[48:51]\n\t"
        "v_mfma_f32_16x16x32_bf16 v[52:55], v[180:183], v[24:27], v[52:55]\n\t"
        "ds_read_b128 v[24:27], v12 offset:10240\n\t"
        "s_waitcnt lgkmcnt(3)\n\t"
        "v_mfma_f32_16x16x32_bf16 v[48:51], v[184:187], v[28:31], v[48:51]\n\t"
        "v_mfma_f32_16x16x32_bf16 v[52:55], v[188:191], v[28:31], v[52:55]\n\t"
        "ds_read_b128 v[28:31], v13 offset:10240\n\t"
        "s_waitcnt lgkmcnt(3)\n\t"
        "v_mfma_f32_16x16x32_bf16 v[48:51], v[192:195], v[16:19], v[48:51]\n\t"
        "v_mfma_f32_16x16x32_bf16 v[52:55], v[196:199], v[16:19], v[52:55]\n\t"
        "ds_read_b128 v[16:19], v12 offset:12288\n\t"
        "s_waitcnt lgkmcnt(3)\n\t"
        "v_mfma_f32_16x16x32_bf16 v[48:51], v[200:203], v[20:23], v[48:51]\n\t"
        "v_mfma_f32_16x16x32_bf16 v[52:55], v[204:207], v[20:23], v[52:55]\n\t"
        "ds_read_b128 v[20:23], v13 offset:12288\n\t"
        "s_waitcnt lgkmcnt(3)\n\t"
        "v_mfma_f32_16x16x32_bf16 v[48:51], v[208:211], v[24:27], v[48:51]\n\t"
        "v_mfma_f32_16x16x32_bf16 v[52:55], v[212:215], v[24:27], v[52:55]\n\t"
        "ds_read_b128 v[24:27], v12 offset:14336\n\t"
        "s_waitcnt lgkmcnt(3)\n\t"
        "v_mfma_f32_16x16x32_bf16 v[48:51], v[216:219], v[28:31], v[48:51]\n\t"
        "v_mfma_f32_16x16x32_bf16 v[52:55], v[220:223], v[28:31], v[52:55]\n\t"
        "ds_read_b128 v[28:31], v13 offset:14336\n\t"
        "s_waitcnt lgkmcnt(3)\n\t"
        "v_mfma_f32_16x16x32_bf16 v[48:51], v[224:227], v[16:19], v[48:51]\n\t"
        "v_mfma_f32_16x16x32_bf16 v[52:55], v[228:231], v[16:19], v[52:55]\n\t"
        "s_waitcnt lgkmcnt(2)\n\t"
        "v_mfma_f32_16x16x32_bf16 v[48:51], v[232:235], v[20:23], v[48:51]\n\t"
        "v_mfma_f32_16x16x32_bf16 v[52:55], v[236:239], v[20:23], v[52:55]\n\t"
        "s_waitcnt lgkmcnt(1)\n\t"
        "v_mfma_f32_16x16x32_bf16 v[48:51], v[240:243], v[24:27], v[48:51]\n\t"
        "v_mfma_f32_16x16x32_bf16 v[52:55], v[244:247], v[24:27], v[52:55]\n\t"
        "s_waitcnt lgkmcnt(0)\n\t"
        "v_mfma_f32_16x16x32_bf16 v[48:51], v[248:251], v[28:31], v[48:51]\n\t"
        "v_mfma_f32_16x16x32_bf16 v[52:55], v[252:255], v[28:31], v[52:55]\n\t"
        "s_waitcnt vmcnt(4) lgkmcnt(0)\n\t"
        "s_barrier\n\t"
        "ds_read_b128 v[16:19], v12 offset:32768\n\t"
        "ds_read_b128 v[20:23], v13 offset:32768\n\t"
        "ds_read_b128 v[24:27], v12 offset:34816\n\t"
        "ds_read_b128 v[64:67], v14 offset:0\n\t"
        "ds_read_b128 v[68:71], v14 offset:1024\n\t"
        "s_add_u32 s50, s50, 0x80000\n\t"
        "s_add_u32 s51, s51, 1\n\t"
        "s_and_b32 s70, s51, 3\n\t"
        "s_cselect_b32 s70, 0, 0x2000\n\t"
        "s_xor_b32 s50, s50, s70\n\t"
        "s_waitcnt lgkmcnt(0)\n\t"
        "v_add_f32 v64, v64, v48\n\t"
        "v_add_f32 v65, v65, v49\n\t"
        "v_add_f32 v66, v66, v50\n\t"
        "v_add_f32 v67, v67, v51\n\t"
        "v_add_f32 v68, v68, v52\n\t"
        "v_add_f32 v69, v69, v53\n\t"
        "v_add_f32 v70, v70, v54\n\t"
        "v_add_f32 v71, v71, v55\n\t"
        "v_cvt_pk_bf16_f32 v72, v64, v65\n\t"
        "v_cvt_pk_bf16_f32 v73, v68, v69\n\t"
        "v_cvt_pk_bf16_f32 v74, v66, v67\n\t"
        "v_cvt_pk_bf16_f32 v75, v70, v71\n\t"
        "s_mov_b64 exec, s[56:57]\n\t"
        "v_swap_b32 v73, v74\n\t"
        "s_nop 1\n\t"
        "v_permlane16_swap_b32 v72, v74\n\t"
        "v_permlane16_swap_b32 v73, v75\n\t"
        "s_mov_b64 exec, -1\n\t"
        "buffer_store_dwordx4 v[72:75], v79, %[rs], s50 offen sc1\n\t"
        "s_add_u32 m0, s47, 65536\n\t"
        "s_nop 0\n\t"
        "buffer_load_dwordx4 v4, %[rx], s49 offen offset:0 lds\n\t"
        "buffer_load_dwordx4 v5, %[rx], s49 offen offset:1024 lds\n\t"
        "buffer_load_dwordx4 v6, %[rx], s49 offen offset:2048 lds\n\t"
        "buffer_load_dwordx4 v7, %[rx], s49 offen offset:3072 lds\n\t"
        "s_add_u32 s49, s49, 32768\n\t"
        "ds_read_b128 v[28:31], v13 offset:34816\n\t"
        "v_mfma_f32_16x16x32_bf16 v[56:59], v[128:131], v[16:19], 0\n\t"
        "v_mfma_f32_16x16x32_bf16 v[60:63], v[132:135], v[16:19], 0\n\t"
        "ds_read_b128 v[16:19], v12 offset:36864\n\t"
        "v_mfma_f32_16x16x32_bf16 v[56:59], v[136:139], v[20:23], v[56:59]\n\t"
        "v_mfma_f32_16x16x32_bf16 v[60:63], v[140:143], v[20:23], v[60:63]\n\t"
        "ds_read_b128 v[20:23], v13 offset:36864\n\t"
        "v_mfma_f32_16x16x32_bf16 v[56:59], v[144:147], v[24:27], v[56:59]\n\t"
        "v_mfma_f32_16x16x32_bf16 v[60:63], v[148:151], v[24:27], v[60:63]\n\t"
        "ds_read_b128 v[24:27], v12 offset:38912\n\t"
        "s_waitcnt lgkmcnt(3)\n\t"
        "v_mfma_f32_16x16x32_bf16 v[56:59], v[152:155], v[28:31], v[56:59]\n\t"
        "v_mfma_f32_16x16x32_bf16 v[60:63], v[156:159], v[28:31], v[60:63]\n\t"
        "ds_read_b128 v[28:31], v13 offset:38912\n\t"
        "s_waitcnt lgkmcnt(3)\n\t"
        "v_mfma_f32_16x16x32_bf16 v[56:59], v[160:163], v[16:19], v[56:59]\n\t"
        "v_mfma_f32_16x16x32_bf16 v[60:63], v[164:167], v[16:19], v[60:63]\n\t"
        "ds_read_b128 v[16:19], v12 offset:40960\n\t"
        "s_waitcnt lgkmcnt(3)\n\t"
        "v_mfma_f32_16x16x32_bf16 v[56:59], v[168:171], v[20:23], v[56:59]\n\t"
        "v_mfma_f32_16x16x32_bf16 v[60:63], v[172:175], v[20:23], v[60:63]\n\t"
        "ds_read_b128 v[20:23], v13 offset:40960\n\t"
        "s_waitcnt lgkmcnt(3)\n\t"
        "v_mfma_f32_16x16x32_bf16 v[56:59], v[176:179], v[24:27], v[56:59]\n\t"
        "v_mfma_f32_16x16x32_bf16 v[60:63], v[180:183], v[24:27], v[60:63]\n\t"
        "ds_read_b128 v[24:27], v12 offset:43008\n\t"
        "s_waitcnt lgkmcnt(3)\n\t"
        "v_mfma_f32_16x16x32_bf16 v[56:59], v[184:187], v[28:31], v[56:59]\n\t"
        "v_mfma_f32_16x16x32_bf16 v[60:63], v[188:191], v[28:31], v[60:63]\n\t"
        "ds_read_b128 v[28:31], v13 offset:43008\n\t"
        "s_waitcnt lgkmcnt(3)\n\t"
        "v_mfma_f32_16x16x32_bf16 v[56:59], v[192:195], v[16:19], v[56:59]\n\t"
        "v_mfma_f32_16x16x32_bf16 v[60:63], v[196:199], v[16:19], v[60:63]\n\t"
        "ds_read_b128 v[16:19], v12 offset:45056\n\t"
        "s_waitcnt lgkmcnt(3)\n\t"
        "v_mfma_f32_16x16x32_bf16 v[56:59], v[200:203], v[20:23], v[56:59]\n\t"
        "v_mfma_f32_16x16x32_bf16 v[60:63], v[204:207], v[20:23], v[60:63]\n\t"
        "ds_read_b128 v[20:23], v13 offset:45056\n\t"
        "s_waitcnt lgkmcnt(3)\n\t"
        "v_mfma_f32_16x16x32_bf16 v[56:59], v[208:211], v[24:27], v[56:59]\n\t"
        "v_mfma_f32_16x16x32_bf16 v[60:63], v[212:215], v[24:27], v[60:63]\n\t"
        "ds_read_b128 v[24:27], v12 offset:47104\n\t"
        "s_waitcnt lgkmcnt(3)\n\t"
        "v_mfma_f32_16x16x32_bf16 v[56:59], v[216:219], v[28:31], v[56:59]\n\t"
        "v_mfma_f32_16x16x32_bf16 v[60:63], v[220:223], v[28:31], v[60:63]\n\t"
        "ds_read_b128 v[28:31], v13 offset:47104\n\t"
        "s_waitcnt lgkmcnt(3)\n\t"
        "v_mfma_f32_16x16x32_bf16 v[56:59], v[224:227], v[16:19], v[56:59]\n\t"
        "v_mfma_f32_16x16x32_bf16 v[60:63], v[228:231], v[16:19], v[60:63]\n\t"
        "s_waitcnt lgkmcnt(2)\n\t"
        "v_mfma_f32_16x16x32_bf16 v[56:59], v[232:235], v[20:23], v[56:59]\n\t"
        "v_mfma_f32_16x16x32_bf16 v[60:63], v[236:239], v[20:23], v[60:63]\n\t"
        "s_waitcnt lgkmcnt(1)\n\t"
        "v_mfma_f32_16x16x32_bf16 v[56:59], v[240:243], v[24:27], v[56:59]\n\t"
        "v_mfma_f32_16x16x32_bf16 v[60:63], v[244:247], v[24:27], v[60:63]\n\t"
        "s_waitcnt lgkmcnt(0)\n\t"
        "v_mfma_f32_16x16x32_bf16 v[56:59], v[248:251], v[28:31], v[56:59]\n\t"
        "v_mfma_f32_16x16x32_bf16 v[60:63], v[252:255], v[28:31], v[60:63]\n\t"
        "s_nop 7\n\t"
        "ds_write_b128 v14, v[56:59] offset:8192\n\t"
        "ds_write_b128 v14, v[60:63] offset:9216\n\t"
        "s_waitcnt vmcnt(4) lgkmcnt(0)\n\t"
        "s_barrier\n\t"
        "ds_read_b128 v[16:19], v10 offset:0\n\t"
        "ds_read_b128 v[20:23], v11 offset:0\n\t"
        "ds_read_b128 v[24:27], v10 offset:2048\n\t"
        "s_add_u32 m0, s47, 98304\n\t"
        "s_nop 0\n\t"
        "buffer_load_dwordx4 v4, %[rx], s49 offen offset:0 lds\n\t"
        "buffer_load_dwordx4 v5, %[rx], s49 offen offset:1024 lds\n\t"
        "buffer_load_dwordx4 v6, %[rx], s49 offen offset:2048 lds\n\t"
        "buffer_load_dwordx4 v7, %[rx], s49 offen offset:3072 lds\n\t"
        "s_add_u32 s49, s49, 32768\n\t"
        "ds_read_b128 v[28:31], v11 offset:2048\n\t"
        "s_waitcnt lgkmcnt(3)\n\t"
        "v_mfma_f32_16x16x32_bf16 v[32:35], v[128:131], v[16:19], 0\n\t"
        "v_mfma_f32_16x16x32_bf16 v[36:39], v[132:135], v[16:19], 0\n\t"
        "ds_read_b128 v[16:19], v10 offset:4096\n\t"
        "s_waitcnt lgkmcnt(3)\n\t"
        "v_mfma_f32_16x16x32_bf16 v[32:35], v[136:139], v[20:23], v[32:35]\n\t"
        "v_mfma_f32_16x16x32_bf16 v[36:39], v[140:143], v[20:23], v[36:39]\n\t"
        "ds_read_b128 v[20:23], v11 offset:4096\n\t"
        "s_waitcnt lgkmcnt(3)\n\t"
        "v_mfma_f32_16x16x32_bf16 v[32:35], v[144:147], v[24:27], v[32:35]\n\t"
        "v_mfma_f32_16x16x32_bf16 v[36:39], v[148:151], v[24:27], v[36:39]\n\t"
        "ds_read_b128 v[24:27], v10 offset:6144\n\t"
        "s_waitcnt lgkmcnt(3)\n\t"
        "v_mfma_f32_16x16x32_bf16 v[32:35], v[152:155], v[28:31], v[32:35]\n\t"
        "v_mfma_f32_16x16x32_bf16 v[36:39], v[156:159], v[28:31], v[36:39]\n\t"
        "ds_read_b128 v[28:31], v11 offset:6144\n\t"
        "s_waitcnt lgkmcnt(3)\n\t"
        "v_mfma_f32_16x16x32_bf16 v[32:35], v[160:163], v[16:19], v[32:35]\n\t"
        "v_mfma_f32_16x16x32_bf16 v[36:39], v[164:167], v[16:19], v[36:39]\n\t"
        "ds_read_b128 v[16:19], v10 offset:8192\n\t"
        "s_waitcnt lgkmcnt(3)\n\t"
        "v_mfma_f32_16x16x32_bf16 v[32:35], v[168:171], v[20:23], v[32:35]\n\t"
        "v_mfma_f32_16x16x32_bf16 v[36:39], v[172:175], v[20:23], v[36:39]\n\t"
        "ds_read_b128 v[20:23], v11 offset:8192\n\t"
        "s_waitcnt lgkmcnt(3)\n\t"
        "v_mfma_f32_16x16x32_bf16 v[32:35], v[176:179], v[24:27], v[32:35]\n\t"
        "v_mfma_f32_16x16x32_bf16 v[36:39], v[180:183], v[24:27], v[36:39]\n\t"
        "ds_read_b128 v[24:27], v10 offset:10240\n\t"
        "s_waitcnt lgkmcnt(3)\n\t"
        "v_mfma_f32_16x16x32_bf16 v[32:35], v[184:187], v[28:31], v[32:35]\n\t"
        "v_mfma_f32_16x16x32_bf16 v[36:39], v[188:191], v[28:31], v[36:39]\n\t"
        "ds_read_b128 v[28:31], v11 offset:10240\n\t"
        "s_waitcnt lgkmcnt(3)\n\t"
        "v_mfma_f32_16x16x32_bf16 v[32:35], v[192:195], v[16:19], v[32:35]\n\t"
        "v_mfma_f32_16x16x32_bf16 v[36:39], v[196:199], v[16:19], v[36:39]\n\t"
        "ds_read_b128 v[16:19], v10 offset:12288\n\t"
        "s_waitcnt lgkmcnt(3)\n\t"
        "v_mfma_f32_16x16x32_bf16 v[32:35], v[200:203], v[20:23], v[32:35]\n\t"
        "v_mfma_f32_16x16x32_bf16 v[36:39], v[204:207], v[20:23], v[36:39]\n\t"
        "ds_read_b128 v[20:23], v11 offset:12288\n\t"
        "s_waitcnt lgkmcnt(3)\n\t"
        "v_mfma_f32_16x16x32_bf16 v[32:35], v[208:211], v[24:27], v[32:35]\n\t"
        "v_mfma_f32_16x16x32_bf16 v[36:39], v[212:215], v[24:27], v[36:39]\n\t"
        "ds_read_b128 v[24:27], v10 offset:14336\n\t"
        "s_waitcnt lgkmcnt(3)\n\t"
        "v_mfma_f32_16x16x32_bf16 v[32:35], v[216:219], v[28:31], v[32:35]\n\t"
        "v_mfma_f32_16x16x32_bf16 v[36:39], v[220:223], v[28:31], v[36:39]\n\t"
        "ds_read_b128 v[28:31], v11 offset:14336\n\t"
        "s_waitcnt lgkmcnt(3)\n\t"
        "v_mfma_f32_16x16x32_bf16 v[32:35], v[224:227], v[16:19], v[32:35]\n\t"
        "v_mfma_f32_16x16x32_bf16 v[36:39], v[228:231], v[16:19], v[36:39]\n\t"
        "s_waitcnt lgkmcnt(2)\n\t"
        "v_mfma_f32_16x16x32_bf16 v[32:35], v[232:235], v[20:23], v[32:35]\n\t"
        "v_mfma_f32_16x16x32_bf16 v[36:39], v[236:239], v[20:23], v[36:39]\n\t"
        "s_waitcnt lgkmcnt(1)\n\t"
        "v_mfma_f32_16x16x32_bf16 v[32:35], v[240:243], v[24:27], v[32:35]\n\t"
        "v_mfma_f32_16x16x32_bf16 v[36:39], v[244:247], v[24:27], v[36:39]\n\t"
        "s_waitcnt lgkmcnt(0)\n\t"
        "v_mfma_f32_16x16x32_bf16 v[32:35], v[248:251], v[28:31], v[32:35]\n\t"
        "v_mfma_f32_16x16x32_bf16 v[36:39], v[252:255], v[28:31], v[36:39]\n\t"
        "s_waitcnt vmcnt(4) lgkmcnt(0)\n\t"
        "s_barrier\n\t"
        "s_add_u32 s54, s54, 1\n\t"
        "v_mov_b32 v76, s54\n\t"
        "s_and_b32 s71, s54, 127\n\t"
        "s_lshl_b32 s71, s71, 7\n\t"
        "buffer_store_dwordx2 v[76:77], v78, %[rf], s71 offen sc1\n\t"
        "ds_read_b128 v[16:19], v10 offset:32768\n\t"
        "ds_read_b128 v[20:23], v11 offset:32768\n\t"
        "ds_read_b128 v[24:27], v10 offset:34816\n\t"
        "ds_read_b128 v[64:67], v14 offset:0\n\t"
        "ds_read_b128 v[68:71], v14 offset:1024\n\t"
        "s_add_u32 s50, s50, 0x80000\n\t"
        "s_add_u32 s51, s51, 1\n\t"
        "s_and_b32 s70, s51, 3\n\t"
        "s_cselect_b32 s70, 0, 0x2000\n\t"
        "s_xor_b32 s50, s50, s70\n\t"
        "s_waitcnt lgkmcnt(0)\n\t"
        "v_add_f32 v64, v64, v32\n\t"
        "v_add_f32 v65, v65, v33\n\t"
        "v_add_f32 v66, v66, v34\n\t"
        "v_add_f32 v67, v67, v35\n\t"
        "v_add_f32 v68, v68, v36\n\t"
        "v_add_f32 v69, v69, v37\n\t"
        "v_add_f32 v70, v70, v38\n\t"
        "v_add_f32 v71, v71, v39\n\t"
        "v_cvt_pk_bf16_f32 v72, v64, v65\n\t"
        "v_cvt_pk_bf16_f32 v73, v68, v69\n\t"
        "v_cvt_pk_bf16_f32 v74, v66, v67\n\t"
        "v_cvt_pk_bf16_f32 v75, v70, v71\n\t"
        "s_mov_b64 exec, s[56:57]\n\t"
        "v_swap_b32 v73, v74\n\t"
        "s_nop 1\n\t"
        "v_permlane16_swap_b32 v72, v74\n\t"
        "v_permlane16_swap_b32 v73, v75\n\t"
        "s_mov_b64 exec, -1\n\t"
        "buffer_store_dwordx4 v[72:75], v79, %[rs], s50 offen sc1\n\t"
        "s_add_u32 m0, s47, 0\n\t"
        "s_nop 0\n\t"
        "buffer_load_dwordx4 v4, %[rx], s49 offen offset:0 lds\n\t"
        "buffer_load_dwordx4 v5, %[rx], s49 offen offset:1024 lds\n\t"
        "buffer_load_dwordx4 v6, %[rx], s49 offen offset:2048 lds\n\t"
        "buffer_load_dwordx4 v7, %[rx], s49 offen offset:3072 lds\n\t"
        "s_add_u32 s49, s49, 32768\n\t"
        "ds_read_b128 v[28:31], v11 offset:34816\n\t"
        "v_mfma_f32_16x16x32_bf16 v[40:43], v[128:131], v[16:19], 0\n\t"
        "v_mfma_f32_16x16x32_bf16 v[44:47], v[132:135], v[16:19], 0\n\t"
        "ds_read_b128 v[16:19], v10 offset:36864\n\t"
        "v_mfma_f32_16x16x32_bf16 v[40:43], v[136:139], v[20:23], v[40:43]\n\t"
        "v_mfma_f32_16x16x32_bf16 v[44:47], v[140:143], v[20:23], v[44:47]\n\t"
        "ds_read_b128 v[20:23], v11 offset:36864\n\t"
        "v_mfma_f32_16x16x32_bf16 v[40:43], v[144:147], v[24:27], v[40:43]\n\t"
        "v_mfma_f32_16x16x32_bf16 v[44:47], v[148:151], v[24:27], v[44:47]\n\t"
        "ds_read_b128 v[24:27], v10 offset:38912\n\t"
        "s_waitcnt lgkmcnt(3)\n\t"
        "v_mfma_f32_16x16x32_bf16 v[40:43], v[152:155], v[28:31], v[40:43]\n\t"
        "v_mfma_f32_16x16x32_bf16 v[44:47], v[156:159], v[28:31], v[44:47]\n\t"
        "ds_read_b128 v[28:31], v11 offset:38912\n\t"
        "s_waitcnt lgkmcnt(3)\n\t"
        "v_mfma_f32_16x16x32_bf16 v[40:43], v[160:163], v[16:19], v[40:43]\n\t"
        "v_mfma_f32_16x16x32_bf16 v[44:47], v[164:167], v[16:19], v[44:47]\n\t"
        "ds_read_b128 v[16:19], v10 offset:40960\n\t"
        "s_waitcnt lgkmcnt(3)\n\t"
        "v_mfma_f32_16x16x32_bf16 v[40:43], v[168:171], v[20:23], v[40:43]\n\t"
        "v_mfma_f32_16x16x32_bf16 v[44:47], v[172:175], v[20:23], v[44:47]\n\t"
        "ds_read_b128 v[20:23], v11 offset:40960\n\t"
        "s_waitcnt lgkmcnt(3)\n\t"
        "v_mfma_f32_16x16x32_bf16 v[40:43], v[176:179], v[24:27], v[40:43]\n\t"
        "v_mfma_f32_16x16x32_bf16 v[44:47], v[180:183], v[24:27], v[44:47]\n\t"
        "ds_read_b128 v[24:27], v10 offset:43008\n\t"
        "s_waitcnt lgkmcnt(3)\n\t"
        "v_mfma_f32_16x16x32_bf16 v[40:43], v[184:187], v[28:31], v[40:43]\n\t"
        "v_mfma_f32_16x16x32_bf16 v[44:47], v[188:191], v[28:31], v[44:47]\n\t"
        "ds_read_b128 v[28:31], v11 offset:43008\n\t"
        "s_waitcnt lgkmcnt(3)\n\t"
        "v_mfma_f32_16x16x32_bf16 v[40:43], v[192:195], v[16:19], v[40:43]\n\t"
        "v_mfma_f32_16x16x32_bf16 v[44:47], v[196:199], v[16:19], v[44:47]\n\t"
        "ds_read_b128 v[16:19], v10 offset:45056\n\t"
        "s_waitcnt lgkmcnt(3)\n\t"
        "v_mfma_f32_16x16x32_bf16 v[40:43], v[200:203], v[20:23], v[40:43]\n\t"
        "v_mfma_f32_16x16x32_bf16 v[44:47], v[204:207], v[20:23], v[44:47]\n\t"
        "ds_read_b128 v[20:23], v11 offset:45056\n\t"
        "s_waitcnt lgkmcnt(3)\n\t"
        "v_mfma_f32_16x16x32_bf16 v[40:43], v[208:211], v[24:27], v[40:43]\n\t"
        "v_mfma_f32_16x16x32_bf16 v[44:47], v[212:215], v[24:27], v[44:47]\n\t"
        "ds_read_b128 v[24:27], v10 offset:47104\n\t"
        "s_waitcnt lgkmcnt(3)\n\t"
        "v_mfma_f32_16x16x32_bf16 v[40:43], v[216:219], v[28:31], v[40:43]\n\t"
        "v_mfma_f32_16x16x32_bf16 v[44:47], v[220:223], v[28:31], v[44:47]\n\t"
        "ds_read_b128 v[28:31], v11 offset:47104\n\t"
        "s_waitcnt lgkmcnt(3)\n\t"
        "v_mfma_f32_16x16x32_bf16 v[40:43], v[224:227], v[16:19], v[40:43]\n\t"
        "v_mfma_f32_16x16x32_bf16 v[44:47], v[228:231], v[16:19], v[44:47]\n\t"
        "s_waitcnt lgkmcnt(2)\n\t"
        "v_mfma_f32_16x16x32_bf16 v[40:43], v[232:235], v[20:23], v[40:43]\n\t"
        "v_mfma_f32_16x16x32_bf16 v[44:47], v[236:239], v[20:23], v[44:47]\n\t"
        "s_waitcnt lgkmcnt(1)\n\t"
        "v_mfma_f32_16x16x32_bf16 v[40:43], v[240:243], v[24:27], v[40:43]\n\t"
        "v_mfma_f32_16x16x32_bf16 v[44:47], v[244:247], v[24:27], v[44:47]\n\t"
        "s_waitcnt lgkmcnt(0)\n\t"
        "v_mfma_f32_16x16x32_bf16 v[40:43], v[248:251], v[28:31], v[40:43]\n\t"
        "v_mfma_f32_16x16x32_bf16 v[44:47], v[252:255], v[28:31], v[44:47]\n\t"
        "s_nop 7\n\t"
        "ds_write_b128 v14, v[40:43] offset:8192\n\t"
        "ds_write_b128 v14, v[44:47] offset:9216\n\t"
        "s_waitcnt vmcnt(4) lgkmcnt(0)\n\t"
        "s_barrier\n\t"
        "ds_read_b128 v[16:19], v12 offset:0\n\t"
        "ds_read_b128 v[20:23], v13 offset:0\n\t"
        "ds_read_b128 v[24:27], v12 offset:2048\n\t"
        "s_add_u32 m0, s47, 32768\n\t"
        "s_nop 0\n\t"
        "buffer_load_dwordx4 v4, %[rx], s49 offen offset:0 lds\n\t"
        "buffer_load_dwordx4 v5, %[rx], s49 offen offset:1024 lds\n\t"
        "buffer_load_dwordx4 v6, %[rx], s49 offen offset:2048 lds\n\t"
        "buffer_load_dwordx4 v7, %[rx], s49 offen offset:3072 lds\n\t"
        "s_add_u32 s49, s49, 32768\n\t"
        "ds_read_b128 v[28:31], v13 offset:2048\n\t"
        "s_waitcnt lgkmcnt(3)\n\t"
        "v_mfma_f32_16x16x32_bf16 v[48:51], v[128:131], v[16:19], 0\n\t"
        "v_mfma_f32_16x16x32_bf16 v[52:55], v[132:135], v[16:19], 0\n\t"
        "ds_read_b128 v[16:19], v12 offset:4096\n\t"
        "s_waitcnt lgkmcnt(3)\n\t"
        "v_mfma_f32_16x16x32_bf16 v[48:51], v[136:139], v[20:23], v[48:51]\n\t"
        "v_mfma_f32_16x16x32_bf16 v[52:55], v[140:143], v[20:23], v[52:55]\n\t"
        "ds_read_b128 v[20:23], v13 offset:4096\n\t"
        "s_waitcnt lgkmcnt(3)\n\t"
        "v_mfma_f32_16x16x32_bf16 v[48:51], v[144:147], v[24:27], v[48:51]\n\t"
        "v_mfma_f32_16x16x32_bf16 v[52:55], v[148:151], v[24:27], v[52:55]\n\t"
        "ds_read_b128 v[24:27], v12 offset:6144\n\t"
        "s_waitcnt lgkmcnt(3)\n\t"
        "v_mfma_f32_16x16x32_bf16 v[48:51], v[152:155], v[28:31], v[48:51]\n\t"
        "v_mfma_f32_16x16x32_bf16 v[52:55], v[156:159], v[28:31], v[52:55]\n\t"
        "ds_read_b128 v[28:31], v13 offset:6144\n\t"
        "s_waitcnt lgkmcnt(3)\n\t"
        "v_mfma_f32_16x16x32_bf16 v[48:51], v[160:163], v[16:19], v[48:51]\n\t"
        "v_mfma_f32_16x16x32_bf16 v[52:55], v[164:167], v[16:19], v[52:55]\n\t"
        "ds_read_b128 v[16:19], v12 offset:8192\n\t"
        "s_waitcnt lgkmcnt(3)\n\t"
        "v_mfma_f32_16x16x32_bf16 v[48:51], v[168:171], v[20:23], v[48:51]\n\t"
        "v_mfma_f32_16x16x32_bf16 v[52:55], v[172:175], v[20:23], v[52:55]\n\t"
        "ds_read_b128 v[20:23], v13 offset:8192\n\t"
        "s_waitcnt lgkmcnt(3)\n\t"
        "v_mfma_f32_16x16x32_bf16 v[48:51], v[176:179], v[24:27], v[48:51]\n\t"
        "v_mfma_f32_16x16x32_bf16 v[52:55], v[180:183], v[24:27], v[52:55]\n\t"
        "ds_read_b128 v[24:27], v12 offset:10240\n\t"
        "s_waitcnt lgkmcnt(3)\n\t"
        "v_mfma_f32_16x16x32_bf16 v[48:51], v[184:187], v[28:31], v[48:51]\n\t"
        "v_mfma_f32_16x16x32_bf16 v[52:55], v[188:191], v[28:31], v[52:55]\n\t"
        "ds_read_b128 v[28:31], v13 offset:10240\n\t"
        "s_waitcnt lgkmcnt(3)\n\t"
        "v_mfma_f32_16x16x32_bf16 v[48:51], v[192:195], v[16:19], v[48:51]\n\t"
        "v_mfma_f32_16x16x32_bf16 v[52:55], v[196:199], v[16:19], v[52:55]\n\t"
        "ds_read_b128 v[16:19], v12 offset:12288\n\t"
        "s_waitcnt lgkmcnt(3)\n\t"
        "v_mfma_f32_16x16x32_bf16 v[48:51], v[200:203], v[20:23], v[48:51]\n\t"
        "v_mfma_f32_16x16x32_bf16 v[52:55], v[204:207], v[20:23], v[52:55]\n\t"
        "ds_read_b128 v[20:23], v13 offset:12288\n\t"
        "s_waitcnt lgkmcnt(3)\n\t"
        "v_mfma_f32_16x16x32_bf16 v[48:51], v[208:211], v[24:27], v[48:51]\n\t"
        "v_mfma_f32_16x16x32_bf16 v[52:55], v[212:215], v[24:27], v[52:55]\n\t"
        "ds_read_b128 v[24:27], v12 offset:14336\n\t"
        "s_waitcnt lgkmcnt(3)\n\t"
        "v_mfma_f32_16x16x32_bf16 v[48:51], v[216:219], v[28:31], v[48:51]\n\t"
        "v_mfma_f32_16x16x32_bf16 v[52:55], v[220:223], v[28:31], v[52:55]\n\t"
        "ds_read_b128 v[28:31], v13 offset:14336\n\t"
        "s_waitcnt lgkmcnt(3)\n\t"
        "v_mfma_f32_16x16x32_bf16 v[48:51], v[224:227], v[16:19], v[48:51]\n\t"
        "v_mfma_f32_16x16x32_bf16 v[52:55], v[228:231], v[16:19], v[52:55]\n\t"
        "s_waitcnt lgkmcnt(2)\n\t"
        "v_mfma_f32_16x16x32_bf16 v[48:51], v[232:235], v[20:23], v[48:51]\n\t"
        "v_mfma_f32_16x16x32_bf16 v[52:55], v[236:239], v[20:23], v[52:55]\n\t"
        "s_waitcnt lgkmcnt(1)\n\t"
        "v_mfma_f32_16x16x32_bf16 v[48:51], v[240:243], v[24:27], v[48:51]\n\t"
        "v_mfma_f32_16x16x32_bf16 v[52:55], v[244:247], v[24:27], v[52:55]\n\t"
        "s_waitcnt lgkmcnt(0)\n\t"
        "v_mfma_f32_16x16x32_bf16 v[48:51], v[248:251], v[28:31], v[48:51]\n\t"
        "v_mfma_f32_16x16x32_bf16 v[52:55], v[252:255], v[28:31], v[52:55]\n\t"
        "s_waitcnt vmcnt(4) lgkmcnt(0)\n\t"
        "s_barrier\n\t"
        "s_add_u32 s54, s54, 1\n\t"
        "v_mov_b32 v76, s54\n\t"
        "s_and_b32 s71, s54, 127\n\t"
        "s_lshl_b32 s71, s71, 7\n\t"
        "buffer_store_dwordx2 v[76:77], v78, %[rf], s71 offen sc1\n\t"
        "ds_read_b128 v[16:19], v12 offset:32768\n\t"
        "ds_read_b128 v[20:23], v13 offset:32768\n\t"
        "ds_read_b128 v[24:27], v12 offset:34816\n\t"
        "ds_read_b128 v[64:67], v14 offset:0\n\t"
        "ds_read_b128 v[68:71], v14 offset:1024\n\t"
        "s_add_u32 s50, s50, 0x80000\n\t"
        "s_add_u32 s51, s51, 1\n\t"
        "s_and_b32 s70, s51, 3\n\t"
        "s_cselect_b32 s70, 0, 0x2000\n\t"
        "s_xor_b32 s50, s50, s70\n\t"
        "s_waitcnt lgkmcnt(0)\n\t"
        "v_add_f32 v64, v64, v48\n\t"
        "v_add_f32 v65, v65, v49\n\t"
        "v_add_f32 v66, v66, v50\n\t"
        "v_add_f32 v67, v67, v51\n\t"
        "v_add_f32 v68, v68, v52\n\t"
        "v_add_f32 v69, v69, v53\n\t"
        "v_add_f32 v70, v70, v54\n\t"
        "v_add_f32 v71, v71, v55\n\t"
        "v_cvt_pk_bf16_f32 v72, v64, v65\n\t"
        "v_cvt_pk_bf16_f32 v73, v68, v69\n\t"
        "v_cvt_pk_bf16_f32 v74, v66, v67\n\t"
        "v_cvt_pk_bf16_f32 v75, v70, v71\n\t"
        "s_mov_b64 exec, s[56:57]\n\t"
        "v_swap_b32 v73, v74\n\t"
        "s_nop 1\n\t"
        "v_permlane16_swap_b32 v72, v74\n\t"
        "v_permlane16_swap_b32 v73, v75\n\t"
        "s_mov_b64 exec, -1\n\t"
        "buffer_store_dwordx4 v[72:75], v79, %[rs], s50 offen sc1\n\t"
        "s_add_u32 m0, s47, 65536\n\t"
        "s_nop 0\n\t"
        "buffer_load_dwordx4 v4, %[rx], s49 offen offset:0 lds\n\t"
        "buffer_load_dwordx4 v5, %[rx], s49 offen offset:1024 lds\n\t"
        "buffer_load_dwordx4 v6, %[rx], s49 offen offset:2048 lds\n\t"
        "buffer_load_dwordx4 v7, %[rx], s49 offen offset:3072 lds\n\t"
        "s_add_u32 s49, s49, 32768\n\t"
        "ds_read_b128 v[28:31], v13 offset:34816\n\t"
        "v_mfma_f32_16x16x32_bf16 v[56:59], v[128:131], v[16:19], 0\n\t"
        "v_mfma_f32_16x16x32_bf16 v[60:63], v[132:135], v[16:19], 0\n\t"
        "ds_read_b128 v[16:19], v12 offset:36864\n\t"
        "v_mfma_f32_16x16x32_bf16 v[56:59], v[136:139], v[20:23], v[56:59]\n\t"
        "v_mfma_f32_16x16x32_bf16 v[60:63], v[140:143], v[20:23], v[60:63]\n\t"
        "ds_read_b128 v[20:23], v13 offset:36864\n\t"
        "v_mfma_f32_16x16x32_bf16 v[56:59], v[144:147], v[24:27], v[56:59]\n\t"
        "v_mfma_f32_16x16x32_bf16 v[60:63], v[148:151], v[24:27], v[60:63]\n\t"
        "ds_read_b128 v[24:27], v12 offset:38912\n\t"
        "s_waitcnt lgkmcnt(3)\n\t"
        "v_mfma_f32_16x16x32_bf16 v[56:59], v[152:155], v[28:31], v[56:59]\n\t"
        "v_mfma_f32_16x16x32_bf16 v[60:63], v[156:159], v[28:31], v[60:63]\n\t"
        "ds_read_b128 v[28:31], v13 offset:38912\n\t"
        "s_waitcnt lgkmcnt(3)\n\t"
        "v_mfma_f32_16x16x32_bf16 v[56:59], v[160:163], v[16:19], v[56:59]\n\t"
        "v_mfma_f32_16x16x32_bf16 v[60:63], v[164:167], v[16:19], v[60:63]\n\t"
        "ds_read_b128 v[16:19], v12 offset:40960\n\t"
        "s_waitcnt lgkmcnt(3)\n\t"
        "v_mfma_f32_16x16x32_bf16 v[56:59], v[168:171], v[20:23], v[56:59]\n\t"
        "v_mfma_f32_16x16x32_bf16 v[60:63], v[172:175], v[20:23], v[60:63]\n\t"
        "ds_read_b128 v[20:23], v13 offset:40960\n\t"
        "s_waitcnt lgkmcnt(3)\n\t"
        "v_mfma_f32_16x16x32_bf16 v[56:59], v[176:179], v[24:27], v[56:59]\n\t"
        "v_mfma_f32_16x16x32_bf16 v[60:63], v[180:183], v[24:27], v[60:63]\n\t"
        "ds_read_b128 v[24:27], v12 offset:43008\n\t"
        "s_waitcnt lgkmcnt(3)\n\t"
        "v_mfma_f32_16x16x32_bf16 v[56:59], v[184:187], v[28:31], v[56:59]\n\t"
        "v_mfma_f32_16x16x32_bf16 v[60:63], v[188:191], v[28:31], v[60:63]\n\t"
        "ds_read_b128 v[28:31], v13 offset:43008\n\t"
        "s_waitcnt lgkmcnt(3)\n\t"
        "v_mfma_f32_16x16x32_bf16 v[56:59], v[192:195], v[16:19], v[56:59]\n\t"
        "v_mfma_f32_16x16x32_bf16 v[60:63], v[196:199], v[16:19], v[60:63]\n\t"
        "ds_read_b128 v[16:19], v12 offset:45056\n\t"
        "s_waitcnt lgkmcnt(3)\n\t"
        "v_mfma_f32_16x16x32_bf16 v[56:59], v[200:203], v[20:23], v[56:59]\n\t"
        "v_mfma_f32_16x16x32_bf16 v[60:63], v[204:207], v[20:23], v[60:63]\n\t"
        "ds_read_b128 v[20:23], v13 offset:45056\n\t"
        "s_waitcnt lgkmcnt(3)\n\t"
        "v_mfma_f32_16x16x32_bf16 v[56:59], v[208:211], v[24:27], v[56:59]\n\t"
        "v_mfma_f32_16x16x32_bf16 v[60:63], v[212:215], v[24:27], v[60:63]\n\t"
        "ds_read_b128 v[24:27], v12 offset:47104\n\t"
        "s_waitcnt lgkmcnt(3)\n\t"
        "v_mfma_f32_16x16x32_bf16 v[56:59], v[216:219], v[28:31], v[56:59]\n\t"
        "v_mfma_f32_16x16x32_bf16 v[60:63], v[220:223], v[28:31], v[60:63]\n\t"
        "ds_read_b128 v[28:31], v13 offset:47104\n\t"
        "s_waitcnt lgkmcnt(3)\n\t"
        "v_mfma_f32_16x16x32_bf16 v[56:59], v[224:227], v[16:19], v[56:59]\n\t"
        "v_mfma_f32_16x16x32_bf16 v[60:63], v[228:231], v[16:19], v[60:63]\n\t"
        "s_waitcnt lgkmcnt(2)\n\t"
        "v_mfma_f32_16x16x32_bf16 v[56:59], v[232:235], v[20:23], v[56:59]\n\t"
        "v_mfma_f32_16x16x32_bf16 v[60:63], v[236:239], v[20:23], v[60:63]\n\t"
        "s_waitcnt lgkmcnt(1)\n\t"
        "v_mfma_f32_16x16x32_bf16 v[56:59], v[240:243], v[24:27], v[56:59]\n\t"
        "v_mfma_f32_16x16x32_bf16 v[60:63], v[244:247], v[24:27], v[60:63]\n\t"
        "s_waitcnt lgkmcnt(0)\n\t"
        "v_mfma_f32_16x16x32_bf16 v[56:59], v[248:251], v[28:31], v[56:59]\n\t"
        "v_mfma_f32_16x16x32_bf16 v[60:63], v[252:255], v[28:31], v[60:63]\n\t"
        "s_nop 7\n\t"
        "ds_write_b128 v14, v[56:59] offset:8192\n\t"
        "ds_write_b128 v14, v[60:63] offset:9216\n\t"
        "s_waitcnt vmcnt(4) lgkmcnt(0)\n\t"
        "s_barrier\n\t"
        "ds_read_b128 v[16:19], v10 offset:0\n\t"
        "ds_read_b128 v[20:23], v11 offset:0\n\t"
        "ds_read_b128 v[24:27], v10 offset:2048\n\t"
        "s_add_u32 m0, s47, 98304\n\t"
        "s_nop 0\n\t"
        "buffer_load_dwordx4 v4, %[rx], s49 offen offset:0 lds\n\t"
        "buffer_load_dwordx4 v5, %[rx], s49 offen offset:1024 lds\n\t"
        "buffer_load_dwordx4 v6, %[rx], s49 offen offset:2048 lds\n\t"
        "buffer_load_dwordx4 v7, %[rx], s49 offen offset:3072 lds\n\t"
        "s_add_u32 s49, s49, 32768\n\t"
        "ds_read_b128 v[28:31], v11 offset:2048\n\t"
        "s_waitcnt lgkmcnt(3)\n\t"
        "v_mfma_f32_16x16x32_bf16 v[32:35], v[128:131], v[16:19], 0\n\t"
        "v_mfma_f32_16x16x32_bf16 v[36:39], v[132:135], v[16:19], 0\n\t"
        "ds_read_b128 v[16:19], v10 offset:4096\n\t"
        "s_waitcnt lgkmcnt(3)\n\t"
        "v_mfma_f32_16x16x32_bf16 v[32:35], v[136:139], v[20:23], v[32:35]\n\t"
        "v_mfma_f32_16x16x32_bf16 v[36:39], v[140:143], v[20:23], v[36:39]\n\t"
        "ds_read_b128 v[20:23], v11 offset:4096\n\t"
        "s_waitcnt lgkmcnt(3)\n\t"
        "v_mfma_f32_16x16x32_bf16 v[32:35], v[144:147], v[24:27], v[32:35]\n\t"
        "v_mfma_f32_16x16x32_bf16 v[36:39], v[148:151], v[24:27], v[36:39]\n\t"
        "ds_read_b128 v[24:27], v10 offset:6144\n\t"
        "s_waitcnt lgkmcnt(3)\n\t"
        "v_mfma_f32_16x16x32_bf16 v[32:35], v[152:155], v[28:31], v[32:35]\n\t"
        "v_mfma_f32_16x16x32_bf16 v[36:39], v[156:159], v[28:31], v[36:39]\n\t"
        "ds_read_b128 v[28:31], v11 offset:6144\n\t"
        "s_waitcnt lgkmcnt(3)\n\t"
        "v_mfma_f32_16x16x32_bf16 v[32:35], v[160:163], v[16:19], v[32:35]\n\t"
        "v_mfma_f32_16x16x32_bf16 v[36:39], v[164:167], v[16:19], v[36:39]\n\t"
        "ds_read_b128 v[16:19], v10 offset:8192\n\t"
        "s_waitcnt lgkmcnt(3)\n\t"
        "v_mfma_f32_16x16x32_bf16 v[32:35], v[168:171], v[20:23], v[32:35]\n\t"
        "v_mfma_f32_16x16x32_bf16 v[36:39], v[172:175], v[20:23], v[36:39]\n\t"
        "ds_read_b128 v[20:23], v11 offset:8192\n\t"
        "s_waitcnt lgkmcnt(3)\n\t"
        "v_mfma_f32_16x16x32_bf16 v[32:35], v[176:179], v[24:27], v[32:35]\n\t"
        "v_mfma_f32_16x16x32_bf16 v[36:39], v[180:183], v[24:27], v[36:39]\n\t"
        "ds_read_b128 v[24:27], v10 offset:10240\n\t"
        "s_waitcnt lgkmcnt(3)\n\t"
        "v_mfma_f32_16x16x32_bf16 v[32:35], v[184:187], v[28:31], v[32:35]\n\t"
        "v_mfma_f32_16x16x32_bf16 v[36:39], v[188:191], v[28:31], v[36:39]\n\t"
        "ds_read_b128 v[28:31], v11 offset:10240\n\t"
        "s_waitcnt lgkmcnt(3)\n\t"
        "v_mfma_f32_16x16x32_bf16 v[32:35], v[192:195], v[16:19], v[32:35]\n\t"
        "v_mfma_f32_16x16x32_bf16 v[36:39], v[196:199], v[16:19], v[36:39]\n\t"
        "ds_read_b128 v[16:19], v10 offset:12288\n\t"
        "s_waitcnt lgkmcnt(3)\n\t"
        "v_mfma_f32_16x16x32_bf16 v[32:35], v[200:203], v[20:23], v[32:35]\n\t"
        "v_mfma_f32_16x16x32_bf16 v[36:39], v[204:207], v[20:23], v[36:39]\n\t"
        "ds_read_b128 v[20:23], v11 offset:12288\n\t"
        "s_waitcnt lgkmcnt(3)\n\t"
        "v_mfma_f32_16x16x32_bf16 v[32:35], v[208:211], v[24:27], v[32:35]\n\t"
        "v_mfma_f32_16x16x32_bf16 v[36:39], v[212:215], v[24:27], v[36:39]\n\t"
        "ds_read_b128 v[24:27], v10 offset:14336\n\t"
        "s_waitcnt lgkmcnt(3)\n\t"
        "v_mfma_f32_16x16x32_bf16 v[32:35], v[216:219], v[28:31], v[32:35]\n\t"
        "v_mfma_f32_16x16x32_bf16 v[36:39], v[220:223], v[28:31], v[36:39]\n\t"
        "ds_read_b128 v[28:31], v11 offset:14336\n\t"
        "s_waitcnt lgkmcnt(3)\n\t"
        "v_mfma_f32_16x16x32_bf16 v[32:35], v[224:227], v[16:19], v[32:35]\n\t"
        "v_mfma_f32_16x16x32_bf16 v[36:39], v[228:231], v[16:19], v[36:39]\n\t"
        "s_waitcnt lgkmcnt(2)\n\t"
        "v_mfma_f32_16x16x32_bf16 v[32:35], v[232:235], v[20:23], v[32:35]\n\t"
        "v_mfma_f32_16x16x32_bf16 v[36:39], v[236:239], v[20:23], v[36:39]\n\t"
        "s_waitcnt lgkmcnt(1)\n\t"
        "v_mfma_f32_16x16x32_bf16 v[32:35], v[240:243], v[24:27], v[32:35]\n\t"
        "v_mfma_f32_16x16x32_bf16 v[36:39], v[244:247], v[24:27], v[36:39]\n\t"
        "s_waitcnt lgkmcnt(0)\n\t"
        "v_mfma_f32_16x16x32_bf16 v[32:35], v[248:251], v[28:31], v[32:35]\n\t"
        "v_mfma_f32_16x16x32_bf16 v[36:39], v[252:255], v[28:31], v[36:39]\n\t"
        "s_waitcnt vmcnt(4) lgkmcnt(0)\n\t"
        "s_barrier\n\t"
        "s_add_u32 s54, s54, 1\n\t"
        "v_mov_b32 v76, s54\n\t"
        "s_and_b32 s71, s54, 127\n\t"
        "s_lshl_b32 s71, s71, 7\n\t"
        "buffer_store_dwordx2 v[76:77], v78, %[rf], s71 offen sc1\n\t"
        "ds_read_b128 v[16:19], v10 offset:32768\n\t"
        "ds_read_b128 v[20:23], v11 offset:32768\n\t"
        "ds_read_b128 v[24:27], v10 offset:34816\n\t"
        "ds_read_b128 v[64:67], v14 offset:0\n\t"
        "ds_read_b128 v[68:71], v14 offset:1024\n\t"
        "s_add_u32 s50, s50, 0x80000\n\t"
        "s_add_u32 s51, s51, 1\n\t"
        "s_and_b32 s70, s51, 3\n\t"
        "s_cselect_b32 s70, 0, 0x2000\n\t"
        "s_xor_b32 s50, s50, s70\n\t"
        "s_waitcnt lgkmcnt(0)\n\t"
        "v_add_f32 v64, v64, v32\n\t"
        "v_add_f32 v65, v65, v33\n\t"
        "v_add_f32 v66, v66, v34\n\t"
        "v_add_f32 v67, v67, v35\n\t"
        "v_add_f32 v68, v68, v36\n\t"
        "v_add_f32 v69, v69, v37\n\t"
        "v_add_f32 v70, v70, v38\n\t"
        "v_add_f32 v71, v71, v39\n\t"
        "v_cvt_pk_bf16_f32 v72, v64, v65\n\t"
        "v_cvt_pk_bf16_f32 v73, v68, v69\n\t"
        "v_cvt_pk_bf16_f32 v74, v66, v67\n\t"
        "v_cvt_pk_bf16_f32 v75, v70, v71\n\t"
        "s_mov_b64 exec, s[56:57]\n\t"
        "v_swap_b32 v73, v74\n\t"
        "s_nop 1\n\t"
        "v_permlane16_swap_b32 v72, v74\n\t"
        "v_permlane16_swap_b32 v73, v75\n\t"
        "s_mov_b64 exec, -1\n\t"
        "buffer_store_dwordx4 v[72:75], v79, %[rs], s50 offen sc1\n\t"
        "s_add_u32 m0, s47, 0\n\t"
        "s_nop 0\n\t"
        "buffer_load_dwordx4 v4, %[rx], s49 offen offset:0 lds\n\t"
        "buffer_load_dwordx4 v5, %[rx], s49 offen offset:1024 lds\n\t"
        "buffer_load_dwordx4 v6, %[rx], s49 offen offset:2048 lds\n\t"
        "buffer_load_dwordx4 v7, %[rx], s49 offen offset:3072 lds\n\t"
        "s_add_u32 s49, s49, 32768\n\t"
        "ds_read_b128 v[28:31], v11 offset:34816\n\t"
        "v_mfma_f32_16x16x32_bf16 v[40:43], v[128:131], v[16:19], 0\n\t"
        "v_mfma_f32_16x16x32_bf16 v[44:47], v[132:135], v[16:19], 0\n\t"
        "ds_read_b128 v[16:19], v10 offset:36864\n\t"
        "v_mfma_f32_16x16x32_bf16 v[40:43], v[136:139], v[20:23], v[40:43]\n\t"
        "v_mfma_f32_16x16x32_bf16 v[44:47], v[140:143], v[20:23], v[44:47]\n\t"
        "ds_read_b128 v[20:23], v11 offset:36864\n\t"
        "v_mfma_f32_16x16x32_bf16 v[40:43], v[144:147], v[24:27], v[40:43]\n\t"
        "v_mfma_f32_16x16x32_bf16 v[44:47], v[148:151], v[24:27], v[44:47]\n\t"
        "ds_read_b128 v[24:27], v10 offset:38912\n\t"
        "s_waitcnt lgkmcnt(3)\n\t"
        "v_mfma_f32_16x16x32_bf16 v[40:43], v[152:155], v[28:31], v[40:43]\n\t"
        "v_mfma_f32_16x16x32_bf16 v[44:47], v[156:159], v[28:31], v[44:47]\n\t"
        "ds_read_b128 v[28:31], v11 offset:38912\n\t"
        "s_waitcnt lgkmcnt(3)\n\t"
        "v_mfma_f32_16x16x32_bf16 v[40:43], v[160:163], v[16:19], v[40:43]\n\t"
        "v_mfma_f32_16x16x32_bf16 v[44:47], v[164:167], v[16:19], v[44:47]\n\t"
        "ds_read_b128 v[16:19], v10 offset:40960\n\t"
        "s_waitcnt lgkmcnt(3)\n\t"
        "v_mfma_f32_16x16x32_bf16 v[40:43], v[168:171], v[20:23], v[40:43]\n\t"
        "v_mfma_f32_16x16x32_bf16 v[44:47], v[172:175], v[20:23], v[44:47]\n\t"
        "ds_read_b128 v[20:23], v11 offset:40960\n\t"
        "s_waitcnt lgkmcnt(3)\n\t"
        "v_mfma_f32_16x16x32_bf16 v[40:43], v[176:179], v[24:27], v[40:43]\n\t"
        "v_mfma_f32_16x16x32_bf16 v[44:47], v[180:183], v[24:27], v[44:47]\n\t"
        "ds_read_b128 v[24:27], v10 offset:43008\n\t"
        "s_waitcnt lgkmcnt(3)\n\t"
        "v_mfma_f32_16x16x32_bf16 v[40:43], v[184:187], v[28:31], v[40:43]\n\t"
        "v_mfma_f32_16x16x32_bf16 v[44:47], v[188:191], v[28:31], v[44:47]\n\t"
        "ds_read_b128 v[28:31], v11 offset:43008\n\t"
        "s_waitcnt lgkmcnt(3)\n\t"
        "v_mfma_f32_16x16x32_bf16 v[40:43], v[192:195], v[16:19], v[40:43]\n\t"
        "v_mfma_f32_16x16x32_bf16 v[44:47], v[196:199], v[16:19], v[44:47]\n\t"
        "ds_read_b128 v[16:19], v10 offset:45056\n\t"
        "s_waitcnt lgkmcnt(3)\n\t"
        "v_mfma_f32_16x16x32_bf16 v[40:43], v[200:203], v[20:23], v[40:43]\n\t"
        "v_mfma_f32_16x16x32_bf16 v[44:47], v[204:207], v[20:23], v[44:47]\n\t"
        "ds_read_b128 v[20:23], v11 offset:45056\n\t"
        "s_waitcnt lgkmcnt(3)\n\t"
        "v_mfma_f32_16x16x32_bf16 v[40:43], v[208:211], v[24:27], v[40:43]\n\t"
        "v_mfma_f32_16x16x32_bf16 v[44:47], v[212:215], v[24:27], v[44:47]\n\t"
        "ds_read_b128 v[24:27], v10 offset:47104\n\t"
        "s_waitcnt lgkmcnt(3)\n\t"
        "v_mfma_f32_16x16x32_bf16 v[40:43], v[216:219], v[28:31], v[40:43]\n\t"
        "v_mfma_f32_16x16x32_bf16 v[44:47], v[220:223], v[28:31], v[44:47]\n\t"
        "ds_read_b128 v[28:31], v11 offset:47104\n\t"
        "s_waitcnt lgkmcnt(3)\n\t"
        "v_mfma_f32_16x16x32_bf16 v[40:43], v[224:227], v[16:19], v[40:43]\n\t"
        "v_mfma_f32_16x16x32_bf16 v[44:47], v[228:231], v[16:19], v[44:47]\n\t"
        "s_waitcnt lgkmcnt(2)\n\t"
        "v_mfma_f32_16x16x32_bf16 v[40:43], v[232:235], v[20:23], v[40:43]\n\t"
        "v_mfma_f32_16x16x32_bf16 v[44:47], v[236:239], v[20:23], v[44:47]\n\t"
        "s_waitcnt lgkmcnt(1)\n\t"
        "v_mfma_f32_16x16x32_bf16 v[40:43], v[240:243], v[24:27], v[40:43]\n\t"
        "v_mfma_f32_16x16x32_bf16 v[44:47], v[244:247], v[24:27], v[44:47]\n\t"
        "s_waitcnt lgkmcnt(0)\n\t"
        "v_mfma_f32_16x16x32_bf16 v[40:43], v[248:251], v[28:31], v[40:43]\n\t"
        "v_mfma_f32_16x16x32_bf16 v[44:47], v[252:255], v[28:31], v[44:47]\n\t"
        "s_nop 7\n\t"
        "ds_write_b128 v14, v[40:43] offset:8192\n\t"
        "ds_write_b128 v14, v[44:47] offset:9216\n\t"
        "s_waitcnt vmcnt(4) lgkmcnt(0)\n\t"
        "s_barrier\n\t"
        "ds_read_b128 v[16:19], v12 offset:0\n\t"
        "ds_read_b128 v[20:23], v13 offset:0\n\t"
        "ds_read_b128 v[24:27], v12 offset:2048\n\t"
        "s_add_u32 m0, s47, 32768\n\t"
        "s_nop 0\n\t"
        "buffer_load_dwordx4 v4, %[rx], s49 offen offset:0 lds\n\t"
        "buffer_load_dwordx4 v5, %[rx], s49 offen offset:1024 lds\n\t"
        "buffer_load_dwordx4 v6, %[rx], s49 offen offset:2048 lds\n\t"
        "buffer_load_dwordx4 v7, %[rx], s49 offen offset:3072 lds\n\t"
        "s_add_u32 s49, s49, 32768\n\t"
        "ds_read_b128 v[28:31], v13 offset:2048\n\t"
        "s_waitcnt lgkmcnt(3)\n\t"
        "v_mfma_f32_16x16x32_bf16 v[48:51], v[128:131], v[16:19], 0\n\t"
        "v_mfma_f32_16x16x32_bf16 v[52:55], v[132:135], v[16:19], 0\n\t"
        "ds_read_b128 v[16:19], v12 offset:4096\n\t"
        "s_waitcnt lgkmcnt(3)\n\t"
        "v_mfma_f32_16x16x32_bf16 v[48:51], v[136:139], v[20:23], v[48:51]\n\t"
        "v_mfma_f32_16x16x32_bf16 v[52:55], v[140:143], v[20:23], v[52:55]\n\t"
        "ds_read_b128 v[20:23], v13 offset:4096\n\t"
        "s_waitcnt lgkmcnt(3)\n\t"
        "v_mfma_f32_16x16x32_bf16 v[48:51], v[144:147], v[24:27], v[48:51]\n\t"
        "v_mfma_f32_16x16x32_bf16 v[52:55], v[148:151], v[24:27], v[52:55]\n\t"
        "ds_read_b128 v[24:27], v12 offset:6144\n\t"
        "s_waitcnt lgkmcnt(3)\n\t"
        "v_mfma_f32_16x16x32_bf16 v[48:51], v[152:155], v[28:31], v[48:51]\n\t"
        "v_mfma_f32_16x16x32_bf16 v[52:55], v[156:159], v[28:31], v[52:55]\n\t"
        "ds_read_b128 v[28:31], v13 offset:6144\n\t"
        "s_waitcnt lgkmcnt(3)\n\t"
        "v_mfma_f32_16x16x32_bf16 v[48:51], v[160:163], v[16:19], v[48:51]\n\t"
        "v_mfma_f32_16x16x32_bf16 v[52:55], v[164:167], v[16:19], v[52:55]\n\t"
        "ds_read_b128 v[16:19], v12 offset:8192\n\t"
        "s_waitcnt lgkmcnt(3)\n\t"
        "v_mfma_f32_16x16x32_bf16 v[48:51], v[168:171], v[20:23], v[48:51]\n\t"
        "v_mfma_f32_16x16x32_bf16 v[52:55], v[172:175], v[20:23], v[52:55]\n\t"
        "ds_read_b128 v[20:23], v13 offset:8192\n\t"
        "s_waitcnt lgkmcnt(3)\n\t"
        "v_mfma_f32_16x16x32_bf16 v[48:51], v[176:179], v[24:27], v[48:51]\n\t"
        "v_mfma_f32_16x16x32_bf16 v[52:55], v[180:183], v[24:27], v[52:55]\n\t"
        "ds_read_b128 v[24:27], v12 offset:10240\n\t"
        "s_waitcnt lgkmcnt(3)\n\t"
        "v_mfma_f32_16x16x32_bf16 v[48:51], v[184:187], v[28:31], v[48:51]\n\t"
        "v_mfma_f32_16x16x32_bf16 v[52:55], v[188:191], v[28:31], v[52:55]\n\t"
        "ds_read_b128 v[28:31], v13 offset:10240\n\t"
        "s_waitcnt lgkmcnt(3)\n\t"
        "v_mfma_f32_16x16x32_bf16 v[48:51], v[192:195], v[16:19], v[48:51]\n\t"
        "v_mfma_f32_16x16x32_bf16 v[52:55], v[196:199], v[16:19], v[52:55]\n\t"
        "ds_read_b128 v[16:19], v12 offset:12288\n\t"
        "s_waitcnt lgkmcnt(3)\n\t"
        "v_mfma_f32_16x16x32_bf16 v[48:51], v[200:203], v[20:23], v[48:51]\n\t"
        "v_mfma_f32_16x16x32_bf16 v[52:55], v[204:207], v[20:23], v[52:55]\n\t"
        "ds_read_b128 v[20:23], v13 offset:12288\n\t"
        "s_waitcnt lgkmcnt(3)\n\t"
        "v_mfma_f32_16x16x32_bf16 v[48:51], v[208:211], v[24:27], v[48:51]\n\t"
        "v_mfma_f32_16x16x32_bf16 v[52:55], v[212:215], v[24:27], v[52:55]\n\t"
        "ds_read_b128 v[24:27], v12 offset:14336\n\t"
        "s_waitcnt lgkmcnt(3)\n\t"
        "v_mfma_f32_16x16x32_bf16 v[48:51], v[216:219], v[28:31], v[48:51]\n\t"
        "v_mfma_f32_16x16x32_bf16 v[52:55], v[220:223], v[28:31], v[52:55]\n\t"
        "ds_read_b128 v[28:31], v13 offset:14336\n\t"
        "s_waitcnt lgkmcnt(3)\n\t"
        "v_mfma_f32_16x16x32_bf16 v[48:51], v[224:227], v[16:19], v[48:51]\n\t"
        "v_mfma_f32_16x16x32_bf16 v[52:55], v[228:231], v[16:19], v[52:55]\n\t"
        "s_waitcnt lgkmcnt(2)\n\t"
        "v_mfma_f32_16x16x32_bf16 v[48:51], v[232:235], v[20:23], v[48:51]\n\t"
        "v_mfma_f32_16x16x32_bf16 v[52:55], v[236:239], v[20:23], v[52:55]\n\t"
        "s_waitcnt lgkmcnt(1)\n\t"
        "v_mfma_f32_16x16x32_bf16 v[48:51], v[240:243], v[24:27], v[48:51]\n\t"
        "v_mfma_f32_16x16x32_bf16 v[52:55], v[244:247], v[24:27], v[52:55]\n\t"
        "s_waitcnt lgkmcnt(0)\n\t"
        "v_mfma_f32_16x16x32_bf16 v[48:51], v[248:251], v[28:31], v[48:51]\n\t"
        "v_mfma_f32_16x16x32_bf16 v[52:55], v[252:255], v[28:31], v[52:55]\n\t"
        "s_waitcnt vmcnt(4) lgkmcnt(0)\n\t"
        "s_barrier\n\t"
        "s_add_u32 s54, s54, 1\n\t"
        "v_mov_b32 v76, s54\n\t"
        "s_and_b32 s71, s54, 127\n\t"
        "s_lshl_b32 s71, s71, 7\n\t"
        "buffer_store_dwordx2 v[76:77], v78, %[rf], s71 offen sc1\n\t"
        "ds_read_b128 v[16:19], v12 offset:32768\n\t"
        "ds_read_b128 v[20:23], v13 offset:32768\n\t"
        "ds_read_b128 v[24:27], v12 offset:34816\n\t"
        "ds_read_b128 v[64:67], v14 offset:0\n\t"
        "ds_read_b128 v[68:71], v14 offset:1024\n\t"
        "s_add_u32 s50, s50, 0x80000\n\t"
        "s_add_u32 s51, s51, 1\n\t"
        "s_and_b32 s70, s51, 3\n\t"
        "s_cselect_b32 s70, 0, 0x2000\n\t"
        "s_xor_b32 s50, s50, s70\n\t"
        "s_waitcnt lgkmcnt(0)\n\t"
        "v_add_f32 v64, v64, v48\n\t"
        "v_add_f32 v65, v65, v49\n\t"
        "v_add_f32 v66, v66, v50\n\t"
        "v_add_f32 v67, v67, v51\n\t"
        "v_add_f32 v68, v68, v52\n\t"
        "v_add_f32 v69, v69, v53\n\t"
        "v_add_f32 v70, v70, v54\n\t"
        "v_add_f32 v71, v71, v55\n\t"
        "v_cvt_pk_bf16_f32 v72, v64, v65\n\t"
        "v_cvt_pk_bf16_f32 v73, v68, v69\n\t"
        "v_cvt_pk_bf16_f32 v74, v66, v67\n\t"
        "v_cvt_pk_bf16_f32 v75, v70, v71\n\t"
        "s_mov_b64 exec, s[56:57]\n\t"
        "v_swap_b32 v73, v74\n\t"
        "s_nop 1\n\t"
        "v_permlane16_swap_b32 v72, v74\n\t"
        "v_permlane16_swap_b32 v73, v75\n\t"
        "s_mov_b64 exec, -1\n\t"
        "buffer_store_dwordx4 v[72:75], v79, %[rs], s50 offen sc1\n\t"
        "s_add_u32 m0, s47, 65536\n\t"
        "s_nop 0\n\t"
        "buffer_load_dwordx4 v4, %[rx], s49 offen offset:0 lds\n\t"
        "buffer_load_dwordx4 v5, %[rx], s49 offen offset:1024 lds\n\t"
        "buffer_load_dwordx4 v6, %[rx], s49 offen offset:2048 lds\n\t"
        "buffer_load_dwordx4 v7, %[rx], s49 offen offset:3072 lds\n\t"
        "s_add_u32 s49, s49, 32768\n\t"
        "ds_read_b128 v[28:31], v13 offset:34816\n\t"
        "v_mfma_f32_16x16x32_bf16 v[56:59], v[128:131], v[16:19], 0\n\t"
        "v_mfma_f32_16x16x32_bf16 v[60:63], v[132:135], v[16:19], 0\n\t"
        "ds_read_b128 v[16:19], v12 offset:36864\n\t"
        "v_mfma_f32_16x16x32_bf16 v[56:59], v[136:139], v[20:23], v[56:59]\n\t"
        "v_mfma_f32_16x16x32_bf16 v[60:63], v[140:143], v[20:23], v[60:63]\n\t"
        "ds_read_b128 v[20:23], v13 offset:36864\n\t"
        "v_mfma_f32_16x16x32_bf16 v[56:59], v[144:147], v[24:27], v[56:59]\n\t"
        "v_mfma_f32_16x16x32_bf16 v[60:63], v[148:151], v[24:27], v[60:63]\n\t"
        "ds_read_b128 v[24:27], v12 offset:38912\n\t"
        "s_waitcnt lgkmcnt(3)\n\t"
        "v_mfma_f32_16x16x32_bf16 v[56:59], v[152:155], v[28:31], v[56:59]\n\t"
        "v_mfma_f32_16x16x32_bf16 v[60:63], v[156:159], v[28:31], v[60:63]\n\t"
        "ds_read_b128 v[28:31], v13 offset:38912\n\t"
        "s_waitcnt lgkmcnt(3)\n\t"
        "v_mfma_f32_16x16x32_bf16 v[56:59], v[160:163], v[16:19], v[56:59]\n\t"
        "v_mfma_f32_16x16x32_bf16 v[60:63], v[164:167], v[16:19], v[60:63]\n\t"
        "ds_read_b128 v[16:19], v12 offset:40960\n\t"
        "s_waitcnt lgkmcnt(3)\n\t"
        "v_mfma_f32_16x16x32_bf16 v[56:59], v[168:171], v[20:23], v[56:59]\n\t"
        "v_mfma_f32_16x16x32_bf16 v[60:63], v[172:175], v[20:23], v[60:63]\n\t"
        "ds_read_b128 v[20:23], v13 offset:40960\n\t"
        "s_waitcnt lgkmcnt(3)\n\t"
        "v_mfma_f32_16x16x32_bf16 v[56:59], v[176:179], v[24:27], v[56:59]\n\t"
        "v_mfma_f32_16x16x32_bf16 v[60:63], v[180:183], v[24:27], v[60:63]\n\t"
        "ds_read_b128 v[24:27], v12 offset:43008\n\t"
        "s_waitcnt lgkmcnt(3)\n\t"
        "v_mfma_f32_16x16x32_bf16 v[56:59], v[184:187], v[28:31], v[56:59]\n\t"
        "v_mfma_f32_16x16x32_bf16 v[60:63], v[188:191], v[28:31], v[60:63]\n\t"
        "ds_read_b128 v[28:31], v13 offset:43008\n\t"
        "s_waitcnt lgkmcnt(3)\n\t"
        "v_mfma_f32_16x16x32_bf16 v[56:59], v[192:195], v[16:19], v[56:59]\n\t"
        "v_mfma_f32_16x16x32_bf16 v[60:63], v[196:199], v[16:19], v[60:63]\n\t"
        "ds_read_b128 v[16:19], v12 offset:45056\n\t"
        "s_waitcnt lgkmcnt(3)\n\t"
        "v_mfma_f32_16x16x32_bf16 v[56:59], v[200:203], v[20:23], v[56:59]\n\t"
        "v_mfma_f32_16x16x32_bf16 v[60:63], v[204:207], v[20:23], v[60:63]\n\t"
        "ds_read_b128 v[20:23], v13 offset:45056\n\t"
        "s_waitcnt lgkmcnt(3)\n\t"
        "v_mfma_f32_16x16x32_bf16 v[56:59], v[208:211], v[24:27], v[56:59]\n\t"
        "v_mfma_f32_16x16x32_bf16 v[60:63], v[212:215], v[24:27], v[60:63]\n\t"
        "ds_read_b128 v[24:27], v12 offset:47104\n\t"
        "s_waitcnt lgkmcnt(3)\n\t"
        "v_mfma_f32_16x16x32_bf16 v[56:59], v[216:219], v[28:31], v[56:59]\n\t"
        "v_mfma_f32_16x16x32_bf16 v[60:63], v[220:223], v[28:31], v[60:63]\n\t"
        "ds_read_b128 v[28:31], v13 offset:47104\n\t"
        "s_waitcnt lgkmcnt(3)\n\t"
        "v_mfma_f32_16x16x32_bf16 v[56:59], v[224:227], v[16:19], v[56:59]\n\t"
        "v_mfma_f32_16x16x32_bf16 v[60:63], v[228:231], v[16:19], v[60:63]\n\t"
        "s_waitcnt lgkmcnt(2)\n\t"
        "v_mfma_f32_16x16x32_bf16 v[56:59], v[232:235], v[20:23], v[56:59]\n\t"
        "v_mfma_f32_16x16x32_bf16 v[60:63], v[236:239], v[20:23], v[60:63]\n\t"
        "s_waitcnt lgkmcnt(1)\n\t"
        "v_mfma_f32_16x16x32_bf16 v[56:59], v[240:243], v[24:27], v[56:59]\n\t"
        "v_mfma_f32_16x16x32_bf16 v[60:63], v[244:247], v[24:27], v[60:63]\n\t"
        "s_waitcnt lgkmcnt(0)\n\t"
        "v_mfma_f32_16x16x32_bf16 v[56:59], v[248:251], v[28:31], v[56:59]\n\t"
        "v_mfma_f32_16x16x32_bf16 v[60:63], v[252:255], v[28:31], v[60:63]\n\t"
        "s_nop 7\n\t"
        "ds_write_b128 v14, v[56:59] offset:8192\n\t"
        "ds_write_b128 v14, v[60:63] offset:9216\n\t"
        "s_waitcnt vmcnt(4) lgkmcnt(0)\n\t"
        "s_barrier\n\t"
        "s_sub_u32 s50, s50, 0x80000\n\t"
        "s_and_b32 s70, s51, 3\n\t"
        "s_cselect_b32 s70, 0, 0x2000\n\t"
        "s_xor_b32 s50, s50, s70\n\t"
        "s_sub_u32 s51, s51, 1\n\t"
        "s_mov_b32 s52, 29\n\t"
        "s_sub_u32 s54, s54, 1\n\t"
        "ds_read_b128 v[16:19], v10 offset:0\n\t"
        "ds_read_b128 v[20:23], v11 offset:0\n\t"
        "ds_read_b128 v[24:27], v10 offset:2048\n\t"
        ".p2align 6\n\t"
        ".Lx_loop0_%=:\n\t"
        "ds_read_b128 v[28:31], v11 offset:2048\n\t"
        "s_waitcnt lgkmcnt(3)\n\t"
        "v_mfma_f32_16x16x32_bf16 v[32:35], v[128:131], v[16:19], 0\n\t"
        "s_add_u32 m0, s47, 98304\n\t"
        "ds_read_b128 v[64:67], v14 offset:0\n\t"
        "v_mfma_f32_16x16x32_bf16 v[36:39], v[132:135], v[16:19], 0\n\t"
        "ds_read_b128 v[68:71], v14 offset:1024\n\t"
        "ds_read_b128 v[16:19], v10 offset:4096\n\t"
        "s_waitcnt lgkmcnt(5)\n\t"
        "v_mfma_f32_16x16x32_bf16 v[32:35], v[136:139], v[20:23], v[32:35]\n\t"
        "buffer_load_dwordx4 v4, %[rx], s49 offen offset:0 lds\n\t"
        "ds_write_b128 v14, v[56:59] offset:8192\n\t"
        "v_mfma_f32_16x16x32_bf16 v[36:39], v[140:143], v[20:23], v[36:39]\n\t"
        "s_add_u32 s50, s50, 0x80000\n\t"
        "s_add_u32 s51, s51, 1\n\t"
        "ds_read_b128 v[20:23], v11 offset:4096\n\t"
        "s_waitcnt lgkmcnt(6)\n\t"
        "v_mfma_f32_16x16x32_bf16 v[32:35], v[144:147], v[24:27], v[32:35]\n\t"
        "ds_write_b128 v14, v[60:63] offset:9216\n\t"
        "s_and_b32 s70, s51, 3\n\t"
        "v_mfma_f32_16x16x32_bf16 v[36:39], v[148:151], v[24:27], v[36:39]\n\t"
        "s_cselect_b32 s70, 0, 0x2000\n\t"
        "ds_read_b128 v[24:27], v10 offset:6144\n\t"
        "s_waitcnt lgkmcnt(7)\n\t"
        "v_mfma_f32_16x16x32_bf16 v[32:35], v[152:155], v[28:31], v[32:35]\n\t"
        "buffer_load_dwordx4 v5, %[rx], s49 offen offset:1024 lds\n\t"
        "v_mfma_f32_16x16x32_bf16 v[36:39], v[156:159], v[28:31], v[36:39]\n\t"
        "s_xor_b32 s50, s50, s70\n\t"
        "ds_read_b128 v[28:31], v11 offset:6144\n\t"
        "s_waitcnt lgkmcnt(5)\n\t"
        "v_mfma_f32_16x16x32_bf16 v[32:35], v[160:163], v[16:19], v[32:35]\n\t"
        "v_add_f32 v64, v64, v48\n\t"
        "v_mfma_f32_16x16x32_bf16 v[36:39], v[164:167], v[16:19], v[36:39]\n\t"
        "v_add_f32 v65, v65, v49\n\t"
        "ds_read_b128 v[16:19], v10 offset:8192\n\t"
        "s_waitcnt lgkmcnt(4)\n\t"
        "v_mfma_f32_16x16x32_bf16 v[32:35], v[168:171], v[20:23], v[32:35]\n\t"
        "buffer_load_dwordx4 v6, %[rx], s49 offen offset:2048 lds\n\t"
        "v_add_f32 v66, v66, v50\n\t"
        "v_mfma_f32_16x16x32_bf16 v[36:39], v[172:175], v[20:23], v[36:39]\n\t"
        "v_add_f32 v67, v67, v51\n\t"
        "ds_read_b128 v[20:23], v11 offset:8192\n\t"
        "s_waitcnt lgkmcnt(3)\n\t"
        "v_mfma_f32_16x16x32_bf16 v[32:35], v[176:179], v[24:27], v[32:35]\n\t"
        "v_add_f32 v68, v68, v52\n\t"
        "v_mfma_f32_16x16x32_bf16 v[36:39], v[180:183], v[24:27], v[36:39]\n\t"
        "v_add_f32 v69, v69, v53\n\t"
        "ds_read_b128 v[24:27], v10 offset:10240\n\t"
        "s_waitcnt lgkmcnt(3)\n\t"
        "v_mfma_f32_16x16x32_bf16 v[32:35], v[184:187], v[28:31], v[32:35]\n\t"
        "buffer_load_dwordx4 v7, %[rx], s49 offen offset:3072 lds\n\t"
        "v_add_f32 v70, v70, v54\n\t"
        "v_mfma_f32_16x16x32_bf16 v[36:39], v[188:191], v[28:31], v[36:39]\n\t"
        "v_add_f32 v71, v71, v55\n\t"
        "ds_read_b128 v[28:31], v11 offset:10240\n\t"
        "s_waitcnt lgkmcnt(3)\n\t"
        "v_mfma_f32_16x16x32_bf16 v[32:35], v[192:195], v[16:19], v[32:35]\n\t"
        "s_add_u32 s49, s49, 32768\n\t"
        "v_cvt_pk_bf16_f32 v72, v64, v65\n\t"
        "v_mfma_f32_16x16x32_bf16 v[36:39], v[196:199], v[16:19], v[36:39]\n\t"
        "v_cvt_pk_bf16_f32 v73, v68, v69\n\t"
        "ds_read_b128 v[16:19], v10 offset:12288\n\t"
        "s_waitcnt lgkmcnt(3)\n\t"
        "v_mfma_f32_16x16x32_bf16 v[32:35], v[200:203], v[20:23], v[32:35]\n\t"
        "v_cvt_pk_bf16_f32 v74, v66, v67\n\t"
        "v_mfma_f32_16x16x32_bf16 v[36:39], v[204:207], v[20:23], v[36:39]\n\t"
        "v_cvt_pk_bf16_f32 v75, v70, v71\n\t"
        "ds_read_b128 v[20:23], v11 offset:12288\n\t"
        "s_waitcnt lgkmcnt(3)\n\t"
        "v_mfma_f32_16x16x32_bf16 v[32:35], v[208:211], v[24:27], v[32:35]\n\t"
        "s_mov_b64 exec, s[56:57]\n\t"
        "v_swap_b32 v73, v74\n\t"
        "s_nop 1\n\t"
        "v_permlane16_swap_b32 v72, v74\n\t"
        "v_permlane16_swap_b32 v73, v75\n\t"
        "s_mov_b64 exec, -1\n\t"
        "v_mfma_f32_16x16x32_bf16 v[36:39], v[212:215], v[24:27], v[36:39]\n\t"
        "buffer_store_dwordx4 v[72:75], v79, %[rs], s50 offen sc1\n\t"
        "ds_read_b128 v[24:27], v10 offset:14336\n\t"
        "s_waitcnt lgkmcnt(3)\n\t"
        "v_mfma_f32_16x16x32_bf16 v[32:35], v[216:219], v[28:31], v[32:35]\n\t"
        "v_mfma_f32_16x16x32_bf16 v[36:39], v[220:223], v[28:31], v[36:39]\n\t"
        "ds_read_b128 v[28:31], v11 offset:14336\n\t"
        "s_waitcnt lgkmcnt(3)\n\t"
        "v_mfma_f32_16x16x32_bf16 v[32:35], v[224:227], v[16:19], v[32:35]\n\t"
        "v_mfma_f32_16x16x32_bf16 v[36:39], v[228:231], v[16:19], v[36:39]\n\t"
        "ds_read_b128 v[16:19], v10 offset:32768\n\t"
        "s_waitcnt lgkmcnt(3)\n\t"
        "v_mfma_f32_16x16x32_bf16 v[32:35], v[232:235], v[20:23], v[32:35]\n\t"
        "v_mfma_f32_16x16x32_bf16 v[36:39], v[236:239], v[20:23], v[36:39]\n\t"
        "ds_read_b128 v[20:23], v11 offset:32768\n\t"
        "s_waitcnt lgkmcnt(3)\n\t"
        "v_mfma_f32_16x16x32_bf16 v[32:35], v[240:243], v[24:27], v[32:35]\n\t"
        "v_mfma_f32_16x16x32_bf16 v[36:39], v[244:247], v[24:27], v[36:39]\n\t"
        "ds_read_b128 v[24:27], v10 offset:34816\n\t"
        "s_waitcnt lgkmcnt(3)\n\t"
        "v_mfma_f32_16x16x32_bf16 v[32:35], v[248:251], v[28:31], v[32:35]\n\t"
        "v_mfma_f32_16x16x32_bf16 v[36:39], v[252:255], v[28:31], v[36:39]\n\t"
        "s_waitcnt vmcnt(5)\n\t"
        "s_barrier\n\t"
        "ds_read_b128 v[28:31], v11 offset:34816\n\t"
        "s_waitcnt lgkmcnt(3)\n\t"
        "v_mfma_f32_16x16x32_bf16 v[40:43], v[128:131], v[16:19], 0\n\t"
        "s_add_u32 m0, s47, 0\n\t"
        "v_mfma_f32_16x16x32_bf16 v[44:47], v[132:135], v[16:19], 0\n\t"
        "ds_read_b128 v[16:19], v10 offset:36864\n\t"
        "s_waitcnt lgkmcnt(3)\n\t"
        "v_mfma_f32_16x16x32_bf16 v[40:43], v[136:139], v[20:23], v[40:43]\n\t"
        "buffer_load_dwordx4 v4, %[rx], s49 offen offset:0 lds\n\t"
        "v_mfma_f32_16x16x32_bf16 v[44:47], v[140:143], v[20:23], v[44:47]\n\t"
        "ds_read_b128 v[20:23], v11 offset:36864\n\t"
        "s_waitcnt lgkmcnt(3)\n\t"
        "v_mfma_f32_16x16x32_bf16 v[40:43], v[144:147], v[24:27], v[40:43]\n\t"
        "v_mfma_f32_16x16x32_bf16 v[44:47], v[148:151], v[24:27], v[44:47]\n\t"
        "ds_read_b128 v[24:27], v10 offset:38912\n\t"
        "s_waitcnt lgkmcnt(3)\n\t"
        "v_mfma_f32_16x16x32_bf16 v[40:43], v[152:155], v[28:31], v[40:43]\n\t"
        "buffer_load_dwordx4 v5, %[rx], s49 offen offset:1024 lds\n\t"
        "v_mfma_f32_16x16x32_bf16 v[44:47], v[156:159], v[28:31], v[44:47]\n\t"
        "ds_read_b128 v[28:31], v11 offset:38912\n\t"
        "s_waitcnt lgkmcnt(3)\n\t"
        "v_mfma_f32_16x16x32_bf16 v[40:43], v[160:163], v[16:19], v[40:43]\n\t"
        "v_mfma_f32_16x16x32_bf16 v[44:47], v[164:167], v[16:19], v[44:47]\n\t"
        "ds_read_b128 v[16:19], v10 offset:40960\n\t"
        "s_waitcnt lgkmcnt(3)\n\t"
        "v_mfma_f32_16x16x32_bf16 v[40:43], v[168:171], v[20:23], v[40:43]\n\t"
        "buffer_load_dwordx4 v6, %[rx], s49 offen offset:2048 lds\n\t"
        "v_mfma_f32_16x16x32_bf16 v[44:47], v[172:175], v[20:23], v[44:47]\n\t"
        "ds_read_b128 v[20:23], v11 offset:40960\n\t"
        "s_waitcnt lgkmcnt(3)\n\t"
        "v_mfma_f32_16x16x32_bf16 v[40:43], v[176:179], v[24:27], v[40:43]\n\t"
        "v_mfma_f32_16x16x32_bf16 v[44:47], v[180:183], v[24:27], v[44:47]\n\t"
        "ds_read_b128 v[24:27], v10 offset:43008\n\t"
        "s_waitcnt lgkmcnt(3)\n\t"
        "v_mfma_f32_16x16x32_bf16 v[40:43], v[184:187], v[28:31], v[40:43]\n\t"
        "buffer_load_dwordx4 v7, %[rx], s49 offen offset:3072 lds\n\t"
        "v_mfma_f32_16x16x32_bf16 v[44:47], v[188:191], v[28:31], v[44:47]\n\t"
        "ds_read_b128 v[28:31], v11 offset:43008\n\t"
        "s_waitcnt lgkmcnt(3)\n\t"
        "v_mfma_f32_16x16x32_bf16 v[40:43], v[192:195], v[16:19], v[40:43]\n\t"
        "s_add_u32 s49, s49, 32768\n\t"
        "v_mfma_f32_16x16x32_bf16 v[44:47], v[196:199], v[16:19], v[44:47]\n\t"
        "s_add_u32 s54, s54, 1\n\t"
        "ds_read_b128 v[16:19], v10 offset:45056\n\t"
        "s_waitcnt lgkmcnt(3)\n\t"
        "v_mfma_f32_16x16x32_bf16 v[40:43], v[200:203], v[20:23], v[40:43]\n\t"
        "v_mov_b32 v76, s54\n\t"
        "v_mfma_f32_16x16x32_bf16 v[44:47], v[204:207], v[20:23], v[44:47]\n\t"
        "ds_read_b128 v[20:23], v11 offset:45056\n\t"
        "s_waitcnt lgkmcnt(3)\n\t"
        "v_mfma_f32_16x16x32_bf16 v[40:43], v[208:211], v[24:27], v[40:43]\n\t"
        "s_and_b32 s71, s54, 127\n\t"
        "s_cmp_le_u32 s71, 16\n\t"
        "s_cselect_b32 s72, 0, 3\n\t"
        "s_mul_i32 s71, s71, 128\n\t"
        "s_and_b32 s72, s72, s54\n\t"
        "s_cselect_b32 s71, 0x70000000, s71\n\t"
        "v_mfma_f32_16x16x32_bf16 v[44:47], v[212:215], v[24:27], v[44:47]\n\t"
        "ds_read_b128 v[24:27], v10 offset:47104\n\t"
        "s_waitcnt lgkmcnt(3)\n\t"
        "v_mfma_f32_16x16x32_bf16 v[40:43], v[216:219], v[28:31], v[40:43]\n\t"
        "buffer_store_dwordx2 v[76:77], v78, %[rf], s71 offen sc1\n\t"
        "v_mfma_f32_16x16x32_bf16 v[44:47], v[220:223], v[28:31], v[44:47]\n\t"
        "ds_read_b128 v[28:31], v11 offset:47104\n\t"
        "s_waitcnt lgkmcnt(3)\n\t"
        "v_mfma_f32_16x16x32_bf16 v[40:43], v[224:227], v[16:19], v[40:43]\n\t"
        "v_mfma_f32_16x16x32_bf16 v[44:47], v[228:231], v[16:19], v[44:47]\n\t"
        "ds_read_b128 v[16:19], v12 offset:0\n\t"
        "s_waitcnt lgkmcnt(3)\n\t"
        "v_mfma_f32_16x16x32_bf16 v[40:43], v[232:235], v[20:23], v[40:43]\n\t"
        "v_mfma_f32_16x16x32_bf16 v[44:47], v[236:239], v[20:23], v[44:47]\n\t"
        "ds_read_b128 v[20:23], v13 offset:0\n\t"
        "s_waitcnt lgkmcnt(3)\n\t"
        "v_mfma_f32_16x16x32_bf16 v[40:43], v[240:243], v[24:27], v[40:43]\n\t"
        "v_mfma_f32_16x16x32_bf16 v[44:47], v[244:247], v[24:27], v[44:47]\n\t"
        "ds_read_b128 v[24:27], v12 offset:2048\n\t"
        "s_waitcnt lgkmcnt(3)\n\t"
        "v_mfma_f32_16x16x32_bf16 v[40:43], v[248:251], v[28:31], v[40:43]\n\t"
        "v_mfma_f32_16x16x32_bf16 v[44:47], v[252:255], v[28:31], v[44:47]\n\t"
        "s_waitcnt vmcnt(5)\n\t"
        "s_barrier\n\t"
        "ds_read_b128 v[28:31], v13 offset:2048\n\t"
        "s_waitcnt lgkmcnt(3)\n\t"
        "v_mfma_f32_16x16x32_bf16 v[48:51], v[128:131], v[16:19], 0\n\t"
        "s_add_u32 m0, s47, 32768\n\t"
        "ds_read_b128 v[64:67], v14 offset:0\n\t"
        "v_mfma_f32_16x16x32_bf16 v[52:55], v[132:135], v[16:19], 0\n\t"
        "ds_read_b128 v[68:71], v14 offset:1024\n\t"
        "ds_read_b128 v[16:19], v12 offset:4096\n\t"
        "s_waitcnt lgkmcnt(5)\n\t"
        "v_mfma_f32_16x16x32_bf16 v[48:51], v[136:139], v[20:23], v[48:51]\n\t"
        "buffer_load_dwordx4 v4, %[rx], s49 offen offset:0 lds\n\t"
        "ds_write_b128 v14, v[40:43] offset:8192\n\t"
        "v_mfma_f32_16x16x32_bf16 v[52:55], v[140:143], v[20:23], v[52:55]\n\t"
        "s_add_u32 s50, s50, 0x80000\n\t"
        "s_add_u32 s51, s51, 1\n\t"
        "ds_read_b128 v[20:23], v13 offset:4096\n\t"
        "s_waitcnt lgkmcnt(6)\n\t"
        "v_mfma_f32_16x16x32_bf16 v[48:51], v[144:147], v[24:27], v[48:51]\n\t"
        "ds_write_b128 v14, v[44:47] offset:9216\n\t"
        "s_and_b32 s70, s51, 3\n\t"
        "v_mfma_f32_16x16x32_bf16 v[52:55], v[148:151], v[24:27], v[52:55]\n\t"
        "s_cselect_b32 s70, 0, 0x2000\n\t"
        "ds_read_b128 v[24:27], v12 offset:6144\n\t"
        "s_waitcnt lgkmcnt(7)\n\t"
        "v_mfma_f32_16x16x32_bf16 v[48:51], v[152:155], v[28:31], v[48:51]\n\t"
        "buffer_load_dwordx4 v5, %[rx], s49 offen offset:1024 lds\n\t"
        "v_mfma_f32_16x16x32_bf16 v[52:55], v[156:159], v[28:31], v[52:55]\n\t"
        "s_xor_b32 s50, s50, s70\n\t"
        "ds_read_b128 v[28:31], v13 offset:6144\n\t"
        "s_waitcnt lgkmcnt(5)\n\t"
        "v_mfma_f32_16x16x32_bf16 v[48:51], v[160:163], v[16:19], v[48:51]\n\t"
        "v_add_f32 v64, v64, v32\n\t"
        "v_mfma_f32_16x16x32_bf16 v[52:55], v[164:167], v[16:19], v[52:55]\n\t"
        "v_add_f32 v65, v65, v33\n\t"
        "ds_read_b128 v[16:19], v12 offset:8192\n\t"
        "s_waitcnt lgkmcnt(4)\n\t"
        "v_mfma_f32_16x16x32_bf16 v[48:51], v[168:171], v[20:23], v[48:51]\n\t"
        "buffer_load_dwordx4 v6, %[rx], s49 offen offset:2048 lds\n\t"
        "v_add_f32 v66, v66, v34\n\t"
        "v_mfma_f32_16x16x32_bf16 v[52:55], v[172:175], v[20:23], v[52:55]\n\t"
        "v_add_f32 v67, v67, v35\n\t"
        "ds_read_b128 v[20:23], v13 offset:8192\n\t"
        "s_waitcnt lgkmcnt(3)\n\t"
        "v_mfma_f32_16x16x32_bf16 v[48:51], v[176:179], v[24:27], v[48:51]\n\t"
        "v_add_f32 v68, v68, v36\n\t"
        "v_mfma_f32_16x16x32_bf16 v[52:55], v[180:183], v[24:27], v[52:55]\n\t"
        "v_add_f32 v69, v69, v37\n\t"
        "ds_read_b128 v[24:27], v12 offset:10240\n\t"
        "s_waitcnt lgkmcnt(3)\n\t"
        "v_mfma_f32_16x16x32_bf16 v[48:51], v[184:187], v[28:31], v[48:51]\n\t"
        "buffer_load_dwordx4 v7, %[rx], s49 offen offset:3072 lds\n\t"
        "v_add_f32 v70, v70, v38\n\t"
        "v_mfma_f32_16x16x32_bf16 v[52:55], v[188:191], v[28:31], v[52:55]\n\t"
        "v_add_f32 v71, v71, v39\n\t"
        "ds_read_b128 v[28:31], v13 offset:10240\n\t"
        "s_waitcnt lgkmcnt(3)\n\t"
        "v_mfma_f32_16x16x32_bf16 v[48:51], v[192:195], v[16:19], v[48:51]\n\t"
        "s_add_u32 s49, s49, 32768\n\t"
        "v_cvt_pk_bf16_f32 v72, v64, v65\n\t"
        "v_mfma_f32_16x16x32_bf16 v[52:55], v[196:199], v[16:19], v[52:55]\n\t"
        "v_cvt_pk_bf16_f32 v73, v68, v69\n\t"
        "ds_read_b128 v[16:19], v12 offset:12288\n\t"
        "s_waitcnt lgkmcnt(3)\n\t"
        "v_mfma_f32_16x16x32_bf16 v[48:51], v[200:203], v[20:23], v[48:51]\n\t"
        "v_cvt_pk_bf16_f32 v74, v66, v67\n\t"
        "v_mfma_f32_16x16x32_bf16 v[52:55], v[204:207], v[20:23], v[52:55]\n\t"
        "v_cvt_pk_bf16_f32 v75, v70, v71\n\t"
        "ds_read_b128 v[20:23], v13 offset:12288\n\t"
        "s_waitcnt lgkmcnt(3)\n\t"
        "v_mfma_f32_16x16x32_bf16 v[48:51], v[208:211], v[24:27], v[48:51]\n\t"
        "s_mov_b64 exec, s[56:57]\n\t"
        "v_swap_b32 v73, v74\n\t"
        "s_nop 1\n\t"
        "v_permlane16_swap_b32 v72, v74\n\t"
        "v_permlane16_swap_b32 v73, v75\n\t"
        "s_mov_b64 exec, -1\n\t"
        "v_mfma_f32_16x16x32_bf16 v[52:55], v[212:215], v[24:27], v[52:55]\n\t"
        "buffer_store_dwordx4 v[72:75], v79, %[rs], s50 offen sc1\n\t"
        "ds_read_b128 v[24:27], v12 offset:14336\n\t"
        "s_waitcnt lgkmcnt(3)\n\t"
        "v_mfma_f32_16x16x32_bf16 v[48:51], v[216:219], v[28:31], v[48:51]\n\t"
        "v_mfma_f32_16x16x32_bf16 v[52:55], v[220:223], v[28:31], v[52:55]\n\t"
        "ds_read_b128 v[28:31], v13 offset:14336\n\t"
        "s_waitcnt lgkmcnt(3)\n\t"
        "v_mfma_f32_16x16x32_bf16 v[48:51], v[224:227], v[16:19], v[48:51]\n\t"
        "v_mfma_f32_16x16x32_bf16 v[52:55], v[228:231], v[16:19], v[52:55]\n\t"
        "ds_read_b128 v[16:19], v12 offset:32768\n\t"
        "s_waitcnt lgkmcnt(3)\n\t"
        "v_mfma_f32_16x16x32_bf16 v[48:51], v[232:235], v[20:23], v[48:51]\n\t"
        "v_mfma_f32_16x16x32_bf16 v[52:55], v[236:239], v[20:23], v[52:55]\n\t"
        "ds_read_b128 v[20:23], v13 offset:32768\n\t"
        "s_waitcnt lgkmcnt(3)\n\t"
        "v_mfma_f32_16x16x32_bf16 v[48:51], v[240:243], v[24:27], v[48:51]\n\t"
        "v_mfma_f32_16x16x32_bf16 v[52:55], v[244:247], v[24:27], v[52:55]\n\t"
        "ds_read_b128 v[24:27], v12 offset:34816\n\t"
        "s_waitcnt lgkmcnt(3)\n\t"
        "v_mfma_f32_16x16x32_bf16 v[48:51], v[248:251], v[28:31], v[48:51]\n\t"
        "v_mfma_f32_16x16x32_bf16 v[52:55], v[252:255], v[28:31], v[52:55]\n\t"
        "s_waitcnt vmcnt(5)\n\t"
        "s_barrier\n\t"
        "ds_read_b128 v[28:31], v13 offset:34816\n\t"
        "s_waitcnt lgkmcnt(3)\n\t"
        "v_mfma_f32_16x16x32_bf16 v[56:59], v[128:131], v[16:19], 0\n\t"
        "s_add_u32 m0, s47, 65536\n\t"
        "v_mfma_f32_16x16x32_bf16 v[60:63], v[132:135], v[16:19], 0\n\t"
        "ds_read_b128 v[16:19], v12 offset:36864\n\t"
        "s_waitcnt lgkmcnt(3)\n\t"
        "v_mfma_f32_16x16x32_bf16 v[56:59], v[136:139], v[20:23], v[56:59]\n\t"
        "buffer_load_dwordx4 v4, %[rx], s49 offen offset:0 lds\n\t"
        "v_mfma_f32_16x16x32_bf16 v[60:63], v[140:143], v[20:23], v[60:63]\n\t"
        "ds_read_b128 v[20:23], v13 offset:36864\n\t"
        "s_waitcnt lgkmcnt(3)\n\t"
        "v_mfma_f32_16x16x32_bf16 v[56:59], v[144:147], v[24:27], v[56:59]\n\t"
        "v_mfma_f32_16x16x32_bf16 v[60:63], v[148:151], v[24:27], v[60:63]\n\t"
        "ds_read_b128 v[24:27], v12 offset:38912\n\t"
        "s_waitcnt lgkmcnt(3)\n\t"
        "v_mfma_f32_16x16x32_bf16 v[56:59], v[152:155], v[28:31], v[56:59]\n\t"
        "buffer_load_dwordx4 v5, %[rx], s49 offen offset:1024 lds\n\t"
        "v_mfma_f32_16x16x32_bf16 v[60:63], v[156:159], v[28:31], v[60:63]\n\t"
        "ds_read_b128 v[28:31], v13 offset:38912\n\t"
        "s_waitcnt lgkmcnt(3)\n\t"
        "v_mfma_f32_16x16x32_bf16 v[56:59], v[160:163], v[16:19], v[56:59]\n\t"
        "v_mfma_f32_16x16x32_bf16 v[60:63], v[164:167], v[16:19], v[60:63]\n\t"
        "ds_read_b128 v[16:19], v12 offset:40960\n\t"
        "s_waitcnt lgkmcnt(3)\n\t"
        "v_mfma_f32_16x16x32_bf16 v[56:59], v[168:171], v[20:23], v[56:59]\n\t"
        "buffer_load_dwordx4 v6, %[rx], s49 offen offset:2048 lds\n\t"
        "v_mfma_f32_16x16x32_bf16 v[60:63], v[172:175], v[20:23], v[60:63]\n\t"
        "ds_read_b128 v[20:23], v13 offset:40960\n\t"
        "s_waitcnt lgkmcnt(3)\n\t"
        "v_mfma_f32_16x16x32_bf16 v[56:59], v[176:179], v[24:27], v[56:59]\n\t"
        "v_mfma_f32_16x16x32_bf16 v[60:63], v[180:183], v[24:27], v[60:63]\n\t"
        "ds_read_b128 v[24:27], v12 offset:43008\n\t"
        "s_waitcnt lgkmcnt(3)\n\t"
        "v_mfma_f32_16x16x32_bf16 v[56:59], v[184:187], v[28:31], v[56:59]\n\t"
        "buffer_load_dwordx4 v7, %[rx], s49 offen offset:3072 lds\n\t"
        "v_mfma_f32_16x16x32_bf16 v[60:63], v[188:191], v[28:31], v[60:63]\n\t"
        "ds_read_b128 v[28:31], v13 offset:43008\n\t"
        "s_waitcnt lgkmcnt(3)\n\t"
        "v_mfma_f32_16x16x32_bf16 v[56:59], v[192:195], v[16:19], v[56:59]\n\t"
        "s_add_u32 s49, s49, 32768\n\t"
        "v_mfma_f32_16x16x32_bf16 v[60:63], v[196:199], v[16:19], v[60:63]\n\t"
        "s_add_u32 s54, s54, 1\n\t"
        "ds_read_b128 v[16:19], v12 offset:45056\n\t"
        "s_waitcnt lgkmcnt(3)\n\t"
        "v_mfma_f32_16x16x32_bf16 v[56:59], v[200:203], v[20:23], v[56:59]\n\t"
        "v_mov_b32 v76, s54\n\t"
        "v_mfma_f32_16x16x32_bf16 v[60:63], v[204:207], v[20:23], v[60:63]\n\t"
        "ds_read_b128 v[20:23], v13 offset:45056\n\t"
        "s_waitcnt lgkmcnt(3)\n\t"
        "v_mfma_f32_16x16x32_bf16 v[56:59], v[208:211], v[24:27], v[56:59]\n\t"
        "s_and_b32 s71, s54, 127\n\t"
        "s_cmp_le_u32 s71, 16\n\t"
        "s_cselect_b32 s72, 0, 3\n\t"
        "s_mul_i32 s71, s71, 128\n\t"
        "s_and_b32 s72, s72, s54\n\t"
        "s_cselect_b32 s71, 0x70000000, s71\n\t"
        "v_mfma_f32_16x16x32_bf16 v[60:63], v[212:215], v[24:27], v[60:63]\n\t"
        "ds_read_b128 v[24:27], v12 offset:47104\n\t"
        "s_waitcnt lgkmcnt(3)\n\t"
        "v_mfma_f32_16x16x32_bf16 v[56:59], v[216:219], v[28:31], v[56:59]\n\t"
        "buffer_store_dwordx2 v[76:77], v78, %[rf], s71 offen sc1\n\t"
        "v_mfma_f32_16x16x32_bf16 v[60:63], v[220:223], v[28:31], v[60:63]\n\t"
        "ds_read_b128 v[28:31], v13 offset:47104\n\t"
        "s_waitcnt lgkmcnt(3)\n\t"
        "v_mfma_f32_16x16x32_bf16 v[56:59], v[224:227], v[16:19], v[56:59]\n\t"
        "v_mfma_f32_16x16x32_bf16 v[60:63], v[228:231], v[16:19], v[60:63]\n\t"
        "ds_read_b128 v[16:19], v10 offset:0\n\t"
        "s_waitcnt lgkmcnt(3)\n\t"
        "v_mfma_f32_16x16x32_bf16 v[56:59], v[232:235], v[20:23], v[56:59]\n\t"
        "v_mfma_f32_16x16x32_bf16 v[60:63], v[236:239], v[20:23], v[60:63]\n\t"
        "ds_read_b128 v[20:23], v11 offset:0\n\t"
        "s_waitcnt lgkmcnt(3)\n\t"
        "v_mfma_f32_16x16x32_bf16 v[56:59], v[240:243], v[24:27], v[56:59]\n\t"
        "v_mfma_f32_16x16x32_bf16 v[60:63], v[244:247], v[24:27], v[60:63]\n\t"
        "ds_read_b128 v[24:27], v10 offset:2048\n\t"
        "s_waitcnt lgkmcnt(3)\n\t"
        "v_mfma_f32_16x16x32_bf16 v[56:59], v[248:251], v[28:31], v[56:59]\n\t"
        "v_mfma_f32_16x16x32_bf16 v[60:63], v[252:255], v[28:31], v[60:63]\n\t"
        "s_sub_u32 s52, s52, 1\n\t"
        "s_waitcnt vmcnt(5)\n\t"
        "s_barrier\n\t"
        "s_cmp_lg_u32 s52, 0\n\t"
        "s_cbranch_scc1 .Lx_loop0_%=\n\t"
        "s_nop 7\n\t"
        "ds_write_b128 v14, v[56:59] offset:8192\n\t"
        "ds_write_b128 v14, v[60:63] offset:9216\n\t"
        "ds_read_b128 v[64:67], v14 offset:0\n\t"
        "ds_read_b128 v[68:71], v14 offset:1024\n\t"
        "s_waitcnt lgkmcnt(0)\n\t"
        "s_barrier\n\t"
        "s_add_u32 s50, s50, 0x80000\n\t"
        "s_add_u32 s51, s51, 1\n\t"
        "s_and_b32 s70, s51, 3\n\t"
        "s_cselect_b32 s70, 0, 0x2000\n\t"
        "s_xor_b32 s50, s50, s70\n\t"
        "s_waitcnt lgkmcnt(0)\n\t"
        "v_add_f32 v64, v64, v48\n\t"
        "v_add_f32 v65, v65, v49\n\t"
        "v_add_f32 v66, v66, v50\n\t"
        "v_add_f32 v67, v67, v51\n\t"
        "v_add_f32 v68, v68, v52\n\t"
        "v_add_f32 v69, v69, v53\n\t"
        "v_add_f32 v70, v70, v54\n\t"
        "v_add_f32 v71, v71, v55\n\t"
        "v_cvt_pk_bf16_f32 v72, v64, v65\n\t"
        "v_cvt_pk_bf16_f32 v73, v68, v69\n\t"
        "v_cvt_pk_bf16_f32 v74, v66, v67\n\t"
        "v_cvt_pk_bf16_f32 v75, v70, v71\n\t"
        "s_mov_b64 exec, s[56:57]\n\t"
        "v_swap_b32 v73, v74\n\t"
        "s_nop 1\n\t"
        "v_permlane16_swap_b32 v72, v74\n\t"
        "v_permlane16_swap_b32 v73, v75\n\t"
        "s_mov_b64 exec, -1\n\t"
        "buffer_store_dwordx4 v[72:75], v79, %[rs], s50 offen sc1\n\t"
        "s_waitcnt vmcnt(0)\n\t"
        "s_barrier\n\t"
        "s_and_b32 s54, s54, 0xffffff80\n\t"
        "s_add_u32 s54, s54, 63\n\t"
        "s_add_u32 s54, s54, 1\n\t"
        "v_mov_b32 v76, s54\n\t"
        "s_and_b32 s71, s54, 127\n\t"
        "s_lshl_b32 s71, s71, 7\n\t"
        "buffer_store_dwordx2 v[76:77], v78, %[rf], s71 offen sc1\n\t"
        "s_branch .Lx_end_%=\n\t"
        ".Lx_k1_%=:\n\t"
        "s_nop 7\n\t"
        "ds_write_b128 v14, v[32:35] offset:0\n\t"
        "ds_write_b128 v14, v[36:39] offset:1024\n\t"
        "s_waitcnt lgkmcnt(0)\n\t"
        "s_barrier\n\t"
        "v_mov_b32 v72, 147456\n\t"
        "ds_read_b64 v[76:77], v72\n\t"
        "ds_read_b128 v[64:67], v14 offset:8192\n\t"
        "ds_read_b128 v[68:71], v14 offset:9216\n\t"
        "s_add_u32 m0, s47, 65536\n\t"
        "s_nop 0\n\t"
        "buffer_load_dwordx4 v4, %[rx], s49 offen offset:0 lds\n\t"
        "buffer_load_dwordx4 v5, %[rx], s49 offen offset:1024 lds\n\t"
        "buffer_load_dwordx4 v6, %[rx], s49 offen offset:2048 lds\n\t"
        "buffer_load_dwordx4 v7, %[rx], s49 offen offset:3072 lds\n\t"
        "s_add_u32 s49, s49, 32768\n\t"
        "s_add_u32 m0, s47, 98304\n\t"
        "s_nop 0\n\t"
        "buffer_load_dwordx4 v4, %[rx], s49 offen offset:0 lds\n\t"
        "buffer_load_dwordx4 v5, %[rx], s49 offen offset:1024 lds\n\t"
        "buffer_load_dwordx4 v6, %[rx], s49 offen offset:2048 lds\n\t"
        "buffer_load_dwordx4 v7, %[rx], s49 offen offset:3072 lds\n\t"
        "s_add_u32 s49, s49, 32768\n\t"
        "s_waitcnt lgkmcnt(0)\n\t"
        "v_readfirstlane_b32 s54, v76\n\t"
        "v_lshrrev_b32 v78, 4, v1\n\t"
        "v_lshlrev_b32 v78, 24, v78\n\t"
        "v_lshl_add_u32 v78, v3, 3, v78\n\t"
        "s_cmp_eq_u32 %[w], 0\n\t"
        "s_cselect_b32 s70, 0, 0x40000000\n\t"
        "v_add_u32 v78, s70, v78\n\t"
        "s_and_b32 s70, s54, 128\n\t"
        "s_lshl_b32 s70, s70, 6\n\t"
        "s_xor_b32 s50, %[co], s70\n\t"
        "s_lshl_b32 s70, %[w], 10\n\t"
        "s_add_u32 s50, s50, s70\n\t"
        "s_mov_b32 s51, 0\n\t"
        "v_add_f32 v64, v64, v40\n\t"
        "v_add_f32 v65, v65, v41\n\t"
        "v_add_f32 v66, v66, v42\n\t"
        "v_add_f32 v67, v67, v43\n\t"
        "v_add_f32 v68, v68, v44\n\t"
        "v_add_f32 v69, v69, v45\n\t"
        "v_add_f32 v70, v70, v46\n\t"
        "v_add_f32 v71, v71, v47\n\t"
        "v_cvt_pk_bf16_f32 v72, v64, v65\n\t"
        "v_cvt_pk_bf16_f32 v73, v68, v69\n\t"
        "v_cvt_pk_bf16_f32 v74, v66, v67\n\t"
        "v_cvt_pk_bf16_f32 v75, v70, v71\n\t"
        "s_mov_b64 exec, s[56:57]\n\t"
        "v_swap_b32 v73, v74\n\t"
        "s_nop 1\n\t"
        "v_permlane16_swap_b32 v72, v74\n\t"
        "v_permlane16_swap_b32 v73, v75\n\t"
        "s_mov_b64 exec, -1\n\t"
        "buffer_store_dwordx4 v[72:75], v79, %[rs], s50 offen sc1\n\t"
        "s_waitcnt vmcnt(0)\n\t"
        "s_barrier\n\t"
        "s_add_u32 s54, s54, 1\n\t"
        "v_mov_b32 v76, s54\n\t"
        "s_and_b32 s71, s54, 127\n\t"
        "s_lshl_b32 s71, s71, 7\n\t"
        "buffer_store_dwordx2 v[76:77], v78, %[rf], s71 offen sc1\n\t"
        "s_add_u32 m0, s47, 0\n\t"
        "s_nop 0\n\t"
        "buffer_load_dwordx4 v4, %[rx], s49 offen offset:0 lds\n\t"
        "buffer_load_dwordx4 v5, %[rx], s49 offen offset:1024 lds\n\t"
        "buffer_load_dwordx4 v6, %[rx], s49 offen offset:2048 lds\n\t"
        "buffer_load_dwordx4 v7, %[rx], s49 offen offset:3072 lds\n\t"
        "s_add_u32 s49, s49, 32768\n\t"
        "ds_read_b128 v[16:19], v12 offset:0\n\t"
        "ds_read_b128 v[20:23], v13 offset:0\n\t"
        "ds_read_b128 v[24:27], v12 offset:2048\n\t"
        "ds_read_b128 v[28:31], v13 offset:2048\n\t"
        "s_waitcnt lgkmcnt(3)\n\t"
        "v_mfma_f32_16x16x32_bf16 v[48:51], v[128:131], v[16:19], 0\n\t"
        "v_mfma_f32_16x16x32_bf16 v[52:55], v[132:135], v[16:19], 0\n\t"
        "ds_read_b128 v[16:19], v12 offset:4096\n\t"
        "s_waitcnt lgkmcnt(3)\n\t"
        "v_mfma_f32_16x16x32_bf16 v[48:51], v[136:139], v[20:23], v[48:51]\n\t"
        "v_mfma_f32_16x16x32_bf16 v[52:55], v[140:143], v[20:23], v[52:55]\n\t"
        "ds_read_b128 v[20:23], v13 offset:4096\n\t"
        "s_waitcnt lgkmcnt(3)\n\t"
        "v_mfma_f32_16x16x32_bf16 v[48:51], v[144:147], v[24:27], v[48:51]\n\t"
        "v_mfma_f32_16x16x32_bf16 v[52:55], v[148:151], v[24:27], v[52:55]\n\t"
        "ds_read_b128 v[24:27], v12 offset:6144\n\t"
        "s_waitcnt lgkmcnt(3)\n\t"
        "v_mfma_f32_16x16x32_bf16 v[48:51], v[152:155], v[28:31], v[48:51]\n\t"
        "v_mfma_f32_16x16x32_bf16 v[52:55], v[156:159], v[28:31], v[52:55]\n\t"
        "ds_read_b128 v[28:31], v13 offset:6144\n\t"
        "s_waitcnt lgkmcnt(3)\n\t"
        "v_mfma_f32_16x16x32_bf16 v[48:51], v[160:163], v[16:19], v[48:51]\n\t"
        "v_mfma_f32_16x16x32_bf16 v[52:55], v[164:167], v[16:19], v[52:55]\n\t"
        "ds_read_b128 v[16:19], v12 offset:8192\n\t"
        "s_waitcnt lgkmcnt(3)\n\t"
        "v_mfma_f32_16x16x32_bf16 v[48:51], v[168:171], v[20:23], v[48:51]\n\t"
        "v_mfma_f32_16x16x32_bf16 v[52:55], v[172:175], v[20:23], v[52:55]\n\t"
        "ds_read_b128 v[20:23], v13 offset:8192\n\t"
        "s_waitcnt lgkmcnt(3)\n\t"
        "v_mfma_f32_16x16x32_bf16 v[48:51], v[176:179], v[24:27], v[48:51]\n\t"
        "v_mfma_f32_16x16x32_bf16 v[52:55], v[180:183], v[24:27], v[52:55]\n\t"
        "ds_read_b128 v[24:27], v12 offset:10240\n\t"
        "s_waitcnt lgkmcnt(3)\n\t"
        "v_mfma_f32_16x16x32_bf16 v[48:51], v[184:187], v[28:31], v[48:51]\n\t"
        "v_mfma_f32_16x16x32_bf16 v[52:55], v[188:191], v[28:31], v[52:55]\n\t"
        "ds_read_b128 v[28:31], v13 offset:10240\n\t"
        "s_waitcnt lgkmcnt(3)\n\t"
        "v_mfma_f32_16x16x32_bf16 v[48:51], v[192:195], v[16:19], v[48:51]\n\t"
        "v_mfma_f32_16x16x32_bf16 v[52:55], v[196:199], v[16:19], v[52:55]\n\t"
        "ds_read_b128 v[16:19], v12 offset:12288\n\t"
        "s_waitcnt lgkmcnt(3)\n\t"
        "v_mfma_f32_16x16x32_bf16 v[48:51], v[200:203], v[20:23], v[48:51]\n\t"
        "v_mfma_f32_16x16x32_bf16 v[52:55], v[204:207], v[20:23], v[52:55]\n\t"
        "ds_read_b128 v[20:23], v13 offset:12288\n\t"
        "s_waitcnt lgkmcnt(3)\n\t"
        "v_mfma_f32_16x16x32_bf16 v[48:51], v[208:211], v[24:27], v[48:51]\n\t"
        "v_mfma_f32_16x16x32_bf16 v[52:55], v[212:215], v[24:27], v[52:55]\n\t"
        "ds_read_b128 v[24:27], v12 offset:14336\n\t"
        "s_waitcnt lgkmcnt(3)\n\t"
        "v_mfma_f32_16x16x32_bf16 v[48:51], v[216:219], v[28:31], v[48:51]\n\t"
        "v_mfma_f32_16x16x32_bf16 v[52:55], v[220:223], v[28:31], v[52:55]\n\t"
        "ds_read_b128 v[28:31], v13 offset:14336\n\t"
        "s_waitcnt lgkmcnt(3)\n\t"
        "v_mfma_f32_16x16x32_bf16 v[48:51], v[224:227], v[16:19], v[48:51]\n\t"
        "v_mfma_f32_16x16x32_bf16 v[52:55], v[228:231], v[16:19], v[52:55]\n\t"
        "s_waitcnt lgkmcnt(2)\n\t"
        "v_mfma_f32_16x16x32_bf16 v[48:51], v[232:235], v[20:23], v[48:51]\n\t"
        "v_mfma_f32_16x16x32_bf16 v[52:55], v[236:239], v[20:23], v[52:55]\n\t"
        "s_waitcnt lgkmcnt(1)\n\t"
        "v_mfma_f32_16x16x32_bf16 v[48:51], v[240:243], v[24:27], v[48:51]\n\t"
        "v_mfma_f32_16x16x32_bf16 v[52:55], v[244:247], v[24:27], v[52:55]\n\t"
        "s_waitcnt lgkmcnt(0)\n\t"
        "v_mfma_f32_16x16x32_bf16 v[48:51], v[248:251], v[28:31], v[48:51]\n\t"
        "v_mfma_f32_16x16x32_bf16 v[52:55], v[252:255], v[28:31], v[52:55]\n\t"
        "s_add_u32 m0, s47, 32768\n\t"
        "s_nop 0\n\t"
        "buffer_load_dwordx4 v4, %[rx], s49 offen offset:0 lds\n\t"
        "buffer_load_dwordx4 v5, %[rx], s49 offen offset:1024 lds\n\t"
        "buffer_load_dwordx4 v6, %[rx], s49 offen offset:2048 lds\n\t"
        "buffer_load_dwordx4 v7, %[rx], s49 offen offset:3072 lds\n\t"
        "s_add_u32 s49, s49, 32768\n\t"
        "s_nop 7\n\t"
        "ds_write_b128 v14, v[48:51] offset:0\n\t"
        "ds_write_b128 v14, v[52:55] offset:1024\n\t"
        "s_waitcnt vmcnt(4) lgkmcnt(0)\n\t"
        "s_barrier\n\t"
        "ds_read_b128 v[16:19], v12 offset:32768\n\t"
        "ds_read_b128 v[20:23], v13 offset:32768\n\t"
        "ds_read_b128 v[24:27], v12 offset:34816\n\t"
        "ds_read_b128 v[28:31], v13 offset:34816\n\t"
        "s_waitcnt lgkmcnt(3)\n\t"
        "v_mfma_f32_16x16x32_bf16 v[56:59], v[128:131], v[16:19], 0\n\t"
        "v_mfma_f32_16x16x32_bf16 v[60:63], v[132:135], v[16:19], 0\n\t"
        "ds_read_b128 v[16:19], v12 offset:36864\n\t"
        "s_waitcnt lgkmcnt(3)\n\t"
        "v_mfma_f32_16x16x32_bf16 v[56:59], v[136:139], v[20:23], v[56:59]\n\t"
        "v_mfma_f32_16x16x32_bf16 v[60:63], v[140:143], v[20:23], v[60:63]\n\t"
        "ds_read_b128 v[20:23], v13 offset:36864\n\t"
        "s_waitcnt lgkmcnt(3)\n\t"
        "v_mfma_f32_16x16x32_bf16 v[56:59], v[144:147], v[24:27], v[56:59]\n\t"
        "v_mfma_f32_16x16x32_bf16 v[60:63], v[148:151], v[24:27], v[60:63]\n\t"
        "ds_read_b128 v[24:27], v12 offset:38912\n\t"
        "s_waitcnt lgkmcnt(3)\n\t"
        "v_mfma_f32_16x16x32_bf16 v[56:59], v[152:155], v[28:31], v[56:59]\n\t"
        "v_mfma_f32_16x16x32_bf16 v[60:63], v[156:159], v[28:31], v[60:63]\n\t"
        "ds_read_b128 v[28:31], v13 offset:38912\n\t"
        "s_waitcnt lgkmcnt(3)\n\t"
        "v_mfma_f32_16x16x32_bf16 v[56:59], v[160:163], v[16:19], v[56:59]\n\t"
        "v_mfma_f32_16x16x32_bf16 v[60:63], v[164:167], v[16:19], v[60:63]\n\t"
        "ds_read_b128 v[16:19], v12 offset:40960\n\t"
        "s_waitcnt lgkmcnt(3)\n\t"
        "v_mfma_f32_16x16x32_bf16 v[56:59], v[168:171], v[20:23], v[56:59]\n\t"
        "v_mfma_f32_16x16x32_bf16 v[60:63], v[172:175], v[20:23], v[60:63]\n\t"
        "ds_read_b128 v[20:23], v13 offset:40960\n\t"
        "s_waitcnt lgkmcnt(3)\n\t"
        "v_mfma_f32_16x16x32_bf16 v[56:59], v[176:179], v[24:27], v[56:59]\n\t"
        "v_mfma_f32_16x16x32_bf16 v[60:63], v[180:183], v[24:27], v[60:63]\n\t"
        "ds_read_b128 v[24:27], v12 offset:43008\n\t"
        "s_waitcnt lgkmcnt(3)\n\t"
        "v_mfma_f32_16x16x32_bf16 v[56:59], v[184:187], v[28:31], v[56:59]\n\t"
        "v_mfma_f32_16x16x32_bf16 v[60:63], v[188:191], v[28:31], v[60:63]\n\t"
        "ds_read_b128 v[28:31], v13 offset:43008\n\t"
        "s_waitcnt lgkmcnt(3)\n\t"
        "v_mfma_f32_16x16x32_bf16 v[56:59], v[192:195], v[16:19], v[56:59]\n\t"
        "v_mfma_f32_16x16x32_bf16 v[60:63], v[196:199], v[16:19], v[60:63]\n\t"
        "ds_read_b128 v[16:19], v12 offset:45056\n\t"
        "s_waitcnt lgkmcnt(3)\n\t"
        "v_mfma_f32_16x16x32_bf16 v[56:59], v[200:203], v[20:23], v[56:59]\n\t"
        "v_mfma_f32_16x16x32_bf16 v[60:63], v[204:207], v[20:23], v[60:63]\n\t"
        "ds_read_b128 v[20:23], v13 offset:45056\n\t"
        "s_waitcnt lgkmcnt(3)\n\t"
        "v_mfma_f32_16x16x32_bf16 v[56:59], v[208:211], v[24:27], v[56:59]\n\t"
        "v_mfma_f32_16x16x32_bf16 v[60:63], v[212:215], v[24:27], v[60:63]\n\t"
        "ds_read_b128 v[24:27], v12 offset:47104\n\t"
        "s_waitcnt lgkmcnt(3)\n\t"
        "v_mfma_f32_16x16x32_bf16 v[56:59], v[216:219], v[28:31], v[56:59]\n\t"
        "v_mfma_f32_16x16x32_bf16 v[60:63], v[220:223], v[28:31], v[60:63]\n\t"
        "ds_read_b128 v[28:31], v13 offset:47104\n\t"
        "s_waitcnt lgkmcnt(3)\n\t"
        "v_mfma_f32_16x16x32_bf16 v[56:59], v[224:227], v[16:19], v[56:59]\n\t"
        "v_mfma_f32_16x16x32_bf16 v[60:63], v[228:231], v[16:19], v[60:63]\n\t"
        "s_waitcnt lgkmcnt(2)\n\t"
        "v_mfma_f32_16x16x32_bf16 v[56:59], v[232:235], v[20:23], v[56:59]\n\t"
        "v_mfma_f32_16x16x32_bf16 v[60:63], v[236:239], v[20:23], v[60:63]\n\t"
        "s_waitcnt lgkmcnt(1)\n\t"
        "v_mfma_f32_16x16x32_bf16 v[56:59], v[240:243], v[24:27], v[56:59]\n\t"
        "v_mfma_f32_16x16x32_bf16 v[60:63], v[244:247], v[24:27], v[60:63]\n\t"
        "s_waitcnt lgkmcnt(0)\n\t"
        "v_mfma_f32_16x16x32_bf16 v[56:59], v[248:251], v[28:31], v[56:59]\n\t"
        "v_mfma_f32_16x16x32_bf16 v[60:63], v[252:255], v[28:31], v[60:63]\n\t"
        "s_add_u32 m0, s47, 65536\n\t"
        "s_nop 0\n\t"
        "buffer_load_dwordx4 v4, %[rx], s49 offen offset:0 lds\n\t"
        "buffer_load_dwordx4 v5, %[rx], s49 offen offset:1024 lds\n\t"
        "buffer_load_dwordx4 v6, %[rx], s49 offen offset:2048 lds\n\t"
        "buffer_load_dwordx4 v7, %[rx], s49 offen offset:3072 lds\n\t"
        "s_add_u32 s49, s49, 32768\n\t"
        "s_waitcnt vmcnt(4) lgkmcnt(0)\n\t"
        "s_barrier\n\t"
        "ds_read_b128 v[16:19], v10 offset:0\n\t"
        "ds_read_b128 v[20:23], v11 offset:0\n\t"
        "ds_read_b128 v[24:27], v10 offset:2048\n\t"
        "ds_read_b128 v[64:67], v14 offset:8192\n\t"
        "ds_read_b128 v[68:71], v14 offset:9216\n\t"
        "s_add_u32 s50, s50, 0x80000\n\t"
        "s_add_u32 s51, s51, 1\n\t"
        "s_and_b32 s70, s51, 3\n\t"
        "s_cselect_b32 s70, 0, 0x2000\n\t"
        "s_xor_b32 s50, s50, s70\n\t"
        "s_waitcnt lgkmcnt(0)\n\t"
        "v_add_f32 v64, v64, v56\n\t"
        "v_add_f32 v65, v65, v57\n\t"
        "v_add_f32 v66, v66, v58\n\t"
        "v_add_f32 v67, v67, v59\n\t"
        "v_add_f32 v68, v68, v60\n\t"
        "v_add_f32 v69, v69, v61\n\t"
        "v_add_f32 v70, v70, v62\n\t"
        "v_add_f32 v71, v71, v63\n\t"
        "v_cvt_pk_bf16_f32 v72, v64, v65\n\t"
        "v_cvt_pk_bf16_f32 v73, v68, v69\n\t"
        "v_cvt_pk_bf16_f32 v74, v66, v67\n\t"
        "v_cvt_pk_bf16_f32 v75, v70, v71\n\t"
        "s_mov_b64 exec, s[56:57]\n\t"
        "v_swap_b32 v73, v74\n\t"
        "s_nop 1\n\t"
        "v_permlane16_swap_b32 v72, v74\n\t"
        "v_permlane16_swap_b32 v73, v75\n\t"
        "s_mov_b64 exec, -1\n\t"
        "buffer_store_dwordx4 v[72:75], v79, %[rs], s50 offen sc1\n\t"
        "ds_read_b128 v[28:31], v11 offset:2048\n\t"
        "v_mfma_f32_16x16x32_bf16 v[32:35], v[128:131], v[16:19], 0\n\t"
        "v_mfma_f32_16x16x32_bf16 v[36:39], v[132:135], v[16:19], 0\n\t"
        "ds_read_b128 v[16:19], v10 offset:4096\n\t"
        "v_mfma_f32_16x16x32_bf16 v[32:35], v[136:139], v[20:23], v[32:35]\n\t"
        "v_mfma_f32_16x16x32_bf16 v[36:39], v[140:143], v[20:23], v[36:39]\n\t"
        "ds_read_b128 v[20:23], v11 offset:4096\n\t"
        "v_mfma_f32_16x16x32_bf16 v[32:35], v[144:147], v[24:27], v[32:35]\n\t"
        "v_mfma_f32_16x16x32_bf16 v[36:39], v[148:151], v[24:27], v[36:39]\n\t"
        "ds_read_b128 v[24:27], v10 offset:6144\n\t"
        "s_waitcnt lgkmcnt(3)\n\t"
        "v_mfma_f32_16x16x32_bf16 v[32:35], v[152:155], v[28:31], v[32:35]\n\t"
        "v_mfma_f32_16x16x32_bf16 v[36:39], v[156:159], v[28:31], v[36:39]\n\t"
        "ds_read_b128 v[28:31], v11 offset:6144\n\t"
        "s_waitcnt lgkmcnt(3)\n\t"
        "v_mfma_f32_16x16x32_bf16 v[32:35], v[160:163], v[16:19], v[32:35]\n\t"
        "v_mfma_f32_16x16x32_bf16 v[36:39], v[164:167], v[16:19], v[36:39]\n\t"
        "ds_read_b128 v[16:19], v10 offset:8192\n\t"
        "s_waitcnt lgkmcnt(3)\n\t"
        "v_mfma_f32_16x16x32_bf16 v[32:35], v[168:171], v[20:23], v[32:35]\n\t"
        "v_mfma_f32_16x16x32_bf16 v[36:39], v[172:175], v[20:23], v[36:39]\n\t"
        "ds_read_b128 v[20:23], v11 offset:8192\n\t"
        "s_waitcnt lgkmcnt(3)\n\t"
        "v_mfma_f32_16x16x32_bf16 v[32:35], v[176:179], v[24:27], v[32:35]\n\t"
        "v_mfma_f32_16x16x32_bf16 v[36:39], v[180:183], v[24:27], v[36:39]\n\t"
        "ds_read_b128 v[24:27], v10 offset:10240\n\t"
        "s_waitcnt lgkmcnt(3)\n\t"
        "v_mfma_f32_16x16x32_bf16 v[32:35], v[184:187], v[28:31], v[32:35]\n\t"
        "v_mfma_f32_16x16x32_bf16 v[36:39], v[188:191], v[28:31], v[36:39]\n\t"
        "ds_read_b128 v[28:31], v11 offset:10240\n\t"
        "s_waitcnt lgkmcnt(3)\n\t"
        "v_mfma_f32_16x16x32_bf16 v[32:35], v[192:195], v[16:19], v[32:35]\n\t"
        "v_mfma_f32_16x16x32_bf16 v[36:39], v[196:199], v[16:19], v[36:39]\n\t"
        "ds_read_b128 v[16:19], v10 offset:12288\n\t"
        "s_waitcnt lgkmcnt(3)\n\t"
        "v_mfma_f32_16x16x32_bf16 v[32:35], v[200:203], v[20:23], v[32:35]\n\t"
        "v_mfma_f32_16x16x32_bf16 v[36:39], v[204:207], v[20:23], v[36:39]\n\t"
        "ds_read_b128 v[20:23], v11 offset:12288\n\t"
        "s_waitcnt lgkmcnt(3)\n\t"
        "v_mfma_f32_16x16x32_bf16 v[32:35], v[208:211], v[24:27], v[32:35]\n\t"
        "v_mfma_f32_16x16x32_bf16 v[36:39], v[212:215], v[24:27], v[36:39]\n\t"
        "ds_read_b128 v[24:27], v10 offset:14336\n\t"
        "s_waitcnt lgkmcnt(3)\n\t"
        "v_mfma_f32_16x16x32_bf16 v[32:35], v[216:219], v[28:31], v[32:35]\n\t"
        "v_mfma_f32_16x16x32_bf16 v[36:39], v[220:223], v[28:31], v[36:39]\n\t"
        "ds_read_b128 v[28:31], v11 offset:14336\n\t"
        "s_waitcnt lgkmcnt(3)\n\t"
        "v_mfma_f32_16x16x32_bf16 v[32:35], v[224:227], v[16:19], v[32:35]\n\t"
        "v_mfma_f32_16x16x32_bf16 v[36:39], v[228:231], v[16:19], v[36:39]\n\t"
        "s_waitcnt lgkmcnt(2)\n\t"
        "v_mfma_f32_16x16x32_bf16 v[32:35], v[232:235], v[20:23], v[32:35]\n\t"
        "v_mfma_f32_16x16x32_bf16 v[36:39], v[236:239], v[20:23], v[36:39]\n\t"
        "s_waitcnt lgkmcnt(1)\n\t"
        "v_mfma_f32_16x16x32_bf16 v[32:35], v[240:243], v[24:27], v[32:35]\n\t"
        "v_mfma_f32_16x16x32_bf16 v[36:39], v[244:247], v[24:27], v[36:39]\n\t"
        "s_waitcnt lgkmcnt(0)\n\t"
        "v_mfma_f32_16x16x32_bf16 v[32:35], v[248:251], v[28:31], v[32:35]\n\t"
        "v_mfma_f32_16x16x32_bf16 v[36:39], v[252:255], v[28:31], v[36:39]\n\t"
        "s_add_u32 m0, s47, 98304\n\t"
        "s_nop 0\n\t"
        "buffer_load_dwordx4 v4, %[rx], s49 offen offset:0 lds\n\t"
        "buffer_load_dwordx4 v5, %[rx], s49 offen offset:1024 lds\n\t"
        "buffer_load_dwordx4 v6, %[rx], s49 offen offset:2048 lds\n\t"
        "buffer_load_dwordx4 v7, %[rx], s49 offen offset:3072 lds\n\t"
        "s_add_u32 s49, s49, 32768\n\t"
        "s_nop 7\n\t"
        "ds_write_b128 v14, v[32:35] offset:0\n\t"
        "ds_write_b128 v14, v[36:39] offset:1024\n\t"
        "s_waitcnt vmcnt(4) lgkmcnt(0)\n\t"
        "s_barrier\n\t"
        "s_add_u32 s54, s54, 1\n\t"
        "v_mov_b32 v76, s54\n\t"
        "s_and_b32 s71, s54, 127\n\t"
        "s_lshl_b32 s71, s71, 7\n\t"
        "buffer_store_dwordx2 v[76:77], v78, %[rf], s71 offen sc1\n\t"
        "ds_read_b128 v[16:19], v10 offset:32768\n\t"
        "ds_read_b128 v[20:23], v11 offset:32768\n\t"
        "ds_read_b128 v[24:27], v10 offset:34816\n\t"
        "ds_read_b128 v[28:31], v11 offset:34816\n\t"
        "s_waitcnt lgkmcnt(3)\n\t"
        "v_mfma_f32_16x16x32_bf16 v[40:43], v[128:131], v[16:19], 0\n\t"
        "v_mfma_f32_16x16x32_bf16 v[44:47], v[132:135], v[16:19], 0\n\t"
        "ds_read_b128 v[16:19], v10 offset:36864\n\t"
        "s_waitcnt lgkmcnt(3)\n\t"
        "v_mfma_f32_16x16x32_bf16 v[40:43], v[136:139], v[20:23], v[40:43]\n\t"
        "v_mfma_f32_16x16x32_bf16 v[44:47], v[140:143], v[20:23], v[44:47]\n\t"
        "ds_read_b128 v[20:23], v11 offset:36864\n\t"
        "s_waitcnt lgkmcnt(3)\n\t"
        "v_mfma_f32_16x16x32_bf16 v[40:43], v[144:147], v[24:27], v[40:43]\n\t"
        "v_mfma_f32_16x16x32_bf16 v[44:47], v[148:151], v[24:27], v[44:47]\n\t"
        "ds_read_b128 v[24:27], v10 offset:38912\n\t"
        "s_waitcnt lgkmcnt(3)\n\t"
        "v_mfma_f32_16x16x32_bf16 v[40:43], v[152:155], v[28:31], v[40:43]\n\t"
        "v_mfma_f32_16x16x32_bf16 v[44:47], v[156:159], v[28:31], v[44:47]\n\t"
        "ds_read_b128 v[28:31], v11 offset:38912\n\t"
        "s_waitcnt lgkmcnt(3)\n\t"
        "v_mfma_f32_16x16x32_bf16 v[40:43], v[160:163], v[16:19], v[40:43]\n\t"
        "v_mfma_f32_16x16x32_bf16 v[44:47], v[164:167], v[16:19], v[44:47]\n\t"
        "ds_read_b128 v[16:19], v10 offset:40960\n\t"
        "s_waitcnt lgkmcnt(3)\n\t"
        "v_mfma_f32_16x16x32_bf16 v[40:43], v[168:171], v[20:23], v[40:43]\n\t"
        "v_mfma_f32_16x16x32_bf16 v[44:47], v[172:175], v[20:23], v[44:47]\n\t"
        "ds_read_b128 v[20:23], v11 offset:40960\n\t"
        "s_waitcnt lgkmcnt(3)\n\t"
        "v_mfma_f32_16x16x32_bf16 v[40:43], v[176:179], v[24:27], v[40:43]\n\t"
        "v_mfma_f32_16x16x32_bf16 v[44:47], v[180:183], v[24:27], v[44:47]\n\t"
        "ds_read_b128 v[24:27], v10 offset:43008\n\t"
        "s_waitcnt lgkmcnt(3)\n\t"
        "v_mfma_f32_16x16x32_bf16 v[40:43], v[184:187], v[28:31], v[40:43]\n\t"
        "v_mfma_f32_16x16x32_bf16 v[44:47], v[188:191], v[28:31], v[44:47]\n\t"
        "ds_read_b128 v[28:31], v11 offset:43008\n\t"
        "s_waitcnt lgkmcnt(3)\n\t"
        "v_mfma_f32_16x16x32_bf16 v[40:43], v[192:195], v[16:19], v[40:43]\n\t"
        "v_mfma_f32_16x16x32_bf16 v[44:47], v[196:199], v[16:19], v[44:47]\n\t"
        "ds_read_b128 v[16:19], v10 offset:45056\n\t"
        "s_waitcnt lgkmcnt(3)\n\t"
        "v_mfma_f32_16x16x32_bf16 v[40:43], v[200:203], v[20:23], v[40:43]\n\t"
        "v_mfma_f32_16x16x32_bf16 v[44:47], v[204:207], v[20:23], v[44:47]\n\t"
        "ds_read_b128 v[20:23], v11 offset:45056\n\t"
        "s_waitcnt lgkmcnt(3)\n\t"
        "v_mfma_f32_16x16x32_bf16 v[40:43], v[208:211], v[24:27], v[40:43]\n\t"
        "v_mfma_f32_16x16x32_bf16 v[44:47], v[212:215], v[24:27], v[44:47]\n\t"
        "ds_read_b128 v[24:27], v10 offset:47104\n\t"
        "s_waitcnt lgkmcnt(3)\n\t"
        "v_mfma_f32_16x16x32_bf16 v[40:43], v[216:219], v[28:31], v[40:43]\n\t"
        "v_mfma_f32_16x16x32_bf16 v[44:47], v[220:223], v[28:31], v[44:47]\n\t"
        "ds_read_b128 v[28:31], v11 offset:47104\n\t"
        "s_waitcnt lgkmcnt(3)\n\t"
        "v_mfma_f32_16x16x32_bf16 v[40:43], v[224:227], v[16:19], v[40:43]\n\t"
        "v_mfma_f32_16x16x32_bf16 v[44:47], v[228:231], v[16:19], v[44:47]\n\t"
        "s_waitcnt lgkmcnt(2)\n\t"
        "v_mfma_f32_16x16x32_bf16 v[40:43], v[232:235], v[20:23], v[40:43]\n\t"
        "v_mfma_f32_16x16x32_bf16 v[44:47], v[236:239], v[20:23], v[44:47]\n\t"
        "s_waitcnt lgkmcnt(1)\n\t"
        "v_mfma_f32_16x16x32_bf16 v[40:43], v[240:243], v[24:27], v[40:43]\n\t"
        "v_mfma_f32_16x16x32_bf16 v[44:47], v[244:247], v[24:27], v[44:47]\n\t"
        "s_waitcnt lgkmcnt(0)\n\t"
        "v_mfma_f32_16x16x32_bf16 v[40:43], v[248:251], v[28:31], v[40:43]\n\t"
        "v_mfma_f32_16x16x32_bf16 v[44:47], v[252:255], v[28:31], v[44:47]\n\t"
        "s_add_u32 m0, s47, 0\n\t"
        "s_nop 0\n\t"
        "buffer_load_dwordx4 v4, %[rx], s49 offen offset:0 lds\n\t"
        "buffer_load_dwordx4 v5, %[rx], s49 offen offset:1024 lds\n\t"
        "buffer_load_dwordx4 v6, %[rx], s49 offen offset:2048 lds\n\t"
        "buffer_load_dwordx4 v7, %[rx], s49 offen offset:3072 lds\n\t"
        "s_add_u32 s49, s49, 32768\n\t"
        "s_waitcnt vmcnt(4) lgkmcnt(0)\n\t"
        "s_barrier\n\t"
        "ds_read_b128 v[16:19], v12 offset:0\n\t"
        "ds_read_b128 v[20:23], v13 offset:0\n\t"
        "ds_read_b128 v[24:27], v12 offset:2048\n\t"
        "ds_read_b128 v[64:67], v14 offset:8192\n\t"
        "ds_read_b128 v[68:71], v14 offset:9216\n\t"
        "s_add_u32 s50, s50, 0x80000\n\t"
        "s_add_u32 s51, s51, 1\n\t"
        "s_and_b32 s70, s51, 3\n\t"
        "s_cselect_b32 s70, 0, 0x2000\n\t"
        "s_xor_b32 s50, s50, s70\n\t"
        "s_waitcnt lgkmcnt(0)\n\t"
        "v_add_f32 v64, v64, v40\n\t"
        "v_add_f32 v65, v65, v41\n\t"
        "v_add_f32 v66, v66, v42\n\t"
        "v_add_f32 v67, v67, v43\n\t"
        "v_add_f32 v68, v68, v44\n\t"
        "v_add_f32 v69, v69, v45\n\t"
        "v_add_f32 v70, v70, v46\n\t"
        "v_add_f32 v71, v71, v47\n\t"
        "v_cvt_pk_bf16_f32 v72, v64, v65\n\t"
        "v_cvt_pk_bf16_f32 v73, v68, v69\n\t"
        "v_cvt_pk_bf16_f32 v74, v66, v67\n\t"
        "v_cvt_pk_bf16_f32 v75, v70, v71\n\t"
        "s_mov_b64 exec, s[56:57]\n\t"
        "v_swap_b32 v73, v74\n\t"
        "s_nop 1\n\t"
        "v_permlane16_swap_b32 v72, v74\n\t"
        "v_permlane16_swap_b32 v73, v75\n\t"
        "s_mov_b64 exec, -1\n\t"
        "buffer_store_dwordx4 v[72:75], v79, %[rs], s50 offen sc1\n\t"
        "ds_read_b128 v[28:31], v13 offset:2048\n\t"
        "v_mfma_f32_16x16x32_bf16 v[48:51], v[128:131], v[16:19], 0\n\t"
        "v_mfma_f32_16x16x32_bf16 v[52:55], v[132:135], v[16:19], 0\n\t"
        "ds_read_b128 v[16:19], v12 offset:4096\n\t"
        "v_mfma_f32_16x16x32_bf16 v[48:51], v[136:139], v[20:23], v[48:51]\n\t"
        "v_mfma_f32_16x16x32_bf16 v[52:55], v[140:143], v[20:23], v[52:55]\n\t"
        "ds_read_b128 v[20:23], v13 offset:4096\n\t"
        "v_mfma_f32_16x16x32_bf16 v[48:51], v[144:147], v[24:27], v[48:51]\n\t"
        "v_mfma_f32_16x16x32_bf16 v[52:55], v[148:151], v[24:27], v[52:55]\n\t"
        "ds_read_b128 v[24:27], v12 offset:6144\n\t"
        "s_waitcnt lgkmcnt(3)\n\t"
        "v_mfma_f32_16x16x32_bf16 v[48:51], v[152:155], v[28:31], v[48:51]\n\t"
        "v_mfma_f32_16x16x32_bf16 v[52:55], v[156:159], v[28:31], v[52:55]\n\t"
        "ds_read_b128 v[28:31], v13 offset:6144\n\t"
        "s_waitcnt lgkmcnt(3)\n\t"
        "v_mfma_f32_16x16x32_bf16 v[48:51], v[160:163], v[16:19], v[48:51]\n\t"
        "v_mfma_f32_16x16x32_bf16 v[52:55], v[164:167], v[16:19], v[52:55]\n\t"
        "ds_read_b128 v[16:19], v12 offset:8192\n\t"
        "s_waitcnt lgkmcnt(3)\n\t"
        "v_mfma_f32_16x16x32_bf16 v[48:51], v[168:171], v[20:23], v[48:51]\n\t"
        "v_mfma_f32_16x16x32_bf16 v[52:55], v[172:175], v[20:23], v[52:55]\n\t"
        "ds_read_b128 v[20:23], v13 offset:8192\n\t"
        "s_waitcnt lgkmcnt(3)\n\t"
        "v_mfma_f32_16x16x32_bf16 v[48:51], v[176:179], v[24:27], v[48:51]\n\t"
        "v_mfma_f32_16x16x32_bf16 v[52:55], v[180:183], v[24:27], v[52:55]\n\t"
        "ds_read_b128 v[24:27], v12 offset:10240\n\t"
        "s_waitcnt lgkmcnt(3)\n\t"
        "v_mfma_f32_16x16x32_bf16 v[48:51], v[184:187], v[28:31], v[48:51]\n\t"
        "v_mfma_f32_16x16x32_bf16 v[52:55], v[188:191], v[28:31], v[52:55]\n\t"
        "ds_read_b128 v[28:31], v13 offset:10240\n\t"
        "s_waitcnt lgkmcnt(3)\n\t"
        "v_mfma_f32_16x16x32_bf16 v[48:51], v[192:195], v[16:19], v[48:51]\n\t"
        "v_mfma_f32_16x16x32_bf16 v[52:55], v[196:199], v[16:19], v[52:55]\n\t"
        "ds_read_b128 v[16:19], v12 offset:12288\n\t"
        "s_waitcnt lgkmcnt(3)\n\t"
        "v_mfma_f32_16x16x32_bf16 v[48:51], v[200:203], v[20:23], v[48:51]\n\t"
        "v_mfma_f32_16x16x32_bf16 v[52:55], v[204:207], v[20:23], v[52:55]\n\t"
        "ds_read_b128 v[20:23], v13 offset:12288\n\t"
        "s_waitcnt lgkmcnt(3)\n\t"
        "v_mfma_f32_16x16x32_bf16 v[48:51], v[208:211], v[24:27], v[48:51]\n\t"
        "v_mfma_f32_16x16x32_bf16 v[52:55], v[212:215], v[24:27], v[52:55]\n\t"
        "ds_read_b128 v[24:27], v12 offset:14336\n\t"
        "s_waitcnt lgkmcnt(3)\n\t"
        "v_mfma_f32_16x16x32_bf16 v[48:51], v[216:219], v[28:31], v[48:51]\n\t"
        "v_mfma_f32_16x16x32_bf16 v[52:55], v[220:223], v[28:31], v[52:55]\n\t"
        "ds_read_b128 v[28:31], v13 offset:14336\n\t"
        "s_waitcnt lgkmcnt(3)\n\t"
        "v_mfma_f32_16x16x32_bf16 v[48:51], v[224:227], v[16:19], v[48:51]\n\t"
        "v_mfma_f32_16x16x32_bf16 v[52:55], v[228:231], v[16:19], v[52:55]\n\t"
        "s_waitcnt lgkmcnt(2)\n\t"
        "v_mfma_f32_16x16x32_bf16 v[48:51], v[232:235], v[20:23], v[48:51]\n\t"
        "v_mfma_f32_16x16x32_bf16 v[52:55], v[236:239], v[20:23], v[52:55]\n\t"
        "s_waitcnt lgkmcnt(1)\n\t"
        "v_mfma_f32_16x16x32_bf16 v[48:51], v[240:243], v[24:27], v[48:51]\n\t"
        "v_mfma_f32_16x16x32_bf16 v[52:55], v[244:247], v[24:27], v[52:55]\n\t"
        "s_waitcnt lgkmcnt(0)\n\t"
        "v_mfma_f32_16x16x32_bf16 v[48:51], v[248:251], v[28:31], v[48:51]\n\t"
        "v_mfma_f32_16x16x32_bf16 v[52:55], v[252:255], v[28:31], v[52:55]\n\t"
        "s_add_u32 m0, s47, 32768\n\t"
        "s_nop 0\n\t"
        "buffer_load_dwordx4 v4, %[rx], s49 offen offset:0 lds\n\t"
        "buffer_load_dwordx4 v5, %[rx], s49 offen offset:1024 lds\n\t"
        "buffer_load_dwordx4 v6, %[rx], s49 offen offset:2048 lds\n\t"
        "buffer_load_dwordx4 v7, %[rx], s49 offen offset:3072 lds\n\t"
        "s_add_u32 s49, s49, 32768\n\t"
        "s_nop 7\n\t"
        "ds_write_b128 v14, v[48:51] offset:0\n\t"
        "ds_write_b128 v14, v[52:55] offset:1024\n\t"
        "s_waitcnt vmcnt(4) lgkmcnt(0)\n\t"
        "s_barrier\n\t"
        "s_add_u32 s54, s54, 1\n\t"
        "v_mov_b32 v76, s54\n\t"
        "s_and_b32 s71, s54, 127\n\t"
        "s_lshl_b32 s71, s71, 7\n\t"
        "buffer_store_dwordx2 v[76:77], v78, %[rf], s71 offen sc1\n\t"
        "ds_read_b128 v[16:19], v12 offset:32768\n\t"
        "ds_read_b128 v[20:23], v13 offset:32768\n\t"
        "ds_read_b128 v[24:27], v12 offset:34816\n\t"
        "ds_read_b128 v[28:31], v13 offset:34816\n\t"
        "s_waitcnt lgkmcnt(3)\n\t"
        "v_mfma_f32_16x16x32_bf16 v[56:59], v[128:131], v[16:19], 0\n\t"
        "v_mfma_f32_16x16x32_bf16 v[60:63], v[132:135], v[16:19], 0\n\t"
        "ds_read_b128 v[16:19], v12 offset:36864\n\t"
        "s_waitcnt lgkmcnt(3)\n\t"
        "v_mfma_f32_16x16x32_bf16 v[56:59], v[136:139], v[20:23], v[56:59]\n\t"
        "v_mfma_f32_16x16x32_bf16 v[60:63], v[140:143], v[20:23], v[60:63]\n\t"
        "ds_read_b128 v[20:23], v13 offset:36864\n\t"
        "s_waitcnt lgkmcnt(3)\n\t"
        "v_mfma_f32_16x16x32_bf16 v[56:59], v[144:147], v[24:27], v[56:59]\n\t"
        "v_mfma_f32_16x16x32_bf16 v[60:63], v[148:151], v[24:27], v[60:63]\n\t"
        "ds_read_b128 v[24:27], v12 offset:38912\n\t"
        "s_waitcnt lgkmcnt(3)\n\t"
        "v_mfma_f32_16x16x32_bf16 v[56:59], v[152:155], v[28:31], v[56:59]\n\t"
        "v_mfma_f32_16x16x32_bf16 v[60:63], v[156:159], v[28:31], v[60:63]\n\t"
        "ds_read_b128 v[28:31], v13 offset:38912\n\t"
        "s_waitcnt lgkmcnt(3)\n\t"
        "v_mfma_f32_16x16x32_bf16 v[56:59], v[160:163], v[16:19], v[56:59]\n\t"
        "v_mfma_f32_16x16x32_bf16 v[60:63], v[164:167], v[16:19], v[60:63]\n\t"
        "ds_read_b128 v[16:19], v12 offset:40960\n\t"
        "s_waitcnt lgkmcnt(3)\n\t"
        "v_mfma_f32_16x16x32_bf16 v[56:59], v[168:171], v[20:23], v[56:59]\n\t"
        "v_mfma_f32_16x16x32_bf16 v[60:63], v[172:175], v[20:23], v[60:63]\n\t"
        "ds_read_b128 v[20:23], v13 offset:40960\n\t"
        "s_waitcnt lgkmcnt(3)\n\t"
        "v_mfma_f32_16x16x32_bf16 v[56:59], v[176:179], v[24:27], v[56:59]\n\t"
        "v_mfma_f32_16x16x32_bf16 v[60:63], v[180:183], v[24:27], v[60:63]\n\t"
        "ds_read_b128 v[24:27], v12 offset:43008\n\t"
        "s_waitcnt lgkmcnt(3)\n\t"
        "v_mfma_f32_16x16x32_bf16 v[56:59], v[184:187], v[28:31], v[56:59]\n\t"
        "v_mfma_f32_16x16x32_bf16 v[60:63], v[188:191], v[28:31], v[60:63]\n\t"
        "ds_read_b128 v[28:31], v13 offset:43008\n\t"
        "s_waitcnt lgkmcnt(3)\n\t"
        "v_mfma_f32_16x16x32_bf16 v[56:59], v[192:195], v[16:19], v[56:59]\n\t"
        "v_mfma_f32_16x16x32_bf16 v[60:63], v[196:199], v[16:19], v[60:63]\n\t"
        "ds_read_b128 v[16:19], v12 offset:45056\n\t"
        "s_waitcnt lgkmcnt(3)\n\t"
        "v_mfma_f32_16x16x32_bf16 v[56:59], v[200:203], v[20:23], v[56:59]\n\t"
        "v_mfma_f32_16x16x32_bf16 v[60:63], v[204:207], v[20:23], v[60:63]\n\t"
        "ds_read_b128 v[20:23], v13 offset:45056\n\t"
        "s_waitcnt lgkmcnt(3)\n\t"
        "v_mfma_f32_16x16x32_bf16 v[56:59], v[208:211], v[24:27], v[56:59]\n\t"
        "v_mfma_f32_16x16x32_bf16 v[60:63], v[212:215], v[24:27], v[60:63]\n\t"
        "ds_read_b128 v[24:27], v12 offset:47104\n\t"
        "s_waitcnt lgkmcnt(3)\n\t"
        "v_mfma_f32_16x16x32_bf16 v[56:59], v[216:219], v[28:31], v[56:59]\n\t"
        "v_mfma_f32_16x16x32_bf16 v[60:63], v[220:223], v[28:31], v[60:63]\n\t"
        "ds_read_b128 v[28:31], v13 offset:47104\n\t"
        "s_waitcnt lgkmcnt(3)\n\t"
        "v_mfma_f32_16x16x32_bf16 v[56:59], v[224:227], v[16:19], v[56:59]\n\t"
        "v_mfma_f32_16x16x32_bf16 v[60:63], v[228:231], v[16:19], v[60:63]\n\t"
        "s_waitcnt lgkmcnt(2)\n\t"
        "v_mfma_f32_16x16x32_bf16 v[56:59], v[232:235], v[20:23], v[56:59]\n\t"
        "v_mfma_f32_16x16x32_bf16 v[60:63], v[236:239], v[20:23], v[60:63]\n\t"
        "s_waitcnt lgkmcnt(1)\n\t"
        "v_mfma_f32_16x16x32_bf16 v[56:59], v[240:243], v[24:27], v[56:59]\n\t"
        "v_mfma_f32_16x16x32_bf16 v[60:63], v[244:247], v[24:27], v[60:63]\n\t"
        "s_waitcnt lgkmcnt(0)\n\t"
        "v_mfma_f32_16x16x32_bf16 v[56:59], v[248:251], v[28:31], v[56:59]\n\t"
        "v_mfma_f32_16x16x32_bf16 v[60:63], v[252:255], v[28:31], v[60:63]\n\t"
        "s_add_u32 m0, s47, 65536\n\t"
        "s_nop 0\n\t"
        "buffer_load_dwordx4 v4, %[rx], s49 offen offset:0 lds\n\t"
        "buffer_load_dwordx4 v5, %[rx], s49 offen offset:1024 lds\n\t"
        "buffer_load_dwordx4 v6, %[rx], s49 offen offset:2048 lds\n\t"
        "buffer_load_dwordx4 v7, %[rx], s49 offen offset:3072 lds\n\t"
        "s_add_u32 s49, s49, 32768\n\t"
        "s_waitcnt vmcnt(4) lgkmcnt(0)\n\t"
        "s_barrier\n\t"
        "ds_read_b128 v[16:19], v10 offset:0\n\t"
        "ds_read_b128 v[20:23], v11 offset:0\n\t"
        "ds_read_b128 v[24:27], v10 offset:2048\n\t"
        "ds_read_b128 v[64:67], v14 offset:8192\n\t"
        "ds_read_b128 v[68:71], v14 offset:9216\n\t"
        "s_add_u32 s50, s50, 0x80000\n\t"
        "s_add_u32 s51, s51, 1\n\t"
        "s_and_b32 s70, s51, 3\n\t"
        "s_cselect_b32 s70, 0, 0x2000\n\t"
        "s_xor_b32 s50, s50, s70\n\t"
        "s_waitcnt lgkmcnt(0)\n\t"
        "v_add_f32 v64, v64, v56\n\t"
        "v_add_f32 v65, v65, v57\n\t"
        "v_add_f32 v66, v66, v58\n\t"
        "v_add_f32 v67, v67, v59\n\t"
        "v_add_f32 v68, v68, v60\n\t"
        "v_add_f32 v69, v69, v61\n\t"
        "v_add_f32 v70, v70, v62\n\t"
        "v_add_f32 v71, v71, v63\n\t"
        "v_cvt_pk_bf16_f32 v72, v64, v65\n\t"
        "v_cvt_pk_bf16_f32 v73, v68, v69\n\t"
        "v_cvt_pk_bf16_f32 v74, v66, v67\n\t"
        "v_cvt_pk_bf16_f32 v75, v70, v71\n\t"
        "s_mov_b64 exec, s[56:57]\n\t"
        "v_swap_b32 v73, v74\n\t"
        "s_nop 1\n\t"
        "v_permlane16_swap_b32 v72, v74\n\t"
        "v_permlane16_swap_b32 v73, v75\n\t"
        "s_mov_b64 exec, -1\n\t"
        "buffer_store_dwordx4 v[72:75], v79, %[rs], s50 offen sc1\n\t"
        "ds_read_b128 v[28:31], v11 offset:2048\n\t"
        "v_mfma_f32_16x16x32_bf16 v[32:35], v[128:131], v[16:19], 0\n\t"
        "v_mfma_f32_16x16x32_bf16 v[36:39], v[132:135], v[16:19], 0\n\t"
        "ds_read_b128 v[16:19], v10 offset:4096\n\t"
        "v_mfma_f32_16x16x32_bf16 v[32:35], v[136:139], v[20:23], v[32:35]\n\t"
        "v_mfma_f32_16x16x32_bf16 v[36:39], v[140:143], v[20:23], v[36:39]\n\t"
        "ds_read_b128 v[20:23], v11 offset:4096\n\t"
        "v_mfma_f32_16x16x32_bf16 v[32:35], v[144:147], v[24:27], v[32:35]\n\t"
        "v_mfma_f32_16x16x32_bf16 v[36:39], v[148:151], v[24:27], v[36:39]\n\t"
        "ds_read_b128 v[24:27], v10 offset:6144\n\t"
        "s_waitcnt lgkmcnt(3)\n\t"
        "v_mfma_f32_16x16x32_bf16 v[32:35], v[152:155], v[28:31], v[32:35]\n\t"
        "v_mfma_f32_16x16x32_bf16 v[36:39], v[156:159], v[28:31], v[36:39]\n\t"
        "ds_read_b128 v[28:31], v11 offset:6144\n\t"
        "s_waitcnt lgkmcnt(3)\n\t"
        "v_mfma_f32_16x16x32_bf16 v[32:35], v[160:163], v[16:19], v[32:35]\n\t"
        "v_mfma_f32_16x16x32_bf16 v[36:39], v[164:167], v[16:19], v[36:39]\n\t"
        "ds_read_b128 v[16:19], v10 offset:8192\n\t"
        "s_waitcnt lgkmcnt(3)\n\t"
        "v_mfma_f32_16x16x32_bf16 v[32:35], v[168:171], v[20:23], v[32:35]\n\t"
        "v_mfma_f32_16x16x32_bf16 v[36:39], v[172:175], v[20:23], v[36:39]\n\t"
        "ds_read_b128 v[20:23], v11 offset:8192\n\t"
        "s_waitcnt lgkmcnt(3)\n\t"
        "v_mfma_f32_16x16x32_bf16 v[32:35], v[176:179], v[24:27], v[32:35]\n\t"
        "v_mfma_f32_16x16x32_bf16 v[36:39], v[180:183], v[24:27], v[36:39]\n\t"
        "ds_read_b128 v[24:27], v10 offset:10240\n\t"
        "s_waitcnt lgkmcnt(3)\n\t"
        "v_mfma_f32_16x16x32_bf16 v[32:35], v[184:187], v[28:31], v[32:35]\n\t"
        "v_mfma_f32_16x16x32_bf16 v[36:39], v[188:191], v[28:31], v[36:39]\n\t"
        "ds_read_b128 v[28:31], v11 offset:10240\n\t"
        "s_waitcnt lgkmcnt(3)\n\t"
        "v_mfma_f32_16x16x32_bf16 v[32:35], v[192:195], v[16:19], v[32:35]\n\t"
        "v_mfma_f32_16x16x32_bf16 v[36:39], v[196:199], v[16:19], v[36:39]\n\t"
        "ds_read_b128 v[16:19], v10 offset:12288\n\t"
        "s_waitcnt lgkmcnt(3)\n\t"
        "v_mfma_f32_16x16x32_bf16 v[32:35], v[200:203], v[20:23], v[32:35]\n\t"
        "v_mfma_f32_16x16x32_bf16 v[36:39], v[204:207], v[20:23], v[36:39]\n\t"
        "ds_read_b128 v[20:23], v11 offset:12288\n\t"
        "s_waitcnt lgkmcnt(3)\n\t"
        "v_mfma_f32_16x16x32_bf16 v[32:35], v[208:211], v[24:27], v[32:35]\n\t"
        "v_mfma_f32_16x16x32_bf16 v[36:39], v[212:215], v[24:27], v[36:39]\n\t"
        "ds_read_b128 v[24:27], v10 offset:14336\n\t"
        "s_waitcnt lgkmcnt(3)\n\t"
        "v_mfma_f32_16x16x32_bf16 v[32:35], v[216:219], v[28:31], v[32:35]\n\t"
        "v_mfma_f32_16x16x32_bf16 v[36:39], v[220:223], v[28:31], v[36:39]\n\t"
        "ds_read_b128 v[28:31], v11 offset:14336\n\t"
        "s_waitcnt lgkmcnt(3)\n\t"
        "v_mfma_f32_16x16x32_bf16 v[32:35], v[224:227], v[16:19], v[32:35]\n\t"
        "v_mfma_f32_16x16x32_bf16 v[36:39], v[228:231], v[16:19], v[36:39]\n\t"
        "s_waitcnt lgkmcnt(2)\n\t"
        "v_mfma_f32_16x16x32_bf16 v[32:35], v[232:235], v[20:23], v[32:35]\n\t"
        "v_mfma_f32_16x16x32_bf16 v[36:39], v[236:239], v[20:23], v[36:39]\n\t"
        "s_waitcnt lgkmcnt(1)\n\t"
        "v_mfma_f32_16x16x32_bf16 v[32:35], v[240:243], v[24:27], v[32:35]\n\t"
        "v_mfma_f32_16x16x32_bf16 v[36:39], v[244:247], v[24:27], v[36:39]\n\t"
        "s_waitcnt lgkmcnt(0)\n\t"
        "v_mfma_f32_16x16x32_bf16 v[32:35], v[248:251], v[28:31], v[32:35]\n\t"
        "v_mfma_f32_16x16x32_bf16 v[36:39], v[252:255], v[28:31], v[36:39]\n\t"
        "s_add_u32 m0, s47, 98304\n\t"
        "s_nop 0\n\t"
        "buffer_load_dwordx4 v4, %[rx], s49 offen offset:0 lds\n\t"
        "buffer_load_dwordx4 v5, %[rx], s49 offen offset:1024 lds\n\t"
        "buffer_load_dwordx4 v6, %[rx], s49 offen offset:2048 lds\n\t"
        "buffer_load_dwordx4 v7, %[rx], s49 offen offset:3072 lds\n\t"
        "s_add_u32 s49, s49, 32768\n\t"
        "s_nop 7\n\t"
        "ds_write_b128 v14, v[32:35] offset:0\n\t"
        "ds_write_b128 v14, v[36:39] offset:1024\n\t"
        "s_waitcnt vmcnt(4) lgkmcnt(0)\n\t"
        "s_barrier\n\t"
        "s_add_u32 s54, s54, 1\n\t"
        "v_mov_b32 v76, s54\n\t"
        "s_and_b32 s71, s54, 127\n\t"
        "s_lshl_b32 s71, s71, 7\n\t"
        "buffer_store_dwordx2 v[76:77], v78, %[rf], s71 offen sc1\n\t"
        "ds_read_b128 v[16:19], v10 offset:32768\n\t"
        "ds_read_b128 v[20:23], v11 offset:32768\n\t"
        "ds_read_b128 v[24:27], v10 offset:34816\n\t"
        "ds_read_b128 v[28:31], v11 offset:34816\n\t"
        "s_waitcnt lgkmcnt(3)\n\t"
        "v_mfma_f32_16x16x32_bf16 v[40:43], v[128:131], v[16:19], 0\n\t"
        "v_mfma_f32_16x16x32_bf16 v[44:47], v[132:135], v[16:19], 0\n\t"
        "ds_read_b128 v[16:19], v10 offset:36864\n\t"
        "s_waitcnt lgkmcnt(3)\n\t"
        "v_mfma_f32_16x16x32_bf16 v[40:43], v[136:139], v[20:23], v[40:43]\n\t"
        "v_mfma_f32_16x16x32_bf16 v[44:47], v[140:143], v[20:23], v[44:47]\n\t"
        "ds_read_b128 v[20:23], v11 offset:36864\n\t"
        "s_waitcnt lgkmcnt(3)\n\t"
        "v_mfma_f32_16x16x32_bf16 v[40:43], v[144:147], v[24:27], v[40:43]\n\t"
        "v_mfma_f32_16x16x32_bf16 v[44:47], v[148:151], v[24:27], v[44:47]\n\t"
        "ds_read_b128 v[24:27], v10 offset:38912\n\t"
        "s_waitcnt lgkmcnt(3)\n\t"
        "v_mfma_f32_16x16x32_bf16 v[40:43], v[152:155], v[28:31], v[40:43]\n\t"
        "v_mfma_f32_16x16x32_bf16 v[44:47], v[156:159], v[28:31], v[44:47]\n\t"
        "ds_read_b128 v[28:31], v11 offset:38912\n\t"
        "s_waitcnt lgkmcnt(3)\n\t"
        "v_mfma_f32_16x16x32_bf16 v[40:43], v[160:163], v[16:19], v[40:43]\n\t"
        "v_mfma_f32_16x16x32_bf16 v[44:47], v[164:167], v[16:19], v[44:47]\n\t"
        "ds_read_b128 v[16:19], v10 offset:40960\n\t"
        "s_waitcnt lgkmcnt(3)\n\t"
        "v_mfma_f32_16x16x32_bf16 v[40:43], v[168:171], v[20:23], v[40:43]\n\t"
        "v_mfma_f32_16x16x32_bf16 v[44:47], v[172:175], v[20:23], v[44:47]\n\t"
        "ds_read_b128 v[20:23], v11 offset:40960\n\t"
        "s_waitcnt lgkmcnt(3)\n\t"
        "v_mfma_f32_16x16x32_bf16 v[40:43], v[176:179], v[24:27], v[40:43]\n\t"
        "v_mfma_f32_16x16x32_bf16 v[44:47], v[180:183], v[24:27], v[44:47]\n\t"
        "ds_read_b128 v[24:27], v10 offset:43008\n\t"
        "s_waitcnt lgkmcnt(3)\n\t"
        "v_mfma_f32_16x16x32_bf16 v[40:43], v[184:187], v[28:31], v[40:43]\n\t"
        "v_mfma_f32_16x16x32_bf16 v[44:47], v[188:191], v[28:31], v[44:47]\n\t"
        "ds_read_b128 v[28:31], v11 offset:43008\n\t"
        "s_waitcnt lgkmcnt(3)\n\t"
        "v_mfma_f32_16x16x32_bf16 v[40:43], v[192:195], v[16:19], v[40:43]\n\t"
        "v_mfma_f32_16x16x32_bf16 v[44:47], v[196:199], v[16:19], v[44:47]\n\t"
        "ds_read_b128 v[16:19], v10 offset:45056\n\t"
        "s_waitcnt lgkmcnt(3)\n\t"
        "v_mfma_f32_16x16x32_bf16 v[40:43], v[200:203], v[20:23], v[40:43]\n\t"
        "v_mfma_f32_16x16x32_bf16 v[44:47], v[204:207], v[20:23], v[44:47]\n\t"
        "ds_read_b128 v[20:23], v11 offset:45056\n\t"
        "s_waitcnt lgkmcnt(3)\n\t"
        "v_mfma_f32_16x16x32_bf16 v[40:43], v[208:211], v[24:27], v[40:43]\n\t"
        "v_mfma_f32_16x16x32_bf16 v[44:47], v[212:215], v[24:27], v[44:47]\n\t"
        "ds_read_b128 v[24:27], v10 offset:47104\n\t"
        "s_waitcnt lgkmcnt(3)\n\t"
        "v_mfma_f32_16x16x32_bf16 v[40:43], v[216:219], v[28:31], v[40:43]\n\t"
        "v_mfma_f32_16x16x32_bf16 v[44:47], v[220:223], v[28:31], v[44:47]\n\t"
        "ds_read_b128 v[28:31], v11 offset:47104\n\t"
        "s_waitcnt lgkmcnt(3)\n\t"
        "v_mfma_f32_16x16x32_bf16 v[40:43], v[224:227], v[16:19], v[40:43]\n\t"
        "v_mfma_f32_16x16x32_bf16 v[44:47], v[228:231], v[16:19], v[44:47]\n\t"
        "s_waitcnt lgkmcnt(2)\n\t"
        "v_mfma_f32_16x16x32_bf16 v[40:43], v[232:235], v[20:23], v[40:43]\n\t"
        "v_mfma_f32_16x16x32_bf16 v[44:47], v[236:239], v[20:23], v[44:47]\n\t"
        "s_waitcnt lgkmcnt(1)\n\t"
        "v_mfma_f32_16x16x32_bf16 v[40:43], v[240:243], v[24:27], v[40:43]\n\t"
        "v_mfma_f32_16x16x32_bf16 v[44:47], v[244:247], v[24:27], v[44:47]\n\t"
        "s_waitcnt lgkmcnt(0)\n\t"
        "v_mfma_f32_16x16x32_bf16 v[40:43], v[248:251], v[28:31], v[40:43]\n\t"
        "v_mfma_f32_16x16x32_bf16 v[44:47], v[252:255], v[28:31], v[44:47]\n\t"
        "s_add_u32 m0, s47, 0\n\t"
        "s_nop 0\n\t"
        "buffer_load_dwordx4 v4, %[rx], s49 offen offset:0 lds\n\t"
        "buffer_load_dwordx4 v5, %[rx], s49 offen offset:1024 lds\n\t"
        "buffer_load_dwordx4 v6, %[rx], s49 offen offset:2048 lds\n\t"
        "buffer_load_dwordx4 v7, %[rx], s49 offen offset:3072 lds\n\t"
        "s_add_u32 s49, s49, 32768\n\t"
        "s_waitcnt vmcnt(4) lgkmcnt(0)\n\t"
        "s_barrier\n\t"
        "ds_read_b128 v[16:19], v12 offset:0\n\t"
        "ds_read_b128 v[20:23], v13 offset:0\n\t"
        "ds_read_b128 v[24:27], v12 offset:2048\n\t"
        "ds_read_b128 v[64:67], v14 offset:8192\n\t"
        "ds_read_b128 v[68:71], v14 offset:9216\n\t"
        "s_add_u32 s50, s50, 0x80000\n\t"
        "s_add_u32 s51, s51, 1\n\t"
        "s_and_b32 s70, s51, 3\n\t"
        "s_cselect_b32 s70, 0, 0x2000\n\t"
        "s_xor_b32 s50, s50, s70\n\t"
        "s_waitcnt lgkmcnt(0)\n\t"
        "v_add_f32 v64, v64, v40\n\t"
        "v_add_f32 v65, v65, v41\n\t"
        "v_add_f32 v66, v66, v42\n\t"
        "v_add_f32 v67, v67, v43\n\t"
        "v_add_f32 v68, v68, v44\n\t"
        "v_add_f32 v69, v69, v45\n\t"
        "v_add_f32 v70, v70, v46\n\t"
        "v_add_f32 v71, v71, v47\n\t"
        "v_cvt_pk_bf16_f32 v72, v64, v65\n\t"
        "v_cvt_pk_bf16_f32 v73, v68, v69\n\t"
        "v_cvt_pk_bf16_f32 v74, v66, v67\n\t"
        "v_cvt_pk_bf16_f32 v75, v70, v71\n\t"
        "s_mov_b64 exec, s[56:57]\n\t"
        "v_swap_b32 v73, v74\n\t"
        "s_nop 1\n\t"
        "v_permlane16_swap_b32 v72, v74\n\t"
        "v_permlane16_swap_b32 v73, v75\n\t"
        "s_mov_b64 exec, -1\n\t"
        "buffer_store_dwordx4 v[72:75], v79, %[rs], s50 offen sc1\n\t"
        "ds_read_b128 v[28:31], v13 offset:2048\n\t"
        "v_mfma_f32_16x16x32_bf16 v[48:51], v[128:131], v[16:19], 0\n\t"
        "v_mfma_f32_16x16x32_bf16 v[52:55], v[132:135], v[16:19], 0\n\t"
        "ds_read_b128 v[16:19], v12 offset:4096\n\t"
        "v_mfma_f32_16x16x32_bf16 v[48:51], v[136:139], v[20:23], v[48:51]\n\t"
        "v_mfma_f32_16x16x32_bf16 v[52:55], v[140:143], v[20:23], v[52:55]\n\t"
        "ds_read_b128 v[20:23], v13 offset:4096\n\t"
        "v_mfma_f32_16x16x32_bf16 v[48:51], v[144:147], v[24:27], v[48:51]\n\t"
        "v_mfma_f32_16x16x32_bf16 v[52:55], v[148:151], v[24:27], v[52:55]\n\t"
        "ds_read_b128 v[24:27], v12 offset:6144\n\t"
        "s_waitcnt lgkmcnt(3)\n\t"
        "v_mfma_f32_16x16x32_bf16 v[48:51], v[152:155], v[28:31], v[48:51]\n\t"
        "v_mfma_f32_16x16x32_bf16 v[52:55], v[156:159], v[28:31], v[52:55]\n\t"
        "ds_read_b128 v[28:31], v13 offset:6144\n\t"
        "s_waitcnt lgkmcnt(3)\n\t"
        "v_mfma_f32_16x16x32_bf16 v[48:51], v[160:163], v[16:19], v[48:51]\n\t"
        "v_mfma_f32_16x16x32_bf16 v[52:55], v[164:167], v[16:19], v[52:55]\n\t"
        "ds_read_b128 v[16:19], v12 offset:8192\n\t"
        "s_waitcnt lgkmcnt(3)\n\t"
        "v_mfma_f32_16x16x32_bf16 v[48:51], v[168:171], v[20:23], v[48:51]\n\t"
        "v_mfma_f32_16x16x32_bf16 v[52:55], v[172:175], v[20:23], v[52:55]\n\t"
        "ds_read_b128 v[20:23], v13 offset:8192\n\t"
        "s_waitcnt lgkmcnt(3)\n\t"
        "v_mfma_f32_16x16x32_bf16 v[48:51], v[176:179], v[24:27], v[48:51]\n\t"
        "v_mfma_f32_16x16x32_bf16 v[52:55], v[180:183], v[24:27], v[52:55]\n\t"
        "ds_read_b128 v[24:27], v12 offset:10240\n\t"
        "s_waitcnt lgkmcnt(3)\n\t"
        "v_mfma_f32_16x16x32_bf16 v[48:51], v[184:187], v[28:31], v[48:51]\n\t"
        "v_mfma_f32_16x16x32_bf16 v[52:55], v[188:191], v[28:31], v[52:55]\n\t"
        "ds_read_b128 v[28:31], v13 offset:10240\n\t"
        "s_waitcnt lgkmcnt(3)\n\t"
        "v_mfma_f32_16x16x32_bf16 v[48:51], v[192:195], v[16:19], v[48:51]\n\t"
        "v_mfma_f32_16x16x32_bf16 v[52:55], v[196:199], v[16:19], v[52:55]\n\t"
        "ds_read_b128 v[16:19], v12 offset:12288\n\t"
        "s_waitcnt lgkmcnt(3)\n\t"
        "v_mfma_f32_16x16x32_bf16 v[48:51], v[200:203], v[20:23], v[48:51]\n\t"
        "v_mfma_f32_16x16x32_bf16 v[52:55], v[204:207], v[20:23], v[52:55]\n\t"
        "ds_read_b128 v[20:23], v13 offset:12288\n\t"
        "s_waitcnt lgkmcnt(3)\n\t"
        "v_mfma_f32_16x16x32_bf16 v[48:51], v[208:211], v[24:27], v[48:51]\n\t"
        "v_mfma_f32_16x16x32_bf16 v[52:55], v[212:215], v[24:27], v[52:55]\n\t"
        "ds_read_b128 v[24:27], v12 offset:14336\n\t"
        "s_waitcnt lgkmcnt(3)\n\t"
        "v_mfma_f32_16x16x32_bf16 v[48:51], v[216:219], v[28:31], v[48:51]\n\t"
        "v_mfma_f32_16x16x32_bf16 v[52:55], v[220:223], v[28:31], v[52:55]\n\t"
        "ds_read_b128 v[28:31], v13 offset:14336\n\t"
        "s_waitcnt lgkmcnt(3)\n\t"
        "v_mfma_f32_16x16x32_bf16 v[48:51], v[224:227], v[16:19], v[48:51]\n\t"
        "v_mfma_f32_16x16x32_bf16 v[52:55], v[228:231], v[16:19], v[52:55]\n\t"
        "s_waitcnt lgkmcnt(2)\n\t"
        "v_mfma_f32_16x16x32_bf16 v[48:51], v[232:235], v[20:23], v[48:51]\n\t"
        "v_mfma_f32_16x16x32_bf16 v[52:55], v[236:239], v[20:23], v[52:55]\n\t"
        "s_waitcnt lgkmcnt(1)\n\t"
        "v_mfma_f32_16x16x32_bf16 v[48:51], v[240:243], v[24:27], v[48:51]\n\t"
        "v_mfma_f32_16x16x32_bf16 v[52:55], v[244:247], v[24:27], v[52:55]\n\t"
        "s_waitcnt lgkmcnt(0)\n\t"
        "v_mfma_f32_16x16x32_bf16 v[48:51], v[248:251], v[28:31], v[48:51]\n\t"
        "v_mfma_f32_16x16x32_bf16 v[52:55], v[252:255], v[28:31], v[52:55]\n\t"
        "s_add_u32 m0, s47, 32768\n\t"
        "s_nop 0\n\t"
        "buffer_load_dwordx4 v4, %[rx], s49 offen offset:0 lds\n\t"
        "buffer_load_dwordx4 v5, %[rx], s49 offen offset:1024 lds\n\t"
        "buffer_load_dwordx4 v6, %[rx], s49 offen offset:2048 lds\n\t"
        "buffer_load_dwordx4 v7, %[rx], s49 offen offset:3072 lds\n\t"
        "s_add_u32 s49, s49, 32768\n\t"
        "s_nop 7\n\t"
        "ds_write_b128 v14, v[48:51] offset:0\n\t"
        "ds_write_b128 v14, v[52:55] offset:1024\n\t"
        "s_waitcnt vmcnt(4) lgkmcnt(0)\n\t"
        "s_barrier\n\t"
        "s_add_u32 s54, s54, 1\n\t"
        "v_mov_b32 v76, s54\n\t"
        "s_and_b32 s71, s54, 127\n\t"
        "s_lshl_b32 s71, s71, 7\n\t"
        "buffer_store_dwordx2 v[76:77], v78, %[rf], s71 offen sc1\n\t"
        "ds_read_b128 v[16:19], v12 offset:32768\n\t"
        "ds_read_b128 v[20:23], v13 offset:32768\n\t"
        "ds_read_b128 v[24:27], v12 offset:34816\n\t"
        "ds_read_b128 v[28:31], v13 offset:34816\n\t"
        "s_waitcnt lgkmcnt(3)\n\t"
        "v_mfma_f32_16x16x32_bf16 v[56:59], v[128:131], v[16:19], 0\n\t"
        "v_mfma_f32_16x16x32_bf16 v[60:63], v[132:135], v[16:19], 0\n\t"
        "ds_read_b128 v[16:19], v12 offset:36864\n\t"
        "s_waitcnt lgkmcnt(3)\n\t"
        "v_mfma_f32_16x16x32_bf16 v[56:59], v[136:139], v[20:23], v[56:59]\n\t"
        "v_mfma_f32_16x16x32_bf16 v[60:63], v[140:143], v[20:23], v[60:63]\n\t"
        "ds_read_b128 v[20:23], v13 offset:36864\n\t"
        "s_waitcnt lgkmcnt(3)\n\t"
        "v_mfma_f32_16x16x32_bf16 v[56:59], v[144:147], v[24:27], v[56:59]\n\t"
        "v_mfma_f32_16x16x32_bf16 v[60:63], v[148:151], v[24:27], v[60:63]\n\t"
        "ds_read_b128 v[24:27], v12 offset:38912\n\t"
        "s_waitcnt lgkmcnt(3)\n\t"
        "v_mfma_f32_16x16x32_bf16 v[56:59], v[152:155], v[28:31], v[56:59]\n\t"
        "v_mfma_f32_16x16x32_bf16 v[60:63], v[156:159], v[28:31], v[60:63]\n\t"
        "ds_read_b128 v[28:31], v13 offset:38912\n\t"
        "s_waitcnt lgkmcnt(3)\n\t"
        "v_mfma_f32_16x16x32_bf16 v[56:59], v[160:163], v[16:19], v[56:59]\n\t"
        "v_mfma_f32_16x16x32_bf16 v[60:63], v[164:167], v[16:19], v[60:63]\n\t"
        "ds_read_b128 v[16:19], v12 offset:40960\n\t"
        "s_waitcnt lgkmcnt(3)\n\t"
        "v_mfma_f32_16x16x32_bf16 v[56:59], v[168:171], v[20:23], v[56:59]\n\t"
        "v_mfma_f32_16x16x32_bf16 v[60:63], v[172:175], v[20:23], v[60:63]\n\t"
        "ds_read_b128 v[20:23], v13 offset:40960\n\t"
        "s_waitcnt lgkmcnt(3)\n\t"
        "v_mfma_f32_16x16x32_bf16 v[56:59], v[176:179], v[24:27], v[56:59]\n\t"
        "v_mfma_f32_16x16x32_bf16 v[60:63], v[180:183], v[24:27], v[60:63]\n\t"
        "ds_read_b128 v[24:27], v12 offset:43008\n\t"
        "s_waitcnt lgkmcnt(3)\n\t"
        "v_mfma_f32_16x16x32_bf16 v[56:59], v[184:187], v[28:31], v[56:59]\n\t"
        "v_mfma_f32_16x16x32_bf16 v[60:63], v[188:191], v[28:31], v[60:63]\n\t"
        "ds_read_b128 v[28:31], v13 offset:43008\n\t"
        "s_waitcnt lgkmcnt(3)\n\t"
        "v_mfma_f32_16x16x32_bf16 v[56:59], v[192:195], v[16:19], v[56:59]\n\t"
        "v_mfma_f32_16x16x32_bf16 v[60:63], v[196:199], v[16:19], v[60:63]\n\t"
        "ds_read_b128 v[16:19], v12 offset:45056\n\t"
        "s_waitcnt lgkmcnt(3)\n\t"
        "v_mfma_f32_16x16x32_bf16 v[56:59], v[200:203], v[20:23], v[56:59]\n\t"
        "v_mfma_f32_16x16x32_bf16 v[60:63], v[204:207], v[20:23], v[60:63]\n\t"
        "ds_read_b128 v[20:23], v13 offset:45056\n\t"
        "s_waitcnt lgkmcnt(3)\n\t"
        "v_mfma_f32_16x16x32_bf16 v[56:59], v[208:211], v[24:27], v[56:59]\n\t"
        "v_mfma_f32_16x16x32_bf16 v[60:63], v[212:215], v[24:27], v[60:63]\n\t"
        "ds_read_b128 v[24:27], v12 offset:47104\n\t"
        "s_waitcnt lgkmcnt(3)\n\t"
        "v_mfma_f32_16x16x32_bf16 v[56:59], v[216:219], v[28:31], v[56:59]\n\t"
        "v_mfma_f32_16x16x32_bf16 v[60:63], v[220:223], v[28:31], v[60:63]\n\t"
        "ds_read_b128 v[28:31], v13 offset:47104\n\t"
        "s_waitcnt lgkmcnt(3)\n\t"
        "v_mfma_f32_16x16x32_bf16 v[56:59], v[224:227], v[16:19], v[56:59]\n\t"
        "v_mfma_f32_16x16x32_bf16 v[60:63], v[228:231], v[16:19], v[60:63]\n\t"
        "s_waitcnt lgkmcnt(2)\n\t"
        "v_mfma_f32_16x16x32_bf16 v[56:59], v[232:235], v[20:23], v[56:59]\n\t"
        "v_mfma_f32_16x16x32_bf16 v[60:63], v[236:239], v[20:23], v[60:63]\n\t"
        "s_waitcnt lgkmcnt(1)\n\t"
        "v_mfma_f32_16x16x32_bf16 v[56:59], v[240:243], v[24:27], v[56:59]\n\t"
        "v_mfma_f32_16x16x32_bf16 v[60:63], v[244:247], v[24:27], v[60:63]\n\t"
        "s_waitcnt lgkmcnt(0)\n\t"
        "v_mfma_f32_16x16x32_bf16 v[56:59], v[248:251], v[28:31], v[56:59]\n\t"
        "v_mfma_f32_16x16x32_bf16 v[60:63], v[252:255], v[28:31], v[60:63]\n\t"
        "s_add_u32 m0, s47, 65536\n\t"
        "s_nop 0\n\t"
        "buffer_load_dwordx4 v4, %[rx], s49 offen offset:0 lds\n\t"
        "buffer_load_dwordx4 v5, %[rx], s49 offen offset:1024 lds\n\t"
        "buffer_load_dwordx4 v6, %[rx], s49 offen offset:2048 lds\n\t"
        "buffer_load_dwordx4 v7, %[rx], s49 offen offset:3072 lds\n\t"
        "s_add_u32 s49, s49, 32768\n\t"
        "s_waitcnt vmcnt(4) lgkmcnt(0)\n\t"
        "s_barrier\n\t"
        "s_mov_b32 s52, 29\n\t"
        "s_sub_u32 s54, s54, 1\n\t"
        "ds_read_b128 v[16:19], v10 offset:0\n\t"
        "ds_read_b128 v[20:23], v11 offset:0\n\t"
        "ds_read_b128 v[24:27], v10 offset:2048\n\t"
        ".p2align 6\n\t"
        ".Lx_loop1_%=:\n\t"
        "ds_read_b128 v[28:31], v11 offset:2048\n\t"
        "s_waitcnt lgkmcnt(3)\n\t"
        "v_mfma_f32_16x16x32_bf16 v[32:35], v[128:131], v[16:19], 0\n\t"
        "s_add_u32 m0, s47, 98304\n\t"
        "v_mfma_f32_16x16x32_bf16 v[36:39], v[132:135], v[16:19], 0\n\t"
        "ds_read_b128 v[16:19], v10 offset:4096\n\t"
        "s_waitcnt lgkmcnt(3)\n\t"
        "v_mfma_f32_16x16x32_bf16 v[32:35], v[136:139], v[20:23], v[32:35]\n\t"
        "buffer_load_dwordx4 v4, %[rx], s49 offen offset:0 lds\n\t"
        "v_mfma_f32_16x16x32_bf16 v[36:39], v[140:143], v[20:23], v[36:39]\n\t"
        "ds_read_b128 v[20:23], v11 offset:4096\n\t"
        "s_waitcnt lgkmcnt(3)\n\t"
        "v_mfma_f32_16x16x32_bf16 v[32:35], v[144:147], v[24:27], v[32:35]\n\t"
        "v_mfma_f32_16x16x32_bf16 v[36:39], v[148:151], v[24:27], v[36:39]\n\t"
        "ds_read_b128 v[24:27], v10 offset:6144\n\t"
        "s_waitcnt lgkmcnt(3)\n\t"
        "v_mfma_f32_16x16x32_bf16 v[32:35], v[152:155], v[28:31], v[32:35]\n\t"
        "buffer_load_dwordx4 v5, %[rx], s49 offen offset:1024 lds\n\t"
        "v_mfma_f32_16x16x32_bf16 v[36:39], v[156:159], v[28:31], v[36:39]\n\t"
        "ds_read_b128 v[28:31], v11 offset:6144\n\t"
        "s_waitcnt lgkmcnt(3)\n\t"
        "v_mfma_f32_16x16x32_bf16 v[32:35], v[160:163], v[16:19], v[32:35]\n\t"
        "v_mfma_f32_16x16x32_bf16 v[36:39], v[164:167], v[16:19], v[36:39]\n\t"
        "ds_read_b128 v[16:19], v10 offset:8192\n\t"
        "s_waitcnt lgkmcnt(3)\n\t"
        "v_mfma_f32_16x16x32_bf16 v[32:35], v[168:171], v[20:23], v[32:35]\n\t"
        "buffer_load_dwordx4 v6, %[rx], s49 offen offset:2048 lds\n\t"
        "v_mfma_f32_16x16x32_bf16 v[36:39], v[172:175], v[20:23], v[36:39]\n\t"
        "ds_read_b128 v[20:23], v11 offset:8192\n\t"
        "s_waitcnt lgkmcnt(3)\n\t"
        "v_mfma_f32_16x16x32_bf16 v[32:35], v[176:179], v[24:27], v[32:35]\n\t"
        "v_mfma_f32_16x16x32_bf16 v[36:39], v[180:183], v[24:27], v[36:39]\n\t"
        "ds_read_b128 v[24:27], v10 offset:10240\n\t"
        "s_waitcnt lgkmcnt(3)\n\t"
        "v_mfma_f32_16x16x32_bf16 v[32:35], v[184:187], v[28:31], v[32:35]\n\t"
        "buffer_load_dwordx4 v7, %[rx], s49 offen offset:3072 lds\n\t"
        "v_mfma_f32_16x16x32_bf16 v[36:39], v[188:191], v[28:31], v[36:39]\n\t"
        "ds_read_b128 v[28:31], v11 offset:10240\n\t"
        "s_waitcnt lgkmcnt(3)\n\t"
        "v_mfma_f32_16x16x32_bf16 v[32:35], v[192:195], v[16:19], v[32:35]\n\t"
        "s_add_u32 s49, s49, 32768\n\t"
        "v_mfma_f32_16x16x32_bf16 v[36:39], v[196:199], v[16:19], v[36:39]\n\t"
        "ds_read_b128 v[16:19], v10 offset:12288\n\t"
        "s_waitcnt lgkmcnt(3)\n\t"
        "v_mfma_f32_16x16x32_bf16 v[32:35], v[200:203], v[20:23], v[32:35]\n\t"
        "v_mfma_f32_16x16x32_bf16 v[36:39], v[204:207], v[20:23], v[36:39]\n\t"
        "ds_read_b128 v[20:23], v11 offset:12288\n\t"
        "s_waitcnt lgkmcnt(3)\n\t"
        "v_mfma_f32_16x16x32_bf16 v[32:35], v[208:211], v[24:27], v[32:35]\n\t"
        "v_mfma_f32_16x16x32_bf16 v[36:39], v[212:215], v[24:27], v[36:39]\n\t"
        "ds_read_b128 v[24:27], v10 offset:14336\n\t"
        "s_waitcnt lgkmcnt(3)\n\t"
        "v_mfma_f32_16x16x32_bf16 v[32:35], v[216:219], v[28:31], v[32:35]\n\t"
        "v_mfma_f32_16x16x32_bf16 v[36:39], v[220:223], v[28:31], v[36:39]\n\t"
        "ds_read_b128 v[28:31], v11 offset:14336\n\t"
        "s_waitcnt lgkmcnt(3)\n\t"
        "v_mfma_f32_16x16x32_bf16 v[32:35], v[224:227], v[16:19], v[32:35]\n\t"
        "v_mfma_f32_16x16x32_bf16 v[36:39], v[228:231], v[16:19], v[36:39]\n\t"
        "ds_read_b128 v[16:19], v10 offset:32768\n\t"
        "s_waitcnt lgkmcnt(3)\n\t"
        "v_mfma_f32_16x16x32_bf16 v[32:35], v[232:235], v[20:23], v[32:35]\n\t"
        "v_mfma_f32_16x16x32_bf16 v[36:39], v[236:239], v[20:23], v[36:39]\n\t"
        "ds_read_b128 v[20:23], v11 offset:32768\n\t"
        "s_waitcnt lgkmcnt(3)\n\t"
        "v_mfma_f32_16x16x32_bf16 v[32:35], v[240:243], v[24:27], v[32:35]\n\t"
        "v_mfma_f32_16x16x32_bf16 v[36:39], v[244:247], v[24:27], v[36:39]\n\t"
        "ds_read_b128 v[24:27], v10 offset:34816\n\t"
        "s_waitcnt lgkmcnt(3)\n\t"
        "v_mfma_f32_16x16x32_bf16 v[32:35], v[248:251], v[28:31], v[32:35]\n\t"
        "v_mfma_f32_16x16x32_bf16 v[36:39], v[252:255], v[28:31], v[36:39]\n\t"
        "s_waitcnt vmcnt(4)\n\t"
        "s_barrier\n\t"
        "ds_read_b128 v[28:31], v11 offset:34816\n\t"
        "s_waitcnt lgkmcnt(3)\n\t"
        "v_mfma_f32_16x16x32_bf16 v[40:43], v[128:131], v[16:19], 0\n\t"
        "s_add_u32 m0, s47, 0\n\t"
        "ds_read_b128 v[64:67], v14 offset:8192\n\t"
        "v_mfma_f32_16x16x32_bf16 v[44:47], v[132:135], v[16:19], 0\n\t"
        "ds_read_b128 v[68:71], v14 offset:9216\n\t"
        "ds_read_b128 v[16:19], v10 offset:36864\n\t"
        "s_waitcnt lgkmcnt(5)\n\t"
        "v_mfma_f32_16x16x32_bf16 v[40:43], v[136:139], v[20:23], v[40:43]\n\t"
        "buffer_load_dwordx4 v4, %[rx], s49 offen offset:0 lds\n\t"
        "ds_write_b128 v14, v[32:35] offset:0\n\t"
        "v_mfma_f32_16x16x32_bf16 v[44:47], v[140:143], v[20:23], v[44:47]\n\t"
        "s_add_u32 s50, s50, 0x80000\n\t"
        "s_add_u32 s51, s51, 1\n\t"
        "ds_read_b128 v[20:23], v11 offset:36864\n\t"
        "s_waitcnt lgkmcnt(6)\n\t"
        "v_mfma_f32_16x16x32_bf16 v[40:43], v[144:147], v[24:27], v[40:43]\n\t"
        "ds_write_b128 v14, v[36:39] offset:1024\n\t"
        "s_and_b32 s70, s51, 3\n\t"
        "v_mfma_f32_16x16x32_bf16 v[44:47], v[148:151], v[24:27], v[44:47]\n\t"
        "s_cselect_b32 s70, 0, 0x2000\n\t"
        "ds_read_b128 v[24:27], v10 offset:38912\n\t"
        "s_waitcnt lgkmcnt(7)\n\t"
        "v_mfma_f32_16x16x32_bf16 v[40:43], v[152:155], v[28:31], v[40:43]\n\t"
        "buffer_load_dwordx4 v5, %[rx], s49 offen offset:1024 lds\n\t"
        "v_mfma_f32_16x16x32_bf16 v[44:47], v[156:159], v[28:31], v[44:47]\n\t"
        "s_xor_b32 s50, s50, s70\n\t"
        "ds_read_b128 v[28:31], v11 offset:38912\n\t"
        "s_waitcnt lgkmcnt(5)\n\t"
        "v_mfma_f32_16x16x32_bf16 v[40:43], v[160:163], v[16:19], v[40:43]\n\t"
        "v_add_f32 v64, v64, v56\n\t"
        "v_mfma_f32_16x16x32_bf16 v[44:47], v[164:167], v[16:19], v[44:47]\n\t"
        "v_add_f32 v65, v65, v57\n\t"
        "ds_read_b128 v[16:19], v10 offset:40960\n\t"
        "s_waitcnt lgkmcnt(4)\n\t"
        "v_mfma_f32_16x16x32_bf16 v[40:43], v[168:171], v[20:23], v[40:43]\n\t"
        "buffer_load_dwordx4 v6, %[rx], s49 offen offset:2048 lds\n\t"
        "v_add_f32 v66, v66, v58\n\t"
        "v_mfma_f32_16x16x32_bf16 v[44:47], v[172:175], v[20:23], v[44:47]\n\t"
        "v_add_f32 v67, v67, v59\n\t"
        "ds_read_b128 v[20:23], v11 offset:40960\n\t"
        "s_waitcnt lgkmcnt(3)\n\t"
        "v_mfma_f32_16x16x32_bf16 v[40:43], v[176:179], v[24:27], v[40:43]\n\t"
        "v_add_f32 v68, v68, v60\n\t"
        "v_mfma_f32_16x16x32_bf16 v[44:47], v[180:183], v[24:27], v[44:47]\n\t"
        "v_add_f32 v69, v69, v61\n\t"
        "ds_read_b128 v[24:27], v10 offset:43008\n\t"
        "s_waitcnt lgkmcnt(3)\n\t"
        "v_mfma_f32_16x16x32_bf16 v[40:43], v[184:187], v[28:31], v[40:43]\n\t"
        "buffer_load_dwordx4 v7, %[rx], s49 offen offset:3072 lds\n\t"
        "v_add_f32 v70, v70, v62\n\t"
        "v_mfma_f32_16x16x32_bf16 v[44:47], v[188:191], v[28:31], v[44:47]\n\t"
        "v_add_f32 v71, v71, v63\n\t"
        "ds_read_b128 v[28:31], v11 offset:43008\n\t"
        "s_waitcnt lgkmcnt(3)\n\t"
        "v_mfma_f32_16x16x32_bf16 v[40:43], v[192:195], v[16:19], v[40:43]\n\t"
        "s_add_u32 s49, s49, 32768\n\t"
        "v_cvt_pk_bf16_f32 v72, v64, v65\n\t"
        "v_mfma_f32_16x16x32_bf16 v[44:47], v[196:199], v[16:19], v[44:47]\n\t"
        "v_cvt_pk_bf16_f32 v73, v68, v69\n\t"
        "s_add_u32 s54, s54, 1\n\t"
        "ds_read_b128 v[16:19], v10 offset:45056\n\t"
        "s_waitcnt lgkmcnt(3)\n\t"
        "v_mfma_f32_16x16x32_bf16 v[40:43], v[200:203], v[20:23], v[40:43]\n\t"
        "v_cvt_pk_bf16_f32 v74, v66, v67\n\t"
        "v_cvt_pk_bf16_f32 v75, v70, v71\n\t"
        "v_mfma_f32_16x16x32_bf16 v[44:47], v[204:207], v[20:23], v[44:47]\n\t"
        "v_mov_b32 v76, s54\n\t"
        "ds_read_b128 v[20:23], v11 offset:45056\n\t"
        "s_waitcnt lgkmcnt(3)\n\t"
        "v_mfma_f32_16x16x32_bf16 v[40:43], v[208:211], v[24:27], v[40:43]\n\t"
        "s_mov_b64 exec, s[56:57]\n\t"
        "v_swap_b32 v73, v74\n\t"
        "s_nop 1\n\t"
        "v_permlane16_swap_b32 v72, v74\n\t"
        "v_permlane16_swap_b32 v73, v75\n\t"
        "s_mov_b64 exec, -1\n\t"
        "buffer_store_dwordx4 v[72:75], v79, %[rs], s50 offen sc1\n\t"
        "v_mfma_f32_16x16x32_bf16 v[44:47], v[212:215], v[24:27], v[44:47]\n\t"
        "s_and_b32 s71, s54, 127\n\t"
        "s_cmp_le_u32 s71, 16\n\t"
        "s_cselect_b32 s72, 0, 3\n\t"
        "s_mul_i32 s71, s71, 128\n\t"
        "s_and_b32 s72, s72, s54\n\t"
        "s_cselect_b32 s71, 0x70000000, s71\n\t"
        "ds_read_b128 v[24:27], v10 offset:47104\n\t"
        "s_waitcnt lgkmcnt(3)\n\t"
        "v_mfma_f32_16x16x32_bf16 v[40:43], v[216:219], v[28:31], v[40:43]\n\t"
        "buffer_store_dwordx2 v[76:77], v78, %[rf], s71 offen sc1\n\t"
        "v_mfma_f32_16x16x32_bf16 v[44:47], v[220:223], v[28:31], v[44:47]\n\t"
        "ds_read_b128 v[28:31], v11 offset:47104\n\t"
        "s_waitcnt lgkmcnt(3)\n\t"
        "v_mfma_f32_16x16x32_bf16 v[40:43], v[224:227], v[16:19], v[40:43]\n\t"
        "v_mfma_f32_16x16x32_bf16 v[44:47], v[228:231], v[16:19], v[44:47]\n\t"
        "ds_read_b128 v[16:19], v12 offset:0\n\t"
        "s_waitcnt lgkmcnt(3)\n\t"
        "v_mfma_f32_16x16x32_bf16 v[40:43], v[232:235], v[20:23], v[40:43]\n\t"
        "v_mfma_f32_16x16x32_bf16 v[44:47], v[236:239], v[20:23], v[44:47]\n\t"
        "ds_read_b128 v[20:23], v13 offset:0\n\t"
        "s_waitcnt lgkmcnt(3)\n\t"
        "v_mfma_f32_16x16x32_bf16 v[40:43], v[240:243], v[24:27], v[40:43]\n\t"
        "v_mfma_f32_16x16x32_bf16 v[44:47], v[244:247], v[24:27], v[44:47]\n\t"
        "ds_read_b128 v[24:27], v12 offset:2048\n\t"
        "s_waitcnt lgkmcnt(3)\n\t"
        "v_mfma_f32_16x16x32_bf16 v[40:43], v[248:251], v[28:31], v[40:43]\n\t"
        "v_mfma_f32_16x16x32_bf16 v[44:47], v[252:255], v[28:31], v[44:47]\n\t"
        "s_waitcnt vmcnt(6)\n\t"
        "s_barrier\n\t"
        "ds_read_b128 v[28:31], v13 offset:2048\n\t"
        "s_waitcnt lgkmcnt(3)\n\t"
        "v_mfma_f32_16x16x32_bf16 v[48:51], v[128:131], v[16:19], 0\n\t"
        "s_add_u32 m0, s47, 32768\n\t"
        "v_mfma_f32_16x16x32_bf16 v[52:55], v[132:135], v[16:19], 0\n\t"
        "ds_read_b128 v[16:19], v12 offset:4096\n\t"
        "s_waitcnt lgkmcnt(3)\n\t"
        "v_mfma_f32_16x16x32_bf16 v[48:51], v[136:139], v[20:23], v[48:51]\n\t"
        "buffer_load_dwordx4 v4, %[rx], s49 offen offset:0 lds\n\t"
        "v_mfma_f32_16x16x32_bf16 v[52:55], v[140:143], v[20:23], v[52:55]\n\t"
        "ds_read_b128 v[20:23], v13 offset:4096\n\t"
        "s_waitcnt lgkmcnt(3)\n\t"
        "v_mfma_f32_16x16x32_bf16 v[48:51], v[144:147], v[24:27], v[48:51]\n\t"
        "v_mfma_f32_16x16x32_bf16 v[52:55], v[148:151], v[24:27], v[52:55]\n\t"
        "ds_read_b128 v[24:27], v12 offset:6144\n\t"
        "s_waitcnt lgkmcnt(3)\n\t"
        "v_mfma_f32_16x16x32_bf16 v[48:51], v[152:155], v[28:31], v[48:51]\n\t"
        "buffer_load_dwordx4 v5, %[rx], s49 offen offset:1024 lds\n\t"
        "v_mfma_f32_16x16x32_bf16 v[52:55], v[156:159], v[28:31], v[52:55]\n\t"
        "ds_read_b128 v[28:31], v13 offset:6144\n\t"
        "s_waitcnt lgkmcnt(3)\n\t"
        "v_mfma_f32_16x16x32_bf16 v[48:51], v[160:163], v[16:19], v[48:51]\n\t"
        "v_mfma_f32_16x16x32_bf16 v[52:55], v[164:167], v[16:19], v[52:55]\n\t"
        "ds_read_b128 v[16:19], v12 offset:8192\n\t"
        "s_waitcnt lgkmcnt(3)\n\t"
        "v_mfma_f32_16x16x32_bf16 v[48:51], v[168:171], v[20:23], v[48:51]\n\t"
        "buffer_load_dwordx4 v6, %[rx], s49 offen offset:2048 lds\n\t"
        "v_mfma_f32_16x16x32_bf16 v[52:55], v[172:175], v[20:23], v[52:55]\n\t"
        "ds_read_b128 v[20:23], v13 offset:8192\n\t"
        "s_waitcnt lgkmcnt(3)\n\t"
        "v_mfma_f32_16x16x32_bf16 v[48:51], v[176:179], v[24:27], v[48:51]\n\t"
        "v_mfma_f32_16x16x32_bf16 v[52:55], v[180:183], v[24:27], v[52:55]\n\t"
        "ds_read_b128 v[24:27], v12 offset:10240\n\t"
        "s_waitcnt lgkmcnt(3)\n\t"
        "v_mfma_f32_16x16x32_bf16 v[48:51], v[184:187], v[28:31], v[48:51]\n\t"
        "buffer_load_dwordx4 v7, %[rx], s49 offen offset:3072 lds\n\t"
        "v_mfma_f32_16x16x32_bf16 v[52:55], v[188:191], v[28:31], v[52:55]\n\t"
        "ds_read_b128 v[28:31], v13 offset:10240\n\t"
        "s_waitcnt lgkmcnt(3)\n\t"
        "v_mfma_f32_16x16x32_bf16 v[48:51], v[192:195], v[16:19], v[48:51]\n\t"
        "s_add_u32 s49, s49, 32768\n\t"
        "v_mfma_f32_16x16x32_bf16 v[52:55], v[196:199], v[16:19], v[52:55]\n\t"
        "ds_read_b128 v[16:19], v12 offset:12288\n\t"
        "s_waitcnt lgkmcnt(3)\n\t"
        "v_mfma_f32_16x16x32_bf16 v[48:51], v[200:203], v[20:23], v[48:51]\n\t"
        "v_mfma_f32_16x16x32_bf16 v[52:55], v[204:207], v[20:23], v[52:55]\n\t"
        "ds_read_b128 v[20:23], v13 offset:12288\n\t"
        "s_waitcnt lgkmcnt(3)\n\t"
        "v_mfma_f32_16x16x32_bf16 v[48:51], v[208:211], v[24:27], v[48:51]\n\t"
        "v_mfma_f32_16x16x32_bf16 v[52:55], v[212:215], v[24:27], v[52:55]\n\t"
        "ds_read_b128 v[24:27], v12 offset:14336\n\t"
        "s_waitcnt lgkmcnt(3)\n\t"
        "v_mfma_f32_16x16x32_bf16 v[48:51], v[216:219], v[28:31], v[48:51]\n\t"
        "v_mfma_f32_16x16x32_bf16 v[52:55], v[220:223], v[28:31], v[52:55]\n\t"
        "ds_read_b128 v[28:31], v13 offset:14336\n\t"
        "s_waitcnt lgkmcnt(3)\n\t"
        "v_mfma_f32_16x16x32_bf16 v[48:51], v[224:227], v[16:19], v[48:51]\n\t"
        "v_mfma_f32_16x16x32_bf16 v[52:55], v[228:231], v[16:19], v[52:55]\n\t"
        "ds_read_b128 v[16:19], v12 offset:32768\n\t"
        "s_waitcnt lgkmcnt(3)\n\t"
        "v_mfma_f32_16x16x32_bf16 v[48:51], v[232:235], v[20:23], v[48:51]\n\t"
        "v_mfma_f32_16x16x32_bf16 v[52:55], v[236:239], v[20:23], v[52:55]\n\t"
        "ds_read_b128 v[20:23], v13 offset:32768\n\t"
        "s_waitcnt lgkmcnt(3)\n\t"
        "v_mfma_f32_16x16x32_bf16 v[48:51], v[240:243], v[24:27], v[48:51]\n\t"
        "v_mfma_f32_16x16x32_bf16 v[52:55], v[244:247], v[24:27], v[52:55]\n\t"
        "ds_read_b128 v[24:27], v12 offset:34816\n\t"
        "s_waitcnt lgkmcnt(3)\n\t"
        "v_mfma_f32_16x16x32_bf16 v[48:51], v[248:251], v[28:31], v[48:51]\n\t"
        "v_mfma_f32_16x16x32_bf16 v[52:55], v[252:255], v[28:31], v[52:55]\n\t"
        "s_waitcnt vmcnt(4)\n\t"
        "s_barrier\n\t"
        "ds_read_b128 v[28:31], v13 offset:34816\n\t"
        "s_waitcnt lgkmcnt(3)\n\t"
        "v_mfma_f32_16x16x32_bf16 v[56:59], v[128:131], v[16:19], 0\n\t"
        "s_add_u32 m0, s47, 65536\n\t"
        "ds_read_b128 v[64:67], v14 offset:8192\n\t"
        "v_mfma_f32_16x16x32_bf16 v[60:63], v[132:135], v[16:19], 0\n\t"
        "ds_read_b128 v[68:71], v14 offset:9216\n\t"
        "ds_read_b128 v[16:19], v12 offset:36864\n\t"
        "s_waitcnt lgkmcnt(5)\n\t"
        "v_mfma_f32_16x16x32_bf16 v[56:59], v[136:139], v[20:23], v[56:59]\n\t"
        "buffer_load_dwordx4 v4, %[rx], s49 offen offset:0 lds\n\t"
        "ds_write_b128 v14, v[48:51] offset:0\n\t"
        "v_mfma_f32_16x16x32_bf16 v[60:63], v[140:143], v[20:23], v[60:63]\n\t"
        "s_add_u32 s50, s50, 0x80000\n\t"
        "s_add_u32 s51, s51, 1\n\t"
        "ds_read_b128 v[20:23], v13 offset:36864\n\t"
        "s_waitcnt lgkmcnt(6)\n\t"
        "v_mfma_f32_16x16x32_bf16 v[56:59], v[144:147], v[24:27], v[56:59]\n\t"
        "ds_write_b128 v14, v[52:55] offset:1024\n\t"
        "s_and_b32 s70, s51, 3\n\t"
        "v_mfma_f32_16x16x32_bf16 v[60:63], v[148:151], v[24:27], v[60:63]\n\t"
        "s_cselect_b32 s70, 0, 0x2000\n\t"
        "ds_read_b128 v[24:27], v12 offset:38912\n\t"
        "s_waitcnt lgkmcnt(7)\n\t"
        "v_mfma_f32_16x16x32_bf16 v[56:59], v[152:155], v[28:31], v[56:59]\n\t"
        "buffer_load_dwordx4 v5, %[rx], s49 offen offset:1024 lds\n\t"
        "v_mfma_f32_16x16x32_bf16 v[60:63], v[156:159], v[28:31], v[60:63]\n\t"
        "s_xor_b32 s50, s50, s70\n\t"
        "ds_read_b128 v[28:31], v13 offset:38912\n\t"
        "s_waitcnt lgkmcnt(5)\n\t"
        "v_mfma_f32_16x16x32_bf16 v[56:59], v[160:163], v[16:19], v[56:59]\n\t"
        "v_add_f32 v64, v64, v40\n\t"
        "v_mfma_f32_16x16x32_bf16 v[60:63], v[164:167], v[16:19], v[60:63]\n\t"
        "v_add_f32 v65, v65, v41\n\t"
        "ds_read_b128 v[16:19], v12 offset:40960\n\t"
        "s_waitcnt lgkmcnt(4)\n\t"
        "v_mfma_f32_16x16x32_bf16 v[56:59], v[168:171], v[20:23], v[56:59]\n\t"
        "buffer_load_dwordx4 v6, %[rx], s49 offen offset:2048 lds\n\t"
        "v_add_f32 v66, v66, v42\n\t"
        "v_mfma_f32_16x16x32_bf16 v[60:63], v[172:175], v[20:23], v[60:63]\n\t"
        "v_add_f32 v67, v67, v43\n\t"
        "ds_read_b128 v[20:23], v13 offset:40960\n\t"
        "s_waitcnt lgkmcnt(3)\n\t"
        "v_mfma_f32_16x16x32_bf16 v[56:59], v[176:179], v[24:27], v[56:59]\n\t"
        "v_add_f32 v68, v68, v44\n\t"
        "v_mfma_f32_16x16x32_bf16 v[60:63], v[180:183], v[24:27], v[60:63]\n\t"
        "v_add_f32 v69, v69, v45\n\t"
        "ds_read_b128 v[24:27], v12 offset:43008\n\t"
        "s_waitcnt lgkmcnt(3)\n\t"
        "v_mfma_f32_16x16x32_bf16 v[56:59], v[184:187], v[28:31], v[56:59]\n\t"
        "buffer_load_dwordx4 v7, %[rx], s49 offen offset:3072 lds\n\t"
        "v_add_f32 v70, v70, v46\n\t"
        "v_mfma_f32_16x16x32_bf16 v[60:63], v[188:191], v[28:31], v[60:63]\n\t"
        "v_add_f32 v71, v71, v47\n\t"
        "ds_read_b128 v[28:31], v13 offset:43008\n\t"
        "s_waitcnt lgkmcnt(3)\n\t"
        "v_mfma_f32_16x16x32_bf16 v[56:59], v[192:195], v[16:19], v[56:59]\n\t"
        "s_add_u32 s49, s49, 32768\n\t"
        "v_cvt_pk_bf16_f32 v72, v64, v65\n\t"
        "v_mfma_f32_16x16x32_bf16 v[60:63], v[196:199], v[16:19], v[60:63]\n\t"
        "v_cvt_pk_bf16_f32 v73, v68, v69\n\t"
        "s_add_u32 s54, s54, 1\n\t"
        "ds_read_b128 v[16:19], v12 offset:45056\n\t"
        "s_waitcnt lgkmcnt(3)\n\t"
        "v_mfma_f32_16x16x32_bf16 v[56:59], v[200:203], v[20:23], v[56:59]\n\t"
        "v_cvt_pk_bf16_f32 v74, v66, v67\n\t"
        "v_cvt_pk_bf16_f32 v75, v70, v71\n\t"
        "v_mfma_f32_16x16x32_bf16 v[60:63], v[204:207], v[20:23], v[60:63]\n\t"
        "v_mov_b32 v76, s54\n\t"
        "ds_read_b128 v[20:23], v13 offset:45056\n\t"
        "s_waitcnt lgkmcnt(3)\n\t"
        "v_mfma_f32_16x16x32_bf16 v[56:59], v[208:211], v[24:27], v[56:59]\n\t"
        "s_mov_b64 exec, s[56:57]\n\t"
        "v_swap_b32 v73, v74\n\t"
        "s_nop 1\n\t"
        "v_permlane16_swap_b32 v72, v74\n\t"
        "v_permlane16_swap_b32 v73, v75\n\t"
        "s_mov_b64 exec, -1\n\t"
        "buffer_store_dwordx4 v[72:75], v79, %[rs], s50 offen sc1\n\t"
        "v_mfma_f32_16x16x32_bf16 v[60:63], v[212:215], v[24:27], v[60:63]\n\t"
        "s_and_b32 s71, s54, 127\n\t"
        "s_cmp_le_u32 s71, 16\n\t"
        "s_cselect_b32 s72, 0, 3\n\t"
        "s_mul_i32 s71, s71, 128\n\t"
        "s_and_b32 s72, s72, s54\n\t"
        "s_cselect_b32 s71, 0x70000000, s71\n\t"
        "ds_read_b128 v[24:27], v12 offset:47104\n\t"
        "s_waitcnt lgkmcnt(3)\n\t"
        "v_mfma_f32_16x16x32_bf16 v[56:59], v[216:219], v[28:31], v[56:59]\n\t"
        "buffer_store_dwordx2 v[76:77], v78, %[rf], s71 offen sc1\n\t"
        "v_mfma_f32_16x16x32_bf16 v[60:63], v[220:223], v[28:31], v[60:63]\n\t"
        "ds_read_b128 v[28:31], v13 offset:47104\n\t"
        "s_waitcnt lgkmcnt(3)\n\t"
        "v_mfma_f32_16x16x32_bf16 v[56:59], v[224:227], v[16:19], v[56:59]\n\t"
        "v_mfma_f32_16x16x32_bf16 v[60:63], v[228:231], v[16:19], v[60:63]\n\t"
        "ds_read_b128 v[16:19], v10 offset:0\n\t"
        "s_waitcnt lgkmcnt(3)\n\t"
        "v_mfma_f32_16x16x32_bf16 v[56:59], v[232:235], v[20:23], v[56:59]\n\t"
        "v_mfma_f32_16x16x32_bf16 v[60:63], v[236:239], v[20:23], v[60:63]\n\t"
        "ds_read_b128 v[20:23], v11 offset:0\n\t"
        "s_waitcnt lgkmcnt(3)\n\t"
        "v_mfma_f32_16x16x32_bf16 v[56:59], v[240:243], v[24:27], v[56:59]\n\t"
        "v_mfma_f32_16x16x32_bf16 v[60:63], v[244:247], v[24:27], v[60:63]\n\t"
        "ds_read_b128 v[24:27], v10 offset:2048\n\t"
        "s_waitcnt lgkmcnt(3)\n\t"
        "v_mfma_f32_16x16x32_bf16 v[56:59], v[248:251], v[28:31], v[56:59]\n\t"
        "v_mfma_f32_16x16x32_bf16 v[60:63], v[252:255], v[28:31], v[60:63]\n\t"
        "s_sub_u32 s52, s52, 1\n\t"
        "s_waitcnt vmcnt(6)\n\t"
        "s_barrier\n\t"
        "s_cmp_lg_u32 s52, 0\n\t"
        "s_cbranch_scc1 .Lx_loop1_%=\n\t"
        "s_nop 7\n\t"
        "s_barrier\n\t"
        "ds_read_b128 v[64:67], v14 offset:8192\n\t"
        "ds_read_b128 v[68:71], v14 offset:9216\n\t"
        "s_add_u32 s50, s50, 0x80000\n\t"
        "s_add_u32 s51, s51, 1\n\t"
        "s_and_b32 s70, s51, 3\n\t"
        "s_cselect_b32 s70, 0, 0x2000\n\t"
        "s_xor_b32 s50, s50, s70\n\t"
        "s_waitcnt lgkmcnt(0)\n\t"
        "v_add_f32 v64, v64, v56\n\t"
        "v_add_f32 v65, v65, v57\n\t"
        "v_add_f32 v66, v66, v58\n\t"
        "v_add_f32 v67, v67, v59\n\t"
        "v_add_f32 v68, v68, v60\n\t"
        "v_add_f32 v69, v69, v61\n\t"
        "v_add_f32 v70, v70, v62\n\t"
        "v_add_f32 v71, v71, v63\n\t"
        "v_cvt_pk_bf16_f32 v72, v64, v65\n\t"
        "v_cvt_pk_bf16_f32 v73, v68, v69\n\t"
        "v_cvt_pk_bf16_f32 v74, v66, v67\n\t"
        "v_cvt_pk_bf16_f32 v75, v70, v71\n\t"
        "s_mov_b64 exec, s[56:57]\n\t"
        "v_swap_b32 v73, v74\n\t"
        "s_nop 1\n\t"
        "v_permlane16_swap_b32 v72, v74\n\t"
        "v_permlane16_swap_b32 v73, v75\n\t"
        "s_mov_b64 exec, -1\n\t"
        "buffer_store_dwordx4 v[72:75], v79, %[rs], s50 offen sc1\n\t"
        "s_waitcnt vmcnt(0)\n\t"
        "s_barrier\n\t"
        "s_and_b32 s54, s54, 0xffffff80\n\t"
        "s_add_u32 s54, s54, 63\n\t"
        "s_add_u32 s54, s54, 1\n\t"
        "v_mov_b32 v76, s54\n\t"
        "s_and_b32 s71, s54, 127\n\t"
        "s_lshl_b32 s71, s71, 7\n\t"
        "buffer_store_dwordx2 v[76:77], v78, %[rf], s71 offen sc1\n\t"
        "s_branch .Lx_end_%=\n\t"
        ".Lcode_x1:\n\t"
        ".Lx_end_%=:\n\t"
        :: [rs] "s"(RSRC(arena(a.slots), ARENA_BYTES)), [rw] "s"(RSRC((kind == 0 ? a.Wk : kind == 1 ? a.Wq : a.Wv) + hcol(h), (DM * HD - hcol(h)) * 2)), [rx] "s"(RSRC(a.x - 2048, S_LEN * DM * 2 + 4096)),
           [rf] "s"(PROG(idx)), [bt] "s"(a.boot), [w] "s"(w), [kn] "s"(kind), [co] "s"((kind == 1 ? Q_OFF : KV_OFF) + h * 16384 + (cls_s(h) ^ (kind >> 1)) * 8192) XSTIN
        : "memory", "vcc", "scc", ALLV, ALLS);
}

template <int R> __device__ __forceinline__ void warm(const int share, const int l) {
    unsigned long long pc; int o0, o1;
#define CODE(r) asm volatile("s_getpc_b64 %0\n1:\n\ts_mov_b32 %1, .Lcode_" r "0-1b\n\ts_mov_b32 %2, .Lcode_" r "1-1b" : "=s"(pc), "=s"(o0), "=s"(o1))
    if (R == 0) CODE("s"); else if (R == 1) CODE("v"); else if (R == 2) CODE("r"); else CODE("x");
    for (int o = o0 + share * 8192; o < o1; o += 64 * 8192) touch((const char*)pc + min(o + l * 128, o1 - 4), 131072);
}
constexpr int MEGA_LDS = 163840;
__global__ __launch_bounds__(512, 2) void mega_kernel(Args) {
    extern __shared__ __attribute__((aligned(256))) char chsm[];
    const int b = BIDX, xi = b & 7, sl = b >> 3, x3 = xi & 3;
    int wv = __builtin_amdgcn_readfirstlane(TIDX >> 6);
    asm volatile("" : "+s"(wv));
    int role, idx;
    if (PL == 0) {
        if (sl < 4) { role = 0; idx = 4 * xi + sl; } else if (sl < 12) { role = 1; idx = VNEAR ? 16 * x3 + 2 * (sl - 4) + (xi >> 2) : 8 * (xi ^ 4) + sl - 4; } else if (sl < 20) { role = 2; idx = 8 * xi + sl - 12; }
        else { role = 3; idx = sl < 28 ? ((sl - 20) >> 2) * 32 + 4 * xi + (sl & 3) : VNEAR ? 64 + 8 * x3 + 4 * (xi >> 2) + (sl & 3) : 64 + 4 * (xi ^ 4) + (sl & 3); }
    } else if (PL == 1) {
        if (xi < 4) { if (sl < 8) { role = 0; idx = 8 * xi + sl; } else if (sl < 16) { role = 3; idx = 8 * xi + sl - 8; } else { role = 2; idx = 16 * xi + sl - 16; } }
        else { if (sl < 16) { role = 1; idx = 16 * x3 + sl; } else { role = 3; idx = (sl < 24 ? 64 : 32) + 8 * x3 + (sl & 7); } }
    } else {
        role = 3; idx = 0;
    }
    if (role == 0) score_wg(wv, idx, REP_S(xi));
    if (role == 1) value_wg(wv, idx >> 1, idx & 1, REP_V(xi));
    if (role == 2) {
        const Args a = get_args();
        const int t = tid_now(wv), l = t & 63, wu = __builtin_amdgcn_readfirstlane(t >> 6);
        const rsrc_t rs = RSRC(arena(a.slots), ARENA_BYTES);
        const unsigned long long tag = boot_tag(a.boot);
#pragma unroll
        for (int e = 0; e < 3; ++e) {
            const int u = wu + 8 * e;
            st_sc1s(rs, l * 16, u < 16 ? (64 + idx) * 16384 + u * 1024 : u < 21 ? (5 * idx + u - 16) * 1024 : u == 21 && idx < 32 ? 21 * 16384 + idx * 1024 : 0x7f000000, v4u{SENT, SENT, SENT, SENT});
        }
        const int cal = CAL_OFF + (idx >> 3) * 16384 + (idx & 7) * 1024;
        if (wu == 0) {
            if (l < 32) __hip_atomic_store((gu32*)(a.sync + idx * 32 + l), 0u, __ATOMIC_RELAXED, __HIP_MEMORY_SCOPE_AGENT);
            if (idx < 2) st_sc1s(rs, l * 16, 20 * 16384 + idx * 8192, v4u{0u, 0u, 0u, 0u});
            __builtin_amdgcn_raw_buffer_store_b32(0u, rs, (l & 3) * 256 + ((l >> 2) & 1) * 8192, cal, AUX_SC1);
        }
        K8_WV(0); K8_BAR;
        volatile unsigned* mb = (volatile unsigned*)(chsm + 114688);
        if (wu) {
            if (wu == 1) touch((const char*)a.x + (idx * 64 + l) * 128, 131072);
            else if (wu == 2) touch_sc1(arena(a.slots) + (size_t)min(l, ARENA_BYTES / MB2 - 1) * MB2 + MB2 - 256, 131072);
            else if (wu == 3) { touch((const char*)a.mem0 + l * 128, 131072); touch((const char*)a.mem0 + 8192 + l * 128, 131072); }
            PREFETCH
            CODEWARM
            __builtin_amdgcn_s_sleep(XSL);
            touch((const char*)a.x + (4096 + idx * 448 + t - 64) * 128, 131072);
            if (wu == 4) return;
        } else {
            unsigned tm[2] = {~0u, ~0u};
            const char* pb = arena(a.slots) + cal;
#pragma unroll
            for (int k = 0; k < 4; ++k) {
                unsigned long long c0, c1; unsigned d;
                asm volatile("s_memtime %0\n\ts_waitcnt lgkmcnt(0)\n\ts_load_dword %2, %3, %4\n\ts_waitcnt lgkmcnt(0)\n\ts_memtime %1\n\ts_waitcnt lgkmcnt(0)"
                             : "=&s"(c0), "=&s"(c1), "=&s"(d) : "s"(pb), "i"((k >> 1) * 256 + (k & 1) * 8192) : "memory");
                tm[k & 1] = min(tm[k & 1], (unsigned)(c1 - c0));
            }
            if (l == 0) __hip_atomic_store((gu64*)(a.boot + idx), (tag << 1) | ((tm[0] > tm[1] ? 1u : 0u) ^ (unsigned)cls_r(idx)), __ATOMIC_RELAXED, __HIP_MEMORY_SCOPE_AGENT);
            __builtin_amdgcn_s_sleep(64);
            if (l < 8) mb[4 + l] = 0u;
            *mb = ((unsigned)boot_wait(a.boot, a.sync + 40 * 32) & 128u) << 6;
            K8_WL(0);
        }
        K8_BAR;
        const unsigned t0 = __builtin_amdgcn_readfirstlane(*mb);
        column_wg(wv, idx, t0);
    }
    WST(0)
    xw_stream(wv, idx);
    WST(3)
    {
        const Args a = get_args();
        cpx* F = (cpx*)a.F;
        const rsrc_t rsF = RSRC(F, 256 * FP * 64), rsW = RSRC(a.Wt, 513 * WP * 4);
        auto stagger = [&](const int n) { for (int k = 0; k < n; k += 128) __builtin_amdgcn_s_sleep(2); };
        stagger(idx * FD1);
        if (FAB & 1) {} else if (idx < 64) fft_rows_fwd(a.bx, rsF, idx, chsm, tid_now(wv));
        else {
            filt_transpose(a.wr, a.wi, rsW, idx - 64, chsm, tid_now(wv));
            __syncthreads();
            filt_transpose(a.wr, a.wi, rsW, idx - 32, chsm, tid_now(wv));
        }
        wg_publish(wv, a.sync, SY_DONE, 96u);
        WST(4)
        wg_wait_acquire(wv, a.sync + (SY_DONE + xi) * 32, a.sync + 40 * 32, 6000u);
        stagger(idx * FD2);
        if (!(FAB & 2) && wv < FCW && FCW * idx + wv < 513) fft_col(F, a.Wt, FCW * idx + wv, chsm + wv * PB2 * 8, lane_now());
        __syncthreads();
        if (!(FAB & 2) && wv < FCW && FCW * idx + wv < 514) fft_col_out(RSRC(OOP ? a.G : a.F, 256 * FP * 64), FCW * idx + wv, chsm + (wv & ~1) * PB2 * 8, lane_now());
        wg_publish(wv, a.sync, SY_DONE + 8, 96u);
        WST(5)
        if (idx < 64) {
            wg_wait_acquire(wv, a.sync + (SY_DONE + 8 + xi) * 32, a.sync + 40 * 32, 6001u);
            stagger(idx * FD3);
            if (!(FAB & 4)) fft_rows_inv((const cpx*)(OOP ? a.G : a.F), a.out, idx, chsm, tid_now(wv));
        }
        WST(6)
    }
}

#if defined(__HIP_DEVICE_COMPILE__)
#pragma clang attribute pop
#endif
extern "C" void kernel_launch(void* const* d_in, const int* in_sizes, int n_in,
                              void* d_out, int out_size, void* d_ws, size_t ws_size,
                              hipStream_t stream) {
    char* ws = (char*)d_ws;
    size_t off = 0;
    auto alloc = [&](size_t bytes) { void* p = ws + off; off += (bytes + 255) & ~(size_t)255; return p; };
    Args a;
    a.sync = (unsigned*)alloc(8192);
    a.boot = (unsigned long long*)alloc((256 + 1040 * 96) * 8);
    a.F = alloc((size_t)256 * FP * 8 * sizeof(cpx));
    a.G = alloc((size_t)256 * FP * 8 * sizeof(cpx));
    a.Wt = (unsigned*)alloc((size_t)513 * WP * 4);
    a.slots = (char*)alloc((size_t)ARENA_BYTES + (2u << 20));
    a.stamps = (unsigned*)(ws + ((size_t)STOFF << 20));
    a.x = (const u16*)d_in[0]; a.mem0 = (const u16*)d_in[1]; a.Wq = (const u16*)d_in[2]; a.Wk = (const u16*)d_in[3]; a.Wv = (const u16*)d_in[4]; a.Wo = (const u16*)d_in[5];
    a.bx = (const u16*)d_in[7]; a.wr = (const u16*)d_in[8]; a.wi = (const u16*)d_in[9]; a.out = (u16*)d_out;
    (void)hipFuncSetAttribute((const void*)mega_kernel, hipFuncAttributeMaxDynamicSharedMemorySize, MEGA_LDS);
    mega_kernel<<<256, 512, MEGA_LDS, stream>>>(a);
}
```

```cpp
#if defined(__HIP_DEVICE_COMPILE__)
#pragma clang attribute push (__attribute__((target("no-packed-fp32-ops"))), apply_to = function)
#endif
#include <hip/hip_runtime.h>
#include <hip/hip_bf16.h>
#include <stdint.h>

typedef unsigned short u16;
#define TIDX ((int)__builtin_amdgcn_workitem_id_x())
#define BIDX ((int)__builtin_amdgcn_workgroup_id_x())
__device__ __forceinline__ int lane_now() { int t; asm volatile("v_mbcnt_lo_u32_b32 %0, -1, 0\n\tv_mbcnt_hi_u32_b32 %0, -1, %0" : "=v"(t)); return t; }
__device__ __forceinline__ int tid_now(int w) { return w * 64 + lane_now(); }
typedef __attribute__((ext_vector_type(8))) short bf16x8;
typedef __attribute__((ext_vector_type(4))) float f32x4;

constexpr int S_LEN = 2048, DM = 1024, NH = 32, DH = 128, SEG = 32;
constexpr int NSEG = S_LEN / SEG;
constexpr int HD = NH * DH;
constexpr int OUTW = 2048;

typedef unsigned v4u __attribute__((ext_vector_type(4)));
typedef unsigned v2u __attribute__((ext_vector_type(2)));
typedef short v4i16 __attribute__((ext_vector_type(4)));
typedef __amdgpu_buffer_rsrc_t rsrc_t;
#define AUX_SC1 16
__device__ __forceinline__ void st_sc1s(rsrc_t rs, int voff, int soff, v4u v) { asm volatile("buffer_store_dwordx4 %0, %1, %2, %3 offen sc1\n\ts_nop 1" :: "v"(v), "v"(voff), "s"(rs), "s"(soff) : "memory"); }
__device__ __forceinline__ void st_sc1(rsrc_t rs, int off, v4u v) { __builtin_amdgcn_raw_buffer_store_b128(v, rs, off, 0, AUX_SC1); }
typedef __bf16 bf16x2_t __attribute__((ext_vector_type(2)));
typedef float f32x2_t __attribute__((ext_vector_type(2)));
__device__ __forceinline__ unsigned cvt_pk_bf16(float lo, float hi) {
    f32x2_t f = {lo, hi};
    return __builtin_bit_cast(unsigned, __builtin_convertvector(f, bf16x2_t));
}
#define SENT 0xFFBFFFBF
constexpr int MB2 = 2097152;
constexpr int YX_OFF = 16777216, KV_OFF = 50331648, Q_OFF = 83886080, ARENA_BYTES = Q_OFF + 16 * MB2, CAL_OFF = 24 * 16384;
__device__ __forceinline__ char* arena(char* slots) { return (char*)(((uintptr_t)slots + 2097151u) & ~(uintptr_t)2097151u); }
constexpr int PL = 0, VNEAR = 1, YXC = 0, SE = 0;
__device__ __forceinline__ int hcol(int h) { return PL == 0 && !SE ? ((h & 3) * 8 + (h >> 2)) * 128 : ((h & 7) * 4 + (h >> 3)) * 128; }
__device__ __forceinline__ int cls_s(int h) { return PL == 0 ? h >> 4 : 0; }
__device__ __forceinline__ int cls_v(int h, int half) { return PL == 0 ? (VNEAR ? half : (h >> 4) ^ 1) : PL == 1 ? 1 : 0; }
__device__ __forceinline__ int cls_r(int j) { return PL == 0 ? j >> 5 : 0; }

struct Args {
    const u16* x; const u16* mem0; const u16* Wq; const u16* Wk; const u16* Wv; const u16* Wo;
    const u16* bx; const u16* wr; const u16* wi;
    u16* out;
    char* slots;
    unsigned* sync;
    unsigned long long* boot;
    void* F; void* G; unsigned* Wt;
    unsigned* stamps;
};
__device__ __forceinline__ Args get_args() {
    typedef __attribute__((address_space(4))) const Args* cargs;
    cargs p = (cargs)__builtin_amdgcn_kernarg_segment_ptr();
    asm volatile("" : "+s"(p));
#if defined(__HIP_DEVICE_COMPILE__)
    return *p;
#else
    return Args{};
#endif
}
constexpr int SY_DONE = 0, SY_FLAGS = 16;
typedef __attribute__((address_space(1))) unsigned gu32;
typedef __attribute__((address_space(1))) unsigned long long gu64;
__device__ __forceinline__ unsigned ld_flag(const unsigned* p) { return __hip_atomic_load((gu32*)p, __ATOMIC_RELAXED, __HIP_MEMORY_SCOPE_AGENT); }
__device__ __forceinline__ unsigned long long boot_tag(const unsigned long long* boot) {
    return __hip_atomic_load((gu64*)(boot + 144), __ATOMIC_RELAXED, __HIP_MEMORY_SCOPE_AGENT) + 1ull;
}
__device__ __forceinline__ unsigned long long boot_wait(const unsigned long long* boot, unsigned* tmo) {
    const int l = lane_now();
    unsigned long long tag = 0, x = 0;
#pragma nounroll
    for (int spin = 0;; ++spin) {
        asm volatile("" ::: "memory");
        x = __hip_atomic_load((gu64*)(boot + l), __ATOMIC_RELAXED, __HIP_MEMORY_SCOPE_AGENT);
        tag = boot_tag(boot);
        if (__all((x | 1u) == ((tag << 1) | 1u))) break;
        if (spin > 100000) { __hip_atomic_store((gu32*)tmo, 9000u, __ATOMIC_RELAXED, __HIP_MEMORY_SCOPE_AGENT); break; }
        __builtin_amdgcn_s_sleep(16);
    }
    asm volatile("" ::: "memory");
    return (tag << 8) | (__popcll(__ballot(x & 1u)) > 32 ? 128u : 0u);
}
__device__ __forceinline__ void wg_wait(const int w, const unsigned* p, unsigned* tmo, unsigned code) {
    if (w == 0) {
#pragma nounroll
        for (int spin = 0;; ++spin) {
            asm volatile("" ::: "memory");
            if (ld_flag(p) >= 1u) break;
            if (spin > 25000) { __hip_atomic_store((gu32*)tmo, code, __ATOMIC_RELAXED, __HIP_MEMORY_SCOPE_AGENT); break; }
            __builtin_amdgcn_s_sleep(16);
        }
    }
    __syncthreads();
}
__device__ __forceinline__ void wg_publish(const int w, unsigned* sync, const int f, const unsigned need) {
    asm volatile("s_waitcnt vmcnt(0)" ::: "memory");
    __syncthreads();
    if (w == 0) {
        const int l = lane_now();
        unsigned old = 0;
        if (l == 0) old = __hip_atomic_fetch_add((gu32*)(sync + (SY_FLAGS + f) * 32), 1u, __ATOMIC_RELAXED, __HIP_MEMORY_SCOPE_AGENT);
        if (__builtin_amdgcn_readfirstlane(old) == need - 1u) {
            if (1) st_sc1s(__builtin_amdgcn_make_buffer_rsrc((void*)(sync + f * 32), 0, 1024, 0x00020000), l * 16, 0, v4u{16u, 16u, 16u, 16u});
            else if (l < 8) __hip_atomic_store((gu32*)(sync + (f + l) * 32), 16u, __ATOMIC_RELAXED, __HIP_MEMORY_SCOPE_AGENT);
        }
    }
}
__device__ __forceinline__ void touch(const void* p, unsigned lds_dst) {
    unsigned keep;
    asm volatile("s_mov_b32 %0, m0\n\ts_mov_b32 m0, %2\n\ts_nop 0\n\tglobal_load_lds_dword %1, off\n\ts_mov_b32 m0, %0" : "=&s"(keep) : "v"(p), "s"(lds_dst) : "memory");
}
__device__ __forceinline__ void touch_sc1(const void* p, unsigned lds_dst) {
    unsigned keep;
    asm volatile("s_mov_b32 %0, m0\n\ts_mov_b32 m0, %2\n\ts_nop 0\n\tglobal_load_lds_dword %1, off sc1\n\ts_mov_b32 m0, %0" : "=&s"(keep) : "v"(p), "s"(lds_dst) : "memory");
}
#define C10(a) "v" #a "0", "v" #a "1", "v" #a "2", "v" #a "3", "v" #a "4", "v" #a "5", "v" #a "6", "v" #a "7", "v" #a "8", "v" #a "9"
#define ALLV C10(), C10(1), C10(2), C10(3), C10(4), C10(5), C10(6), C10(7), C10(8), C10(9), C10(10), C10(11), C10(12), C10(13), C10(14), C10(15), C10(16), C10(17), C10(18), C10(19), C10(20), C10(21), C10(22), \
    C10(23), C10(24), "v250", "v251", "v252", "v253", "v254", "v255"
#define S10(a) "s" #a "0", "s" #a "1", "s" #a "2", "s" #a "3", "s" #a "4", "s" #a "5", "s" #a "6", "s" #a "7", "s" #a "8", "s" #a "9"
#define ALLS "s44", "s45", "s46", "s47", "s48", "s49", S10(5), S10(6), S10(7), S10(8), "s90", "s91", "s92", "s93", "s94", "s95", "s96", "s97", "s98", "s99" STCL
#define RSRC(p, n) __builtin_amdgcn_make_buffer_rsrc((void*)(p), 0, (n), 0x00020000)
#define REP_S(x) (((x) >> 0) & 1)
#define REP_V(x) (2 + ((x) & 1))
#define KW 4
#define EF 0
#define SD 0
#define KD 0
#define QD 0
#define OT 0
#define IT 1
#define MULTI 1
#define FLA 1
#define OOP 0
#define STOFF 244
#define FCW 6
#define FAB 0
#define FD1 0
#define FD2 90
#define FD3 120
#define CODEWARM if (wu >= 4) { if ((idx & 3) == 0) warm<0>(wu - 4, l); else if ((idx & 3) == 1) warm<1>(wu - 4, l); else if ((idx & 3) == 2) warm<3>(wu - 4, l); else warm<2>(wu - 4, l); }
#define DGIN
#define DGT 0u
#define FTIN
#define XSL 127
#define XD 4
#define PREFETCH
#define WST(n)
#define FST(n)
#define FWT

#define PROG(n) RSRC(FLA ? arena(a.slots) + (MULTI ? ((n) >> 5) * MB2 + 32 * 16384 + ((n) & 31) * 8320 : 32 * 16384 + (n) * 128) : (char*)(a.boot + 256 + 1040 * (n)), 65 * 128)
#define TW(c) (20 * 16384 + (c) * 8192)
#define STCL
#define STIN(u)
#define XSTIN

__device__ __forceinline__ void score_wg(const int w, const int h, const int rep) {
    const Args a = get_args();
    const int co = hcol(h) + 64 * ((w >> 1) & 1);
    asm volatile(
        ".Lcode_s0:\n\t"
        "v_mbcnt_lo_u32_b32 v52, -1, 0\n\t"
        "v_mbcnt_hi_u32_b32 v52, -1, v52\n\t"
        "s_load_dwordx2 s[54:55], %[ep], 0\n\t"
        "s_mov_b32 s79, 0\n\t"
        "s_mov_b32 s44, -1\n\t"
        "s_mov_b32 s45, 0\n\t"
        "s_mov_b32 s46, 200000\n\t"
        "s_mov_b32 s51, 0\n\t"
        "v_lshlrev_b32 v0, 4, v52\n\t"
        "v_lshrrev_b32 v53, 3, v52\n\t"
        "v_and_b32 v54, 7, v52\n\t"
        "v_and_b32 v8, 2, v53\n\t"
        "v_xor_b32 v54, v54, v8\n\t"
        "v_lshlrev_b32 v54, 4, v54\n\t"
        "v_lshl_add_u32 v8, v53, 13, v54\n\t"
        "v_xor_b32 v9, 64, v8\n\t"
        "s_lshl_b32 s68, %[w], 20\n\t"
        "s_lshl_b32 s69, %[w], 12\n\t"
        "v_lshrrev_b32 v11, 4, v52\n\t"
        "s_add_u32 m0, s69, 65536\n\t"
        "s_xor_b32 s70, s68, 0x0\n\t"
        "buffer_load_dwordx4 v8, %[rw], s70 offen offset:0 lds\n\t"
        "s_add_u32 s70, s70, 64512\n\t"
        "buffer_load_dwordx4 v9, %[rw], s70 offen offset:1024 lds\n\t"
        "s_add_u32 s70, s70, 64512\n\t"
        "buffer_load_dwordx4 v8, %[rw], s70 offen offset:2048 lds\n\t"
        "s_add_u32 s70, s70, 64512\n\t"
        "buffer_load_dwordx4 v9, %[rw], s70 offen offset:3072 lds\n\t"
        "v_mov_b32 v20, 0\n\t"
        "s_mov_b32 s72, 52428544\n\t"
        "s_mov_b32 s73, 85982976\n\t"
        "s_mov_b32 s74, 376832\n\t"
        "buffer_load_dword v21, v20, %[rf], 0 offen sc1\n\t"
        "buffer_load_dword v21, v20, %[rs], s72 offen sc1\n\t"
        "buffer_load_dword v21, v20, %[rs], s73 offen sc1\n\t"
        "buffer_load_dword v21, v20, %[rs], s74 offen sc1\n\t"
        "s_add_u32 m0, s69, 98304\n\t"
        "s_xor_b32 s70, s68, 0x200000\n\t"
        "buffer_load_dwordx4 v8, %[rw], s70 offen offset:0 lds\n\t"
        "s_add_u32 s70, s70, 64512\n\t"
        "buffer_load_dwordx4 v9, %[rw], s70 offen offset:1024 lds\n\t"
        "s_add_u32 s70, s70, 64512\n\t"
        "buffer_load_dwordx4 v8, %[rw], s70 offen offset:2048 lds\n\t"
        "s_add_u32 s70, s70, 64512\n\t"
        "buffer_load_dwordx4 v9, %[rw], s70 offen offset:3072 lds\n\t"
        "v_lshrrev_b32 v4, 2, v52\n\t"
        "v_lshl_add_u32 v4, v11, 2, v4\n\t"
        "v_lshlrev_b32 v4, 7, v4\n\t"
        "v_and_b32 v5, 3, v52\n\t"
        "v_lshl_add_u32 v4, v5, 3, v4\n\t"
        "s_and_b32 s70, %[w], 1\n\t"
        "s_lshl_b32 s70, s70, 1\n\t"
        "v_bfe_u32 v5, v52, 3, 2\n\t"
        "v_xor_b32 v5, s70, v5\n\t"
        "v_lshl_add_u32 v4, v5, 5, v4\n\t"
        "v_add_u32 v4, s69, v4\n\t"
        "v_add_u32 v4, 65536, v4\n\t"
        "v_xor_b32 v5, 32, v4\n\t"
        "v_xor_b32 v6, 0x1000, v4\n\t"
        "v_xor_b32 v7, 0x1000, v5\n\t"
        "v_mov_b32 v53, 0x4444\n\t"
        "v_mov_b32 v54, 0xeeee\n\t"
        "v_and_b32 v1, 1, v52\n\t"
        "v_cmp_eq_u32 vcc, 0, v1\n\t"
        "s_lshl_b32 s70, %[w], 11\n\t"
        "v_add_u32 v3, s70, v0\n\t"
        "v_cndmask_b32 v1, v54, v53, vcc\n\t"
        "v_and_b32 v2, 0x1fff, v3\n\t"
        "s_waitcnt lgkmcnt(0)\n\t"
        "s_add_u32 s54, s54, 1\n\t"
        "s_addc_u32 s55, s55, 0\n\t"
        "s_lshl_b64 s[54:55], s[54:55], 8\n\t"
        "s_cmp_ge_u32 %[w], 4\n\t"
        "s_cbranch_scc1 .Ls_stk_%=\n\t"
        "v_add_u32 v10, s69, v0\n\t"
        "v_add_u32 v11, 131072, v10\n\t"
        "v_add_u32 v10, 65536, v10\n\t"
        "s_waitcnt vmcnt(4)\n\t"
        "s_barrier\n\t"
        "s_add_u32 m0, s69, 131072\n\t"
        "s_xor_b32 s70, s68, 0x400000\n\t"
        "buffer_load_dwordx4 v8, %[rw], s70 offen offset:0 lds\n\t"
        "s_add_u32 s70, s70, 64512\n\t"
        "buffer_load_dwordx4 v9, %[rw], s70 offen offset:1024 lds\n\t"
        "s_add_u32 s70, s70, 64512\n\t"
        "buffer_load_dwordx4 v8, %[rw], s70 offen offset:2048 lds\n\t"
        "s_add_u32 s70, s70, 64512\n\t"
        "buffer_load_dwordx4 v9, %[rw], s70 offen offset:3072 lds\n\t"
        "ds_read_b64_tr_b16 v[56:57], v4\n\t"
        "ds_read_b64_tr_b16 v[58:59], v4 offset:512\n\t"
        "ds_read_b64_tr_b16 v[64:65], v5\n\t"
        "ds_read_b64_tr_b16 v[66:67], v5 offset:512\n\t"
        "ds_read_b64_tr_b16 v[88:89], v6\n\t"
        "ds_read_b64_tr_b16 v[90:91], v6 offset:512\n\t"
        "ds_read_b64_tr_b16 v[28:29], v7\n\t"
        "ds_read_b64_tr_b16 v[30:31], v7 offset:512\n\t"
        "s_waitcnt lgkmcnt(0)\n\t"
        "v_add_u32 v4, 32768, v4\n\t"
        "v_add_u32 v5, 32768, v5\n\t"
        "v_add_u32 v6, 32768, v6\n\t"
        "v_add_u32 v7, 32768, v7\n\t"
        "s_waitcnt vmcnt(4)\n\t"
        "s_barrier\n\t"
        "s_add_u32 m0, s69, 65536\n\t"
        "s_xor_b32 s70, s68, 0x600000\n\t"
        "buffer_load_dwordx4 v8, %[rw], s70 offen offset:0 lds\n\t"
        "s_add_u32 s70, s70, 64512\n\t"
        "buffer_load_dwordx4 v9, %[rw], s70 offen offset:1024 lds\n\t"
        "s_add_u32 s70, s70, 64512\n\t"
        "buffer_load_dwordx4 v8, %[rw], s70 offen offset:2048 lds\n\t"
        "s_add_u32 s70, s70, 64512\n\t"
        "buffer_load_dwordx4 v9, %[rw], s70 offen offset:3072 lds\n\t"
        "ds_read_b64_tr_b16 v[112:113], v4\n\t"
        "ds_read_b64_tr_b16 v[114:115], v4 offset:512\n\t"
        "ds_read_b64_tr_b16 v[36:37], v5\n\t"
        "ds_read_b64_tr_b16 v[38:39], v5 offset:512\n\t"
        "ds_read_b64_tr_b16 v[136:137], v6\n\t"
        "ds_read_b64_tr_b16 v[138:139], v6 offset:512\n\t"
        "ds_read_b64_tr_b16 v[44:45], v7\n\t"
        "ds_read_b64_tr_b16 v[46:47], v7 offset:512\n\t"
        "s_waitcnt lgkmcnt(0)\n\t"
        "v_add_u32 v4, 32768, v4\n\t"
        "v_add_u32 v5, 32768, v5\n\t"
        "v_add_u32 v6, 32768, v6\n\t"
        "v_add_u32 v7, 32768, v7\n\t"
        "s_waitcnt vmcnt(4)\n\t"
        "s_barrier\n\t"
        "s_add_u32 m0, s69, 98304\n\t"
        "s_xor_b32 s70, s68, 0x40000\n\t"
        "buffer_load_dwordx4 v8, %[rw], s70 offen offset:0 lds\n\t"
        "s_add_u32 s70, s70, 64512\n\t"
        "buffer_load_dwordx4 v9, %[rw], s70 offen offset:1024 lds\n\t"
        "s_add_u32 s70, s70, 64512\n\t"
        "buffer_load_dwordx4 v8, %[rw], s70 offen offset:2048 lds\n\t"
        "s_add_u32 s70, s70, 64512\n\t"
        "buffer_load_dwordx4 v9, %[rw], s70 offen offset:3072 lds\n\t"
        "ds_read_b64_tr_b16 v[160:161], v4\n\t"
        "ds_read_b64_tr_b16 v[162:163], v4 offset:512\n\t"
        "ds_read_b64_tr_b16 v[168:169], v5\n\t"
        "ds_read_b64_tr_b16 v[170:171], v5 offset:512\n\t"
        "ds_read_b64_tr_b16 v[184:185], v6\n\t"
        "ds_read_b64_tr_b16 v[186:187], v6 offset:512\n\t"
        "ds_read_b64_tr_b16 v[192:193], v7\n\t"
        "ds_read_b64_tr_b16 v[194:195], v7 offset:512\n\t"
        "s_waitcnt lgkmcnt(0)\n\t"
        "v_add_u32 v4, -65536, v4\n\t"
        "v_add_u32 v5, -65536, v5\n\t"
        "v_add_u32 v6, -65536, v6\n\t"
        "v_add_u32 v7, -65536, v7\n\t"
        "s_waitcnt vmcnt(4)\n\t"
        "s_barrier\n\t"
        "s_add_u32 m0, s69, 131072\n\t"
        "s_xor_b32 s70, s68, 0x240000\n\t"
        "buffer_load_dwordx4 v8, %[rw], s70 offen offset:0 lds\n\t"
        "s_add_u32 s70, s70, 64512\n\t"
        "buffer_load_dwordx4 v9, %[rw], s70 offen offset:1024 lds\n\t"
        "s_add_u32 s70, s70, 64512\n\t"
        "buffer_load_dwordx4 v8, %[rw], s70 offen offset:2048 lds\n\t"
        "s_add_u32 s70, s70, 64512\n\t"
        "buffer_load_dwordx4 v9, %[rw], s70 offen offset:3072 lds\n\t"
        "ds_read_b64_tr_b16 v[208:209], v4\n\t"
        "ds_read_b64_tr_b16 v[210:211], v4 offset:512\n\t"
        "ds_read_b64_tr_b16 v[216:217], v5\n\t"
        "ds_read_b64_tr_b16 v[218:219], v5 offset:512\n\t"
        "ds_read_b64_tr_b16 v[232:233], v6\n\t"
        "ds_read_b64_tr_b16 v[234:235], v6 offset:512\n\t"
        "ds_read_b64_tr_b16 v[240:241], v7\n\t"
        "ds_read_b64_tr_b16 v[242:243], v7 offset:512\n\t"
        "s_waitcnt lgkmcnt(0)\n\t"
        "v_add_u32 v4, 32768, v4\n\t"
        "v_add_u32 v5, 32768, v5\n\t"
        "v_add_u32 v6, 32768, v6\n\t"
        "v_add_u32 v7, 32768, v7\n\t"
        "s_waitcnt vmcnt(4)\n\t"
        "s_barrier\n\t"
        "s_add_u32 m0, s69, 65536\n\t"
        "s_xor_b32 s70, s68, 0x440000\n\t"
        "buffer_load_dwordx4 v8, %[rw], s70 offen offset:0 lds\n\t"
        "s_add_u32 s70, s70, 64512\n\t"
        "buffer_load_dwordx4 v9, %[rw], s70 offen offset:1024 lds\n\t"
        "s_add_u32 s70, s70, 64512\n\t"
        "buffer_load_dwordx4 v8, %[rw], s70 offen offset:2048 lds\n\t"
        "s_add_u32 s70, s70, 64512\n\t"
        "buffer_load_dwordx4 v9, %[rw], s70 offen offset:3072 lds\n\t"
        "ds_read_b64_tr_b16 v[60:61], v4\n\t"
        "ds_read_b64_tr_b16 v[62:63], v4 offset:512\n\t"
        "ds_read_b64_tr_b16 v[68:69], v5\n\t"
        "ds_read_b64_tr_b16 v[70:71], v5 offset:512\n\t"
        "ds_read_b64_tr_b16 v[92:93], v6\n\t"
        "ds_read_b64_tr_b16 v[94:95], v6 offset:512\n\t"
        "ds_read_b64_tr_b16 v[32:33], v7\n\t"
        "ds_read_b64_tr_b16 v[34:35], v7 offset:512\n\t"
        "s_waitcnt lgkmcnt(0)\n\t"
        "v_add_u32 v4, 32768, v4\n\t"
        "v_add_u32 v5, 32768, v5\n\t"
        "v_add_u32 v6, 32768, v6\n\t"
        "v_add_u32 v7, 32768, v7\n\t"
        "s_waitcnt vmcnt(4)\n\t"
        "s_barrier\n\t"
        "s_add_u32 m0, s69, 98304\n\t"
        "s_xor_b32 s70, s68, 0x640000\n\t"
        "buffer_load_dwordx4 v8, %[rw], s70 offen offset:0 lds\n\t"
        "s_add_u32 s70, s70, 64512\n\t"
        "buffer_load_dwordx4 v9, %[rw], s70 offen offset:1024 lds\n\t"
        "s_add_u32 s70, s70, 64512\n\t"
        "buffer_load_dwordx4 v8, %[rw], s70 offen offset:2048 lds\n\t"
        "s_add_u32 s70, s70, 64512\n\t"
        "buffer_load_dwordx4 v9, %[rw], s70 offen offset:3072 lds\n\t"
        "ds_read_b64_tr_b16 v[116:117], v4\n\t"
        "ds_read_b64_tr_b16 v[118:119], v4 offset:512\n\t"
        "ds_read_b64_tr_b16 v[40:41], v5\n\t"
        "ds_read_b64_tr_b16 v[42:43], v5 offset:512\n\t"
        "ds_read_b64_tr_b16 v[140:141], v6\n\t"
        "ds_read_b64_tr_b16 v[142:143], v6 offset:512\n\t"
        "ds_read_b64_tr_b16 v[48:49], v7\n\t"
        "ds_read_b64_tr_b16 v[50:51], v7 offset:512\n\t"
        "s_waitcnt lgkmcnt(0)\n\t"
        "v_add_u32 v4, -65536, v4\n\t"
        "v_add_u32 v5, -65536, v5\n\t"
        "v_add_u32 v6, -65536, v6\n\t"
        "v_add_u32 v7, -65536, v7\n\t"
        "s_waitcnt vmcnt(4)\n\t"
        "s_barrier\n\t"
        "s_add_u32 m0, s69, 131072\n\t"
        "s_xor_b32 s70, s68, 0x80000\n\t"
        "buffer_load_dwordx4 v8, %[rw], s70 offen offset:0 lds\n\t"
        "s_add_u32 s70, s70, 64512\n\t"
        "buffer_load_dwordx4 v9, %[rw], s70 offen offset:1024 lds\n\t"
        "s_add_u32 s70, s70, 64512\n\t"
        "buffer_load_dwordx4 v8, %[rw], s70 offen offset:2048 lds\n\t"
        "s_add_u32 s70, s70, 64512\n\t"
        "buffer_load_dwordx4 v9, %[rw], s70 offen offset:3072 lds\n\t"
        "ds_read_b64_tr_b16 v[164:165], v4\n\t"
        "ds_read_b64_tr_b16 v[166:167], v4 offset:512\n\t"
        "ds_read_b64_tr_b16 v[172:173], v5\n\t"
        "ds_read_b64_tr_b16 v[174:175], v5 offset:512\n\t"
        "ds_read_b64_tr_b16 v[188:189], v6\n\t"
        "ds_read_b64_tr_b16 v[190:191], v6 offset:512\n\t"
        "ds_read_b64_tr_b16 v[196:197], v7\n\t"
        "ds_read_b64_tr_b16 v[198:199], v7 offset:512\n\t"
        "s_waitcnt lgkmcnt(0)\n\t"
        "v_add_u32 v4, 32768, v4\n\t"
        "v_add_u32 v5, 32768, v5\n\t"
        "v_add_u32 v6, 32768, v6\n\t"
        "v_add_u32 v7, 32768, v7\n\t"
        "s_waitcnt vmcnt(4)\n\t"
        "s_barrier\n\t"
        "s_add_u32 m0, s69, 65536\n\t"
        "s_xor_b32 s70, s68, 0x280000\n\t"
        "buffer_load_dwordx4 v8, %[rw], s70 offen offset:0 lds\n\t"
        "s_add_u32 s70, s70, 64512\n\t"
        "buffer_load_dwordx4 v9, %[rw], s70 offen offset:1024 lds\n\t"
        "s_add_u32 s70, s70, 64512\n\t"
        "buffer_load_dwordx4 v8, %[rw], s70 offen offset:2048 lds\n\t"
        "s_add_u32 s70, s70, 64512\n\t"
        "buffer_load_dwordx4 v9, %[rw], s70 offen offset:3072 lds\n\t"
        "ds_read_b64_tr_b16 v[212:213], v4\n\t"
        "ds_read_b64_tr_b16 v[214:215], v4 offset:512\n\t"
        "ds_read_b64_tr_b16 v[220:221], v5\n\t"
        "ds_read_b64_tr_b16 v[222:223], v5 offset:512\n\t"
        "ds_read_b64_tr_b16 v[236:237], v6\n\t"
        "ds_read_b64_tr_b16 v[238:239], v6 offset:512\n\t"
        "ds_read_b64_tr_b16 v[244:245], v7\n\t"
        "ds_read_b64_tr_b16 v[246:247], v7 offset:512\n\t"
        "s_waitcnt lgkmcnt(0)\n\t"
        "v_add_u32 v4, 32768, v4\n\t"
        "v_add_u32 v5, 32768, v5\n\t"
        "v_add_u32 v6, 32768, v6\n\t"
        "v_add_u32 v7, 32768, v7\n\t"
        "s_waitcnt vmcnt(4)\n\t"
        "s_barrier\n\t"
        "s_add_u32 m0, s69, 98304\n\t"
        "s_xor_b32 s70, s68, 0x480000\n\t"
        "buffer_load_dwordx4 v8, %[rw], s70 offen offset:0 lds\n\t"
        "s_add_u32 s70, s70, 64512\n\t"
        "buffer_load_dwordx4 v9, %[rw], s70 offen offset:1024 lds\n\t"
        "s_add_u32 s70, s70, 64512\n\t"
        "buffer_load_dwordx4 v8, %[rw], s70 offen offset:2048 lds\n\t"
        "s_add_u32 s70, s70, 64512\n\t"
        "buffer_load_dwordx4 v9, %[rw], s70 offen offset:3072 lds\n\t"
        "ds_read_b64_tr_b16 v[72:73], v4\n\t"
        "ds_read_b64_tr_b16 v[74:75], v4 offset:512\n\t"
        "ds_read_b64_tr_b16 v[80:81], v5\n\t"
        "ds_read_b64_tr_b16 v[82:83], v5 offset:512\n\t"
        "ds_read_b64_tr_b16 v[96:97], v6\n\t"
        "ds_read_b64_tr_b16 v[98:99], v6 offset:512\n\t"
        "ds_read_b64_tr_b16 v[104:105], v7\n\t"
        "ds_read_b64_tr_b16 v[106:107], v7 offset:512\n\t"
        "s_waitcnt lgkmcnt(0)\n\t"
        "v_add_u32 v4, -65536, v4\n\t"
        "v_add_u32 v5, -65536, v5\n\t"
        "v_add_u32 v6, -65536, v6\n\t"
        "v_add_u32 v7, -65536, v7\n\t"
        "s_waitcnt vmcnt(4)\n\t"
        "s_barrier\n\t"
        "s_add_u32 m0, s69, 131072\n\t"
        "s_xor_b32 s70, s68, 0x680000\n\t"
        "buffer_load_dwordx4 v8, %[rw], s70 offen offset:0 lds\n\t"
        "s_add_u32 s70, s70, 64512\n\t"
        "buffer_load_dwordx4 v9, %[rw], s70 offen offset:1024 lds\n\t"
        "s_add_u32 s70, s70, 64512\n\t"
        "buffer_load_dwordx4 v8, %[rw], s70 offen offset:2048 lds\n\t"
        "s_add_u32 s70, s70, 64512\n\t"
        "buffer_load_dwordx4 v9, %[rw], s70 offen offset:3072 lds\n\t"
        "ds_read_b64_tr_b16 v[120:121], v4\n\t"
        "ds_read_b64_tr_b16 v[122:123], v4 offset:512\n\t"
        "ds_read_b64_tr_b16 v[128:129], v5\n\t"
        "ds_read_b64_tr_b16 v[130:131], v5 offset:512\n\t"
        "ds_read_b64_tr_b16 v[144:145], v6\n\t"
        "ds_read_b64_tr_b16 v[146:147], v6 offset:512\n\t"
        "ds_read_b64_tr_b16 v[152:153], v7\n\t"
        "ds_read_b64_tr_b16 v[154:155], v7 offset:512\n\t"
        "s_waitcnt lgkmcnt(0)\n\t"
        "v_add_u32 v4, 32768, v4\n\t"
        "v_add_u32 v5, 32768, v5\n\t"
        "v_add_u32 v6, 32768, v6\n\t"
        "v_add_u32 v7, 32768, v7\n\t"
        "s_waitcnt vmcnt(4)\n\t"
        "s_barrier\n\t"
        "s_add_u32 m0, s69, 65536\n\t"
        "s_xor_b32 s70, s68, 0xc0000\n\t"
        "buffer_load_dwordx4 v8, %[rw], s70 offen offset:0 lds\n\t"
        "s_add_u32 s70, s70, 64512\n\t"
        "buffer_load_dwordx4 v9, %[rw], s70 offen offset:1024 lds\n\t"
        "s_add_u32 s70, s70, 64512\n\t"
        "buffer_load_dwordx4 v8, %[rw], s70 offen offset:2048 lds\n\t"
        "s_add_u32 s70, s70, 64512\n\t"
        "buffer_load_dwordx4 v9, %[rw], s70 offen offset:3072 lds\n\t"
        "ds_read_b64_tr_b16 v[176:177], v4\n\t"
        "ds_read_b64_tr_b16 v[178:179], v4 offset:512\n\t"
        "ds_read_b64_tr_b16 v[52:53], v5\n\t"
        "ds_read_b64_tr_b16 v[54:55], v5 offset:512\n\t"
        "ds_read_b64_tr_b16 v[200:201], v6\n\t"
        "ds_read_b64_tr_b16 v[202:203], v6 offset:512\n\t"
        "ds_read_b64_tr_b16 v[12:13], v7\n\t"
        "ds_read_b64_tr_b16 v[14:15], v7 offset:512\n\t"
        "s_waitcnt lgkmcnt(0)\n\t"
        "v_add_u32 v4, 32768, v4\n\t"
        "v_add_u32 v5, 32768, v5\n\t"
        "v_add_u32 v6, 32768, v6\n\t"
        "v_add_u32 v7, 32768, v7\n\t"
        "ds_write_b128 v3, v[52:55] offset:16384\n\t"
        "ds_write_b128 v3, v[12:15] offset:32768\n\t"
        "s_waitcnt vmcnt(4)\n\t"
        "s_barrier\n\t"
        "s_add_u32 m0, s69, 98304\n\t"
        "s_xor_b32 s70, s68, 0x2c0000\n\t"
        "buffer_load_dwordx4 v8, %[rw], s70 offen offset:0 lds\n\t"
        "s_add_u32 s70, s70, 64512\n\t"
        "buffer_load_dwordx4 v9, %[rw], s70 offen offset:1024 lds\n\t"
        "s_add_u32 s70, s70, 64512\n\t"
        "buffer_load_dwordx4 v8, %[rw], s70 offen offset:2048 lds\n\t"
        "s_add_u32 s70, s70, 64512\n\t"
        "buffer_load_dwordx4 v9, %[rw], s70 offen offset:3072 lds\n\t"
        "ds_read_b64_tr_b16 v[224:225], v4\n\t"
        "ds_read_b64_tr_b16 v[226:227], v4 offset:512\n\t"
        "ds_read_b64_tr_b16 v[52:53], v5\n\t"
        "ds_read_b64_tr_b16 v[54:55], v5 offset:512\n\t"
        "ds_read_b64_tr_b16 v[248:249], v6\n\t"
        "ds_read_b64_tr_b16 v[250:251], v6 offset:512\n\t"
        "ds_read_b64_tr_b16 v[20:21], v7\n\t"
        "ds_read_b64_tr_b16 v[22:23], v7 offset:512\n\t"
        "s_waitcnt lgkmcnt(0)\n\t"
        "v_add_u32 v4, -65536, v4\n\t"
        "v_add_u32 v5, -65536, v5\n\t"
        "v_add_u32 v6, -65536, v6\n\t"
        "v_add_u32 v7, -65536, v7\n\t"
        "ds_write_b128 v3, v[52:55] offset:49152\n\t"
        "s_waitcnt vmcnt(4)\n\t"
        "s_barrier\n\t"
        "s_add_u32 m0, s69, 131072\n\t"
        "s_xor_b32 s70, s68, 0x4c0000\n\t"
        "buffer_load_dwordx4 v8, %[rw], s70 offen offset:0 lds\n\t"
        "s_add_u32 s70, s70, 64512\n\t"
        "buffer_load_dwordx4 v9, %[rw], s70 offen offset:1024 lds\n\t"
        "s_add_u32 s70, s70, 64512\n\t"
        "buffer_load_dwordx4 v8, %[rw], s70 offen offset:2048 lds\n\t"
        "s_add_u32 s70, s70, 64512\n\t"
        "buffer_load_dwordx4 v9, %[rw], s70 offen offset:3072 lds\n\t"
        "ds_read_b64_tr_b16 v[76:77], v4\n\t"
        "ds_read_b64_tr_b16 v[78:79], v4 offset:512\n\t"
        "ds_read_b64_tr_b16 v[84:85], v5\n\t"
        "ds_read_b64_tr_b16 v[86:87], v5 offset:512\n\t"
        "ds_read_b64_tr_b16 v[100:101], v6\n\t"
        "ds_read_b64_tr_b16 v[102:103], v6 offset:512\n\t"
        "ds_read_b64_tr_b16 v[108:109], v7\n\t"
        "ds_read_b64_tr_b16 v[110:111], v7 offset:512\n\t"
        "s_waitcnt lgkmcnt(0)\n\t"
        "v_add_u32 v4, 32768, v4\n\t"
        "v_add_u32 v5, 32768, v5\n\t"
        "v_add_u32 v6, 32768, v6\n\t"
        "v_add_u32 v7, 32768, v7\n\t"
        "s_waitcnt vmcnt(4)\n\t"
        "s_barrier\n\t"
        "s_add_u32 m0, s69, 65536\n\t"
        "s_xor_b32 s70, s68, 0x6c0000\n\t"
        "buffer_load_dwordx4 v8, %[rw], s70 offen offset:0 lds\n\t"
        "s_add_u32 s70, s70, 64512\n\t"
        "buffer_load_dwordx4 v9, %[rw], s70 offen offset:1024 lds\n\t"
        "s_add_u32 s70, s70, 64512\n\t"
        "buffer_load_dwordx4 v8, %[rw], s70 offen offset:2048 lds\n\t"
        "s_add_u32 s70, s70, 64512\n\t"
        "buffer_load_dwordx4 v9, %[rw], s70 offen offset:3072 lds\n\t"
        "ds_read_b64_tr_b16 v[124:125], v4\n\t"
        "ds_read_b64_tr_b16 v[126:127], v4 offset:512\n\t"
        "ds_read_b64_tr_b16 v[132:133], v5\n\t"
        "ds_read_b64_tr_b16 v[134:135], v5 offset:512\n\t"
        "ds_read_b64_tr_b16 v[148:149], v6\n\t"
        "ds_read_b64_tr_b16 v[150:151], v6 offset:512\n\t"
        "ds_read_b64_tr_b16 v[156:157], v7\n\t"
        "ds_read_b64_tr_b16 v[158:159], v7 offset:512\n\t"
        "s_waitcnt lgkmcnt(0)\n\t"
        "v_add_u32 v4, 32768, v4\n\t"
        "v_add_u32 v5, 32768, v5\n\t"
        "v_add_u32 v6, 32768, v6\n\t"
        "v_add_u32 v7, 32768, v7\n\t"
        "s_waitcnt vmcnt(4)\n\t"
        "s_barrier\n\t"
        "v_mov_b32 v10, 128\n\t"
        "buffer_load_dwordx2 v[10:11], v10, %[rf], 0 offen sc1\n\t"
        "ds_read_b64_tr_b16 v[180:181], v4\n\t"
        "ds_read_b64_tr_b16 v[182:183], v4 offset:512\n\t"
        "ds_read_b64_tr_b16 v[52:53], v5\n\t"
        "ds_read_b64_tr_b16 v[54:55], v5 offset:512\n\t"
        "ds_read_b64_tr_b16 v[204:205], v6\n\t"
        "ds_read_b64_tr_b16 v[206:207], v6 offset:512\n\t"
        "ds_read_b64_tr_b16 v[12:13], v7\n\t"
        "ds_read_b64_tr_b16 v[14:15], v7 offset:512\n\t"
        "s_waitcnt lgkmcnt(0)\n\t"
        "v_add_u32 v4, -65536, v4\n\t"
        "v_add_u32 v5, -65536, v5\n\t"
        "v_add_u32 v6, -65536, v6\n\t"
        "v_add_u32 v7, -65536, v7\n\t"
        "ds_write_b128 v3, v[52:55] offset:17408\n\t"
        "ds_write_b128 v3, v[12:15] offset:33792\n\t"
        "s_waitcnt vmcnt(0)\n\t"
        "s_barrier\n\t"
        "v_bfe_u32 v19, v0, 5, 3\n\t"
        "v_lshlrev_b32 v19, 11, v19\n\t"
        "v_lshrrev_b32 v18, 8, v0\n\t"
        "v_lshl_add_u32 v19, v18, 5, v19\n\t"
        "v_bfe_u32 v18, v0, 4, 1\n\t"
        "v_lshl_add_u32 v19, v18, 2, v19\n\t"
        "s_lshl_b32 s71, %[w], 8\n\t"
        "buffer_load_dword v12, v19, %[rm], s71 offen offset:0\n\t"
        "buffer_load_dword v13, v19, %[rm], s71 offen offset:8\n\t"
        "buffer_load_dword v14, v19, %[rm], s71 offen offset:16\n\t"
        "buffer_load_dword v15, v19, %[rm], s71 offen offset:24\n\t"
        "buffer_load_dword v16, v19, %[rm], s71 offen offset:128\n\t"
        "buffer_load_dword v17, v19, %[rm], s71 offen offset:136\n\t"
        "buffer_load_dword v18, v19, %[rm], s71 offen offset:144\n\t"
        "buffer_load_dword v19, v19, %[rm], s71 offen offset:152\n\t"
        "ds_read_b64_tr_b16 v[228:229], v4\n\t"
        "ds_read_b64_tr_b16 v[230:231], v4 offset:512\n\t"
        "ds_read_b64_tr_b16 v[52:53], v5\n\t"
        "ds_read_b64_tr_b16 v[54:55], v5 offset:512\n\t"
        "ds_read_b64_tr_b16 v[252:253], v6\n\t"
        "ds_read_b64_tr_b16 v[254:255], v6 offset:512\n\t"
        "ds_read_b64_tr_b16 v[24:25], v7\n\t"
        "ds_read_b64_tr_b16 v[26:27], v7 offset:512\n\t"
        "s_waitcnt lgkmcnt(0)\n\t"
        "v_add_u32 v4, 32768, v4\n\t"
        "v_add_u32 v5, 32768, v5\n\t"
        "v_add_u32 v6, 32768, v6\n\t"
        "v_add_u32 v7, 32768, v7\n\t"
        "ds_write_b128 v3, v[52:55] offset:50176\n\t"
        "s_branch .Ls_std_%=\n\t"
        ".Ls_stk_%=:\n\t"
        "v_add_u32 v10, s69, v0\n\t"
        "v_add_u32 v11, 131072, v10\n\t"
        "v_add_u32 v10, 65536, v10\n\t"
        "s_waitcnt vmcnt(4)\n\t"
        "s_barrier\n\t"
        "s_add_u32 m0, s69, 131072\n\t"
        "s_xor_b32 s70, s68, 0x400000\n\t"
        "buffer_load_dwordx4 v8, %[rw], s70 offen offset:0 lds\n\t"
        "s_add_u32 s70, s70, 64512\n\t"
        "buffer_load_dwordx4 v9, %[rw], s70 offen offset:1024 lds\n\t"
        "s_add_u32 s70, s70, 64512\n\t"
        "buffer_load_dwordx4 v8, %[rw], s70 offen offset:2048 lds\n\t"
        "s_add_u32 s70, s70, 64512\n\t"
        "buffer_load_dwordx4 v9, %[rw], s70 offen offset:3072 lds\n\t"
        "ds_read_b64_tr_b16 v[56:57], v4\n\t"
        "ds_read_b64_tr_b16 v[58:59], v4 offset:512\n\t"
        "ds_read_b64_tr_b16 v[64:65], v5\n\t"
        "ds_read_b64_tr_b16 v[66:67], v5 offset:512\n\t"
        "ds_read_b64_tr_b16 v[88:89], v6\n\t"
        "ds_read_b64_tr_b16 v[90:91], v6 offset:512\n\t"
        "ds_read_b64_tr_b16 v[28:29], v7\n\t"
        "ds_read_b64_tr_b16 v[30:31], v7 offset:512\n\t"
        "s_waitcnt lgkmcnt(0)\n\t"
        "v_add_u32 v4, 32768, v4\n\t"
        "v_add_u32 v5, 32768, v5\n\t"
        "v_add_u32 v6, 32768, v6\n\t"
        "v_add_u32 v7, 32768, v7\n\t"
        "s_waitcnt vmcnt(4)\n\t"
        "s_barrier\n\t"
        "s_add_u32 m0, s69, 65536\n\t"
        "s_xor_b32 s70, s68, 0x600000\n\t"
        "buffer_load_dwordx4 v8, %[rw], s70 offen offset:0 lds\n\t"
        "s_add_u32 s70, s70, 64512\n\t"
        "buffer_load_dwordx4 v9, %[rw], s70 offen offset:1024 lds\n\t"
        "s_add_u32 s70, s70, 64512\n\t"
        "buffer_load_dwordx4 v8, %[rw], s70 offen offset:2048 lds\n\t"
        "s_add_u32 s70, s70, 64512\n\t"
        "buffer_load_dwordx4 v9, %[rw], s70 offen offset:3072 lds\n\t"
        "ds_read_b64_tr_b16 v[112:113], v4\n\t"
        "ds_read_b64_tr_b16 v[114:115], v4 offset:512\n\t"
        "ds_read_b64_tr_b16 v[36:37], v5\n\t"
        "ds_read_b64_tr_b16 v[38:39], v5 offset:512\n\t"
        "ds_read_b64_tr_b16 v[136:137], v6\n\t"
        "ds_read_b64_tr_b16 v[138:139], v6 offset:512\n\t"
        "ds_read_b64_tr_b16 v[44:45], v7\n\t"
        "ds_read_b64_tr_b16 v[46:47], v7 offset:512\n\t"
        "s_waitcnt lgkmcnt(0)\n\t"
        "v_add_u32 v4, 32768, v4\n\t"
        "v_add_u32 v5, 32768, v5\n\t"
        "v_add_u32 v6, 32768, v6\n\t"
        "v_add_u32 v7, 32768, v7\n\t"
        "s_waitcnt vmcnt(4)\n\t"
        "s_barrier\n\t"
        "s_add_u32 m0, s69, 98304\n\t"
        "s_xor_b32 s70, s68, 0x40000\n\t"
        "buffer_load_dwordx4 v8, %[rw], s70 offen offset:0 lds\n\t"
        "s_add_u32 s70, s70, 64512\n\t"
        "buffer_load_dwordx4 v9, %[rw], s70 offen offset:1024 lds\n\t"
        "s_add_u32 s70, s70, 64512\n\t"
        "buffer_load_dwordx4 v8, %[rw], s70 offen offset:2048 lds\n\t"
        "s_add_u32 s70, s70, 64512\n\t"
        "buffer_load_dwordx4 v9, %[rw], s70 offen offset:3072 lds\n\t"
        "ds_read_b64_tr_b16 v[152:153], v4\n\t"
        "ds_read_b64_tr_b16 v[154:155], v4 offset:512\n\t"
        "ds_read_b64_tr_b16 v[52:53], v5\n\t"
        "ds_read_b64_tr_b16 v[54:55], v5 offset:512\n\t"
        "ds_read_b64_tr_b16 v[168:169], v6\n\t"
        "ds_read_b64_tr_b16 v[170:171], v6 offset:512\n\t"
        "ds_read_b64_tr_b16 v[20:21], v7\n\t"
        "ds_read_b64_tr_b16 v[22:23], v7 offset:512\n\t"
        "s_waitcnt lgkmcnt(0)\n\t"
        "v_add_u32 v4, -65536, v4\n\t"
        "v_add_u32 v5, -65536, v5\n\t"
        "v_add_u32 v6, -65536, v6\n\t"
        "v_add_u32 v7, -65536, v7\n\t"
        "ds_write_b128 v3, v[52:55] offset:32768\n\t"
        "s_waitcnt vmcnt(4)\n\t"
        "s_barrier\n\t"
        "s_add_u32 m0, s69, 131072\n\t"
        "s_xor_b32 s70, s68, 0x240000\n\t"
        "buffer_load_dwordx4 v8, %[rw], s70 offen offset:0 lds\n\t"
        "s_add_u32 s70, s70, 64512\n\t"
        "buffer_load_dwordx4 v9, %[rw], s70 offen offset:1024 lds\n\t"
        "s_add_u32 s70, s70, 64512\n\t"
        "buffer_load_dwordx4 v8, %[rw], s70 offen offset:2048 lds\n\t"
        "s_add_u32 s70, s70, 64512\n\t"
        "buffer_load_dwordx4 v9, %[rw], s70 offen offset:3072 lds\n\t"
        "ds_read_b64_tr_b16 v[192:193], v4\n\t"
        "ds_read_b64_tr_b16 v[194:195], v4 offset:512\n\t"
        "ds_read_b64_tr_b16 v[200:201], v5\n\t"
        "ds_read_b64_tr_b16 v[202:203], v5 offset:512\n\t"
        "ds_read_b64_tr_b16 v[224:225], v6\n\t"
        "ds_read_b64_tr_b16 v[226:227], v6 offset:512\n\t"
        "ds_read_b64_tr_b16 v[232:233], v7\n\t"
        "ds_read_b64_tr_b16 v[234:235], v7 offset:512\n\t"
        "s_waitcnt lgkmcnt(0)\n\t"
        "v_add_u32 v4, 32768, v4\n\t"
        "v_add_u32 v5, 32768, v5\n\t"
        "v_add_u32 v6, 32768, v6\n\t"
        "v_add_u32 v7, 32768, v7\n\t"
        "s_waitcnt vmcnt(4)\n\t"
        "s_barrier\n\t"
        "s_add_u32 m0, s69, 65536\n\t"
        "s_xor_b32 s70, s68, 0x440000\n\t"
        "buffer_load_dwordx4 v8, %[rw], s70 offen offset:0 lds\n\t"
        "s_add_u32 s70, s70, 64512\n\t"
        "buffer_load_dwordx4 v9, %[rw], s70 offen offset:1024 lds\n\t"
        "s_add_u32 s70, s70, 64512\n\t"
        "buffer_load_dwordx4 v8, %[rw], s70 offen offset:2048 lds\n\t"
        "s_add_u32 s70, s70, 64512\n\t"
        "buffer_load_dwordx4 v9, %[rw], s70 offen offset:3072 lds\n\t"
        "ds_read_b64_tr_b16 v[60:61], v4\n\t"
        "ds_read_b64_tr_b16 v[62:63], v4 offset:512\n\t"
        "ds_read_b64_tr_b16 v[68:69], v5\n\t"
        "ds_read_b64_tr_b16 v[70:71], v5 offset:512\n\t"
        "ds_read_b64_tr_b16 v[92:93], v6\n\t"
        "ds_read_b64_tr_b16 v[94:95], v6 offset:512\n\t"
        "ds_read_b64_tr_b16 v[32:33], v7\n\t"
        "ds_read_b64_tr_b16 v[34:35], v7 offset:512\n\t"
        "s_waitcnt lgkmcnt(0)\n\t"
        "v_add_u32 v4, 32768, v4\n\t"
        "v_add_u32 v5, 32768, v5\n\t"
        "v_add_u32 v6, 32768, v6\n\t"
        "v_add_u32 v7, 32768, v7\n\t"
        "s_waitcnt vmcnt(4)\n\t"
        "s_barrier\n\t"
        "s_add_u32 m0, s69, 98304\n\t"
        "s_xor_b32 s70, s68, 0x640000\n\t"
        "buffer_load_dwordx4 v8, %[rw], s70 offen offset:0 lds\n\t"
        "s_add_u32 s70, s70, 64512\n\t"
        "buffer_load_dwordx4 v9, %[rw], s70 offen offset:1024 lds\n\t"
        "s_add_u32 s70, s70, 64512\n\t"
        "buffer_load_dwordx4 v8, %[rw], s70 offen offset:2048 lds\n\t"
        "s_add_u32 s70, s70, 64512\n\t"
        "buffer_load_dwordx4 v9, %[rw], s70 offen offset:3072 lds\n\t"
        "ds_read_b64_tr_b16 v[116:117], v4\n\t"
        "ds_read_b64_tr_b16 v[118:119], v4 offset:512\n\t"
        "ds_read_b64_tr_b16 v[40:41], v5\n\t"
        "ds_read_b64_tr_b16 v[42:43], v5 offset:512\n\t"
        "ds_read_b64_tr_b16 v[140:141], v6\n\t"
        "ds_read_b64_tr_b16 v[142:143], v6 offset:512\n\t"
        "ds_read_b64_tr_b16 v[48:49], v7\n\t"
        "ds_read_b64_tr_b16 v[50:51], v7 offset:512\n\t"
        "s_waitcnt lgkmcnt(0)\n\t"
        "v_add_u32 v4, -65536, v4\n\t"
        "v_add_u32 v5, -65536, v5\n\t"
        "v_add_u32 v6, -65536, v6\n\t"
        "v_add_u32 v7, -65536, v7\n\t"
        "s_waitcnt vmcnt(4)\n\t"
        "s_barrier\n\t"
        "s_add_u32 m0, s69, 131072\n\t"
        "s_xor_b32 s70, s68, 0x80000\n\t"
        "buffer_load_dwordx4 v8, %[rw], s70 offen offset:0 lds\n\t"
        "s_add_u32 s70, s70, 64512\n\t"
        "buffer_load_dwordx4 v9, %[rw], s70 offen offset:1024 lds\n\t"
        "s_add_u32 s70, s70, 64512\n\t"
        "buffer_load_dwordx4 v8, %[rw], s70 offen offset:2048 lds\n\t"
        "s_add_u32 s70, s70, 64512\n\t"
        "buffer_load_dwordx4 v9, %[rw], s70 offen offset:3072 lds\n\t"
        "ds_read_b64_tr_b16 v[156:157], v4\n\t"
        "ds_read_b64_tr_b16 v[158:159], v4 offset:512\n\t"
        "ds_read_b64_tr_b16 v[52:53], v5\n\t"
        "ds_read_b64_tr_b16 v[54:55], v5 offset:512\n\t"
        "ds_read_b64_tr_b16 v[172:173], v6\n\t"
        "ds_read_b64_tr_b16 v[174:175], v6 offset:512\n\t"
        "ds_read_b64_tr_b16 v[24:25], v7\n\t"
        "ds_read_b64_tr_b16 v[26:27], v7 offset:512\n\t"
        "s_waitcnt lgkmcnt(0)\n\t"
        "v_add_u32 v4, 32768, v4\n\t"
        "v_add_u32 v5, 32768, v5\n\t"
        "v_add_u32 v6, 32768, v6\n\t"
        "v_add_u32 v7, 32768, v7\n\t"
        "ds_write_b128 v3, v[52:55] offset:33792\n\t"
        "s_waitcnt vmcnt(4)\n\t"
        "s_barrier\n\t"
        "s_add_u32 m0, s69, 65536\n\t"
        "s_xor_b32 s70, s68, 0x280000\n\t"
        "buffer_load_dwordx4 v8, %[rw], s70 offen offset:0 lds\n\t"
        "s_add_u32 s70, s70, 64512\n\t"
        "buffer_load_dwordx4 v9, %[rw], s70 offen offset:1024 lds\n\t"
        "s_add_u32 s70, s70, 64512\n\t"
        "buffer_load_dwordx4 v8, %[rw], s70 offen offset:2048 lds\n\t"
        "s_add_u32 s70, s70, 64512\n\t"
        "buffer_load_dwordx4 v9, %[rw], s70 offen offset:3072 lds\n\t"
        "ds_read_b64_tr_b16 v[196:197], v4\n\t"
        "ds_read_b64_tr_b16 v[198:199], v4 offset:512\n\t"
        "ds_read_b64_tr_b16 v[204:205], v5\n\t"
        "ds_read_b64_tr_b16 v[206:207], v5 offset:512\n\t"
        "ds_read_b64_tr_b16 v[228:229], v6\n\t"
        "ds_read_b64_tr_b16 v[230:231], v6 offset:512\n\t"
        "ds_read_b64_tr_b16 v[236:237], v7\n\t"
        "ds_read_b64_tr_b16 v[238:239], v7 offset:512\n\t"
        "s_waitcnt lgkmcnt(0)\n\t"
        "v_add_u32 v4, 32768, v4\n\t"
        "v_add_u32 v5, 32768, v5\n\t"
        "v_add_u32 v6, 32768, v6\n\t"
        "v_add_u32 v7, 32768, v7\n\t"
        "s_waitcnt vmcnt(4)\n\t"
        "s_barrier\n\t"
        "s_add_u32 m0, s69, 98304\n\t"
        "s_xor_b32 s70, s68, 0x480000\n\t"
        "buffer_load_dwordx4 v8, %[rw], s70 offen offset:0 lds\n\t"
        "s_add_u32 s70, s70, 64512\n\t"
        "buffer_load_dwordx4 v9, %[rw], s70 offen offset:1024 lds\n\t"
        "s_add_u32 s70, s70, 64512\n\t"
        "buffer_load_dwordx4 v8, %[rw], s70 offen offset:2048 lds\n\t"
        "s_add_u32 s70, s70, 64512\n\t"
        "buffer_load_dwordx4 v9, %[rw], s70 offen offset:3072 lds\n\t"
        "ds_read_b64_tr_b16 v[72:73], v4\n\t"
        "ds_read_b64_tr_b16 v[74:75], v4 offset:512\n\t"
        "ds_read_b64_tr_b16 v[80:81], v5\n\t"
        "ds_read_b64_tr_b16 v[82:83], v5 offset:512\n\t"
        "ds_read_b64_tr_b16 v[96:97], v6\n\t"
        "ds_read_b64_tr_b16 v[98:99], v6 offset:512\n\t"
        "ds_read_b64_tr_b16 v[104:105], v7\n\t"
        "ds_read_b64_tr_b16 v[106:107], v7 offset:512\n\t"
        "s_waitcnt lgkmcnt(0)\n\t"
        "v_add_u32 v4, -65536, v4\n\t"
        "v_add_u32 v5, -65536, v5\n\t"
        "v_add_u32 v6, -65536, v6\n\t"
        "v_add_u32 v7, -65536, v7\n\t"
        "s_waitcnt vmcnt(4)\n\t"
        "s_barrier\n\t"
        "s_add_u32 m0, s69, 131072\n\t"
        "s_xor_b32 s70, s68, 0x680000\n\t"
        "buffer_load_dwordx4 v8, %[rw], s70 offen offset:0 lds\n\t"
        "s_add_u32 s70, s70, 64512\n\t"
        "buffer_load_dwordx4 v9, %[rw], s70 offen offset:1024 lds\n\t"
        "s_add_u32 s70, s70, 64512\n\t"
        "buffer_load_dwordx4 v8, %[rw], s70 offen offset:2048 lds\n\t"
        "s_add_u32 s70, s70, 64512\n\t"
        "buffer_load_dwordx4 v9, %[rw], s70 offen offset:3072 lds\n\t"
        "v_bfe_u32 v19, v0, 5, 3\n\t"
        "v_lshlrev_b32 v19, 11, v19\n\t"
        "v_lshrrev_b32 v18, 8, v0\n\t"
        "v_lshl_add_u32 v19, v18, 5, v19\n\t"
        "v_bfe_u32 v18, v0, 4, 1\n\t"
        "v_lshl_add_u32 v19, v18, 2, v19\n\t"
        "s_lshl_b32 s71, %[w], 8\n\t"
        "buffer_load_dword v12, v19, %[rm], s71 offen offset:0\n\t"
        "buffer_load_dword v13, v19, %[rm], s71 offen offset:8\n\t"
        "buffer_load_dword v14, v19, %[rm], s71 offen offset:16\n\t"
        "buffer_load_dword v15, v19, %[rm], s71 offen offset:24\n\t"
        "buffer_load_dword v16, v19, %[rm], s71 offen offset:128\n\t"
        "buffer_load_dword v17, v19, %[rm], s71 offen offset:136\n\t"
        "buffer_load_dword v18, v19, %[rm], s71 offen offset:144\n\t"
        "buffer_load_dword v19, v19, %[rm], s71 offen offset:152\n\t"
        "ds_read_b64_tr_b16 v[120:121], v4\n\t"
        "ds_read_b64_tr_b16 v[122:123], v4 offset:512\n\t"
        "ds_read_b64_tr_b16 v[128:129], v5\n\t"
        "ds_read_b64_tr_b16 v[130:131], v5 offset:512\n\t"
        "ds_read_b64_tr_b16 v[144:145], v6\n\t"
        "ds_read_b64_tr_b16 v[146:147], v6 offset:512\n\t"
        "ds_read_b64_tr_b16 v[52:53], v7\n\t"
        "ds_read_b64_tr_b16 v[54:55], v7 offset:512\n\t"
        "s_waitcnt lgkmcnt(0)\n\t"
        "v_add_u32 v4, 32768, v4\n\t"
        "v_add_u32 v5, 32768, v5\n\t"
        "v_add_u32 v6, 32768, v6\n\t"
        "v_add_u32 v7, 32768, v7\n\t"
        "ds_write_b128 v3, v[52:55] offset:16384\n\t"
        "s_waitcnt vmcnt(12)\n\t"
        "s_barrier\n\t"
        "s_add_u32 m0, s69, 65536\n\t"
        "s_xor_b32 s70, s68, 0xc0000\n\t"
        "buffer_load_dwordx4 v8, %[rw], s70 offen offset:0 lds\n\t"
        "s_add_u32 s70, s70, 64512\n\t"
        "buffer_load_dwordx4 v9, %[rw], s70 offen offset:1024 lds\n\t"
        "s_add_u32 s70, s70, 64512\n\t"
        "buffer_load_dwordx4 v8, %[rw], s70 offen offset:2048 lds\n\t"
        "s_add_u32 s70, s70, 64512\n\t"
        "buffer_load_dwordx4 v9, %[rw], s70 offen offset:3072 lds\n\t"
        "ds_read_b64_tr_b16 v[160:161], v4\n\t"
        "ds_read_b64_tr_b16 v[162:163], v4 offset:512\n\t"
        "ds_read_b64_tr_b16 v[52:53], v5\n\t"
        "ds_read_b64_tr_b16 v[54:55], v5 offset:512\n\t"
        "ds_read_b64_tr_b16 v[176:177], v6\n\t"
        "ds_read_b64_tr_b16 v[178:179], v6 offset:512\n\t"
        "ds_read_b64_tr_b16 v[184:185], v7\n\t"
        "ds_read_b64_tr_b16 v[186:187], v7 offset:512\n\t"
        "s_waitcnt lgkmcnt(0)\n\t"
        "v_add_u32 v4, 32768, v4\n\t"
        "v_add_u32 v5, 32768, v5\n\t"
        "v_add_u32 v6, 32768, v6\n\t"
        "v_add_u32 v7, 32768, v7\n\t"
        "ds_write_b128 v3, v[52:55] offset:49152\n\t"
        "s_waitcnt vmcnt(12)\n\t"
        "s_barrier\n\t"
        "s_add_u32 m0, s69, 98304\n\t"
        "s_xor_b32 s70, s68, 0x2c0000\n\t"
        "buffer_load_dwordx4 v8, %[rw], s70 offen offset:0 lds\n\t"
        "s_add_u32 s70, s70, 64512\n\t"
        "buffer_load_dwordx4 v9, %[rw], s70 offen offset:1024 lds\n\t"
        "s_add_u32 s70, s70, 64512\n\t"
        "buffer_load_dwordx4 v8, %[rw], s70 offen offset:2048 lds\n\t"
        "s_add_u32 s70, s70, 64512\n\t"
        "buffer_load_dwordx4 v9, %[rw], s70 offen offset:3072 lds\n\t"
        "ds_read_b64_tr_b16 v[208:209], v4\n\t"
        "ds_read_b64_tr_b16 v[210:211], v4 offset:512\n\t"
        "ds_read_b64_tr_b16 v[216:217], v5\n\t"
        "ds_read_b64_tr_b16 v[218:219], v5 offset:512\n\t"
        "ds_read_b64_tr_b16 v[240:241], v6\n\t"
        "ds_read_b64_tr_b16 v[242:243], v6 offset:512\n\t"
        "ds_read_b64_tr_b16 v[248:249], v7\n\t"
        "ds_read_b64_tr_b16 v[250:251], v7 offset:512\n\t"
        "s_waitcnt lgkmcnt(0)\n\t"
        "v_add_u32 v4, -65536, v4\n\t"
        "v_add_u32 v5, -65536, v5\n\t"
        "v_add_u32 v6, -65536, v6\n\t"
        "v_add_u32 v7, -65536, v7\n\t"
        "s_waitcnt vmcnt(4)\n\t"
        "s_barrier\n\t"
        "s_add_u32 m0, s69, 131072\n\t"
        "s_xor_b32 s70, s68, 0x4c0000\n\t"
        "buffer_load_dwordx4 v8, %[rw], s70 offen offset:0 lds\n\t"
        "s_add_u32 s70, s70, 64512\n\t"
        "buffer_load_dwordx4 v9, %[rw], s70 offen offset:1024 lds\n\t"
        "s_add_u32 s70, s70, 64512\n\t"
        "buffer_load_dwordx4 v8, %[rw], s70 offen offset:2048 lds\n\t"
        "s_add_u32 s70, s70, 64512\n\t"
        "buffer_load_dwordx4 v9, %[rw], s70 offen offset:3072 lds\n\t"
        "ds_read_b64_tr_b16 v[76:77], v4\n\t"
        "ds_read_b64_tr_b16 v[78:79], v4 offset:512\n\t"
        "ds_read_b64_tr_b16 v[84:85], v5\n\t"
        "ds_read_b64_tr_b16 v[86:87], v5 offset:512\n\t"
        "ds_read_b64_tr_b16 v[100:101], v6\n\t"
        "ds_read_b64_tr_b16 v[102:103], v6 offset:512\n\t"
        "ds_read_b64_tr_b16 v[108:109], v7\n\t"
        "ds_read_b64_tr_b16 v[110:111], v7 offset:512\n\t"
        "s_waitcnt lgkmcnt(0)\n\t"
        "v_add_u32 v4, 32768, v4\n\t"
        "v_add_u32 v5, 32768, v5\n\t"
        "v_add_u32 v6, 32768, v6\n\t"
        "v_add_u32 v7, 32768, v7\n\t"
        "s_waitcnt vmcnt(4)\n\t"
        "s_barrier\n\t"
        "s_add_u32 m0, s69, 65536\n\t"
        "s_xor_b32 s70, s68, 0x6c0000\n\t"
        "buffer_load_dwordx4 v8, %[rw], s70 offen offset:0 lds\n\t"
        "s_add_u32 s70, s70, 64512\n\t"
        "buffer_load_dwordx4 v9, %[rw], s70 offen offset:1024 lds\n\t"
        "s_add_u32 s70, s70, 64512\n\t"
        "buffer_load_dwordx4 v8, %[rw], s70 offen offset:2048 lds\n\t"
        "s_add_u32 s70, s70, 64512\n\t"
        "buffer_load_dwordx4 v9, %[rw], s70 offen offset:3072 lds\n\t"
        "ds_read_b64_tr_b16 v[124:125], v4\n\t"
        "ds_read_b64_tr_b16 v[126:127], v4 offset:512\n\t"
        "ds_read_b64_tr_b16 v[132:133], v5\n\t"
        "ds_read_b64_tr_b16 v[134:135], v5 offset:512\n\t"
        "ds_read_b64_tr_b16 v[148:149], v6\n\t"
        "ds_read_b64_tr_b16 v[150:151], v6 offset:512\n\t"
        "ds_read_b64_tr_b16 v[52:53], v7\n\t"
        "ds_read_b64_tr_b16 v[54:55], v7 offset:512\n\t"
        "s_waitcnt lgkmcnt(0)\n\t"
        "v_add_u32 v4, 32768, v4\n\t"
        "v_add_u32 v5, 32768, v5\n\t"
        "v_add_u32 v6, 32768, v6\n\t"
        "v_add_u32 v7, 32768, v7\n\t"
        "ds_write_b128 v3, v[52:55] offset:17408\n\t"
        "s_waitcnt vmcnt(4)\n\t"
        "s_barrier\n\t"
        "v_mov_b32 v10, 128\n\t"
        "buffer_load_dwordx2 v[10:11], v10, %[rf], 0 offen sc1\n\t"
        "ds_read_b64_tr_b16 v[164:165], v4\n\t"
        "ds_read_b64_tr_b16 v[166:167], v4 offset:512\n\t"
        "ds_read_b64_tr_b16 v[52:53], v5\n\t"
        "ds_read_b64_tr_b16 v[54:55], v5 offset:512\n\t"
        "ds_read_b64_tr_b16 v[180:181], v6\n\t"
        "ds_read_b64_tr_b16 v[182:183], v6 offset:512\n\t"
        "ds_read_b64_tr_b16 v[188:189], v7\n\t"
        "ds_read_b64_tr_b16 v[190:191], v7 offset:512\n\t"
        "s_waitcnt lgkmcnt(0)\n\t"
        "v_add_u32 v4, -65536, v4\n\t"
        "v_add_u32 v5, -65536, v5\n\t"
        "v_add_u32 v6, -65536, v6\n\t"
        "v_add_u32 v7, -65536, v7\n\t"
        "ds_write_b128 v3, v[52:55] offset:50176\n\t"
        "s_waitcnt vmcnt(0)\n\t"
        "s_barrier\n\t"
        "ds_read_b64_tr_b16 v[212:213], v4\n\t"
        "ds_read_b64_tr_b16 v[214:215], v4 offset:512\n\t"
        "ds_read_b64_tr_b16 v[220:221], v5\n\t"
        "ds_read_b64_tr_b16 v[222:223], v5 offset:512\n\t"
        "ds_read_b64_tr_b16 v[244:245], v6\n\t"
        "ds_read_b64_tr_b16 v[246:247], v6 offset:512\n\t"
        "ds_read_b64_tr_b16 v[252:253], v7\n\t"
        "ds_read_b64_tr_b16 v[254:255], v7 offset:512\n\t"
        "s_waitcnt lgkmcnt(0)\n\t"
        "v_add_u32 v4, 32768, v4\n\t"
        "v_add_u32 v5, 32768, v5\n\t"
        "v_add_u32 v6, 32768, v6\n\t"
        "v_add_u32 v7, 32768, v7\n\t"
        ".Ls_std_%=:\n\t"
        "s_waitcnt lgkmcnt(0)\n\t"
        "s_barrier\n\t"
        "v_add_u32 v53, 65536, v3\n\t"
        "ds_write_b128 v53, v[28:31] offset:0\n\t"
        "ds_write_b128 v53, v[32:35] offset:1024\n\t"
        "ds_write_b128 v53, v[36:39] offset:16384\n\t"
        "ds_write_b128 v53, v[40:43] offset:17408\n\t"
        "ds_write_b128 v53, v[44:47] offset:32768\n\t"
        "ds_write_b128 v53, v[48:51] offset:33792\n\t"
        "ds_write_b128 v53, v[20:23] offset:49152\n\t"
        "ds_write_b128 v53, v[24:27] offset:50176\n\t"
        "v_xor_b32 v3, 0x1800, v2\n\t"
        "s_mov_b32 s56, 0x55555555\n\t"
        "s_mov_b32 s57, 0x55555555\n\t"
        "s_mov_b32 s66, 0x3e0293ee\n\t"
        "s_mov_b32 s58, 0xfffcffff\n\t"
        "s_mov_b32 s59, 0xffc0fff0\n\t"
        "s_mov_b32 s60, 0xfc00ff00\n\t"
        "s_mov_b32 s61, 0xc000f000\n\t"
        "s_mov_b32 s62, 0xfff8fffe\n\t"
        "s_mov_b32 s63, 0xff80ffe0\n\t"
        "s_mov_b32 s64, 0xf800fe00\n\t"
        "s_mov_b32 s65, 0x8000e000\n\t"
        "s_memtime s[92:93]\n\t"
        "s_memrealtime s[94:95]\n\t"
        "s_mov_b32 s98, 0x3d380000\n\t"
        "s_waitcnt lgkmcnt(0)\n\t"
        "s_sub_u32 s96, s92, 4500\n\t"
        "s_sub_u32 s97, s94, 200\n\t"
        "s_waitcnt vmcnt(0)\n\t"
        "v_readfirstlane_b32 s68, v10\n\t"
        "v_readfirstlane_b32 s69, v11\n\t"
        "s_xor_b32 s68, s68, s54\n\t"
        "s_xor_b32 s69, s69, s55\n\t"
        "s_lshr_b32 s70, s68, 8\n\t"
        "s_or_b32 s70, s70, s69\n\t"
        "s_cmp_eq_u32 s70, 0\n\t"
        "s_cselect_b32 s68, s68, 0\n\t"
        "s_and_b32 s69, s68, 127\n\t"
        "s_max_u32 s51, s51, s69\n\t"
        "s_cmp_ge_u32 s51, 1\n\t"
        "s_cbranch_scc0 .Ls_f0p_%=\n\t"
        ".Ls_f0k_%=:\n\t"
        "s_lshr_b32 s48, s68, 7\n\t"
        "s_lshl_b32 s48, s48, 13\n\t"
        "s_lshl_b32 s70, %[cs], 13\n\t"
        "s_xor_b32 s70, s70, s48\n\t"
        "s_lshr_b32 s71, %[w], 2\n\t"
        "s_lshl_b32 s71, s71, 14\n\t"
        "s_and_b32 s72, %[w], 3\n\t"
        "s_lshl_b32 s73, s72, 11\n\t"
        "s_add_u32 s71, s71, s73\n\t"
        "s_add_u32 s47, s71, %[ra]\n\t"
        "s_add_u32 s47, s47, s70\n\t"
        "s_xor_b32 s47, s47, 0x2000\n\t"
        "s_sub_u32 s47, s47, 2097152\n\t"
        "s_lshl_b32 s71, %[h], 14\n\t"
        "s_add_u32 s49, s71, s70\n\t"
        "s_mov_b32 s50, s48\n\t"
        "s_lshr_b32 s71, %[h], 4\n\t"
        "s_lshl_b32 s74, s71, 14\n\t"
        "s_and_b32 s71, %[h], 15\n\t"
        "s_lshl_b32 s71, s71, 9\n\t"
        "s_add_u32 s74, s74, s71\n\t"
        "s_lshr_b32 s71, %[h], 3\n\t"
        "s_lshl_b32 s75, s71, 10\n\t"
        "s_bfe_u32 s72, %[h], 0x20001\n\t"
        "s_lshl_b32 s73, s72, 8\n\t"
        "s_add_u32 s75, s75, s73\n\t"
        "s_and_b32 s73, %[h], 1\n\t"
        "s_lshl_b32 s73, s73, 3\n\t"
        "s_add_u32 s75, s75, s73\n\t"
        "s_add_u32 s75, s75, 32768\n\t"
        "s_lshl_b32 s71, s71, 15\n\t"
        "s_add_u32 s71, s71, 196608\n\t"
        "s_and_b32 s70, %[h], 7\n\t"
        "s_lshl_b32 s76, s70, 10\n\t"
        "s_add_u32 s76, s76, s71\n\t"
        "s_lshl_b32 s77, s72, 10\n\t"
        "s_add_u32 s77, s77, s73\n\t"
        "s_add_u32 s77, s77, s71\n\t"
        "s_add_u32 s77, s77, 16384\n\t"
        "s_lshl_b32 s78, s70, 8\n\t"
        "s_add_u32 s78, s78, s71\n\t"
        "s_add_u32 s78, s78, 20480\n\t"
        "v_mbcnt_lo_u32_b32 v52, -1, 0\n\t"
        "v_mbcnt_hi_u32_b32 v52, -1, v52\n\t"
        "v_mov_b64 v[4:5], 0\n\t"
        "v_mov_b64 v[6:7], 0\n\t"
        "v_mov_b64 v[8:9], 0\n\t"
        "v_mov_b64 v[10:11], 0\n\t"
        "v_lshrrev_b32 v53, 4, v52\n\t"
        "v_lshlrev_b32 v53, 3, v53\n\t"
        "s_cmp_ge_u32 %[w], 4\n\t"
        "s_cbranch_scc1 .Ls_kpro_%=\n\t"
        "s_mov_b32 s52, 147456\n\t"
        "s_add_u32 s49, s49, 83886080\n\t"
        "s_sub_u32 s70, %[w], 1\n\t"
        "s_lshl_b32 s70, s70, 12\n\t"
        "s_add_u32 s49, s49, s70\n\t"
        "v_mov_b32 v20, v12\n\t"
        "v_mov_b32 v21, v13\n\t"
        "v_mov_b32 v22, v14\n\t"
        "v_mov_b32 v23, v15\n\t"
        "v_mov_b32 v24, v16\n\t"
        "v_mov_b32 v25, v17\n\t"
        "v_mov_b32 v26, v18\n\t"
        "v_mov_b32 v27, v19\n\t"
        "s_and_b32 s70, s45, 1\n\t"
        "s_lshl_b32 s70, s70, 13\n\t"
        "s_add_u32 s71, s70, %[kx]\n\t"
        "v_bfe_u32 v12, v0, 4, 1\n\t"
        "v_bfe_u32 v13, v0, 8, 1\n\t"
        "v_lshrrev_b32 v14, 9, v0\n\t"
        "v_and_b32 v15, 0xe0, v0\n\t"
        "v_and_b32 v16, 0xf0, v0\n\t"
        "v_bfe_u32 v17, v0, 6, 2\n\t"
        "v_lshrrev_b32 v18, 8, v0\n\t"
        "v_lshrrev_b32 v4, 1, v15\n\t"
        "v_lshl_add_u32 v4, v12, 9, v4\n\t"
        "v_lshl_add_u32 v4, v13, 8, v4\n\t"
        "v_lshl_add_u32 v4, v14, 10, v4\n\t"
        "v_add_u32 v4, s71, v4\n\t"
        "v_bfe_u32 v5, v0, 4, 4\n\t"
        "v_xor_b32 v5, v5, v18\n\t"
        "v_lshlrev_b32 v5, 3, v5\n\t"
        "v_mul_u32_u24 v6, 384, v18\n\t"
        "v_add3_u32 v5, v5, v6, s52\n\t"
        "s_lshl_b32 s71, %[w], 2\n\t"
        "s_add_u32 s71, s71, 156672\n\t"
        "v_add_u32 v6, s71, v0\n\t"
        "v_lshl_add_u32 v7, v12, 2, v18\n\t"
        "v_xor_b32 v7, v7, v17\n\t"
        "v_lshlrev_b32 v7, 3, v7\n\t"
        "v_lshl_add_u32 v7, v15, 2, v7\n\t"
        "v_lshl_add_u32 v7, v17, 7, v7\n\t"
        "s_mul_i32 s71, %[w], 1536\n\t"
        "s_add_u32 s71, s71, s52\n\t"
        "v_add_u32 v7, s71, v7\n\t"
        "v_lshl_add_u32 v8, v13, 3, v16\n\t"
        "v_lshl_add_u32 v8, v14, 8, v8\n\t"
        "v_lshl_add_u32 v9, v13, 4, v15\n\t"
        "v_lshl_add_u32 v9, v14, 2, v9\n\t"
        "v_lshlrev_b32 v10, 13, v12\n\t"
        "s_and_b32 s71, s50, 0x2000\n\t"
        "v_xor_b32 v10, s71, v10\n\t"
        "v_add_u32 v9, v9, v10\n\t"
        "s_lshl_b32 s71, %[w], 10\n\t"
        "s_add_u32 s71, s71, 158720\n\t"
        "v_add_u32 v55, s71, v0\n\t"
        "v_add_u32 v14, 162816, v0\n\t"
        "v_and_b32 v10, 0x1f0, v0\n\t"
        "v_lshlrev_b32 v10, 1, v10\n\t"
        "ds_write_b128 v55, v[4:7]\n\t"
        "ds_write_b128 v14, v[8:11]\n\t"
        "s_xor_b32 s48, s50, %[tw]\n\t"
        "v_mov_b32 v12, 0xffbfffbf\n\t"
        "v_mov_b32 v13, v12\n\t"
        "ds_write_b64 v7, v[12:13]\n\t"
        "ds_write_b64 v7, v[12:13] offset:64\n\t"
        "s_add_u32 s72, s50, s74\n\t"
        "s_xor_b32 s73, s72, 0x2000\n\t"
        "s_andn2_b32 s67, s50, 0x2000\n\t"
        "s_add_u32 s67, s67, s75\n\t"
        "v_mov_b32 v12, v20\n\t"
        "v_mov_b32 v13, v21\n\t"
        "v_mov_b32 v14, v22\n\t"
        "v_mov_b32 v15, v23\n\t"
        "v_mov_b32 v16, v24\n\t"
        "v_mov_b32 v17, v25\n\t"
        "v_mov_b32 v18, v26\n\t"
        "v_mov_b32 v19, v27\n\t"
        "v_mov_b64 v[4:5], 0\n\t"
        "v_mov_b64 v[6:7], 0\n\t"
        "v_mov_b64 v[8:9], 0\n\t"
        "v_mov_b64 v[10:11], 0\n\t"
        "v_xor_b32 v52, 0x800, v2\n\t"
        "v_xor_b32 v53, 0x1000, v2\n\t"
        "s_setprio 2\n\t"
        "s_waitcnt lgkmcnt(0)\n\t"
        "s_branch .Ls_q0r_%=\n\t"
        ".p2align 6\n\t"
        ".Ls_qloop_%=:\n\t"
        "s_barrier\n\t"
        "buffer_load_dwordx4 v[12:15], v0, %[rs], s47 offen sc1\n\t"
        "s_nop 4\n\t"
        "buffer_load_dwordx4 v[16:19], v0, %[rs], s47 offen offset:1024 sc1\n\t"
        ".Ls_q0r_%=:\n\t"
        "s_waitcnt vmcnt(1)\n\t"
        "ds_write_b128 v2, v[12:15] offset:0\n\t"
        "v_smfmac_f32_16x16x64_bf16 v[4:7], v[12:15], v[56:63], v1\n\t"
        "v_max_u32 v54, v12, v13\n\t"
        "v_smfmac_f32_16x16x64_bf16 v[8:11], v[12:15], v[64:71], v1\n\t"
        "v_max3_u32 v54, v54, v14, v15\n\t"
        "s_waitcnt vmcnt(0)\n\t"
        "ds_write_b128 v2, v[16:19] offset:1024\n\t"
        "v_max3_u32 v54, v54, v16, v17\n\t"
        "v_max3_u32 v54, v54, v18, v19\n\t"
        "v_smfmac_f32_16x16x64_bf16 v[4:7], v[16:19], v[72:79], v1\n\t"
        "v_cmp_eq_u32 vcc, 0xffbfffbf, v54\n\t"
        "v_smfmac_f32_16x16x64_bf16 v[8:11], v[16:19], v[80:87], v1\n\t"
        "s_waitcnt lgkmcnt(0)\n\t"
        "s_cbranch_vccnz .Ls_q0f_%=\n\t"
        ".Ls_q0k_%=:\n\t"
        "s_barrier\n\t"
        "ds_read_b128 v[20:23], v52 offset:0\n\t"
        "ds_read_b128 v[24:27], v52 offset:1024\n\t"
        "ds_read_b128 v[12:15], v53 offset:0\n\t"
        "ds_read_b128 v[16:19], v53 offset:1024\n\t"
        "s_cmp_eq_u32 s45, 63\n\t"
        "s_cbranch_scc1 .Ls_qlast_%=\n\t"
        "s_waitcnt lgkmcnt(3)\n\t"
        "v_smfmac_f32_16x16x64_bf16 v[4:7], v[20:23], v[88:95], v1\n\t"
        "v_smfmac_f32_16x16x64_bf16 v[8:11], v[20:23], v[28:35], v1\n\t"
        "ds_read_b128 v[20:23], v3 offset:0\n\t"
        "s_waitcnt lgkmcnt(3)\n\t"
        "v_smfmac_f32_16x16x64_bf16 v[4:7], v[24:27], v[96:103], v1\n\t"
        "v_smfmac_f32_16x16x64_bf16 v[8:11], v[24:27], v[104:111], v1\n\t"
        "ds_read_b128 v[28:31], v2 offset:16384\n\t"
        "ds_read_b128 v[32:35], v2 offset:17408\n\t"
        "ds_read_b128 v[24:27], v3 offset:1024\n\t"
        "s_waitcnt lgkmcnt(5)\n\t"
        "v_smfmac_f32_16x16x64_bf16 v[4:7], v[12:15], v[112:119], v1\n\t"
        "v_smfmac_f32_16x16x64_bf16 v[8:11], v[12:15], v[36:43], v1\n\t"
        "ds_read_b128 v[12:15], v2 offset:8192\n\t"
        "s_waitcnt lgkmcnt(5)\n\t"
        "v_smfmac_f32_16x16x64_bf16 v[4:7], v[16:19], v[120:127], v1\n\t"
        "v_smfmac_f32_16x16x64_bf16 v[8:11], v[16:19], v[128:135], v1\n\t"
        "ds_read_b128 v[36:39], v2 offset:32768\n\t"
        "ds_read_b128 v[40:43], v2 offset:33792\n\t"
        "ds_read_b128 v[16:19], v2 offset:9216\n\t"
        "s_waitcnt lgkmcnt(7)\n\t"
        "v_smfmac_f32_16x16x64_bf16 v[4:7], v[20:23], v[136:143], v1\n\t"
        "v_smfmac_f32_16x16x64_bf16 v[8:11], v[20:23], v[44:51], v1\n\t"
        "ds_read_b128 v[20:23], v52 offset:8192\n\t"
        "s_waitcnt lgkmcnt(5)\n\t"
        "v_smfmac_f32_16x16x64_bf16 v[4:7], v[24:27], v[144:151], v1\n\t"
        "v_smfmac_f32_16x16x64_bf16 v[8:11], v[24:27], v[152:159], v1\n\t"
        "ds_read_b128 v[44:47], v2 offset:49152\n\t"
        "ds_read_b128 v[48:51], v2 offset:50176\n\t"
        "ds_read_b128 v[24:27], v52 offset:9216\n\t"
        "s_waitcnt lgkmcnt(7)\n\t"
        "v_smfmac_f32_16x16x64_bf16 v[4:7], v[12:15], v[160:167], v1\n\t"
        "v_smfmac_f32_16x16x64_bf16 v[8:11], v[12:15], v[168:175], v1\n\t"
        "ds_read_b128 v[12:15], v53 offset:8192\n\t"
        "s_waitcnt lgkmcnt(5)\n\t"
        "v_smfmac_f32_16x16x64_bf16 v[4:7], v[16:19], v[176:183], v1\n\t"
        "v_smfmac_f32_16x16x64_bf16 v[8:11], v[16:19], v[28:35], v1\n\t"
        "v_add_u32 v54, 114688, v2\n\t"
        "ds_read_b128 v[28:31], v54 offset:0\n\t"
        "ds_read_b128 v[32:35], v54 offset:1024\n\t"
        "ds_read_b128 v[16:19], v53 offset:9216\n\t"
        "ds_read_b96 v[52:54], v55\n\t"
        "s_waitcnt lgkmcnt(8)\n\t"
        "v_smfmac_f32_16x16x64_bf16 v[4:7], v[20:23], v[184:191], v1\n\t"
        "v_smfmac_f32_16x16x64_bf16 v[8:11], v[20:23], v[192:199], v1\n\t"
        "ds_read_b128 v[20:23], v3 offset:8192\n\t"
        "s_waitcnt lgkmcnt(6)\n\t"
        "v_smfmac_f32_16x16x64_bf16 v[4:7], v[24:27], v[200:207], v1\n\t"
        "v_smfmac_f32_16x16x64_bf16 v[8:11], v[24:27], v[36:43], v1\n\t"
        "ds_read_b128 v[24:27], v3 offset:9216\n\t"
        "s_waitcnt lgkmcnt(6)\n\t"
        "v_smfmac_f32_16x16x64_bf16 v[4:7], v[12:15], v[208:215], v1\n\t"
        "v_smfmac_f32_16x16x64_bf16 v[8:11], v[12:15], v[216:223], v1\n\t"
        "s_waitcnt lgkmcnt(3)\n\t"
        "v_smfmac_f32_16x16x64_bf16 v[4:7], v[16:19], v[224:231], v1\n\t"
        "v_mov_b64 v[12:13], 0\n\t"
        "v_smfmac_f32_16x16x64_bf16 v[8:11], v[16:19], v[44:51], v1\n\t"
        "v_mov_b64 v[14:15], 0\n\t"
        "s_waitcnt lgkmcnt(2)\n\t"
        "ds_read_b128 v[36:39], v52 offset:0\n\t"
        "ds_read_b128 v[40:43], v52 offset:2048\n\t"
        "ds_read_b128 v[16:19], v55 offset:4096\n\t"
        "ds_read_b32 v55, v55 offset:12\n\t"
        "s_waitcnt lgkmcnt(5)\n\t"
        "v_smfmac_f32_16x16x64_bf16 v[4:7], v[20:23], v[232:239], v1\n\t"
        "v_smfmac_f32_16x16x64_bf16 v[8:11], v[20:23], v[240:247], v1\n\t"
        "s_waitcnt lgkmcnt(4)\n\t"
        "v_smfmac_f32_16x16x64_bf16 v[4:7], v[24:27], v[248:255], v1\n\t"
        "v_mov_b64 v[20:21], 0\n\t"
        "v_smfmac_f32_16x16x64_bf16 v[8:11], v[24:27], v[28:35], v1\n\t"
        "v_mov_b64 v[22:23], 0\n\t"
        "s_nop 4\n\t"
        "v_add_f32 v4, v4, v5\n\t"
        "v_add_f32 v6, v6, v7\n\t"
        "v_mul_f32 v4, s66, v4\n\t"
        "v_add_f32 v8, v8, v9\n\t"
        "v_add_f32 v10, v10, v11\n\t"
        "v_mul_f32 v6, s66, v6\n\t"
        "v_mul_f32 v8, s66, v8\n\t"
        "v_mul_f32 v10, s66, v10\n\t"
        "v_cvt_pk_bf16_f32 v4, v4, v4\n\t"
        "v_cvt_pk_bf16_f32 v5, v6, v6\n\t"
        "v_cvt_pk_bf16_f32 v8, v8, v8\n\t"
        "v_cvt_pk_bf16_f32 v9, v10, v10\n\t"
        "s_waitcnt lgkmcnt(0)\n\t"
        "ds_write_b64 v55, v[4:5]\n\t"
        "ds_write_b64 v55, v[8:9] offset:64\n\t"
        "s_waitcnt lgkmcnt(0)\n\t"
        "s_barrier\n\t"
        ".Ls_xs_%=:\n\t"
        "ds_read_b64_tr_b16 v[28:29], v53 offset:0\n\t"
        "ds_read_b64_tr_b16 v[30:31], v53 offset:128\n\t"
        "ds_read_b64_tr_b16 v[32:33], v53 offset:1536\n\t"
        "ds_read_b64_tr_b16 v[34:35], v53 offset:1664\n\t"
        "ds_read_b64_tr_b16 v[44:45], v53 offset:3072\n\t"
        "ds_read_b64_tr_b16 v[46:47], v53 offset:3200\n\t"
        "ds_read_b64_tr_b16 v[48:49], v53 offset:4608\n\t"
        "ds_read_b64_tr_b16 v[50:51], v53 offset:4736\n\t"
        "s_waitcnt lgkmcnt(4)\n\t"
        "v_smfmac_f32_16x16x64_bf16 v[12:15], v[36:39], v[28:35], v1\n\t"
        "s_waitcnt lgkmcnt(0)\n\t"
        "v_smfmac_f32_16x16x64_bf16 v[12:15], v[40:43], v[44:51], v1\n\t"
        "s_nop 7\n\t"
        "v_cndmask_b32_e64 v8, v14, v12, s[56:57]\n\t"
        "v_cndmask_b32_e64 v9, v15, v13, s[56:57]\n\t"
        "v_add_f32 v8, v8, v9\n\t"
        "v_exp_f32 v9, v8\n\t"
        "v_cmp_u_f32 vcc, v8, v8\n\t"
        "s_cmp_eq_u32 %[w], 0\n\t"
        ".Ls_xk_%=:\n\t"
        "ds_write2st64_b32 v54, v9, v8 offset0:0 offset1:4\n\t"
        "s_cbranch_scc0 .Ls_qoth_%=\n\t"
        "buffer_load_dword v36, v18, %[rs], s48 offen sc1\n\t"
        "s_setprio 3\n\t"
        "s_waitcnt lgkmcnt(0)\n\t"
        "s_barrier\n\t"
        "ds_read_b64_tr_b16 v[4:5], v53 offset:6144\n\t"
        "ds_read_b64_tr_b16 v[6:7], v53 offset:6272\n\t"
        "ds_read_b64_tr_b16 v[8:9], v53 offset:7680\n\t"
        "ds_read_b64_tr_b16 v[10:11], v53 offset:7808\n\t"
        "ds_read_b128 v[24:27], v54\n\t"
        "s_waitcnt lgkmcnt(3)\n\t"
        "v_smfmac_f32_16x16x64_bf16 v[20:23], v[4:7], v[28:35], v1\n\t"
        "s_waitcnt lgkmcnt(1)\n\t"
        "v_smfmac_f32_16x16x64_bf16 v[20:23], v[8:11], v[44:51], v1\n\t"
        "s_waitcnt lgkmcnt(0)\n\t"
        "v_add_f32 v8, v24, v25\n\t"
        "v_add_f32 v9, v26, v27\n\t"
        "v_add_f32 v8, v8, v9\n\t"
        "s_nop 1\n\t"
        "v_add_f32_dpp v8, v8, v8 quad_perm:[1,0,3,2] row_mask:0xf bank_mask:0xf\n\t"
        "s_nop 0\n\t"
        "v_add_f32 v4, v20, v21\n\t"
        "v_add_f32 v6, v22, v23\n\t"
        "v_exp_f32 v5, v4\n\t"
        "v_exp_f32 v7, v6\n\t"
        ".Ls_psum_%=:\n\t"
        "v_add_f32 v8, v8, v5\n\t"
        "v_add_f32 v9, v8, v7\n\t"
        "v_add_f32 v10, v8, v7\n\t"
        "s_nop 1\n\t"
        "v_permlane16_swap_b32 v9, v10\n\t"
        "v_add_f32 v8, v9, v10\n\t"
        "v_add_f32 v11, v9, v10\n\t"
        "s_nop 1\n\t"
        "v_permlane32_swap_b32 v8, v11\n\t"
        "v_add_f32 v8, v8, v11\n\t"
        "v_rcp_f32 v9, v8\n\t"
        "v_subrev_u32 v10, 0x0d800000, v8\n\t"
        "v_cmp_lt_u32 vcc, 0x64000000, v10\n\t"
        "v_mul_f32 v24, v24, v9\n\t"
        "v_mul_f32 v25, v25, v9\n\t"
        "v_mul_f32 v26, v26, v9\n\t"
        "v_mul_f32 v27, v27, v9\n\t"
        "s_cbranch_vccnz .Ls_pmax_%=\n\t"
        ".Ls_pok_%=:\n\t"
        "v_cvt_pk_bf16_f32 v24, v24, v25\n\t"
        "v_cvt_pk_bf16_f32 v25, v26, v27\n\t"
        "v_mul_f32 v5, v5, v9\n\t"
        "buffer_store_dwordx2 v[24:25], v16, %[rs], s72 offen sc1\n\t"
        "v_mul_f32 v7, v7, v9\n\t"
        "buffer_store_dwordx2 v[24:25], v16, %[rs], s73 offen sc1\n\t"
        "v_cvt_pk_bf16_f32 v5, v5, v7\n\t"
        "buffer_store_dword v5, v17, %[rs], s67 offen sc1\n\t"
        "s_sub_u32 s68, s92, s96\n\t"
        "s_sub_u32 s69, s94, s97\n\t"
        "s_mov_b32 s96, s92\n\t"
        "s_mov_b32 s97, s94\n\t"
        "v_cvt_f32_u32 v39, s68\n\t"
        "v_cvt_f32_u32 v38, s69\n\t"
        "v_rcp_f32 v39, v39\n\t"
        "s_nop 0\n\t"
        "v_mul_f32 v39, v39, v38\n\t"
        "v_max_f32 v39, 0x3d2aaaab, v39\n\t"
        "v_min_f32 v39, 0x3d638e39, v39\n\t"
        "v_subrev_f32 v39, s98, v39\n\t"
        "v_mov_b32 v38, s98\n\t"
        "v_fmac_f32 v38, 0x3f000000, v39\n\t"
        "v_mul_f32 v39, 0x44898000, v38\n\t"
        "v_cvt_u32_f32 v39, v39\n\t"
        "v_readfirstlane_b32 s98, v38\n\t"
        ".Ls_twm_%=:\n\t"
        "s_waitcnt vmcnt(3)\n\t"
        "v_cmp_eq_u32 s[68:69], 0, v36\n\t"
        "v_max_u32_dpp v36, v36, v36 row_shr:1 row_mask:0xf bank_mask:0xf\n\t"
        "s_nop 1\n\t"
        "v_max_u32_dpp v36, v36, v36 row_shr:2 row_mask:0xf bank_mask:0xf\n\t"
        "s_nop 1\n\t"
        "v_max_u32_dpp v36, v36, v36 row_shr:4 row_mask:0xf bank_mask:0xf\n\t"
        "s_nop 1\n\t"
        "v_max_u32_dpp v36, v36, v36 row_shr:8 row_mask:0xf bank_mask:0xf\n\t"
        "s_nop 1\n\t"
        "v_max_u32_dpp v36, v36, v36 row_bcast:15 row_mask:0xf bank_mask:0xf\n\t"
        "s_nop 1\n\t"
        "v_readlane_b32 s70, v36, 63\n\t"
        "s_cmp_lg_u64 s[68:69], 0\n\t"
        "v_readfirstlane_b32 s68, v39\n\t"
        "s_cbranch_scc1 .Ls_twx_%=\n\t"
        ".Ls_twy_%=:\n\t"
        "s_add_i32 s70, s70, 26\n\t"
        "s_add_u32 s48, s70, s68\n\t"
        "s_mov_b32 s99, s48\n\t"
        "v_add_u32 v54, 65536, v2\n\t"
        "ds_read_b128 v[28:31], v54 offset:0\n\t"
        "ds_read_b128 v[32:35], v54 offset:1024\n\t"
        "ds_read_b128 v[36:39], v54 offset:16384\n\t"
        "ds_read_b128 v[40:43], v54 offset:17408\n\t"
        "ds_read_b128 v[44:47], v54 offset:32768\n\t"
        "ds_read_b128 v[48:51], v54 offset:33792\n\t"
        "v_xor_b32 v52, 0x800, v2\n\t"
        "v_xor_b32 v53, 0x1000, v2\n\t"
        "v_xor_b32 v3, 0x1800, v2\n\t"
        "s_add_u32 s45, s45, 1\n\t"
        "s_add_u32 s47, s47, 2097152\n\t"
        "s_and_b32 s47, s47, 0xffffff\n\t"
        "s_xor_b32 s47, s47, 0x2000\n\t"
        "s_add_u32 s50, s50, 2097152\n\t"
        "s_and_b32 s50, s50, 0xffffff\n\t"
        "s_xor_b32 s50, s50, 0x2000\n\t"
        "s_add_u32 s49, s49, 0x80000\n\t"
        "s_add_u32 s70, s45, 0\n\t"
        "s_and_b32 s70, s70, 3\n\t"
        "s_cselect_b32 s70, 0, 0x2000\n\t"
        "s_xor_b32 s49, s49, s70\n\t"
        "s_and_b32 s70, s45, 1\n\t"
        "s_lshl_b32 s70, s70, 13\n\t"
        "s_add_u32 s71, s70, %[kx]\n\t"
        "v_bfe_u32 v12, v0, 4, 1\n\t"
        "v_bfe_u32 v13, v0, 8, 1\n\t"
        "v_lshrrev_b32 v14, 9, v0\n\t"
        "v_and_b32 v15, 0xe0, v0\n\t"
        "v_and_b32 v16, 0xf0, v0\n\t"
        "v_bfe_u32 v17, v0, 6, 2\n\t"
        "v_lshrrev_b32 v18, 8, v0\n\t"
        "v_lshrrev_b32 v4, 1, v15\n\t"
        "v_lshl_add_u32 v4, v12, 9, v4\n\t"
        "v_lshl_add_u32 v4, v13, 8, v4\n\t"
        "v_lshl_add_u32 v4, v14, 10, v4\n\t"
        "v_add_u32 v4, s71, v4\n\t"
        "v_bfe_u32 v5, v0, 4, 4\n\t"
        "v_xor_b32 v5, v5, v18\n\t"
        "v_lshlrev_b32 v5, 3, v5\n\t"
        "v_mul_u32_u24 v6, 384, v18\n\t"
        "v_add3_u32 v5, v5, v6, s52\n\t"
        "s_lshl_b32 s71, %[w], 2\n\t"
        "s_add_u32 s71, s71, 156672\n\t"
        "v_add_u32 v6, s71, v0\n\t"
        "v_lshl_add_u32 v7, v12, 2, v18\n\t"
        "v_xor_b32 v7, v7, v17\n\t"
        "v_lshlrev_b32 v7, 3, v7\n\t"
        "v_lshl_add_u32 v7, v15, 2, v7\n\t"
        "v_lshl_add_u32 v7, v17, 7, v7\n\t"
        "s_mul_i32 s71, %[w], 1536\n\t"
        "s_add_u32 s71, s71, s52\n\t"
        "v_add_u32 v7, s71, v7\n\t"
        "v_lshl_add_u32 v8, v13, 3, v16\n\t"
        "v_lshl_add_u32 v8, v14, 8, v8\n\t"
        "v_lshl_add_u32 v9, v13, 4, v15\n\t"
        "v_lshl_add_u32 v9, v14, 2, v9\n\t"
        "v_lshlrev_b32 v10, 13, v12\n\t"
        "s_and_b32 s71, s50, 0x2000\n\t"
        "v_xor_b32 v10, s71, v10\n\t"
        "v_add_u32 v9, v9, v10\n\t"
        "s_lshl_b32 s71, %[w], 10\n\t"
        "s_add_u32 s71, s71, 158720\n\t"
        "v_add_u32 v55, s71, v0\n\t"
        "v_add_u32 v14, 162816, v0\n\t"
        "v_and_b32 v10, 0x1f0, v0\n\t"
        "v_lshlrev_b32 v10, 1, v10\n\t"
        "ds_write_b128 v55, v[4:7]\n\t"
        "ds_write_b128 v14, v[8:11]\n\t"
        "s_xor_b32 s48, s50, %[tw]\n\t"
        "v_mov_b32 v12, 0xffbfffbf\n\t"
        "v_mov_b32 v13, v12\n\t"
        "ds_write_b64 v7, v[12:13]\n\t"
        "ds_write_b64 v7, v[12:13] offset:64\n\t"
        "s_add_u32 s72, s50, s74\n\t"
        "s_xor_b32 s73, s72, 0x2000\n\t"
        "s_andn2_b32 s67, s50, 0x2000\n\t"
        "s_add_u32 s67, s67, s75\n\t"
        "s_nop 0\n\t"
        "v_mov_b64 v[4:5], 0\n\t"
        "v_mov_b64 v[6:7], 0\n\t"
        "v_mov_b64 v[8:9], 0\n\t"
        "v_mov_b64 v[10:11], 0\n\t"
        "s_waitcnt lgkmcnt(0)\n\t"
        "s_setprio 2\n\t"
        ".Ls_twa_%=:\n\t"
        "s_memtime s[92:93]\n\t"
        "s_memrealtime s[94:95]\n\t"
        "s_waitcnt lgkmcnt(0)\n\t"
        "s_sub_i32 s68, s99, s94\n\t"
        "s_sub_u32 s69, s68, 5\n\t"
        "s_cmp_lt_u32 s69, 400\n\t"
        "s_cbranch_scc1 .Ls_twa_%=\n\t"
        "s_cmp_lt_i32 s68, 1\n\t"
        "s_cbranch_scc1 .Ls_twb_%=\n\t"
        "s_nop 2\n\t"
        "s_cmp_lt_i32 s68, 2\n\t"
        "s_cbranch_scc1 .Ls_twb_%=\n\t"
        "s_nop 2\n\t"
        "s_cmp_lt_i32 s68, 3\n\t"
        "s_cbranch_scc1 .Ls_twb_%=\n\t"
        "s_nop 2\n\t"
        "s_cmp_lt_i32 s68, 4\n\t"
        "s_cbranch_scc1 .Ls_twb_%=\n\t"
        "s_nop 2\n\t"
        "s_branch .Ls_twb_%=\n\t"
        ".Ls_twb_%=:\n\t"
        "s_cmp_lt_u32 s45, 64\n\t"
        "s_cbranch_scc1 .Ls_qloop_%=\n\t"
        "s_branch .Ls_end_%=\n\t"
        ".Ls_qoth_%=:\n\t"
        "s_cmp_eq_u32 %[w], 3\n\t"
        "s_cbranch_scc1 .Ls_qarm_%=\n\t"
        "buffer_load_dwordx4 v[28:31], v0, %[rs], s49 offen offset:0 sc1\n\t"
        "buffer_load_dwordx4 v[32:35], v0, %[rs], s49 offen offset:1024 sc1\n\t"
        "buffer_load_dwordx4 v[36:39], v0, %[rs], s49 offen offset:2048 sc1\n\t"
        "buffer_load_dwordx4 v[40:43], v0, %[rs], s49 offen offset:3072 sc1\n\t"
        "s_sub_u32 s73, s45, %[w]\n\t"
        "s_add_u32 s71, s73, 4\n\t"
        "s_min_u32 s71, s71, 64\n\t"
        "s_add_u32 s70, s71, 3\n\t"
        "s_andn2_b32 s70, s70, 3\n\t"
        "s_cmp_le_u32 s71, 16\n\t"
        "s_cselect_b32 s70, s71, s70\n\t"
        "s_lshl_b32 s70, s70, 7\n\t"
        "v_mov_b32 v16, s70\n\t"
        "s_cmp_lt_u32 s51, s71\n\t"
        "s_cselect_b32 s70, 0, 0x70000000\n\t"
        "buffer_load_dwordx2 v[16:17], v16, %[rf], s70 offen sc1\n\t"
        ".Ls_qoth2_%=:\n\t"
        "s_setprio 0\n\t"
        "s_waitcnt lgkmcnt(0)\n\t"
        "s_barrier\n\t"
        "v_bfe_u32 v18, v0, 8, 1\n\t"
        "v_lshrrev_b32 v19, 9, v0\n\t"
        "v_lshlrev_b32 v19, 8, v19\n\t"
        "v_lshl_add_u32 v19, v18, 3, v19\n\t"
        "v_bfe_u32 v18, v0, 4, 1\n\t"
        "v_lshl_add_u32 v19, v18, 7, v19\n\t"
        "v_bfe_u32 v18, v0, 6, 2\n\t"
        "v_lshl_add_u32 v19, v18, 5, v19\n\t"
        "v_bfe_u32 v18, v0, 5, 1\n\t"
        "v_lshl_add_u32 v19, v18, 4, v19\n\t"
        "s_and_b32 s70, s45, 1\n\t"
        "s_lshl_b32 s70, s70, 13\n\t"
        "s_add_u32 s70, s70, 131072\n\t"
        "v_add_u32 v19, s70, v19\n\t"
        "s_cmp_eq_u32 %[w], 1\n\t"
        "s_cbranch_scc0 .Ls_qx1_%=\n\t"
        "ds_read_b64_tr_b16 v[4:5], v53 offset:6144\n\t"
        "ds_read_b64_tr_b16 v[6:7], v53 offset:6272\n\t"
        "ds_read_b64_tr_b16 v[8:9], v53 offset:7680\n\t"
        "ds_read_b64_tr_b16 v[10:11], v53 offset:7808\n\t"
        "ds_read2_b32 v[44:45], v19 offset0:0 offset1:128\n\t"
        "ds_read2_b32 v[46:47], v19 offset0:1 offset1:129\n\t"
        "v_add_u32 v19, 1024, v19\n\t"
        "ds_read2_b32 v[48:49], v19 offset0:0 offset1:128\n\t"
        "ds_read2_b32 v[50:51], v19 offset0:1 offset1:129\n\t"
        "v_add_u32 v19, 1024, v19\n\t"
        "ds_read2_b32 v[24:25], v19 offset0:0 offset1:128\n\t"
        "ds_read2_b32 v[26:27], v19 offset0:1 offset1:129\n\t"
        "v_add_u32 v19, 1024, v19\n\t"
        "ds_read2_b32 v[52:53], v19 offset0:0 offset1:128\n\t"
        "ds_read2_b32 v[54:55], v19 offset0:1 offset1:129\n\t"
        "v_add_u32 v19, 1024, v19\n\t"
        "s_waitcnt vmcnt(0)\n\t"
        "v_readfirstlane_b32 s68, v16\n\t"
        "v_readfirstlane_b32 s69, v17\n\t"
        "s_xor_b32 s68, s68, s54\n\t"
        "s_xor_b32 s69, s69, s55\n\t"
        "s_lshr_b32 s70, s68, 8\n\t"
        "s_or_b32 s70, s70, s69\n\t"
        "s_cmp_eq_u32 s70, 0\n\t"
        "s_cselect_b32 s68, s68, 0\n\t"
        "s_and_b32 s69, s68, 127\n\t"
        "s_max_u32 s51, s51, s69\n\t"
        "s_waitcnt lgkmcnt(10)\n\t"
        "v_smfmac_f32_16x16x64_bf16 v[20:23], v[4:7], v[28:35], v1\n\t"
        "s_waitcnt lgkmcnt(8)\n\t"
        "v_smfmac_f32_16x16x64_bf16 v[20:23], v[8:11], v[36:43], v1\n\t"
        "s_waitcnt lgkmcnt(6)\n\t"
        "v_mfma_f32_16x16x32_bf16 v[12:15], v[44:47], v[28:31], 0\n\t"
        "s_waitcnt lgkmcnt(4)\n\t"
        "v_mfma_f32_16x16x32_bf16 v[12:15], v[48:51], v[32:35], v[12:15]\n\t"
        "s_waitcnt lgkmcnt(2)\n\t"
        "v_mfma_f32_16x16x32_bf16 v[12:15], v[24:27], v[36:39], v[12:15]\n\t"
        "s_waitcnt lgkmcnt(0)\n\t"
        "v_mfma_f32_16x16x32_bf16 v[12:15], v[52:55], v[40:43], v[12:15]\n\t"
        "v_and_b32 v16, 0xf0, v0\n\t"
        "v_lshrrev_b32 v17, 8, v0\n\t"
        "v_lshl_add_u32 v16, v17, 2, v16\n\t"
        "s_nop 0\n\t"
        "v_add_f32 v20, v20, v21\n\t"
        "v_add_f32 v21, v22, v23\n\t"
        "v_mov_b32 v22, 0xff7fffff\n\t"
        "s_nop 1\n\t"
        "v_cndmask_b32_e64 v12, v22, v12, s[58:59]\n\t"
        "v_cndmask_b32_e64 v13, v22, v13, s[60:61]\n\t"
        "v_cndmask_b32_e64 v14, v22, v14, s[62:63]\n\t"
        "v_cndmask_b32_e64 v15, v22, v15, s[64:65]\n\t"
        "v_max3_f32 v8, v20, v21, v12\n\t"
        "v_max3_f32 v8, v8, v13, v14\n\t"
        "v_max_f32 v8, v8, v15\n\t"
        "v_mov_b32 v9, v8\n\t"
        "s_nop 1\n\t"
        "v_permlane16_swap_b32 v8, v9\n\t"
        "v_max_f32 v10, v8, v9\n\t"
        "v_max_f32 v11, v8, v9\n\t"
        "s_nop 1\n\t"
        "v_permlane32_swap_b32 v10, v11\n\t"
        "v_max_f32 v8, v10, v11\n\t"
        "v_mul_f32 v8, s66, v8\n\t"
        "v_fma_f32 v20, v20, s66, -v8\n\t"
        "v_fma_f32 v21, v21, s66, -v8\n\t"
        "v_fma_f32 v12, v12, s66, -v8\n\t"
        "v_fma_f32 v13, v13, s66, -v8\n\t"
        "v_fma_f32 v14, v14, s66, -v8\n\t"
        "v_fma_f32 v15, v15, s66, -v8\n\t"
        "v_exp_f32 v20, v20\n\t"
        "v_exp_f32 v21, v21\n\t"
        "v_exp_f32 v12, v12\n\t"
        "v_exp_f32 v13, v13\n\t"
        "v_exp_f32 v14, v14\n\t"
        "v_exp_f32 v15, v15\n\t"
        "v_add_f32 v8, v20, v21\n\t"
        "v_add_f32 v9, v12, v13\n\t"
        "v_add_f32 v8, v8, v14\n\t"
        "v_add_f32 v9, v9, v15\n\t"
        "v_add_f32 v10, v8, v9\n\t"
        "v_add_f32 v11, v8, v9\n\t"
        "s_nop 1\n\t"
        "v_permlane16_swap_b32 v10, v11\n\t"
        "v_add_f32 v8, v10, v11\n\t"
        "v_add_f32 v9, v10, v11\n\t"
        "s_nop 1\n\t"
        "v_permlane32_swap_b32 v8, v9\n\t"
        "v_add_f32 v8, v8, v9\n\t"
        "v_rcp_f32 v8, v8\n\t"
        "s_add_u32 s70, s50, s77\n\t"
        "s_add_u32 s71, s50, s78\n\t"
        "v_mul_f32 v20, v20, v8\n\t"
        "v_mul_f32 v21, v21, v8\n\t"
        "v_mul_f32 v12, v12, v8\n\t"
        "v_mul_f32 v13, v13, v8\n\t"
        "v_mul_f32 v14, v14, v8\n\t"
        "v_mul_f32 v15, v15, v8\n\t"
        "v_cvt_pk_bf16_f32 v20, v20, v21\n\t"
        "v_cvt_pk_bf16_f32 v24, v12, v13\n\t"
        "v_cvt_pk_bf16_f32 v25, v14, v15\n\t"
        "buffer_store_dwordx2 v[24:25], v0, %[rs], s70 offen sc1\n\t"
        "buffer_store_dword v20, v16, %[rs], s71 offen offset:0 sc1\n\t"
        "s_xor_b32 s70, s70, 0x2000\n\t"
        "s_xor_b32 s71, s71, 0x2000\n\t"
        "buffer_store_dwordx2 v[24:25], v0, %[rs], s70 offen sc1\n\t"
        "buffer_store_dword v20, v16, %[rs], s71 offen offset:0 sc1\n\t"
        "s_add_u32 s71, s45, 3\n\t"
        "s_min_u32 s71, s71, 64\n\t"
        "s_cmp_lt_u32 s45, 3\n\t"
        "s_cselect_b32 s71, 0, s71\n\t"
        "s_cmp_ge_u32 s51, s71\n\t"
        "s_cbranch_scc0 .Ls_fq0p_%=\n\t"
        ".Ls_fq0k_%=:\n\t"
        "s_branch .Ls_qend_%=\n\t"
        ".Ls_qx1_%=:\n\t"
        "ds_read_b64_tr_b16 v[4:5], v53 offset:6144\n\t"
        "ds_read_b64_tr_b16 v[6:7], v53 offset:6272\n\t"
        "ds_read_b64_tr_b16 v[8:9], v53 offset:7680\n\t"
        "ds_read_b64_tr_b16 v[10:11], v53 offset:7808\n\t"
        "ds_read2_b32 v[44:45], v19 offset0:0 offset1:128\n\t"
        "ds_read2_b32 v[46:47], v19 offset0:1 offset1:129\n\t"
        "v_add_u32 v19, 1024, v19\n\t"
        "ds_read2_b32 v[48:49], v19 offset0:0 offset1:128\n\t"
        "ds_read2_b32 v[50:51], v19 offset0:1 offset1:129\n\t"
        "v_add_u32 v19, 1024, v19\n\t"
        "ds_read2_b32 v[24:25], v19 offset0:0 offset1:128\n\t"
        "ds_read2_b32 v[26:27], v19 offset0:1 offset1:129\n\t"
        "v_add_u32 v19, 1024, v19\n\t"
        "ds_read2_b32 v[52:53], v19 offset0:0 offset1:128\n\t"
        "ds_read2_b32 v[54:55], v19 offset0:1 offset1:129\n\t"
        "v_add_u32 v19, 1024, v19\n\t"
        "s_waitcnt vmcnt(0)\n\t"
        "v_readfirstlane_b32 s68, v16\n\t"
        "v_readfirstlane_b32 s69, v17\n\t"
        "s_xor_b32 s68, s68, s54\n\t"
        "s_xor_b32 s69, s69, s55\n\t"
        "s_lshr_b32 s70, s68, 8\n\t"
        "s_or_b32 s70, s70, s69\n\t"
        "s_cmp_eq_u32 s70, 0\n\t"
        "s_cselect_b32 s68, s68, 0\n\t"
        "s_and_b32 s69, s68, 127\n\t"
        "s_max_u32 s51, s51, s69\n\t"
        "s_waitcnt lgkmcnt(10)\n\t"
        "v_smfmac_f32_16x16x64_bf16 v[20:23], v[4:7], v[28:35], v1\n\t"
        "s_waitcnt lgkmcnt(8)\n\t"
        "v_smfmac_f32_16x16x64_bf16 v[20:23], v[8:11], v[36:43], v1\n\t"
        "s_waitcnt lgkmcnt(6)\n\t"
        "v_mfma_f32_16x16x32_bf16 v[12:15], v[44:47], v[28:31], 0\n\t"
        "ds_read2_b32 v[44:45], v19 offset0:0 offset1:128\n\t"
        "ds_read2_b32 v[46:47], v19 offset0:1 offset1:129\n\t"
        "v_add_u32 v19, 1024, v19\n\t"
        "s_waitcnt lgkmcnt(6)\n\t"
        "v_mfma_f32_16x16x32_bf16 v[12:15], v[48:51], v[32:35], v[12:15]\n\t"
        "ds_read2_b32 v[48:49], v19 offset0:0 offset1:128\n\t"
        "ds_read2_b32 v[50:51], v19 offset0:1 offset1:129\n\t"
        "v_add_u32 v19, 1024, v19\n\t"
        "s_waitcnt lgkmcnt(6)\n\t"
        "v_mfma_f32_16x16x32_bf16 v[12:15], v[24:27], v[36:39], v[12:15]\n\t"
        "ds_read2_b32 v[24:25], v19 offset0:0 offset1:128\n\t"
        "ds_read2_b32 v[26:27], v19 offset0:1 offset1:129\n\t"
        "v_add_u32 v19, 1024, v19\n\t"
        "s_waitcnt lgkmcnt(6)\n\t"
        "v_mfma_f32_16x16x32_bf16 v[12:15], v[52:55], v[40:43], v[12:15]\n\t"
        "ds_read2_b32 v[52:53], v19 offset0:0 offset1:128\n\t"
        "ds_read2_b32 v[54:55], v19 offset0:1 offset1:129\n\t"
        "v_add_u32 v19, 1024, v19\n\t"
        "s_waitcnt lgkmcnt(6)\n\t"
        "v_mfma_f32_16x16x32_bf16 v[4:7], v[44:47], v[28:31], 0\n\t"
        "s_waitcnt lgkmcnt(4)\n\t"
        "v_mfma_f32_16x16x32_bf16 v[4:7], v[48:51], v[32:35], v[4:7]\n\t"
        "s_waitcnt lgkmcnt(2)\n\t"
        "v_mfma_f32_16x16x32_bf16 v[4:7], v[24:27], v[36:39], v[4:7]\n\t"
        "s_waitcnt lgkmcnt(0)\n\t"
        "v_mfma_f32_16x16x32_bf16 v[4:7], v[52:55], v[40:43], v[4:7]\n\t"
        "v_and_b32 v16, 0xf0, v0\n\t"
        "v_lshrrev_b32 v17, 8, v0\n\t"
        "v_lshl_add_u32 v16, v17, 2, v16\n\t"
        "s_nop 0\n\t"
        "v_add_f32 v20, v20, v21\n\t"
        "v_add_f32 v21, v22, v23\n\t"
        "v_mov_b32 v22, 0xff7fffff\n\t"
        "s_nop 1\n\t"
        "v_cndmask_b32_e64 v4, v22, v4, s[58:59]\n\t"
        "v_cndmask_b32_e64 v5, v22, v5, s[60:61]\n\t"
        "v_cndmask_b32_e64 v6, v22, v6, s[62:63]\n\t"
        "v_cndmask_b32_e64 v7, v22, v7, s[64:65]\n\t"
        "v_max3_f32 v8, v20, v21, v12\n\t"
        "v_max3_f32 v8, v8, v13, v14\n\t"
        "v_max_f32 v8, v8, v15\n\t"
        "v_max3_f32 v8, v8, v4, v5\n\t"
        "v_max3_f32 v8, v8, v6, v7\n\t"
        "v_mov_b32 v9, v8\n\t"
        "s_nop 1\n\t"
        "v_permlane16_swap_b32 v8, v9\n\t"
        "v_max_f32 v10, v8, v9\n\t"
        "v_max_f32 v11, v8, v9\n\t"
        "s_nop 1\n\t"
        "v_permlane32_swap_b32 v10, v11\n\t"
        "v_max_f32 v8, v10, v11\n\t"
        "v_mul_f32 v8, s66, v8\n\t"
        "v_fma_f32 v20, v20, s66, -v8\n\t"
        "v_fma_f32 v21, v21, s66, -v8\n\t"
        "v_fma_f32 v12, v12, s66, -v8\n\t"
        "v_fma_f32 v13, v13, s66, -v8\n\t"
        "v_fma_f32 v14, v14, s66, -v8\n\t"
        "v_fma_f32 v15, v15, s66, -v8\n\t"
        "v_fma_f32 v4, v4, s66, -v8\n\t"
        "v_fma_f32 v5, v5, s66, -v8\n\t"
        "v_fma_f32 v6, v6, s66, -v8\n\t"
        "v_fma_f32 v7, v7, s66, -v8\n\t"
        "v_exp_f32 v20, v20\n\t"
        "v_exp_f32 v21, v21\n\t"
        "v_exp_f32 v12, v12\n\t"
        "v_exp_f32 v13, v13\n\t"
        "v_exp_f32 v14, v14\n\t"
        "v_exp_f32 v15, v15\n\t"
        "v_exp_f32 v4, v4\n\t"
        "v_exp_f32 v5, v5\n\t"
        "v_exp_f32 v6, v6\n\t"
        "v_exp_f32 v7, v7\n\t"
        "v_add_f32 v8, v20, v21\n\t"
        "v_add_f32 v9, v12, v13\n\t"
        "v_add_f32 v8, v8, v14\n\t"
        "v_add_f32 v9, v9, v15\n\t"
        "v_add_f32 v8, v8, v4\n\t"
        "v_add_f32 v9, v9, v5\n\t"
        "v_add_f32 v8, v8, v6\n\t"
        "v_add_f32 v9, v9, v7\n\t"
        "v_add_f32 v10, v8, v9\n\t"
        "v_add_f32 v11, v8, v9\n\t"
        "s_nop 1\n\t"
        "v_permlane16_swap_b32 v10, v11\n\t"
        "v_add_f32 v8, v10, v11\n\t"
        "v_add_f32 v9, v10, v11\n\t"
        "s_nop 1\n\t"
        "v_permlane32_swap_b32 v8, v9\n\t"
        "v_add_f32 v8, v8, v9\n\t"
        "v_rcp_f32 v8, v8\n\t"
        "s_add_u32 s70, s50, s76\n\t"
        "s_add_u32 s71, s50, s78\n\t"
        "v_mul_f32 v20, v20, v8\n\t"
        "v_mul_f32 v21, v21, v8\n\t"
        "v_mul_f32 v12, v12, v8\n\t"
        "v_mul_f32 v13, v13, v8\n\t"
        "v_mul_f32 v14, v14, v8\n\t"
        "v_mul_f32 v15, v15, v8\n\t"
        "v_mul_f32 v4, v4, v8\n\t"
        "v_mul_f32 v5, v5, v8\n\t"
        "v_mul_f32 v6, v6, v8\n\t"
        "v_mul_f32 v7, v7, v8\n\t"
        "v_cvt_pk_bf16_f32 v20, v20, v21\n\t"
        "v_cvt_pk_bf16_f32 v24, v12, v13\n\t"
        "v_cvt_pk_bf16_f32 v25, v14, v15\n\t"
        "v_cvt_pk_bf16_f32 v26, v4, v5\n\t"
        "v_cvt_pk_bf16_f32 v27, v6, v7\n\t"
        "buffer_store_dwordx4 v[24:27], v0, %[rs], s70 offen sc1\n\t"
        "buffer_store_dword v20, v16, %[rs], s71 offen offset:2048 sc1\n\t"
        "s_xor_b32 s70, s70, 0x2000\n\t"
        "s_xor_b32 s71, s71, 0x2000\n\t"
        "buffer_store_dwordx4 v[24:27], v0, %[rs], s70 offen sc1\n\t"
        "buffer_store_dword v20, v16, %[rs], s71 offen offset:2048 sc1\n\t"
        "s_add_u32 s71, s45, 2\n\t"
        "s_min_u32 s71, s71, 64\n\t"
        "s_cmp_ge_u32 s51, s71\n\t"
        "s_cbranch_scc0 .Ls_fq1p_%=\n\t"
        ".Ls_fq1k_%=:\n\t"
        "s_branch .Ls_qend_%=\n\t"
        ".Ls_qlast_%=:\n\t"
        "ds_read_b96 v[52:54], v55\n\t"
        "s_waitcnt lgkmcnt(0)\n\t"
        "v_mov_b64 v[20:21], 0\n\t"
        "v_mov_b64 v[22:23], 0\n\t"
        "s_sub_u32 s70, %[w], 1\n\t"
        "s_cmp_lt_u32 s70, 2\n\t"
        "s_cbranch_scc0 .Ls_qlast2_%=\n\t"
        "buffer_load_dwordx4 v[28:31], v0, %[rs], s49 offen offset:0 sc1\n\t"
        "buffer_load_dwordx4 v[32:35], v0, %[rs], s49 offen offset:1024 sc1\n\t"
        "buffer_load_dwordx4 v[36:39], v0, %[rs], s49 offen offset:2048 sc1\n\t"
        "buffer_load_dwordx4 v[40:43], v0, %[rs], s49 offen offset:3072 sc1\n\t"
        "s_barrier\n\t"
        "s_branch .Ls_qoth2_%=\n\t"
        ".Ls_qlast2_%=:\n\t"
        "s_barrier\n\t"
        "s_barrier\n\t"
        "s_branch .Ls_end_%=\n\t"
        ".Ls_qarm_%=:\n\t"
        "s_setprio 0\n\t"
        "s_waitcnt lgkmcnt(0)\n\t"
        "s_barrier\n\t"
        "s_nop 15\n\t"
        "s_nop 15\n\t"
        "s_nop 15\n\t"
        "s_nop 15\n\t"
        "s_nop 15\n\t"
        "s_nop 15\n\t"
        "s_nop 3\n\t"
        "s_add_u32 s70, s50, 4194304\n\t"
        "s_and_b32 s70, s70, 0xffffff\n\t"
        "s_cmp_lt_u32 s45, 62\n\t"
        "s_cselect_b32 s70, s70, 0x7f000000\n\t"
        "v_mov_b32 v24, 0xffbfffbf\n\t"
        "v_mov_b32 v25, v24\n\t"
        "v_mov_b32 v26, v24\n\t"
        "v_mov_b32 v27, v24\n\t"
        "s_bitcmp0_b32 %[h], 0\n\t"
        "s_cselect_b32 s71, s70, 0x7f000000\n\t"
        "v_add_u32 v20, s74, v0\n\t"
        "v_and_b32 v21, 0xf0, v0\n\t"
        "v_bfe_u32 v22, v0, 8, 1\n\t"
        "v_lshl_add_u32 v21, v22, 11, v21\n\t"
        "v_add_u32 v21, s78, v21\n\t"
        "v_cmp_gt_u32 vcc, 512, v0\n\t"
        "v_lshrrev_b32 v22, 8, v0\n\t"
        "v_lshl_add_u32 v22, v22, 29, v0\n\t"
        "v_cndmask_b32 v20, v21, v20, vcc\n\t"
        "s_add_u32 s72, s70, s76\n\t"
        "s_add_u32 s73, s71, s77\n\t"
        "buffer_store_dwordx4 v[24:27], v0, %[rs], s72 offen sc1\n\t"
        "buffer_store_dwordx4 v[24:27], v0, %[rs], s73 offen sc1\n\t"
        "s_add_u32 s72, s71, s75\n\t"
        "buffer_store_dwordx4 v[24:27], v20, %[rs], s70 offen sc1\n\t"
        "buffer_store_dwordx4 v[24:27], v22, %[rs], s72 offen sc1\n\t"
        "s_xor_b32 s70, s70, 0x2000\n\t"
        "s_xor_b32 s71, s71, 0x2000\n\t"
        "s_add_u32 s72, s70, s76\n\t"
        "s_add_u32 s73, s71, s77\n\t"
        "buffer_store_dwordx4 v[24:27], v0, %[rs], s72 offen sc1\n\t"
        "buffer_store_dwordx4 v[24:27], v0, %[rs], s73 offen sc1\n\t"
        "s_add_u32 s72, s71, s75\n\t"
        "buffer_store_dwordx4 v[24:27], v20, %[rs], s70 offen sc1\n\t"
        "buffer_store_dwordx4 v[24:27], v22, %[rs], s72 offen sc1\n\t"
        ".Ls_qend_%=:\n\t"
        "v_add_u32 v54, 65536, v2\n\t"
        "ds_read_b128 v[28:31], v54 offset:0\n\t"
        "ds_read_b128 v[32:35], v54 offset:1024\n\t"
        "ds_read_b128 v[36:39], v54 offset:16384\n\t"
        "ds_read_b128 v[40:43], v54 offset:17408\n\t"
        "ds_read_b128 v[44:47], v54 offset:32768\n\t"
        "ds_read_b128 v[48:51], v54 offset:33792\n\t"
        "v_xor_b32 v52, 0x800, v2\n\t"
        "v_xor_b32 v53, 0x1000, v2\n\t"
        "v_xor_b32 v3, 0x1800, v2\n\t"
        "s_add_u32 s45, s45, 1\n\t"
        "s_add_u32 s47, s47, 2097152\n\t"
        "s_and_b32 s47, s47, 0xffffff\n\t"
        "s_xor_b32 s47, s47, 0x2000\n\t"
        "s_add_u32 s50, s50, 2097152\n\t"
        "s_and_b32 s50, s50, 0xffffff\n\t"
        "s_xor_b32 s50, s50, 0x2000\n\t"
        "s_add_u32 s49, s49, 0x80000\n\t"
        "s_add_u32 s70, s45, 0\n\t"
        "s_and_b32 s70, s70, 3\n\t"
        "s_cselect_b32 s70, 0, 0x2000\n\t"
        "s_xor_b32 s49, s49, s70\n\t"
        "s_and_b32 s70, s45, 1\n\t"
        "s_lshl_b32 s70, s70, 13\n\t"
        "s_add_u32 s71, s70, %[kx]\n\t"
        "v_bfe_u32 v12, v0, 4, 1\n\t"
        "v_bfe_u32 v13, v0, 8, 1\n\t"
        "v_lshrrev_b32 v14, 9, v0\n\t"
        "v_and_b32 v15, 0xe0, v0\n\t"
        "v_and_b32 v16, 0xf0, v0\n\t"
        "v_bfe_u32 v17, v0, 6, 2\n\t"
        "v_lshrrev_b32 v18, 8, v0\n\t"
        "v_lshrrev_b32 v4, 1, v15\n\t"
        "v_lshl_add_u32 v4, v12, 9, v4\n\t"
        "v_lshl_add_u32 v4, v13, 8, v4\n\t"
        "v_lshl_add_u32 v4, v14, 10, v4\n\t"
        "v_add_u32 v4, s71, v4\n\t"
        "v_bfe_u32 v5, v0, 4, 4\n\t"
        "v_xor_b32 v5, v5, v18\n\t"
        "v_lshlrev_b32 v5, 3, v5\n\t"
        "v_mul_u32_u24 v6, 384, v18\n\t"
        "v_add3_u32 v5, v5, v6, s52\n\t"
        "s_lshl_b32 s71, %[w], 2\n\t"
        "s_add_u32 s71, s71, 156672\n\t"
        "v_add_u32 v6, s71, v0\n\t"
        "v_lshl_add_u32 v7, v12, 2, v18\n\t"
        "v_xor_b32 v7, v7, v17\n\t"
        "v_lshlrev_b32 v7, 3, v7\n\t"
        "v_lshl_add_u32 v7, v15, 2, v7\n\t"
        "v_lshl_add_u32 v7, v17, 7, v7\n\t"
        "s_mul_i32 s71, %[w], 1536\n\t"
        "s_add_u32 s71, s71, s52\n\t"
        "v_add_u32 v7, s71, v7\n\t"
        "v_lshl_add_u32 v8, v13, 3, v16\n\t"
        "v_lshl_add_u32 v8, v14, 8, v8\n\t"
        "v_lshl_add_u32 v9, v13, 4, v15\n\t"
        "v_lshl_add_u32 v9, v14, 2, v9\n\t"
        "v_lshlrev_b32 v10, 13, v12\n\t"
        "s_and_b32 s71, s50, 0x2000\n\t"
        "v_xor_b32 v10, s71, v10\n\t"
        "v_add_u32 v9, v9, v10\n\t"
        "s_lshl_b32 s71, %[w], 10\n\t"
        "s_add_u32 s71, s71, 158720\n\t"
        "v_add_u32 v55, s71, v0\n\t"
        "v_add_u32 v14, 162816, v0\n\t"
        "v_and_b32 v10, 0x1f0, v0\n\t"
        "v_lshlrev_b32 v10, 1, v10\n\t"
        "ds_write_b128 v55, v[4:7]\n\t"
        "ds_write_b128 v14, v[8:11]\n\t"
        "s_xor_b32 s48, s50, %[tw]\n\t"
        "v_mov_b32 v12, 0xffbfffbf\n\t"
        "v_mov_b32 v13, v12\n\t"
        "ds_write_b64 v7, v[12:13]\n\t"
        "ds_write_b64 v7, v[12:13] offset:64\n\t"
        "s_add_u32 s72, s50, s74\n\t"
        "s_xor_b32 s73, s72, 0x2000\n\t"
        "s_andn2_b32 s67, s50, 0x2000\n\t"
        "s_add_u32 s67, s67, s75\n\t"
        "s_nop 0\n\t"
        "v_mov_b64 v[4:5], 0\n\t"
        "v_mov_b64 v[6:7], 0\n\t"
        "v_mov_b64 v[8:9], 0\n\t"
        "v_mov_b64 v[10:11], 0\n\t"
        "s_waitcnt lgkmcnt(0)\n\t"
        "s_setprio 2\n\t"
        "s_cmp_lt_u32 s45, 64\n\t"
        "s_cbranch_scc1 .Ls_qloop_%=\n\t"
        "s_branch .Ls_end_%=\n\t"
        ".Ls_kpro_%=:\n\t"
        "s_mov_b64 s[56:57], exec\n\t"
        "s_and_b64 s[58:59], exec, 1\n\t"
        "v_and_b32 v55, 3, v52\n\t"
        "v_lshrrev_b32 v53, 4, v52\n\t"
        "v_lshl_add_u32 v55, v55, 2, v53\n\t"
        "v_bfe_u32 v53, v52, 3, 1\n\t"
        "v_xor_b32 v55, v55, v53\n\t"
        "s_and_b32 s70, %[w], 1\n\t"
        "s_lshl_b32 s70, s70, 1\n\t"
        "v_xor_b32 v55, s70, v55\n\t"
        "v_lshlrev_b32 v55, 3, v55\n\t"
        "v_bfe_u32 v54, v52, 2, 2\n\t"
        "v_add_u32 v53, v53, v54\n\t"
        "v_lshl_add_u32 v55, v53, 7, v55\n\t"
        "s_mul_i32 s70, %[w], 768\n\t"
        "s_add_u32 s70, s70, 150528\n\t"
        "v_add_u32 v55, s70, v55\n\t"
        "s_add_u32 s49, s49, 50331648\n\t"
        "s_lshl_b32 s70, %[w], 11\n\t"
        "s_sub_u32 s70, s70, 8192\n\t"
        "s_add_u32 s49, s49, s70\n\t"
        "s_add_u32 s52, s70, 131072\n\t"
        "s_mov_b32 m0, s52\n\t"
        "s_add_u32 s67, s49, 33554432\n\t"
        "buffer_load_dwordx4 v0, %[rs], s49 offen sc1 lds\n\t"
        "buffer_load_dwordx4 v0, %[rs], s49 offen offset:1024 sc1 lds\n\t"
        "s_add_u32 s49, s49, 0x80000\n\t"
        "v_xor_b32 v52, 0x800, v2\n\t"
        "v_xor_b32 v53, 0x1000, v2\n\t"
        "s_setprio 1\n\t"
        "s_waitcnt lgkmcnt(0)\n\t"
        "s_branch .Ls_kprep_%=\n\t"
        ".p2align 6\n\t"
        ".Ls_kloop_%=:\n\t"
        "s_barrier\n\t"
        "buffer_load_dwordx4 v[12:15], v0, %[rs], s47 offen sc1\n\t"
        "s_nop 4\n\t"
        "buffer_load_dwordx4 v[16:19], v0, %[rs], s47 offen offset:1024 sc1\n\t"
        ".Ls_k0r_%=:\n\t"
        "s_waitcnt vmcnt(1)\n\t"
        "ds_write_b128 v2, v[12:15] offset:8192\n\t"
        "v_smfmac_f32_16x16x64_bf16 v[4:7], v[12:15], v[56:63], v1\n\t"
        "v_max_u32 v54, v12, v13\n\t"
        "v_smfmac_f32_16x16x64_bf16 v[8:11], v[12:15], v[64:71], v1\n\t"
        "v_max3_u32 v54, v54, v14, v15\n\t"
        "s_waitcnt vmcnt(0)\n\t"
        "ds_write_b128 v2, v[16:19] offset:9216\n\t"
        "v_max3_u32 v54, v54, v16, v17\n\t"
        "v_max3_u32 v54, v54, v18, v19\n\t"
        "v_smfmac_f32_16x16x64_bf16 v[4:7], v[16:19], v[72:79], v1\n\t"
        "v_cmp_eq_u32 vcc, 0xffbfffbf, v54\n\t"
        "v_smfmac_f32_16x16x64_bf16 v[8:11], v[16:19], v[80:87], v1\n\t"
        "s_waitcnt lgkmcnt(0)\n\t"
        "s_cbranch_vccnz .Ls_k0f_%=\n\t"
        ".Ls_k0k_%=:\n\t"
        "s_barrier\n\t"
        "s_memtime s[92:93]\n\t"
        "s_memrealtime s[94:95]\n\t"
        "ds_read_b128 v[20:23], v52 offset:8192\n\t"
        "ds_read_b128 v[24:27], v52 offset:9216\n\t"
        "ds_read_b128 v[12:15], v53 offset:8192\n\t"
        "ds_read_b128 v[16:19], v53 offset:9216\n\t"
        "s_setprio 3\n\t"
        "s_mov_b64 exec, s[58:59]\n\t"
        "s_waitcnt lgkmcnt(0)\n\t"
        "s_add_u32 s68, s94, s99\n\t"
        "s_or_b32 s68, s68, 1\n\t"
        "v_mov_b32 v54, s68\n\t"
        "buffer_store_dword v54, v0, %[rs], s53 offen sc1\n\t"
        "s_mov_b64 exec, s[56:57]\n\t"
        "s_setprio 1\n\t"
        "v_smfmac_f32_16x16x64_bf16 v[4:7], v[20:23], v[88:95], v1\n\t"
        "v_smfmac_f32_16x16x64_bf16 v[8:11], v[20:23], v[28:35], v1\n\t"
        "ds_read_b128 v[20:23], v3 offset:8192\n\t"
        "v_smfmac_f32_16x16x64_bf16 v[4:7], v[24:27], v[96:103], v1\n\t"
        "v_smfmac_f32_16x16x64_bf16 v[8:11], v[24:27], v[104:111], v1\n\t"
        "ds_read_b128 v[24:27], v3 offset:9216\n\t"
        "ds_read_b128 v[28:31], v2 offset:24576\n\t"
        "ds_read_b128 v[32:35], v2 offset:25600\n\t"
        "v_smfmac_f32_16x16x64_bf16 v[4:7], v[12:15], v[112:119], v1\n\t"
        "v_smfmac_f32_16x16x64_bf16 v[8:11], v[12:15], v[36:43], v1\n\t"
        "ds_read_b128 v[12:15], v2 offset:0\n\t"
        "v_smfmac_f32_16x16x64_bf16 v[4:7], v[16:19], v[120:127], v1\n\t"
        "v_smfmac_f32_16x16x64_bf16 v[8:11], v[16:19], v[128:135], v1\n\t"
        "ds_read_b128 v[16:19], v2 offset:1024\n\t"
        "ds_read_b128 v[36:39], v2 offset:40960\n\t"
        "ds_read_b128 v[40:43], v2 offset:41984\n\t"
        "s_waitcnt lgkmcnt(7)\n\t"
        "v_smfmac_f32_16x16x64_bf16 v[4:7], v[20:23], v[136:143], v1\n\t"
        "v_smfmac_f32_16x16x64_bf16 v[8:11], v[20:23], v[44:51], v1\n\t"
        "ds_read_b128 v[20:23], v52 offset:0\n\t"
        "ds_read_b128 v[44:47], v2 offset:57344\n\t"
        "ds_read_b128 v[48:51], v2 offset:58368\n\t"
        "s_waitcnt lgkmcnt(9)\n\t"
        "v_smfmac_f32_16x16x64_bf16 v[4:7], v[24:27], v[144:151], v1\n\t"
        "s_waitcnt lgkmcnt(7)\n\t"
        "v_smfmac_f32_16x16x64_bf16 v[8:11], v[24:27], v[28:35], v1\n\t"
        "v_add_u32 v54, 122880, v2\n\t"
        "ds_read_b128 v[24:27], v52 offset:1024\n\t"
        "ds_read_b128 v[28:31], v54 offset:0\n\t"
        "ds_read_b128 v[32:35], v54 offset:1024\n\t"
        "s_waitcnt lgkmcnt(9)\n\t"
        "v_smfmac_f32_16x16x64_bf16 v[4:7], v[12:15], v[152:159], v1\n\t"
        "s_waitcnt lgkmcnt(6)\n\t"
        "v_smfmac_f32_16x16x64_bf16 v[8:11], v[12:15], v[36:43], v1\n\t"
        "ds_read_b128 v[12:15], v53 offset:0\n\t"
        "ds_read_b128 v[36:39], v53 offset:1024\n\t"
        "ds_read_b128 v[40:43], v3 offset:0\n\t"
        "v_smfmac_f32_16x16x64_bf16 v[4:7], v[16:19], v[160:167], v1\n\t"
        "s_waitcnt lgkmcnt(6)\n\t"
        "v_smfmac_f32_16x16x64_bf16 v[8:11], v[16:19], v[44:51], v1\n\t"
        "ds_read_b128 v[16:19], v3 offset:1024\n\t"
        "v_smfmac_f32_16x16x64_bf16 v[4:7], v[20:23], v[168:175], v1\n\t"
        "s_waitcnt lgkmcnt(4)\n\t"
        "v_smfmac_f32_16x16x64_bf16 v[8:11], v[20:23], v[28:35], v1\n\t"
        "s_barrier\n\t"
        "v_smfmac_f32_16x16x64_bf16 v[4:7], v[24:27], v[176:183], v1\n\t"
        "v_smfmac_f32_16x16x64_bf16 v[8:11], v[24:27], v[184:191], v1\n\t"
        "buffer_load_dwordx4 v0, %[rs], s72 offen sc1 lds\n\t"
        "buffer_load_dwordx4 v0, %[rs], s72 offen offset:1024 sc1 lds\n\t"
        "s_waitcnt lgkmcnt(3)\n\t"
        "v_smfmac_f32_16x16x64_bf16 v[4:7], v[12:15], v[192:199], v1\n\t"
        "v_smfmac_f32_16x16x64_bf16 v[8:11], v[12:15], v[200:207], v1\n\t"
        "s_waitcnt lgkmcnt(2)\n\t"
        "v_smfmac_f32_16x16x64_bf16 v[4:7], v[36:39], v[208:215], v1\n\t"
        "v_smfmac_f32_16x16x64_bf16 v[8:11], v[36:39], v[216:223], v1\n\t"
        "s_waitcnt lgkmcnt(1)\n\t"
        "v_smfmac_f32_16x16x64_bf16 v[4:7], v[40:43], v[224:231], v1\n\t"
        "v_smfmac_f32_16x16x64_bf16 v[8:11], v[40:43], v[232:239], v1\n\t"
        "s_waitcnt lgkmcnt(0)\n\t"
        "v_smfmac_f32_16x16x64_bf16 v[4:7], v[16:19], v[240:247], v1\n\t"
        "v_smfmac_f32_16x16x64_bf16 v[8:11], v[16:19], v[248:255], v1\n\t"
        "s_nop 6\n\t"
        "v_add_f32 v4, v4, v5\n\t"
        "v_add_f32 v6, v6, v7\n\t"
        "v_add_f32 v8, v8, v9\n\t"
        "v_add_f32 v10, v10, v11\n\t"
        "v_cvt_pk_bf16_f32 v4, v4, v8\n\t"
        "v_cvt_pk_bf16_f32 v5, v6, v10\n\t"
        "ds_write_b64 v55, v[4:5]\n\t"
        "s_waitcnt lgkmcnt(0)\n\t"
        "s_barrier\n\t"
        "s_setprio 0\n\t"
        "s_nop 15\n\t"
        "s_nop 15\n\t"
        "s_nop 15\n\t"
        "s_nop 15\n\t"
        "s_nop 15\n\t"
        "s_nop 15\n\t"
        "s_nop 3\n\t"
        "s_cmp_lt_u32 s45, 4\n\t"
        "s_cbranch_scc1 .Ls_kearly_%=\n\t"
        ".Ls_klate_%=:\n\t"
        "v_mov_b32 v21, 0\n\t"
        "s_add_u32 s53, s53, 4194304\n\t"
        "s_and_b32 s53, s53, 0x70ffffff\n\t"
        "s_cmp_lt_u32 s45, 62\n\t"
        "s_cselect_b32 s53, s53, 0x7f000000\n\t"
        "s_mov_b64 exec, s[58:59]\n\t"
        "buffer_store_dword v21, v0, %[rs], s53 offen sc1\n\t"
        "s_mov_b64 exec, s[56:57]\n\t"
        "s_sub_u32 s68, s92, s96\n\t"
        "s_sub_u32 s69, s94, s97\n\t"
        "s_mov_b32 s96, s92\n\t"
        "s_mov_b32 s97, s94\n\t"
        "v_cvt_f32_u32 v17, s68\n\t"
        "v_cvt_f32_u32 v16, s69\n\t"
        "v_rcp_f32 v17, v17\n\t"
        "s_nop 0\n\t"
        "v_mul_f32 v17, v17, v16\n\t"
        "v_max_f32 v17, 0x3d2aaaab, v17\n\t"
        "v_min_f32 v17, 0x3d638e39, v17\n\t"
        "v_subrev_f32 v17, s98, v17\n\t"
        "v_mov_b32 v16, s98\n\t"
        "v_fmac_f32 v16, 0x3ec00000, v17\n\t"
        "v_mul_f32 v17, 0x3f800000, v16\n\t"
        "v_cvt_u32_f32 v17, v17\n\t"
        "v_readfirstlane_b32 s98, v16\n\t"
        "v_mul_f32 v16, 0x44d48000, v16\n\t"
        "v_cvt_u32_f32 v16, v16\n\t"
        "s_nop 0\n\t"
        "v_readfirstlane_b32 s99, v16\n\t"
        "s_add_u32 s67, s49, 33554432\n\t"
        "v_add_u32 v54, 73728, v2\n\t"
        "ds_read_b128 v[28:31], v54 offset:0\n\t"
        "ds_read_b128 v[32:35], v54 offset:1024\n\t"
        "ds_read_b128 v[36:39], v54 offset:16384\n\t"
        "ds_read_b128 v[40:43], v54 offset:17408\n\t"
        "ds_read_b128 v[44:47], v54 offset:32768\n\t"
        "ds_read_b128 v[48:51], v54 offset:33792\n\t"
        "v_xor_b32 v52, 0x800, v2\n\t"
        "v_xor_b32 v53, 0x1000, v2\n\t"
        "v_xor_b32 v3, 0x1800, v2\n\t"
        "s_add_u32 s45, s45, 1\n\t"
        "s_add_u32 s47, s47, 2097152\n\t"
        "s_and_b32 s47, s47, 0xffffff\n\t"
        "s_xor_b32 s47, s47, 0x2000\n\t"
        "s_add_u32 s50, s50, 2097152\n\t"
        "s_and_b32 s50, s50, 0xffffff\n\t"
        "s_xor_b32 s50, s50, 0x2000\n\t"
        "s_add_u32 s49, s49, 0x80000\n\t"
        "s_add_u32 s70, s45, 1\n\t"
        "s_and_b32 s70, s70, 3\n\t"
        "s_cselect_b32 s70, 0, 0x2000\n\t"
        "s_xor_b32 s49, s49, s70\n\t"
        "s_nop 0\n\t"
        "v_mov_b64 v[4:5], 0\n\t"
        "v_mov_b64 v[6:7], 0\n\t"
        "v_mov_b64 v[8:9], 0\n\t"
        "v_mov_b64 v[10:11], 0\n\t"
        "s_waitcnt lgkmcnt(0)\n\t"
        "s_and_b32 s70, %[w], 1\n\t"
        "s_lshl_b32 s70, s70, 13\n\t"
        "s_xor_b32 s53, s50, s70\n\t"
        "s_lshl_b32 s70, %[h], 5\n\t"
        "s_add_u32 s53, s53, s70\n\t"
        "s_add_u32 s53, s53, 327680\n\t"
        "s_cmp_lt_u32 %[w], 6\n\t"
        "s_cselect_b32 s53, s53, 0x70000000\n\t"
        "s_add_u32 s70, s45, 1\n\t"
        "s_and_b32 s70, s70, 1\n\t"
        "s_lshl_b32 s70, s70, 13\n\t"
        "s_add_u32 m0, s70, s52\n\t"
        "s_cmp_lt_u32 s45, 63\n\t"
        "s_cselect_b32 s72, s49, 0x7f000000\n\t"
        "s_add_u32 s73, s45, 2\n\t"
        "s_min_u32 s73, s73, 64\n\t"
        "s_cmp_lt_u32 s45, 4\n\t"
        "s_cselect_b32 s72, 0x7f000000, s72\n\t"
        "s_cselect_b32 m0, 163840, m0\n\t"
        "s_cselect_b32 s73, 0, s73\n\t"
        "s_cselect_b32 s67, 0x7f000000, s67\n\t"
        "s_setprio 1\n\t"
        "s_cmp_lt_u32 s45, 64\n\t"
        "s_cbranch_scc1 .Ls_kloop_%=\n\t"
        "s_branch .Ls_end_%=\n\t"
        ".Ls_kprep_%=:\n\t"
        "s_mov_b32 s99, 80\n\t"
        "s_and_b32 s70, %[w], 1\n\t"
        "s_lshl_b32 s70, s70, 13\n\t"
        "s_xor_b32 s53, s50, s70\n\t"
        "s_lshl_b32 s70, %[h], 5\n\t"
        "s_add_u32 s53, s53, s70\n\t"
        "s_add_u32 s53, s53, 327680\n\t"
        "s_cmp_lt_u32 %[w], 6\n\t"
        "s_cselect_b32 s53, s53, 0x70000000\n\t"
        "v_mov_b32 v21, 0\n\t"
        "s_add_u32 s53, s53, 2097152\n\t"
        "s_mov_b64 exec, s[58:59]\n\t"
        "buffer_store_dword v21, v0, %[rs], s53 offen sc1\n\t"
        "s_mov_b64 exec, s[56:57]\n\t"
        "s_and_b32 s70, %[w], 1\n\t"
        "s_lshl_b32 s70, s70, 13\n\t"
        "s_xor_b32 s53, s50, s70\n\t"
        "s_lshl_b32 s70, %[h], 5\n\t"
        "s_add_u32 s53, s53, s70\n\t"
        "s_add_u32 s53, s53, 327680\n\t"
        "s_cmp_lt_u32 %[w], 6\n\t"
        "s_cselect_b32 s53, s53, 0x70000000\n\t"
        "s_mov_b32 s72, 0x7f000000\n\t"
        "s_mov_b32 s67, s72\n\t"
        "s_mov_b32 s73, 0\n\t"
        "v_mov_b64 v[24:25], 0\n\t"
        "s_mov_b32 m0, 163840\n\t"
        "s_branch .Ls_k0r_%=\n\t"
        ".Ls_kearly_%=:\n\t"
        "s_add_u32 s71, s45, 2\n\t"
        "s_cmp_ge_u32 s51, s71\n\t"
        "s_cbranch_scc0 .Ls_fep_%=\n\t"
        ".Ls_fek_%=:\n\t"
        "s_add_u32 s70, s45, 1\n\t"
        "s_and_b32 s70, s70, 1\n\t"
        "s_lshl_b32 s70, s70, 13\n\t"
        "s_add_u32 m0, s70, s52\n\t"
        "s_cmp_lt_u32 s45, 63\n\t"
        "s_cselect_b32 s72, s49, 0x7f000000\n\t"
        "s_add_u32 s73, s45, 2\n\t"
        "s_min_u32 s73, s73, 64\n\t"
        "buffer_load_dwordx4 v0, %[rs], s72 offen sc1 lds\n\t"
        "buffer_load_dwordx4 v0, %[rs], s72 offen offset:1024 sc1 lds\n\t"
        "s_branch .Ls_klate_%=\n\t"
        ".Ls_f0p_%=:\n\t"
        "s_add_u32 s70, 1, 3\n\t"
        "s_andn2_b32 s70, s70, 3\n\t"
        "s_cmp_le_u32 1, 16\n\t"
        "s_cselect_b32 s70, 1, s70\n\t"
        "s_lshl_b32 s70, s70, 7\n\t"
        "v_mov_b32 v52, 0\n\t"
        "buffer_load_dwordx2 v[52:53], v52, %[rf], s70 offen sc1\n\t"
        "s_waitcnt vmcnt(0)\n\t"
        "v_readfirstlane_b32 s68, v52\n\t"
        "v_readfirstlane_b32 s69, v53\n\t"
        "s_xor_b32 s68, s68, s54\n\t"
        "s_xor_b32 s69, s69, s55\n\t"
        "s_lshr_b32 s70, s68, 8\n\t"
        "s_or_b32 s70, s70, s69\n\t"
        "s_cmp_eq_u32 s70, 0\n\t"
        "s_cselect_b32 s68, s68, 0\n\t"
        "s_and_b32 s69, s68, 127\n\t"
        "s_max_u32 s51, s51, s69\n\t"
        "s_cmp_ge_u32 s51, 1\n\t"
        "s_cbranch_scc1 .Ls_f0k_%=\n\t"
        "s_sleep 4\n\t"
        "s_sub_u32 s46, s46, 1\n\t"
        "s_cbranch_scc0 .Ls_f0p_%=\n\t"
        "s_mov_b32 s46, 0\n\t"
        "s_mov_b32 s46, 0\n\t"
        "v_mov_b32 v52, 0\n\t"
        "s_add_u32 s68, s45, 3000\n\t"
        "v_mov_b32 v53, s68\n\t"
        "global_store_dword v52, v53, %[tm] sc1\n\t"
        "s_branch .Ls_f0k_%=\n\t"
        ".Ls_q0f_%=:\n\t"
        "s_cmp_eq_u32 s45, 0\n\t"
        "s_cbranch_scc1 .Ls_q0k_%=\n\t"
        "s_add_u32 s79, s79, 1\n\t"
        "buffer_load_dwordx4 v[12:15], v0, %[rs], s47 offen sc1\n\t"
        "buffer_load_dwordx4 v[16:19], v0, %[rs], s47 offen offset:1024 sc1\n\t"
        "s_nop 7\n\t"
        "v_mov_b64 v[4:5], 0\n\t"
        "v_mov_b64 v[6:7], 0\n\t"
        "v_mov_b64 v[8:9], 0\n\t"
        "v_mov_b64 v[10:11], 0\n\t"
        "s_waitcnt vmcnt(0)\n\t"
        "s_sub_u32 s46, s46, 1\n\t"
        "s_cbranch_scc0 .Ls_q0r_%=\n\t"
        "s_mov_b32 s46, 0\n\t"
        "s_branch .Ls_q0k_%=\n\t"
        ".Ls_xf_%=:\n\t"
        "s_add_u32 s79, s79, 256\n\t"
        "v_max_u32 v4, v28, v29\n\t"
        "v_max3_u32 v4, v4, v30, v31\n\t"
        "v_max3_u32 v4, v4, v32, v33\n\t"
        "v_max3_u32 v4, v4, v34, v35\n\t"
        "v_max3_u32 v4, v4, v44, v45\n\t"
        "v_max3_u32 v4, v4, v46, v47\n\t"
        "v_max3_u32 v4, v4, v48, v49\n\t"
        "v_max3_u32 v4, v4, v50, v51\n\t"
        "v_cmp_eq_u32 vcc, 0xffbfffbf, v4\n\t"
        "s_cmp_eq_u32 %[w], 0\n\t"
        "s_cbranch_vccz .Ls_xk_%=\n\t"
        "v_mov_b64 v[12:13], 0\n\t"
        "v_mov_b64 v[14:15], 0\n\t"
        "s_branch .Ls_xs_%=\n\t"
        ".Ls_pmax_%=:\n\t"
        "s_cmp_eq_u32 s44, s45\n\t"
        "s_cbranch_scc1 .Ls_pok_%=\n\t"
        "s_mov_b32 s44, s45\n\t"
        "ds_read_b128 v[24:27], v54 offset:1024\n\t"
        "s_waitcnt lgkmcnt(0)\n\t"
        "v_max3_f32 v8, v24, v25, v26\n\t"
        "v_max3_f32 v8, v8, v27, v4\n\t"
        "v_max_f32 v8, v8, v6\n\t"
        "s_nop 1\n\t"
        "v_max_f32_dpp v9, v8, v8 quad_perm:[1,0,3,2] row_mask:0xf bank_mask:0xf\n\t"
        "v_max_f32_dpp v10, v8, v8 quad_perm:[1,0,3,2] row_mask:0xf bank_mask:0xf\n\t"
        "s_nop 1\n\t"
        "v_permlane16_swap_b32 v9, v10\n\t"
        "v_max_f32 v8, v9, v10\n\t"
        "v_max_f32 v11, v9, v10\n\t"
        "s_nop 1\n\t"
        "v_permlane32_swap_b32 v8, v11\n\t"
        "v_max_f32 v8, v8, v11\n\t"
        "v_sub_f32 v24, v24, v8\n\t"
        "v_sub_f32 v25, v25, v8\n\t"
        "v_sub_f32 v26, v26, v8\n\t"
        "v_sub_f32 v27, v27, v8\n\t"
        "v_sub_f32 v5, v4, v8\n\t"
        "v_sub_f32 v7, v6, v8\n\t"
        "v_exp_f32 v24, v24\n\t"
        "v_exp_f32 v25, v25\n\t"
        "v_exp_f32 v26, v26\n\t"
        "v_exp_f32 v27, v27\n\t"
        "v_exp_f32 v5, v5\n\t"
        "v_exp_f32 v7, v7\n\t"
        "v_add_f32 v8, v24, v25\n\t"
        "v_add_f32 v9, v26, v27\n\t"
        "v_add_f32 v8, v8, v9\n\t"
        "s_nop 1\n\t"
        "v_add_f32_dpp v8, v8, v8 quad_perm:[1,0,3,2] row_mask:0xf bank_mask:0xf\n\t"
        "s_branch .Ls_psum_%=\n\t"
        ".Ls_twx_%=:\n\t"
        "s_add_u32 s79, s79, 0x1000000\n\t"
        "s_sleep 4\n\t"
        "buffer_load_dword v36, v18, %[rs], s48 offen sc1\n\t"
        "s_waitcnt vmcnt(0)\n\t"
        "s_sub_u32 s46, s46, 1\n\t"
        "s_cbranch_scc0 .Ls_twm_%=\n\t"
        "s_mov_b32 s46, 0\n\t"
        "s_branch .Ls_twy_%=\n\t"
        ".Ls_fq0p_%=:\n\t"
        "s_add_u32 s70, s71, 3\n\t"
        "s_andn2_b32 s70, s70, 3\n\t"
        "s_cmp_le_u32 s71, 16\n\t"
        "s_cselect_b32 s70, s71, s70\n\t"
        "s_lshl_b32 s70, s70, 7\n\t"
        "v_mov_b32 v12, 0\n\t"
        "buffer_load_dwordx2 v[12:13], v12, %[rf], s70 offen sc1\n\t"
        "s_waitcnt vmcnt(0)\n\t"
        "v_readfirstlane_b32 s68, v12\n\t"
        "v_readfirstlane_b32 s69, v13\n\t"
        "s_xor_b32 s68, s68, s54\n\t"
        "s_xor_b32 s69, s69, s55\n\t"
        "s_lshr_b32 s70, s68, 8\n\t"
        "s_or_b32 s70, s70, s69\n\t"
        "s_cmp_eq_u32 s70, 0\n\t"
        "s_cselect_b32 s68, s68, 0\n\t"
        "s_and_b32 s69, s68, 127\n\t"
        "s_max_u32 s51, s51, s69\n\t"
        "s_cmp_ge_u32 s51, s71\n\t"
        "s_cbranch_scc1 .Ls_fq0k_%=\n\t"
        "s_sleep 4\n\t"
        "s_sub_u32 s46, s46, 1\n\t"
        "s_cbranch_scc0 .Ls_fq0p_%=\n\t"
        "s_mov_b32 s46, 0\n\t"
        "s_mov_b32 s46, 0\n\t"
        "v_mov_b32 v12, 0\n\t"
        "s_add_u32 s68, s45, 3000\n\t"
        "v_mov_b32 v13, s68\n\t"
        "global_store_dword v12, v13, %[tm] sc1\n\t"
        "s_branch .Ls_fq0k_%=\n\t"
        ".Ls_fq1p_%=:\n\t"
        "s_add_u32 s70, s71, 3\n\t"
        "s_andn2_b32 s70, s70, 3\n\t"
        "s_cmp_le_u32 s71, 16\n\t"
        "s_cselect_b32 s70, s71, s70\n\t"
        "s_lshl_b32 s70, s70, 7\n\t"
        "v_mov_b32 v12, 0\n\t"
        "buffer_load_dwordx2 v[12:13], v12, %[rf], s70 offen sc1\n\t"
        "s_waitcnt vmcnt(0)\n\t"
        "v_readfirstlane_b32 s68, v12\n\t"
        "v_readfirstlane_b32 s69, v13\n\t"
        "s_xor_b32 s68, s68, s54\n\t"
        "s_xor_b32 s69, s69, s55\n\t"
        "s_lshr_b32 s70, s68, 8\n\t"
        "s_or_b32 s70, s70, s69\n\t"
        "s_cmp_eq_u32 s70, 0\n\t"
        "s_cselect_b32 s68, s68, 0\n\t"
        "s_and_b32 s69, s68, 127\n\t"
        "s_max_u32 s51, s51, s69\n\t"
        "s_cmp_ge_u32 s51, s71\n\t"
        "s_cbranch_scc1 .Ls_fq1k_%=\n\t"
        "s_sleep 4\n\t"
        "s_sub_u32 s46, s46, 1\n\t"
        "s_cbranch_scc0 .Ls_fq1p_%=\n\t"
        "s_mov_b32 s46, 0\n\t"
        "s_mov_b32 s46, 0\n\t"
        "v_mov_b32 v12, 0\n\t"
        "s_add_u32 s68, s45, 3000\n\t"
        "v_mov_b32 v13, s68\n\t"
        "global_store_dword v12, v13, %[tm] sc1\n\t"
        "s_branch .Ls_fq1k_%=\n\t"
        ".Ls_k0f_%=:\n\t"
        "s_cmp_eq_u32 s45, 0\n\t"
        "s_cbranch_scc1 .Ls_k0k_%=\n\t"
        "s_add_u32 s79, s79, 1\n\t"
        "buffer_load_dwordx4 v[12:15], v0, %[rs], s47 offen sc1\n\t"
        "buffer_load_dwordx4 v[16:19], v0, %[rs], s47 offen offset:1024 sc1\n\t"
        "s_nop 7\n\t"
        "v_mov_b64 v[4:5], 0\n\t"
        "v_mov_b64 v[6:7], 0\n\t"
        "v_mov_b64 v[8:9], 0\n\t"
        "v_mov_b64 v[10:11], 0\n\t"
        "s_waitcnt vmcnt(0)\n\t"
        "s_sub_u32 s46, s46, 1\n\t"
        "s_cbranch_scc0 .Ls_k0r_%=\n\t"
        "s_mov_b32 s46, 0\n\t"
        "s_branch .Ls_k0k_%=\n\t"
        ".Ls_fep_%=:\n\t"
        "s_add_u32 s70, s71, 3\n\t"
        "s_andn2_b32 s70, s70, 3\n\t"
        "s_cmp_le_u32 s71, 16\n\t"
        "s_cselect_b32 s70, s71, s70\n\t"
        "s_lshl_b32 s70, s70, 7\n\t"
        "v_mov_b32 v24, 0\n\t"
        "buffer_load_dwordx2 v[24:25], v24, %[rf], s70 offen sc1\n\t"
        "s_waitcnt vmcnt(0)\n\t"
        "v_readfirstlane_b32 s68, v24\n\t"
        "v_readfirstlane_b32 s69, v25\n\t"
        "s_xor_b32 s68, s68, s54\n\t"
        "s_xor_b32 s69, s69, s55\n\t"
        "s_lshr_b32 s70, s68, 8\n\t"
        "s_or_b32 s70, s70, s69\n\t"
        "s_cmp_eq_u32 s70, 0\n\t"
        "s_cselect_b32 s68, s68, 0\n\t"
        "s_and_b32 s69, s68, 127\n\t"
        "s_max_u32 s51, s51, s69\n\t"
        "s_cmp_ge_u32 s51, s71\n\t"
        "s_cbranch_scc1 .Ls_fek_%=\n\t"
        "s_sleep 4\n\t"
        "s_sub_u32 s46, s46, 1\n\t"
        "s_cbranch_scc0 .Ls_fep_%=\n\t"
        "s_mov_b32 s46, 0\n\t"
        "s_mov_b32 s46, 0\n\t"
        "v_mov_b32 v24, 0\n\t"
        "s_add_u32 s68, s45, 3000\n\t"
        "v_mov_b32 v25, s68\n\t"
        "global_store_dword v24, v25, %[tm] sc1\n\t"
        "s_branch .Ls_fek_%=\n\t"
        ".Lcode_s1:\n\t"
        ".Ls_end_%=:\n\t"
        "s_endpgm\n\t"
        :: [rs] "s"(RSRC(arena(a.slots), ARENA_BYTES)), [rw] "s"(RSRC((w < 4 ? a.Wq : a.Wk) + co, (DM * HD - co) * 2)), [rm] "s"(RSRC(a.mem0, 8 * DM * 2)), [ep] "s"(a.boot + 144),
           [rf] "s"(PROG((w == 2 ? 32 : 0) + h)), [tm] "s"(a.sync + 40 * 32), [w] "s"(w), [h] "s"(h), [cs] "s"(cls_s(h)), [ra] "s"((4 + 2 * rep) * 16384),
           [kx] "s"(131072 + (w >> 1) * 4096 + (w & 1) * 128), [tw] "s"(TW(cls_s(h))) STIN(w == 0 ? h : w == KW ? 192 + h : 251)
        : "memory", "vcc", "scc", ALLV, ALLS);
    __builtin_unreachable();
}
__device__ __forceinline__ void value_wg(const int w, const int h, const int half, const int rep) {
    const Args a = get_args();
    const int co = hcol(h) + 64 * (w >> 2), j0 = 32 * half + 4 * w, oo = hcol(h) * DM + 16 * j0;
    asm volatile(
        ".Lcode_v0:\n\t"
        "v_mbcnt_lo_u32_b32 v52, -1, 0\n\t"
        "v_mbcnt_hi_u32_b32 v52, -1, v52\n\t"
        "s_load_dwordx2 s[54:55], %[ep], 0\n\t"
        "s_mov_b32 s79, 0\n\t"
        "s_mov_b32 s45, 0\n\t"
        "s_mov_b32 s46, 200000\n\t"
        "s_mov_b32 s51, 0\n\t"
        "v_lshlrev_b32 v0, 4, v52\n\t"
        "v_lshrrev_b32 v53, 3, v52\n\t"
        "v_and_b32 v54, 7, v52\n\t"
        "v_and_b32 v8, 2, v53\n\t"
        "v_xor_b32 v54, v54, v8\n\t"
        "v_lshlrev_b32 v54, 4, v54\n\t"
        "v_lshl_add_u32 v8, v53, 13, v54\n\t"
        "v_xor_b32 v9, 64, v8\n\t"
        "s_lshl_b32 s69, %[w], 12\n\t"
        "v_lshrrev_b32 v12, 2, v53\n\t"
        "v_lshlrev_b32 v12, 1, v12\n\t"
        "v_and_b32 v13, 1, v53\n\t"
        "v_add_u32 v12, v12, v13\n\t"
        "v_and_b32 v13, 2, v53\n\t"
        "v_lshl_add_u32 v12, v13, 3, v12\n\t"
        "v_lshl_add_u32 v12, v12, 11, v54\n\t"
        "v_xor_b32 v13, 64, v12\n\t"
        "v_lshrrev_b32 v11, 4, v52\n\t"
        "s_lshl_b32 s68, %[wa], 20\n\t"
        "s_add_u32 m0, s69, 0\n\t"
        "s_xor_b32 s70, s68, 0x0\n\t"
        "buffer_load_dwordx4 v8, %[rw], s70 offen offset:0 lds\n\t"
        "s_add_u32 s70, s70, 64512\n\t"
        "buffer_load_dwordx4 v9, %[rw], s70 offen offset:1024 lds\n\t"
        "s_add_u32 s70, s70, 64512\n\t"
        "buffer_load_dwordx4 v8, %[rw], s70 offen offset:2048 lds\n\t"
        "s_add_u32 s70, s70, 64512\n\t"
        "buffer_load_dwordx4 v9, %[rw], s70 offen offset:3072 lds\n\t"
        "v_bfe_u32 v60, v0, 5, 3\n\t"
        "v_lshlrev_b32 v60, 11, v60\n\t"
        "v_lshrrev_b32 v61, 8, v0\n\t"
        "v_lshl_add_u32 v60, v61, 5, v60\n\t"
        "v_bfe_u32 v61, v0, 4, 1\n\t"
        "v_lshl_add_u32 v60, v61, 2, v60\n\t"
        "s_lshl_b32 s71, %[wa], 8\n\t"
        "buffer_load_dword v20, v60, %[rm], s71 offen offset:0\n\t"
        "buffer_load_dword v21, v60, %[rm], s71 offen offset:8\n\t"
        "buffer_load_dword v22, v60, %[rm], s71 offen offset:16\n\t"
        "buffer_load_dword v23, v60, %[rm], s71 offen offset:24\n\t"
        "buffer_load_dword v24, v60, %[rm], s71 offen offset:128\n\t"
        "buffer_load_dword v25, v60, %[rm], s71 offen offset:136\n\t"
        "buffer_load_dword v26, v60, %[rm], s71 offen offset:144\n\t"
        "buffer_load_dword v27, v60, %[rm], s71 offen offset:152\n\t"
        "v_mov_b32 v28, 0\n\t"
        "s_mov_b32 s72, 52428544\n\t"
        "s_mov_b32 s74, 376832\n\t"
        "buffer_load_dword v29, v28, %[rf], 0 offen sc1\n\t"
        "buffer_load_dword v29, v28, %[rs], s72 offen sc1\n\t"
        "buffer_load_dword v29, v28, %[rs], s74 offen sc1\n\t"
        "v_lshlrev_b32 v28, 18, v0\n\t"
        "s_or_b32 s73, %[oy], 0x1fff00\n\t"
        "s_mov_b64 exec, 0x1\n\t"
        "buffer_load_dword v29, v28, %[rs], s73 offen sc1\n\t"
        "s_mov_b64 exec, -1\n\t"
        "s_lshl_b32 s68, %[wa], 20\n\t"
        "s_add_u32 m0, s69, 32768\n\t"
        "s_xor_b32 s70, s68, 0x400000\n\t"
        "buffer_load_dwordx4 v8, %[rw], s70 offen offset:0 lds\n\t"
        "s_add_u32 s70, s70, 64512\n\t"
        "buffer_load_dwordx4 v9, %[rw], s70 offen offset:1024 lds\n\t"
        "s_add_u32 s70, s70, 64512\n\t"
        "buffer_load_dwordx4 v8, %[rw], s70 offen offset:2048 lds\n\t"
        "s_add_u32 s70, s70, 64512\n\t"
        "buffer_load_dwordx4 v9, %[rw], s70 offen offset:3072 lds\n\t"
        "v_lshrrev_b32 v4, 2, v52\n\t"
        "v_lshl_add_u32 v4, v11, 2, v4\n\t"
        "v_lshlrev_b32 v4, 7, v4\n\t"
        "v_and_b32 v5, 3, v52\n\t"
        "v_lshl_add_u32 v4, v5, 3, v4\n\t"
        "v_add_u32 v4, s69, v4\n\t"
        "v_bfe_u32 v5, v52, 3, 2\n\t"
        "v_lshl_add_u32 v14, v5, 5, v4\n\t"
        "v_xor_b32 v15, 32, v14\n\t"
        "v_xor_b32 v16, 64, v14\n\t"
        "v_xor_b32 v17, 96, v14\n\t"
        "s_and_b32 s70, %[w], 3\n\t"
        "s_lshl_b32 s70, s70, 5\n\t"
        "v_xor_b32 v4, s70, v14\n\t"
        "v_xor_b32 v5, 0x1000, v4\n\t"
        "v_xor_b32 v6, 0x2000, v4\n\t"
        "v_xor_b32 v7, 0x3000, v4\n\t"
        "v_mov_b32 v53, 0x4444\n\t"
        "v_mov_b32 v54, 0xeeee\n\t"
        "v_and_b32 v1, 1, v52\n\t"
        "v_cmp_eq_u32 vcc, 0, v1\n\t"
        "s_lshl_b32 s70, %[wa], 11\n\t"
        "v_add_u32 v2, s70, v0\n\t"
        "v_cndmask_b32 v1, v54, v53, vcc\n\t"
        "s_waitcnt lgkmcnt(0)\n\t"
        "s_add_u32 s54, s54, 1\n\t"
        "s_addc_u32 s55, s55, 0\n\t"
        "s_lshl_b64 s[54:55], s[54:55], 8\n\t"
        "s_waitcnt vmcnt(4)\n\t"
        "s_barrier\n\t"
        "s_lshl_b32 s68, %[wa], 20\n\t"
        "s_add_u32 m0, s69, 65536\n\t"
        "s_xor_b32 s70, s68, 0x40000\n\t"
        "buffer_load_dwordx4 v8, %[rw], s70 offen offset:0 lds\n\t"
        "s_add_u32 s70, s70, 64512\n\t"
        "buffer_load_dwordx4 v9, %[rw], s70 offen offset:1024 lds\n\t"
        "s_add_u32 s70, s70, 64512\n\t"
        "buffer_load_dwordx4 v8, %[rw], s70 offen offset:2048 lds\n\t"
        "s_add_u32 s70, s70, 64512\n\t"
        "buffer_load_dwordx4 v9, %[rw], s70 offen offset:3072 lds\n\t"
        "ds_read_b64_tr_b16 v[64:65], v4\n\t"
        "ds_read_b64_tr_b16 v[66:67], v4 offset:512\n\t"
        "ds_read_b64_tr_b16 v[80:81], v5\n\t"
        "ds_read_b64_tr_b16 v[82:83], v5 offset:512\n\t"
        "ds_read_b64_tr_b16 v[96:97], v6\n\t"
        "ds_read_b64_tr_b16 v[98:99], v6 offset:512\n\t"
        "ds_read_b64_tr_b16 v[112:113], v7\n\t"
        "ds_read_b64_tr_b16 v[114:115], v7 offset:512\n\t"
        "s_waitcnt lgkmcnt(0)\n\t"
        "v_add_u32 v4, 32768, v4\n\t"
        "v_add_u32 v5, 32768, v5\n\t"
        "v_add_u32 v6, 32768, v6\n\t"
        "v_add_u32 v7, 32768, v7\n\t"
        "v_add_u32 v14, 32768, v14\n\t"
        "v_add_u32 v15, 32768, v15\n\t"
        "v_add_u32 v16, 32768, v16\n\t"
        "v_add_u32 v17, 32768, v17\n\t"
        "s_waitcnt vmcnt(4)\n\t"
        "s_barrier\n\t"
        "s_lshl_b32 s68, %[wa], 20\n\t"
        "s_add_u32 m0, s69, 0\n\t"
        "s_xor_b32 s70, s68, 0x440000\n\t"
        "buffer_load_dwordx4 v8, %[rw], s70 offen offset:0 lds\n\t"
        "s_add_u32 s70, s70, 64512\n\t"
        "buffer_load_dwordx4 v9, %[rw], s70 offen offset:1024 lds\n\t"
        "s_add_u32 s70, s70, 64512\n\t"
        "buffer_load_dwordx4 v8, %[rw], s70 offen offset:2048 lds\n\t"
        "s_add_u32 s70, s70, 64512\n\t"
        "buffer_load_dwordx4 v9, %[rw], s70 offen offset:3072 lds\n\t"
        "ds_read_b64_tr_b16 v[128:129], v4\n\t"
        "ds_read_b64_tr_b16 v[130:131], v4 offset:512\n\t"
        "ds_read_b64_tr_b16 v[144:145], v5\n\t"
        "ds_read_b64_tr_b16 v[146:147], v5 offset:512\n\t"
        "ds_read_b64_tr_b16 v[160:161], v6\n\t"
        "ds_read_b64_tr_b16 v[162:163], v6 offset:512\n\t"
        "ds_read_b64_tr_b16 v[176:177], v7\n\t"
        "ds_read_b64_tr_b16 v[178:179], v7 offset:512\n\t"
        "s_waitcnt lgkmcnt(0)\n\t"
        "v_add_u32 v4, 32768, v4\n\t"
        "v_add_u32 v5, 32768, v5\n\t"
        "v_add_u32 v6, 32768, v6\n\t"
        "v_add_u32 v7, 32768, v7\n\t"
        "v_add_u32 v14, 32768, v14\n\t"
        "v_add_u32 v15, 32768, v15\n\t"
        "v_add_u32 v16, 32768, v16\n\t"
        "v_add_u32 v17, 32768, v17\n\t"
        "s_waitcnt vmcnt(4)\n\t"
        "s_barrier\n\t"
        "s_lshl_b32 s68, %[wa], 20\n\t"
        "s_add_u32 m0, s69, 32768\n\t"
        "s_xor_b32 s70, s68, 0x80000\n\t"
        "buffer_load_dwordx4 v8, %[rw], s70 offen offset:0 lds\n\t"
        "s_add_u32 s70, s70, 64512\n\t"
        "buffer_load_dwordx4 v9, %[rw], s70 offen offset:1024 lds\n\t"
        "s_add_u32 s70, s70, 64512\n\t"
        "buffer_load_dwordx4 v8, %[rw], s70 offen offset:2048 lds\n\t"
        "s_add_u32 s70, s70, 64512\n\t"
        "buffer_load_dwordx4 v9, %[rw], s70 offen offset:3072 lds\n\t"
        "ds_read_b64_tr_b16 v[68:69], v4\n\t"
        "ds_read_b64_tr_b16 v[70:71], v4 offset:512\n\t"
        "ds_read_b64_tr_b16 v[84:85], v5\n\t"
        "ds_read_b64_tr_b16 v[86:87], v5 offset:512\n\t"
        "ds_read_b64_tr_b16 v[100:101], v6\n\t"
        "ds_read_b64_tr_b16 v[102:103], v6 offset:512\n\t"
        "ds_read_b64_tr_b16 v[116:117], v7\n\t"
        "ds_read_b64_tr_b16 v[118:119], v7 offset:512\n\t"
        "s_waitcnt lgkmcnt(0)\n\t"
        "v_add_u32 v4, -65536, v4\n\t"
        "v_add_u32 v5, -65536, v5\n\t"
        "v_add_u32 v6, -65536, v6\n\t"
        "v_add_u32 v7, -65536, v7\n\t"
        "v_add_u32 v14, -65536, v14\n\t"
        "v_add_u32 v15, -65536, v15\n\t"
        "v_add_u32 v16, -65536, v16\n\t"
        "v_add_u32 v17, -65536, v17\n\t"
        "s_waitcnt vmcnt(4)\n\t"
        "s_barrier\n\t"
        "s_lshl_b32 s68, %[wa], 20\n\t"
        "s_add_u32 m0, s69, 65536\n\t"
        "s_xor_b32 s70, s68, 0x480000\n\t"
        "buffer_load_dwordx4 v8, %[rw], s70 offen offset:0 lds\n\t"
        "s_add_u32 s70, s70, 64512\n\t"
        "buffer_load_dwordx4 v9, %[rw], s70 offen offset:1024 lds\n\t"
        "s_add_u32 s70, s70, 64512\n\t"
        "buffer_load_dwordx4 v8, %[rw], s70 offen offset:2048 lds\n\t"
        "s_add_u32 s70, s70, 64512\n\t"
        "buffer_load_dwordx4 v9, %[rw], s70 offen offset:3072 lds\n\t"
        "ds_read_b64_tr_b16 v[132:133], v4\n\t"
        "ds_read_b64_tr_b16 v[134:135], v4 offset:512\n\t"
        "ds_read_b64_tr_b16 v[148:149], v5\n\t"
        "ds_read_b64_tr_b16 v[150:151], v5 offset:512\n\t"
        "ds_read_b64_tr_b16 v[164:165], v6\n\t"
        "ds_read_b64_tr_b16 v[166:167], v6 offset:512\n\t"
        "ds_read_b64_tr_b16 v[180:181], v7\n\t"
        "ds_read_b64_tr_b16 v[182:183], v7 offset:512\n\t"
        "s_waitcnt lgkmcnt(0)\n\t"
        "v_add_u32 v4, 32768, v4\n\t"
        "v_add_u32 v5, 32768, v5\n\t"
        "v_add_u32 v6, 32768, v6\n\t"
        "v_add_u32 v7, 32768, v7\n\t"
        "v_add_u32 v14, 32768, v14\n\t"
        "v_add_u32 v15, 32768, v15\n\t"
        "v_add_u32 v16, 32768, v16\n\t"
        "v_add_u32 v17, 32768, v17\n\t"
        "s_waitcnt vmcnt(4)\n\t"
        "s_barrier\n\t"
        "s_lshl_b32 s68, %[wa], 20\n\t"
        "s_add_u32 m0, s69, 0\n\t"
        "s_xor_b32 s70, s68, 0xc0000\n\t"
        "buffer_load_dwordx4 v8, %[rw], s70 offen offset:0 lds\n\t"
        "s_add_u32 s70, s70, 64512\n\t"
        "buffer_load_dwordx4 v9, %[rw], s70 offen offset:1024 lds\n\t"
        "s_add_u32 s70, s70, 64512\n\t"
        "buffer_load_dwordx4 v8, %[rw], s70 offen offset:2048 lds\n\t"
        "s_add_u32 s70, s70, 64512\n\t"
        "buffer_load_dwordx4 v9, %[rw], s70 offen offset:3072 lds\n\t"
        "ds_read_b64_tr_b16 v[72:73], v4\n\t"
        "ds_read_b64_tr_b16 v[74:75], v4 offset:512\n\t"
        "ds_read_b64_tr_b16 v[88:89], v5\n\t"
        "ds_read_b64_tr_b16 v[90:91], v5 offset:512\n\t"
        "ds_read_b64_tr_b16 v[104:105], v6\n\t"
        "ds_read_b64_tr_b16 v[106:107], v6 offset:512\n\t"
        "ds_read_b64_tr_b16 v[120:121], v7\n\t"
        "ds_read_b64_tr_b16 v[122:123], v7 offset:512\n\t"
        "s_waitcnt lgkmcnt(0)\n\t"
        "v_add_u32 v4, 32768, v4\n\t"
        "v_add_u32 v5, 32768, v5\n\t"
        "v_add_u32 v6, 32768, v6\n\t"
        "v_add_u32 v7, 32768, v7\n\t"
        "v_add_u32 v14, 32768, v14\n\t"
        "v_add_u32 v15, 32768, v15\n\t"
        "v_add_u32 v16, 32768, v16\n\t"
        "v_add_u32 v17, 32768, v17\n\t"
        "s_waitcnt vmcnt(4)\n\t"
        "s_barrier\n\t"
        "s_lshl_b32 s68, %[wa], 20\n\t"
        "s_add_u32 m0, s69, 32768\n\t"
        "s_xor_b32 s70, s68, 0x4c0000\n\t"
        "buffer_load_dwordx4 v8, %[rw], s70 offen offset:0 lds\n\t"
        "s_add_u32 s70, s70, 64512\n\t"
        "buffer_load_dwordx4 v9, %[rw], s70 offen offset:1024 lds\n\t"
        "s_add_u32 s70, s70, 64512\n\t"
        "buffer_load_dwordx4 v8, %[rw], s70 offen offset:2048 lds\n\t"
        "s_add_u32 s70, s70, 64512\n\t"
        "buffer_load_dwordx4 v9, %[rw], s70 offen offset:3072 lds\n\t"
        "ds_read_b64_tr_b16 v[136:137], v4\n\t"
        "ds_read_b64_tr_b16 v[138:139], v4 offset:512\n\t"
        "ds_read_b64_tr_b16 v[152:153], v5\n\t"
        "ds_read_b64_tr_b16 v[154:155], v5 offset:512\n\t"
        "ds_read_b64_tr_b16 v[168:169], v6\n\t"
        "ds_read_b64_tr_b16 v[170:171], v6 offset:512\n\t"
        "ds_read_b64_tr_b16 v[184:185], v7\n\t"
        "ds_read_b64_tr_b16 v[186:187], v7 offset:512\n\t"
        "s_waitcnt lgkmcnt(0)\n\t"
        "v_add_u32 v4, -65536, v4\n\t"
        "v_add_u32 v5, -65536, v5\n\t"
        "v_add_u32 v6, -65536, v6\n\t"
        "v_add_u32 v7, -65536, v7\n\t"
        "v_add_u32 v14, -65536, v14\n\t"
        "v_add_u32 v15, -65536, v15\n\t"
        "v_add_u32 v16, -65536, v16\n\t"
        "v_add_u32 v17, -65536, v17\n\t"
        "s_waitcnt vmcnt(4)\n\t"
        "s_barrier\n\t"
        "s_mov_b32 s68, 0\n\t"
        "s_add_u32 m0, s69, 65536\n\t"
        "s_xor_b32 s70, s68, 0x0\n\t"
        "buffer_load_dwordx4 v12, %[rx], s70 offen offset:0 nt lds\n\t"
        "s_add_u32 s70, s70, 7168\n\t"
        "buffer_load_dwordx4 v13, %[rx], s70 offen offset:1024 nt lds\n\t"
        "s_add_u32 s70, s70, 7168\n\t"
        "buffer_load_dwordx4 v12, %[rx], s70 offen offset:2048 nt lds\n\t"
        "s_add_u32 s70, s70, 7168\n\t"
        "buffer_load_dwordx4 v13, %[rx], s70 offen offset:3072 nt lds\n\t"
        "ds_read_b64_tr_b16 v[76:77], v4\n\t"
        "ds_read_b64_tr_b16 v[78:79], v4 offset:512\n\t"
        "ds_read_b64_tr_b16 v[92:93], v5\n\t"
        "ds_read_b64_tr_b16 v[94:95], v5 offset:512\n\t"
        "ds_read_b64_tr_b16 v[108:109], v6\n\t"
        "ds_read_b64_tr_b16 v[110:111], v6 offset:512\n\t"
        "ds_read_b64_tr_b16 v[124:125], v7\n\t"
        "ds_read_b64_tr_b16 v[126:127], v7 offset:512\n\t"
        "s_waitcnt lgkmcnt(0)\n\t"
        "v_add_u32 v4, 32768, v4\n\t"
        "v_add_u32 v5, 32768, v5\n\t"
        "v_add_u32 v6, 32768, v6\n\t"
        "v_add_u32 v7, 32768, v7\n\t"
        "v_add_u32 v14, 32768, v14\n\t"
        "v_add_u32 v15, 32768, v15\n\t"
        "v_add_u32 v16, 32768, v16\n\t"
        "v_add_u32 v17, 32768, v17\n\t"
        "s_waitcnt vmcnt(4)\n\t"
        "s_barrier\n\t"
        "s_mov_b32 s68, 0\n\t"
        "s_add_u32 m0, s69, 0\n\t"
        "s_xor_b32 s70, s68, 0x10000\n\t"
        "buffer_load_dwordx4 v12, %[rx], s70 offen offset:0 nt lds\n\t"
        "s_add_u32 s70, s70, 7168\n\t"
        "buffer_load_dwordx4 v13, %[rx], s70 offen offset:1024 nt lds\n\t"
        "s_add_u32 s70, s70, 7168\n\t"
        "buffer_load_dwordx4 v12, %[rx], s70 offen offset:2048 nt lds\n\t"
        "s_add_u32 s70, s70, 7168\n\t"
        "buffer_load_dwordx4 v13, %[rx], s70 offen offset:3072 nt lds\n\t"
        "ds_read_b64_tr_b16 v[140:141], v4\n\t"
        "ds_read_b64_tr_b16 v[142:143], v4 offset:512\n\t"
        "ds_read_b64_tr_b16 v[156:157], v5\n\t"
        "ds_read_b64_tr_b16 v[158:159], v5 offset:512\n\t"
        "ds_read_b64_tr_b16 v[172:173], v6\n\t"
        "ds_read_b64_tr_b16 v[174:175], v6 offset:512\n\t"
        "ds_read_b64_tr_b16 v[188:189], v7\n\t"
        "ds_read_b64_tr_b16 v[190:191], v7 offset:512\n\t"
        "s_waitcnt lgkmcnt(0)\n\t"
        "v_add_u32 v4, 32768, v4\n\t"
        "v_add_u32 v5, 32768, v5\n\t"
        "v_add_u32 v6, 32768, v6\n\t"
        "v_add_u32 v7, 32768, v7\n\t"
        "v_add_u32 v14, 32768, v14\n\t"
        "v_add_u32 v15, 32768, v15\n\t"
        "v_add_u32 v16, 32768, v16\n\t"
        "v_add_u32 v17, 32768, v17\n\t"
        "s_waitcnt vmcnt(4)\n\t"
        "s_barrier\n\t"
        "s_mov_b32 s68, 0\n\t"
        "s_add_u32 m0, s69, 32768\n\t"
        "s_xor_b32 s70, s68, 0x20000\n\t"
        "buffer_load_dwordx4 v12, %[rx], s70 offen offset:0 nt lds\n\t"
        "s_add_u32 s70, s70, 7168\n\t"
        "buffer_load_dwordx4 v13, %[rx], s70 offen offset:1024 nt lds\n\t"
        "s_add_u32 s70, s70, 7168\n\t"
        "buffer_load_dwordx4 v12, %[rx], s70 offen offset:2048 nt lds\n\t"
        "s_add_u32 s70, s70, 7168\n\t"
        "buffer_load_dwordx4 v13, %[rx], s70 offen offset:3072 nt lds\n\t"
        "ds_read_b64_tr_b16 v[192:193], v14\n\t"
        "ds_read_b64_tr_b16 v[194:195], v14 offset:512\n\t"
        "ds_read_b64_tr_b16 v[208:209], v15\n\t"
        "ds_read_b64_tr_b16 v[210:211], v15 offset:512\n\t"
        "ds_read_b64_tr_b16 v[224:225], v16\n\t"
        "ds_read_b64_tr_b16 v[226:227], v16 offset:512\n\t"
        "ds_read_b64_tr_b16 v[240:241], v17\n\t"
        "ds_read_b64_tr_b16 v[242:243], v17 offset:512\n\t"
        "s_waitcnt lgkmcnt(0)\n\t"
        "v_add_u32 v4, -65536, v4\n\t"
        "v_add_u32 v5, -65536, v5\n\t"
        "v_add_u32 v6, -65536, v6\n\t"
        "v_add_u32 v7, -65536, v7\n\t"
        "v_add_u32 v14, -65536, v14\n\t"
        "v_add_u32 v15, -65536, v15\n\t"
        "v_add_u32 v16, -65536, v16\n\t"
        "v_add_u32 v17, -65536, v17\n\t"
        "s_waitcnt vmcnt(4)\n\t"
        "s_barrier\n\t"
        "s_mov_b32 s68, 0\n\t"
        "s_add_u32 m0, s69, 65536\n\t"
        "s_xor_b32 s70, s68, 0x30000\n\t"
        "buffer_load_dwordx4 v12, %[rx], s70 offen offset:0 nt lds\n\t"
        "s_add_u32 s70, s70, 7168\n\t"
        "buffer_load_dwordx4 v13, %[rx], s70 offen offset:1024 nt lds\n\t"
        "s_add_u32 s70, s70, 7168\n\t"
        "buffer_load_dwordx4 v12, %[rx], s70 offen offset:2048 nt lds\n\t"
        "s_add_u32 s70, s70, 7168\n\t"
        "buffer_load_dwordx4 v13, %[rx], s70 offen offset:3072 nt lds\n\t"
        "ds_read_b64_tr_b16 v[196:197], v14\n\t"
        "ds_read_b64_tr_b16 v[198:199], v14 offset:512\n\t"
        "ds_read_b64_tr_b16 v[212:213], v15\n\t"
        "ds_read_b64_tr_b16 v[214:215], v15 offset:512\n\t"
        "ds_read_b64_tr_b16 v[228:229], v16\n\t"
        "ds_read_b64_tr_b16 v[230:231], v16 offset:512\n\t"
        "ds_read_b64_tr_b16 v[244:245], v17\n\t"
        "ds_read_b64_tr_b16 v[246:247], v17 offset:512\n\t"
        "s_waitcnt lgkmcnt(0)\n\t"
        "v_add_u32 v4, 32768, v4\n\t"
        "v_add_u32 v5, 32768, v5\n\t"
        "v_add_u32 v6, 32768, v6\n\t"
        "v_add_u32 v7, 32768, v7\n\t"
        "v_add_u32 v14, 32768, v14\n\t"
        "v_add_u32 v15, 32768, v15\n\t"
        "v_add_u32 v16, 32768, v16\n\t"
        "v_add_u32 v17, 32768, v17\n\t"
        "s_waitcnt vmcnt(4)\n\t"
        "s_barrier\n\t"
        "ds_read_b64_tr_b16 v[200:201], v14\n\t"
        "ds_read_b64_tr_b16 v[202:203], v14 offset:512\n\t"
        "ds_read_b64_tr_b16 v[216:217], v15\n\t"
        "ds_read_b64_tr_b16 v[218:219], v15 offset:512\n\t"
        "ds_read_b64_tr_b16 v[232:233], v16\n\t"
        "ds_read_b64_tr_b16 v[234:235], v16 offset:512\n\t"
        "ds_read_b64_tr_b16 v[248:249], v17\n\t"
        "ds_read_b64_tr_b16 v[250:251], v17 offset:512\n\t"
        "s_waitcnt lgkmcnt(0)\n\t"
        "v_add_u32 v4, 32768, v4\n\t"
        "v_add_u32 v5, 32768, v5\n\t"
        "v_add_u32 v6, 32768, v6\n\t"
        "v_add_u32 v7, 32768, v7\n\t"
        "v_add_u32 v14, 32768, v14\n\t"
        "v_add_u32 v15, 32768, v15\n\t"
        "v_add_u32 v16, 32768, v16\n\t"
        "v_add_u32 v17, 32768, v17\n\t"
        "s_waitcnt vmcnt(0)\n\t"
        "s_barrier\n\t"
        "ds_read_b64_tr_b16 v[204:205], v14\n\t"
        "ds_read_b64_tr_b16 v[206:207], v14 offset:512\n\t"
        "ds_read_b64_tr_b16 v[220:221], v15\n\t"
        "ds_read_b64_tr_b16 v[222:223], v15 offset:512\n\t"
        "ds_read_b64_tr_b16 v[236:237], v16\n\t"
        "ds_read_b64_tr_b16 v[238:239], v16 offset:512\n\t"
        "ds_read_b64_tr_b16 v[252:253], v17\n\t"
        "ds_read_b64_tr_b16 v[254:255], v17 offset:512\n\t"
        "s_waitcnt lgkmcnt(0)\n\t"
        "v_add_u32 v4, -65536, v4\n\t"
        "v_add_u32 v5, -65536, v5\n\t"
        "v_add_u32 v6, -65536, v6\n\t"
        "v_add_u32 v7, -65536, v7\n\t"
        "v_add_u32 v14, -65536, v14\n\t"
        "v_add_u32 v15, -65536, v15\n\t"
        "v_add_u32 v16, -65536, v16\n\t"
        "v_add_u32 v17, -65536, v17\n\t"
        "v_mbcnt_lo_u32_b32 v52, -1, 0\n\t"
        "v_mbcnt_hi_u32_b32 v52, -1, v52\n\t"
        "s_lshr_b32 s72, %[w], 1\n\t"
        "v_and_b32 v55, 3, v52\n\t"
        "v_lshrrev_b32 v53, 4, v52\n\t"
        "v_lshl_add_u32 v55, v55, 2, v53\n\t"
        "v_bfe_u32 v53, v52, 3, 1\n\t"
        "v_xor_b32 v55, v55, v53\n\t"
        "s_and_b32 s70, s72, 1\n\t"
        "s_lshl_b32 s70, s70, 1\n\t"
        "v_xor_b32 v55, s70, v55\n\t"
        "v_lshlrev_b32 v55, 3, v55\n\t"
        "v_bfe_u32 v54, v52, 2, 2\n\t"
        "v_add_u32 v53, v53, v54\n\t"
        "v_lshl_add_u32 v55, v53, 7, v55\n\t"
        "v_and_b32 v53, 15, v52\n\t"
        "v_lshrrev_b32 v54, 4, v52\n\t"
        "v_xor_b32 v3, v53, v54\n\t"
        "v_lshlrev_b32 v3, 3, v3\n\t"
        "v_mul_u32_u24 v56, 384, v54\n\t"
        "v_add_u32 v3, v3, v56\n\t"
        "v_add_u32 v3, 131072, v3\n\t"
        "s_mul_i32 s70, s72, 768\n\t"
        "s_and_b32 s71, %[w], 1\n\t"
        "s_lshl_b32 s71, s71, 1\n\t"
        "s_add_u32 s70, s70, s71\n\t"
        "s_add_u32 s70, s70, 131072\n\t"
        "v_add_u32 v55, s70, v55\n\t"
        "v_bfe_u32 v56, v0, 4, 1\n\t"
        "v_lshlrev_b32 v56, 7, v56\n\t"
        "v_bfe_u32 v57, v0, 6, 2\n\t"
        "v_lshl_add_u32 v56, v57, 5, v56\n\t"
        "v_bfe_u32 v57, v0, 5, 1\n\t"
        "v_lshl_add_u32 v56, v57, 4, v56\n\t"
        "v_lshl_add_u32 v56, v54, 8, v56\n\t"
        "v_lshlrev_b32 v57, 4, v53\n\t"
        "v_lshl_add_u32 v58, v54, 14, v57\n\t"
        "v_lshl_add_u32 v57, v54, 2, v57\n\t"
        "s_barrier\n\t"
        "v_mov_b64 v[4:5], 0\n\t"
        "v_mov_b64 v[6:7], 0\n\t"
        "v_xor_b32 v8, 0x800, v2\n\t"
        "v_xor_b32 v9, 0x1000, v2\n\t"
        "v_xor_b32 v10, 0x1800, v2\n\t"
        "v_xor_b32 v11, 0x2000, v2\n\t"
        "v_xor_b32 v12, 0x2800, v2\n\t"
        "v_xor_b32 v13, 0x3000, v2\n\t"
        "v_xor_b32 v14, 0x3800, v2\n\t"
        "v_mov_b32 v36, 128\n\t"
        "buffer_load_dwordx2 v[36:37], v36, %[rf], 0 offen sc1\n\t"
        ".Lv_u0r_%=:\n\t"
        "s_waitcnt vmcnt(1)\n\t"
        "ds_write_b128 v2, v[20:23] offset:0\n\t"
        "v_smfmac_f32_16x16x64_bf16 v[4:7], v[20:23], v[64:71], v1\n\t"
        "v_max_u32 v54, v20, v21\n\t"
        "v_max3_u32 v54, v54, v22, v23\n\t"
        "s_waitcnt vmcnt(0)\n\t"
        "ds_write_b128 v2, v[24:27] offset:1024\n\t"
        "v_max3_u32 v54, v54, v24, v25\n\t"
        "v_max3_u32 v54, v54, v26, v27\n\t"
        "v_smfmac_f32_16x16x64_bf16 v[4:7], v[24:27], v[72:79], v1\n\t"
        "v_cmp_eq_u32 vcc, 0xffbfffbf, v54\n\t"
        "s_waitcnt lgkmcnt(0)\n\t"
        "s_cbranch_vccnz .Lv_u0f_%=\n\t"
        ".Lv_u0k_%=:\n\t"
        "s_barrier\n\t"
        "ds_read_b128 v[28:31], v8 offset:0\n\t"
        "ds_read_b128 v[32:35], v8 offset:1024\n\t"
        "ds_read_b128 v[20:23], v9 offset:0\n\t"
        "ds_read_b128 v[24:27], v9 offset:1024\n\t"
        "s_waitcnt lgkmcnt(3)\n\t"
        "v_smfmac_f32_16x16x64_bf16 v[4:7], v[28:31], v[80:87], v1\n\t"
        "ds_read_b128 v[28:31], v10 offset:0\n\t"
        "s_waitcnt lgkmcnt(3)\n\t"
        "v_smfmac_f32_16x16x64_bf16 v[4:7], v[32:35], v[88:95], v1\n\t"
        "ds_read_b128 v[32:35], v10 offset:1024\n\t"
        "s_waitcnt lgkmcnt(3)\n\t"
        "v_smfmac_f32_16x16x64_bf16 v[4:7], v[20:23], v[96:103], v1\n\t"
        "ds_read_b128 v[20:23], v11 offset:0\n\t"
        "s_waitcnt lgkmcnt(3)\n\t"
        "v_smfmac_f32_16x16x64_bf16 v[4:7], v[24:27], v[104:111], v1\n\t"
        "ds_read_b128 v[24:27], v11 offset:1024\n\t"
        "s_waitcnt lgkmcnt(3)\n\t"
        "v_smfmac_f32_16x16x64_bf16 v[4:7], v[28:31], v[112:119], v1\n\t"
        "ds_read_b128 v[28:31], v12 offset:0\n\t"
        "s_waitcnt lgkmcnt(3)\n\t"
        "v_smfmac_f32_16x16x64_bf16 v[4:7], v[32:35], v[120:127], v1\n\t"
        "ds_read_b128 v[32:35], v12 offset:1024\n\t"
        "s_waitcnt lgkmcnt(3)\n\t"
        "v_smfmac_f32_16x16x64_bf16 v[4:7], v[20:23], v[128:135], v1\n\t"
        "ds_read_b128 v[20:23], v13 offset:0\n\t"
        "s_waitcnt lgkmcnt(3)\n\t"
        "v_smfmac_f32_16x16x64_bf16 v[4:7], v[24:27], v[136:143], v1\n\t"
        "ds_read_b128 v[24:27], v13 offset:1024\n\t"
        "s_waitcnt lgkmcnt(3)\n\t"
        "v_smfmac_f32_16x16x64_bf16 v[4:7], v[28:31], v[144:151], v1\n\t"
        "ds_read_b128 v[28:31], v14 offset:0\n\t"
        "s_waitcnt lgkmcnt(3)\n\t"
        "v_smfmac_f32_16x16x64_bf16 v[4:7], v[32:35], v[152:159], v1\n\t"
        "ds_read_b128 v[32:35], v14 offset:1024\n\t"
        "s_waitcnt lgkmcnt(3)\n\t"
        "v_smfmac_f32_16x16x64_bf16 v[4:7], v[20:23], v[160:167], v1\n\t"
        "s_waitcnt lgkmcnt(2)\n\t"
        "v_smfmac_f32_16x16x64_bf16 v[4:7], v[24:27], v[168:175], v1\n\t"
        "s_waitcnt lgkmcnt(1)\n\t"
        "v_smfmac_f32_16x16x64_bf16 v[4:7], v[28:31], v[176:183], v1\n\t"
        "s_waitcnt lgkmcnt(0)\n\t"
        "v_smfmac_f32_16x16x64_bf16 v[4:7], v[32:35], v[184:191], v1\n\t"
        "v_mov_b64 v[8:9], 0\n\t"
        "v_mov_b64 v[10:11], 0\n\t"
        "v_mov_b64 v[12:13], 0\n\t"
        "v_mov_b64 v[14:15], 0\n\t"
        "v_mov_b64 v[16:17], 0\n\t"
        "v_mov_b64 v[18:19], 0\n\t"
        "s_nop 1\n\t"
        "v_add_f32 v4, v4, v5\n\t"
        "v_add_f32 v6, v6, v7\n\t"
        "v_cvt_pk_bf16_f32 v52, v4, v6\n\t"
        "ds_write_b16 v55, v52\n\t"
        "ds_write_b16_d16_hi v55, v52 offset:4\n\t"
        "v_mov_b64 v[4:5], 0\n\t"
        "v_mov_b64 v[6:7], 0\n\t"
        "s_waitcnt lgkmcnt(0)\n\t"
        "s_barrier\n\t"
        "ds_read_b64_tr_b16 v[20:21], v3 offset:0\n\t"
        "ds_read_b64_tr_b16 v[22:23], v3 offset:128\n\t"
        "ds_read_b64_tr_b16 v[24:25], v3 offset:1536\n\t"
        "ds_read_b64_tr_b16 v[26:27], v3 offset:1664\n\t"
        "s_waitcnt lgkmcnt(2)\n\t"
        "v_smfmac_f32_16x16x64_bf16 v[4:7], v[20:23], v[192:199], v1\n\t"
        "s_waitcnt lgkmcnt(0)\n\t"
        "v_smfmac_f32_16x16x64_bf16 v[4:7], v[24:27], v[200:207], v1\n\t"
        "v_smfmac_f32_16x16x64_bf16 v[12:15], v[20:23], v[224:231], v1\n\t"
        "v_smfmac_f32_16x16x64_bf16 v[12:15], v[24:27], v[232:239], v1\n\t"
        "v_smfmac_f32_16x16x64_bf16 v[8:11], v[20:23], v[208:215], v1\n\t"
        "v_smfmac_f32_16x16x64_bf16 v[8:11], v[24:27], v[216:223], v1\n\t"
        "v_smfmac_f32_16x16x64_bf16 v[16:19], v[20:23], v[240:247], v1\n\t"
        "v_smfmac_f32_16x16x64_bf16 v[16:19], v[24:27], v[248:255], v1\n\t"
        "s_nop 1\n\t"
        "v_add_f32 v4, v4, v5\n\t"
        "v_add_f32 v6, v6, v7\n\t"
        "v_cvt_pk_bf16_f32 v60, v4, v6\n\t"
        "v_add_f32 v12, v12, v13\n\t"
        "v_add_f32 v14, v14, v15\n\t"
        "v_cvt_pk_bf16_f32 v62, v12, v14\n\t"
        "s_nop 1\n\t"
        "v_add_f32 v8, v8, v9\n\t"
        "v_add_f32 v10, v10, v11\n\t"
        "v_cvt_pk_bf16_f32 v61, v8, v10\n\t"
        "v_permlane32_swap_b32 v60, v62\n\t"
        "v_add_f32 v16, v16, v17\n\t"
        "v_add_f32 v18, v18, v19\n\t"
        "v_cvt_pk_bf16_f32 v63, v16, v18\n\t"
        "s_nop 1\n\t"
        "v_permlane32_swap_b32 v61, v63\n\t"
        "s_nop 1\n\t"
        "v_permlane16_swap_b32 v60, v61\n\t"
        "v_permlane16_swap_b32 v62, v63\n\t"
        "s_memtime s[92:93]\n\t"
        "s_memrealtime s[94:95]\n\t"
        "s_mov_b32 s98, 0x3d380000\n\t"
        "s_waitcnt lgkmcnt(0)\n\t"
        "s_sub_u32 s96, s92, 4500\n\t"
        "s_sub_u32 s97, s94, 200\n\t"
        "v_readfirstlane_b32 s68, v36\n\t"
        "v_readfirstlane_b32 s69, v37\n\t"
        "s_xor_b32 s68, s68, s54\n\t"
        "s_xor_b32 s69, s69, s55\n\t"
        "s_lshr_b32 s70, s68, 8\n\t"
        "s_or_b32 s70, s70, s69\n\t"
        "s_cmp_eq_u32 s70, 0\n\t"
        "s_cselect_b32 s68, s68, 0\n\t"
        "s_and_b32 s69, s68, 127\n\t"
        "s_max_u32 s51, s51, s69\n\t"
        "s_cmp_ge_u32 s51, 1\n\t"
        "s_cbranch_scc0 .Lv_f0p_%=\n\t"
        ".Lv_f0k_%=:\n\t"
        "s_lshr_b32 s48, s68, 7\n\t"
        "s_lshl_b32 s48, s48, 13\n\t"
        "s_lshl_b32 s70, %[cv], 13\n\t"
        "s_xor_b32 s70, s70, s48\n\t"
        "s_lshr_b32 s71, %[wa], 2\n\t"
        "s_lshl_b32 s71, s71, 14\n\t"
        "s_and_b32 s72, %[wa], 3\n\t"
        "s_lshl_b32 s73, s72, 11\n\t"
        "s_add_u32 s71, s71, s73\n\t"
        "s_add_u32 s47, s71, %[ra]\n\t"
        "s_add_u32 s47, s47, s70\n\t"
        "s_xor_b32 s47, s47, 0x2000\n\t"
        "s_sub_u32 s47, s47, 2097152\n\t"
        "s_xor_b32 s49, %[ov], s48\n\t"
        "s_xor_b32 s50, %[om], s48\n\t"
        "s_xor_b32 s52, %[oy], s48\n\t"
        "s_mov_b32 s53, 0\n\t"
        "s_lshl_b32 s67, %[w], 10\n\t"
        "s_add_u32 s67, s67, 98304\n\t"
        "s_mov_b32 s60, 0\n\t"
        "s_mov_b32 s61, %[of]\n\t"
        "v_lshlrev_b32 v57, 3, v0\n\t"
        "s_mov_b32 s62, 0x7f000000\n\t"
        "s_mov_b32 s63, 0x7f000000\n\t"
        "s_mov_b32 s64, 0x7f000000\n\t"
        "s_mov_b32 s65, 0x7f000000\n\t"
        "s_getpc_b64 s[56:57]\n\t"
        ".Lv_pc_%=:\n\t"
        "s_add_u32 s56, s56, .Lv_yx_%=-.Lv_pc_%=\n\t"
        "s_addc_u32 s57, s57, 0\n\t"
        "s_add_u32 s71, s60, 1\n\t"
        "s_min_u32 s71, s71, 64\n\t"
        "s_cmp_ge_u32 s51, s71\n\t"
        "s_cbranch_scc0 .Lv_g1p_%=\n\t"
        ".Lv_g1k_%=:\n\t"
        "s_and_b32 s70, s60, 3\n\t"
        "s_lshl_b32 s70, s70, 13\n\t"
        "s_add_u32 m0, s70, s67\n\t"
        "s_cmp_lt_u32 s60, 64\n\t"
        "s_cselect_b32 s70, s49, 0x7f000000\n\t"
        "buffer_load_dwordx4 v0, %[rs], s70 offen sc1 lds\n\t"
        "s_add_u32 s60, s60, 1\n\t"
        "s_add_u32 s49, s49, 0x80000\n\t"
        "s_and_b32 s70, s60, 3\n\t"
        "s_cselect_b32 s70, 0, 0x2000\n\t"
        "s_xor_b32 s49, s49, s70\n\t"
        "s_branch .Lv_ym_%=\n\t"
        ".p2align 6\n\t"
        ".Lv_loop_%=:\n\t"
        "s_barrier\n\t"
        "buffer_load_dwordx4 v[20:23], v0, %[rs], s47 offen sc1\n\t"
        "s_nop 4\n\t"
        "buffer_load_dwordx4 v[24:27], v0, %[rs], s47 offen offset:1024 sc1\n\t"
        ".Lv_v0r_%=:\n\t"
        "s_waitcnt vmcnt(1)\n\t"
        "ds_write_b128 v2, v[20:23] offset:0\n\t"
        "v_smfmac_f32_16x16x64_bf16 v[4:7], v[20:23], v[64:71], v1\n\t"
        "v_max_u32 v54, v20, v21\n\t"
        "v_max3_u32 v54, v54, v22, v23\n\t"
        "s_waitcnt vmcnt(0)\n\t"
        "ds_write_b128 v2, v[24:27] offset:1024\n\t"
        "v_max3_u32 v54, v54, v24, v25\n\t"
        "v_max3_u32 v54, v54, v26, v27\n\t"
        "v_smfmac_f32_16x16x64_bf16 v[4:7], v[24:27], v[72:79], v1\n\t"
        "v_cmp_eq_u32 vcc, 0xffbfffbf, v54\n\t"
        "s_waitcnt lgkmcnt(0)\n\t"
        "s_cbranch_vccnz .Lv_v0f_%=\n\t"
        ".Lv_v0k_%=:\n\t"
        "s_barrier\n\t"
        "ds_read_b128 v[28:31], v8 offset:0\n\t"
        "ds_read_b128 v[32:35], v8 offset:1024\n\t"
        "ds_read_b128 v[20:23], v9 offset:0\n\t"
        "ds_read_b128 v[24:27], v9 offset:1024\n\t"
        "s_waitcnt lgkmcnt(3)\n\t"
        "v_smfmac_f32_16x16x64_bf16 v[4:7], v[28:31], v[80:87], v1\n\t"
        "ds_read_b128 v[28:31], v10 offset:0\n\t"
        "s_waitcnt lgkmcnt(3)\n\t"
        "v_smfmac_f32_16x16x64_bf16 v[4:7], v[32:35], v[88:95], v1\n\t"
        "ds_read_b128 v[32:35], v10 offset:1024\n\t"
        "s_waitcnt lgkmcnt(3)\n\t"
        "v_smfmac_f32_16x16x64_bf16 v[4:7], v[20:23], v[96:103], v1\n\t"
        "ds_read_b128 v[20:23], v11 offset:0\n\t"
        "s_waitcnt lgkmcnt(3)\n\t"
        "v_smfmac_f32_16x16x64_bf16 v[4:7], v[24:27], v[104:111], v1\n\t"
        "ds_read_b128 v[24:27], v11 offset:1024\n\t"
        "s_waitcnt lgkmcnt(3)\n\t"
        "v_smfmac_f32_16x16x64_bf16 v[4:7], v[28:31], v[112:119], v1\n\t"
        "ds_read_b128 v[28:31], v12 offset:0\n\t"
        "s_waitcnt lgkmcnt(3)\n\t"
        "v_smfmac_f32_16x16x64_bf16 v[4:7], v[32:35], v[120:127], v1\n\t"
        "ds_read_b128 v[32:35], v12 offset:1024\n\t"
        "s_waitcnt lgkmcnt(3)\n\t"
        "v_smfmac_f32_16x16x64_bf16 v[4:7], v[20:23], v[128:135], v1\n\t"
        "ds_read_b128 v[20:23], v13 offset:0\n\t"
        "s_waitcnt lgkmcnt(3)\n\t"
        "v_smfmac_f32_16x16x64_bf16 v[4:7], v[24:27], v[136:143], v1\n\t"
        "ds_read_b128 v[24:27], v13 offset:1024\n\t"
        "s_waitcnt lgkmcnt(3)\n\t"
        "v_smfmac_f32_16x16x64_bf16 v[4:7], v[28:31], v[144:151], v1\n\t"
        "ds_read_b128 v[28:31], v14 offset:0\n\t"
        "s_waitcnt lgkmcnt(3)\n\t"
        "v_smfmac_f32_16x16x64_bf16 v[4:7], v[32:35], v[152:159], v1\n\t"
        "ds_read_b128 v[32:35], v14 offset:1024\n\t"
        "s_waitcnt lgkmcnt(3)\n\t"
        "v_smfmac_f32_16x16x64_bf16 v[4:7], v[20:23], v[160:167], v1\n\t"
        "s_waitcnt lgkmcnt(2)\n\t"
        "v_smfmac_f32_16x16x64_bf16 v[4:7], v[24:27], v[168:175], v1\n\t"
        "s_waitcnt lgkmcnt(1)\n\t"
        "v_smfmac_f32_16x16x64_bf16 v[4:7], v[28:31], v[176:183], v1\n\t"
        "s_waitcnt lgkmcnt(0)\n\t"
        "v_smfmac_f32_16x16x64_bf16 v[4:7], v[32:35], v[184:191], v1\n\t"
        "v_mov_b64 v[8:9], 0\n\t"
        "v_mov_b64 v[10:11], 0\n\t"
        "v_mov_b64 v[12:13], 0\n\t"
        "v_mov_b64 v[14:15], 0\n\t"
        "v_mov_b64 v[16:17], 0\n\t"
        "v_mov_b64 v[18:19], 0\n\t"
        "s_nop 1\n\t"
        "v_add_f32 v4, v4, v5\n\t"
        "v_add_f32 v6, v6, v7\n\t"
        "v_cvt_pk_bf16_f32 v52, v4, v6\n\t"
        "ds_write_b16 v55, v52\n\t"
        "ds_write_b16_d16_hi v55, v52 offset:4\n\t"
        "v_mov_b64 v[4:5], 0\n\t"
        "v_mov_b64 v[6:7], 0\n\t"
        "s_waitcnt lgkmcnt(0)\n\t"
        "s_barrier\n\t"
        "ds_read_b64_tr_b16 v[20:21], v3 offset:0\n\t"
        "ds_read_b64_tr_b16 v[22:23], v3 offset:128\n\t"
        "ds_read_b64_tr_b16 v[24:25], v3 offset:1536\n\t"
        "ds_read_b64_tr_b16 v[26:27], v3 offset:1664\n\t"
        "s_waitcnt lgkmcnt(2)\n\t"
        "v_smfmac_f32_16x16x64_bf16 v[4:7], v[20:23], v[192:199], v1\n\t"
        "s_waitcnt lgkmcnt(0)\n\t"
        "v_smfmac_f32_16x16x64_bf16 v[4:7], v[24:27], v[200:207], v1\n\t"
        "v_smfmac_f32_16x16x64_bf16 v[12:15], v[20:23], v[224:231], v1\n\t"
        "v_smfmac_f32_16x16x64_bf16 v[12:15], v[24:27], v[232:239], v1\n\t"
        "v_smfmac_f32_16x16x64_bf16 v[8:11], v[20:23], v[208:215], v1\n\t"
        "v_smfmac_f32_16x16x64_bf16 v[8:11], v[24:27], v[216:223], v1\n\t"
        "v_smfmac_f32_16x16x64_bf16 v[16:19], v[20:23], v[240:247], v1\n\t"
        "v_smfmac_f32_16x16x64_bf16 v[16:19], v[24:27], v[248:255], v1\n\t"
        "s_nop 1\n\t"
        "v_add_f32 v4, v4, v5\n\t"
        "v_add_f32 v6, v6, v7\n\t"
        "v_cvt_pk_bf16_f32 v60, v4, v6\n\t"
        "v_add_f32 v12, v12, v13\n\t"
        "v_add_f32 v14, v14, v15\n\t"
        "v_cvt_pk_bf16_f32 v62, v12, v14\n\t"
        "s_nop 1\n\t"
        "v_add_f32 v8, v8, v9\n\t"
        "v_add_f32 v10, v10, v11\n\t"
        "v_cvt_pk_bf16_f32 v61, v8, v10\n\t"
        "v_permlane32_swap_b32 v60, v62\n\t"
        "v_add_f32 v16, v16, v17\n\t"
        "v_add_f32 v18, v18, v19\n\t"
        "v_cvt_pk_bf16_f32 v63, v16, v18\n\t"
        "s_nop 1\n\t"
        "v_permlane32_swap_b32 v61, v63\n\t"
        "s_nop 1\n\t"
        "v_permlane16_swap_b32 v60, v61\n\t"
        "v_permlane16_swap_b32 v62, v63\n\t"
        ".Lv_ym_%=:\n\t"
        "buffer_store_dwordx4 v[60:63], v58, %[rs], s50 offen sc1\n\t"
        "s_lshr_b32 s71, s50, 21\n\t"
        "s_lshl_b32 s71, s71, 21\n\t"
        "s_and_b32 s70, s45, 1\n\t"
        "s_lshl_b32 s70, s70, 13\n\t"
        "s_xor_b32 s70, s70, s48\n\t"
        "s_add_u32 s71, s71, s70\n\t"
        "s_cmp_eq_u32 %[w], 0\n\t"
        "s_cselect_b32 s77, s71, 0x70000000\n\t"
        "s_cmp_eq_u32 s45, 0\n\t"
        "s_cbranch_scc1 .Lv_early_%=\n\t"
        "s_nop 15\n\t"
        "s_nop 15\n\t"
        "s_nop 15\n\t"
        "s_nop 1\n\t"
        "s_xor_b32 s76, s77, %[tw]\n\t"
        "v_and_b32 v52, 0x1f0, v0\n\t"
        "v_lshlrev_b32 v52, 1, v52\n\t"
        "buffer_load_dword v54, v52, %[rs], s76 offen sc1\n\t"
        "s_cmp_eq_u32 s45, 1\n\t"
        "s_cbranch_scc1 .Lv_second_%=\n\t"
        "s_cmp_eq_u32 s45, 2\n\t"
        "s_cbranch_scc1 .Lv_second_%=\n\t"
        ".Lv_third_%=:\n\t"
        "s_add_u32 s71, s60, 1\n\t"
        "s_min_u32 s71, s71, 64\n\t"
        "s_cmp_ge_u32 s51, s71\n\t"
        "s_cbranch_scc0 .Lv_g2p_%=\n\t"
        ".Lv_g2k_%=:\n\t"
        "s_and_b32 s70, s60, 3\n\t"
        "s_lshl_b32 s70, s70, 13\n\t"
        "s_add_u32 m0, s70, s67\n\t"
        "s_cmp_lt_u32 s60, 64\n\t"
        "s_cselect_b32 s70, s49, 0x7f000000\n\t"
        "buffer_load_dwordx4 v0, %[rs], s70 offen sc1 lds\n\t"
        "s_add_u32 s60, s60, 1\n\t"
        "s_add_u32 s49, s49, 0x80000\n\t"
        "s_and_b32 s70, s60, 3\n\t"
        "s_cselect_b32 s70, 0, 0x2000\n\t"
        "s_xor_b32 s49, s49, s70\n\t"
        "s_add_u32 s71, s60, 1\n\t"
        "s_min_u32 s71, s71, 64\n\t"
        "s_add_u32 s70, s71, 3\n\t"
        "s_andn2_b32 s70, s70, 3\n\t"
        "s_cmp_le_u32 s71, 16\n\t"
        "s_cselect_b32 s70, s71, s70\n\t"
        "s_lshl_b32 s70, s70, 7\n\t"
        "v_mov_b32 v62, s70\n\t"
        "s_cmp_lt_u32 s51, s71\n\t"
        "s_cselect_b32 s70, 0, 0x70000000\n\t"
        "buffer_load_dwordx2 v[62:63], v62, %[rf], s70 offen sc1\n\t"
        "s_swappc_b64 s[58:59], s[56:57]\n\t"
        "s_waitcnt vmcnt(4)\n\t"
        "v_readfirstlane_b32 s68, v62\n\t"
        "v_readfirstlane_b32 s69, v63\n\t"
        "s_xor_b32 s68, s68, s54\n\t"
        "s_xor_b32 s69, s69, s55\n\t"
        "s_lshr_b32 s70, s68, 8\n\t"
        "s_or_b32 s70, s70, s69\n\t"
        "s_cmp_eq_u32 s70, 0\n\t"
        "s_cselect_b32 s68, s68, 0\n\t"
        "s_and_b32 s69, s68, 127\n\t"
        "s_max_u32 s51, s51, s69\n\t"
        ".Lv_eg_%=:\n\t"
        "v_xor_b32 v8, 0x800, v2\n\t"
        "v_xor_b32 v9, 0x1000, v2\n\t"
        "v_xor_b32 v10, 0x1800, v2\n\t"
        "v_xor_b32 v11, 0x2000, v2\n\t"
        "v_xor_b32 v12, 0x2800, v2\n\t"
        "v_xor_b32 v13, 0x3000, v2\n\t"
        "v_xor_b32 v14, 0x3800, v2\n\t"
        "v_mov_b64 v[4:5], 0\n\t"
        "v_mov_b64 v[6:7], 0\n\t"
        "s_add_u32 s45, s45, 1\n\t"
        "s_add_u32 s47, s47, 2097152\n\t"
        "s_and_b32 s47, s47, 0xffffff\n\t"
        "s_xor_b32 s47, s47, 0x2000\n\t"
        "s_add_u32 s50, s50, 2097152\n\t"
        "s_and_b32 s50, s50, 0xffffff\n\t"
        "s_xor_b32 s50, s50, 0x2000\n\t"
        "s_cmp_eq_u32 %[w], 0\n\t"
        "s_cbranch_scc0 .Lv_nw_%=\n\t"
        "s_sub_u32 s68, s92, s96\n\t"
        "s_sub_u32 s69, s94, s97\n\t"
        "s_mov_b32 s96, s92\n\t"
        "s_mov_b32 s97, s94\n\t"
        "v_cvt_f32_u32 v61, s68\n\t"
        "v_cvt_f32_u32 v60, s69\n\t"
        "v_rcp_f32 v61, v61\n\t"
        "s_nop 0\n\t"
        "v_mul_f32 v61, v61, v60\n\t"
        "v_max_f32 v61, 0x3d2aaaab, v61\n\t"
        "v_min_f32 v61, 0x3d638e39, v61\n\t"
        "v_subrev_f32 v61, s98, v61\n\t"
        "v_mov_b32 v60, s98\n\t"
        "v_fmac_f32 v60, 0x3f000000, v61\n\t"
        "v_mul_f32 v61, 0x44898000, v60\n\t"
        "v_cvt_u32_f32 v61, v61\n\t"
        "v_readfirstlane_b32 s98, v60\n\t"
        ".Lv_twm_%=:\n\t"
        "s_waitcnt vmcnt(8)\n\t"
        "v_cmp_eq_u32 s[68:69], 0, v54\n\t"
        "v_max_u32_dpp v54, v54, v54 row_shr:1 row_mask:0xf bank_mask:0xf\n\t"
        "s_nop 1\n\t"
        "v_max_u32_dpp v54, v54, v54 row_shr:2 row_mask:0xf bank_mask:0xf\n\t"
        "s_nop 1\n\t"
        "v_max_u32_dpp v54, v54, v54 row_shr:4 row_mask:0xf bank_mask:0xf\n\t"
        "s_nop 1\n\t"
        "v_max_u32_dpp v54, v54, v54 row_shr:8 row_mask:0xf bank_mask:0xf\n\t"
        "s_nop 1\n\t"
        "v_max_u32_dpp v54, v54, v54 row_bcast:15 row_mask:0xf bank_mask:0xf\n\t"
        "s_nop 1\n\t"
        "v_readlane_b32 s70, v54, 63\n\t"
        "s_cmp_lg_u64 s[68:69], 0\n\t"
        "v_readfirstlane_b32 s68, v61\n\t"
        "s_cbranch_scc1 .Lv_twx_%=\n\t"
        ".Lv_twy_%=:\n\t"
        "s_add_i32 s70, s70, 28\n\t"
        "s_add_u32 s76, s70, s68\n\t"
        ".Lv_twa_%=:\n\t"
        "s_memtime s[92:93]\n\t"
        "s_memrealtime s[94:95]\n\t"
        "s_waitcnt lgkmcnt(0)\n\t"
        "s_sub_i32 s68, s76, s94\n\t"
        "s_sub_u32 s69, s68, 5\n\t"
        "s_cmp_lt_u32 s69, 400\n\t"
        "s_cbranch_scc1 .Lv_twa_%=\n\t"
        "s_cmp_lt_i32 s68, 1\n\t"
        "s_cbranch_scc1 .Lv_twb_%=\n\t"
        "s_nop 2\n\t"
        "s_cmp_lt_i32 s68, 2\n\t"
        "s_cbranch_scc1 .Lv_twb_%=\n\t"
        "s_nop 2\n\t"
        "s_cmp_lt_i32 s68, 3\n\t"
        "s_cbranch_scc1 .Lv_twb_%=\n\t"
        "s_nop 2\n\t"
        "s_cmp_lt_i32 s68, 4\n\t"
        "s_cbranch_scc1 .Lv_twb_%=\n\t"
        "s_nop 2\n\t"
        "s_branch .Lv_twb_%=\n\t"
        ".Lv_twb_%=:\n\t"
        ".Lv_nw_%=:\n\t"
        "s_cmp_lt_u32 s45, 64\n\t"
        "s_cbranch_scc1 .Lv_loop_%=\n\t"
        "s_branch .Lv_end_%=\n\t"
        ".Lv_early_%=:\n\t"
        "s_add_u32 s71, s60, 1\n\t"
        "s_min_u32 s71, s71, 64\n\t"
        "s_add_u32 s70, s71, 3\n\t"
        "s_andn2_b32 s70, s70, 3\n\t"
        "s_cmp_le_u32 s71, 16\n\t"
        "s_cselect_b32 s70, s71, s70\n\t"
        "s_lshl_b32 s70, s70, 7\n\t"
        "v_mov_b32 v62, s70\n\t"
        "s_cmp_lt_u32 s51, s71\n\t"
        "s_cselect_b32 s70, 0, 0x70000000\n\t"
        "buffer_load_dwordx2 v[62:63], v62, %[rf], s70 offen sc1\n\t"
        "s_waitcnt vmcnt(2)\n\t"
        "s_barrier\n\t"
        "s_swappc_b64 s[58:59], s[56:57]\n\t"
        "s_waitcnt vmcnt(4)\n\t"
        "v_readfirstlane_b32 s68, v62\n\t"
        "v_readfirstlane_b32 s69, v63\n\t"
        "s_xor_b32 s68, s68, s54\n\t"
        "s_xor_b32 s69, s69, s55\n\t"
        "s_lshr_b32 s70, s68, 8\n\t"
        "s_or_b32 s70, s70, s69\n\t"
        "s_cmp_eq_u32 s70, 0\n\t"
        "s_cselect_b32 s68, s68, 0\n\t"
        "s_and_b32 s69, s68, 127\n\t"
        "s_max_u32 s51, s51, s69\n\t"
        "s_add_u32 s71, s60, 1\n\t"
        "s_min_u32 s71, s71, 64\n\t"
        "s_cmp_ge_u32 s51, s71\n\t"
        "s_cbranch_scc0 .Lv_g3p_%=\n\t"
        ".Lv_g3k_%=:\n\t"
        "s_and_b32 s70, s60, 3\n\t"
        "s_lshl_b32 s70, s70, 13\n\t"
        "s_add_u32 m0, s70, s67\n\t"
        "s_cmp_lt_u32 s60, 64\n\t"
        "s_cselect_b32 s70, s49, 0x7f000000\n\t"
        "buffer_load_dwordx4 v0, %[rs], s70 offen sc1 lds\n\t"
        "s_add_u32 s60, s60, 1\n\t"
        "s_add_u32 s49, s49, 0x80000\n\t"
        "s_and_b32 s70, s60, 3\n\t"
        "s_cselect_b32 s70, 0, 0x2000\n\t"
        "s_xor_b32 s49, s49, s70\n\t"
        "s_add_u32 s71, s60, 1\n\t"
        "s_min_u32 s71, s71, 64\n\t"
        "s_add_u32 s70, s71, 3\n\t"
        "s_andn2_b32 s70, s70, 3\n\t"
        "s_cmp_le_u32 s71, 16\n\t"
        "s_cselect_b32 s70, s71, s70\n\t"
        "s_lshl_b32 s70, s70, 7\n\t"
        "v_mov_b32 v62, s70\n\t"
        "s_cmp_lt_u32 s51, s71\n\t"
        "s_cselect_b32 s70, 0, 0x70000000\n\t"
        "buffer_load_dwordx2 v[62:63], v62, %[rf], s70 offen sc1\n\t"
        "buffer_store_dwordx4 v[36:39], v0, %[rs], s62 offen sc1\n\t"
        "buffer_store_dwordx4 v[40:43], v0, %[rs], s63 offen sc1\n\t"
        "buffer_store_dwordx4 v[44:47], v0, %[rs], s64 offen sc1\n\t"
        "buffer_store_dwordx4 v[48:51], v0, %[rs], s65 offen sc1\n\t"
        "s_mov_b32 s62, 0x7f000000\n\t"
        "s_mov_b32 s63, 0x7f000000\n\t"
        "s_mov_b32 s64, 0x7f000000\n\t"
        "s_mov_b32 s65, 0x7f000000\n\t"
        "s_mov_b64 exec, 15\n\t"
        "s_waitcnt vmcnt(0)\n\t"
        "buffer_store_dword v1, v57, %[rs], s61 offen sc1\n\t"
        "s_mov_b64 exec, -1\n\t"
        "s_add_u32 s61, s61, 8192\n\t"
        "s_waitcnt vmcnt(1)\n\t"
        "s_barrier\n\t"
        "s_swappc_b64 s[58:59], s[56:57]\n\t"
        "v_readfirstlane_b32 s68, v62\n\t"
        "v_readfirstlane_b32 s69, v63\n\t"
        "s_xor_b32 s68, s68, s54\n\t"
        "s_xor_b32 s69, s69, s55\n\t"
        "s_lshr_b32 s70, s68, 8\n\t"
        "s_or_b32 s70, s70, s69\n\t"
        "s_cmp_eq_u32 s70, 0\n\t"
        "s_cselect_b32 s68, s68, 0\n\t"
        "s_and_b32 s69, s68, 127\n\t"
        "s_max_u32 s51, s51, s69\n\t"
        "s_add_u32 s71, s60, 1\n\t"
        "s_min_u32 s71, s71, 64\n\t"
        "s_cmp_ge_u32 s51, s71\n\t"
        "s_cbranch_scc0 .Lv_g4p_%=\n\t"
        ".Lv_g4k_%=:\n\t"
        "s_and_b32 s70, s60, 3\n\t"
        "s_lshl_b32 s70, s70, 13\n\t"
        "s_add_u32 m0, s70, s67\n\t"
        "s_cmp_lt_u32 s60, 64\n\t"
        "s_cselect_b32 s70, s49, 0x7f000000\n\t"
        "buffer_load_dwordx4 v0, %[rs], s70 offen sc1 lds\n\t"
        "s_add_u32 s60, s60, 1\n\t"
        "s_add_u32 s49, s49, 0x80000\n\t"
        "s_and_b32 s70, s60, 3\n\t"
        "s_cselect_b32 s70, 0, 0x2000\n\t"
        "s_xor_b32 s49, s49, s70\n\t"
        "s_add_u32 s71, s60, 1\n\t"
        "s_min_u32 s71, s71, 64\n\t"
        "s_add_u32 s70, s71, 3\n\t"
        "s_andn2_b32 s70, s70, 3\n\t"
        "s_cmp_le_u32 s71, 16\n\t"
        "s_cselect_b32 s70, s71, s70\n\t"
        "s_lshl_b32 s70, s70, 7\n\t"
        "v_mov_b32 v62, s70\n\t"
        "s_cmp_lt_u32 s51, s71\n\t"
        "s_cselect_b32 s70, 0, 0x70000000\n\t"
        "buffer_load_dwordx2 v[62:63], v62, %[rf], s70 offen sc1\n\t"
        "buffer_store_dwordx4 v[36:39], v0, %[rs], s62 offen sc1\n\t"
        "buffer_store_dwordx4 v[40:43], v0, %[rs], s63 offen sc1\n\t"
        "buffer_store_dwordx4 v[44:47], v0, %[rs], s64 offen sc1\n\t"
        "buffer_store_dwordx4 v[48:51], v0, %[rs], s65 offen sc1\n\t"
        "s_mov_b32 s62, 0x7f000000\n\t"
        "s_mov_b32 s63, 0x7f000000\n\t"
        "s_mov_b32 s64, 0x7f000000\n\t"
        "s_mov_b32 s65, 0x7f000000\n\t"
        "s_waitcnt vmcnt(5)\n\t"
        "s_barrier\n\t"
        "s_swappc_b64 s[58:59], s[56:57]\n\t"
        "s_waitcnt vmcnt(8)\n\t"
        "v_readfirstlane_b32 s68, v62\n\t"
        "v_readfirstlane_b32 s69, v63\n\t"
        "s_xor_b32 s68, s68, s54\n\t"
        "s_xor_b32 s69, s69, s55\n\t"
        "s_lshr_b32 s70, s68, 8\n\t"
        "s_or_b32 s70, s70, s69\n\t"
        "s_cmp_eq_u32 s70, 0\n\t"
        "s_cselect_b32 s68, s68, 0\n\t"
        "s_and_b32 s69, s68, 127\n\t"
        "s_max_u32 s51, s51, s69\n\t"
        "s_add_u32 s71, s60, 1\n\t"
        "s_min_u32 s71, s71, 64\n\t"
        "s_cmp_ge_u32 s51, s71\n\t"
        "s_cbranch_scc0 .Lv_g5p_%=\n\t"
        ".Lv_g5k_%=:\n\t"
        "s_and_b32 s70, s60, 3\n\t"
        "s_lshl_b32 s70, s70, 13\n\t"
        "s_add_u32 m0, s70, s67\n\t"
        "s_cmp_lt_u32 s60, 64\n\t"
        "s_cselect_b32 s70, s49, 0x7f000000\n\t"
        "buffer_load_dwordx4 v0, %[rs], s70 offen sc1 lds\n\t"
        "s_add_u32 s60, s60, 1\n\t"
        "s_add_u32 s49, s49, 0x80000\n\t"
        "s_and_b32 s70, s60, 3\n\t"
        "s_cselect_b32 s70, 0, 0x2000\n\t"
        "s_xor_b32 s49, s49, s70\n\t"
        "buffer_store_dwordx4 v[36:39], v0, %[rs], s62 offen sc1\n\t"
        "buffer_store_dwordx4 v[40:43], v0, %[rs], s63 offen sc1\n\t"
        "buffer_store_dwordx4 v[44:47], v0, %[rs], s64 offen sc1\n\t"
        "buffer_store_dwordx4 v[48:51], v0, %[rs], s65 offen sc1\n\t"
        "s_mov_b32 s62, 0x7f000000\n\t"
        "s_mov_b32 s63, 0x7f000000\n\t"
        "s_mov_b32 s64, 0x7f000000\n\t"
        "s_mov_b32 s65, 0x7f000000\n\t"
        "s_mov_b64 exec, 15\n\t"
        "s_waitcnt vmcnt(9)\n\t"
        "buffer_store_dword v1, v57, %[rs], s61 offen sc1\n\t"
        "s_mov_b64 exec, -1\n\t"
        "s_add_u32 s61, s61, 8192\n\t"
        "s_xor_b32 s76, s77, %[tw]\n\t"
        "v_and_b32 v52, 0x1f0, v0\n\t"
        "v_lshlrev_b32 v52, 1, v52\n\t"
        "buffer_load_dword v54, v52, %[rs], s76 offen sc1\n\t"
        "s_waitcnt vmcnt(0)\n\t"
        "s_branch .Lv_eg_%=\n\t"
        ".Lv_second_%=:\n\t"
        "s_add_u32 s71, s60, 1\n\t"
        "s_min_u32 s71, s71, 64\n\t"
        "s_cmp_ge_u32 s51, s71\n\t"
        "s_cbranch_scc0 .Lv_g6p_%=\n\t"
        ".Lv_g6k_%=:\n\t"
        "s_and_b32 s70, s60, 3\n\t"
        "s_lshl_b32 s70, s70, 13\n\t"
        "s_add_u32 m0, s70, s67\n\t"
        "s_cmp_lt_u32 s60, 64\n\t"
        "s_cselect_b32 s70, s49, 0x7f000000\n\t"
        "buffer_load_dwordx4 v0, %[rs], s70 offen sc1 lds\n\t"
        "s_add_u32 s60, s60, 1\n\t"
        "s_add_u32 s49, s49, 0x80000\n\t"
        "s_and_b32 s70, s60, 3\n\t"
        "s_cselect_b32 s70, 0, 0x2000\n\t"
        "s_xor_b32 s49, s49, s70\n\t"
        "s_add_u32 s71, s60, 1\n\t"
        "s_min_u32 s71, s71, 64\n\t"
        "s_add_u32 s70, s71, 3\n\t"
        "s_andn2_b32 s70, s70, 3\n\t"
        "s_cmp_le_u32 s71, 16\n\t"
        "s_cselect_b32 s70, s71, s70\n\t"
        "s_lshl_b32 s70, s70, 7\n\t"
        "v_mov_b32 v62, s70\n\t"
        "s_cmp_lt_u32 s51, s71\n\t"
        "s_cselect_b32 s70, 0, 0x70000000\n\t"
        "buffer_load_dwordx2 v[62:63], v62, %[rf], s70 offen sc1\n\t"
        "s_swappc_b64 s[58:59], s[56:57]\n\t"
        "s_waitcnt vmcnt(4)\n\t"
        "v_readfirstlane_b32 s68, v62\n\t"
        "v_readfirstlane_b32 s69, v63\n\t"
        "s_xor_b32 s68, s68, s54\n\t"
        "s_xor_b32 s69, s69, s55\n\t"
        "s_lshr_b32 s70, s68, 8\n\t"
        "s_or_b32 s70, s70, s69\n\t"
        "s_cmp_eq_u32 s70, 0\n\t"
        "s_cselect_b32 s68, s68, 0\n\t"
        "s_and_b32 s69, s68, 127\n\t"
        "s_max_u32 s51, s51, s69\n\t"
        "s_barrier\n\t"
        "s_branch .Lv_third_%=\n\t"
        ".Lv_yx_%=:\n\t"
        "s_cmp_lt_u32 s53, 64\n\t"
        "s_cbranch_scc0 .Lv_yxl_%=\n\t"
        "s_and_b32 s70, s53, 3\n\t"
        "s_lshl_b32 s70, s70, 13\n\t"
        "s_add_u32 s70, s70, 98304\n\t"
        "v_add_u32 v59, s70, v56\n\t"
        "ds_read_b128 v[20:23], v59 offset:0\n\t"
        "ds_read_b128 v[24:27], v59 offset:1024\n\t"
        "ds_read_b128 v[28:31], v59 offset:2048\n\t"
        "ds_read_b128 v[32:35], v59 offset:3072\n\t"
        "buffer_store_dwordx4 v[36:39], v0, %[rs], s62 offen sc1\n\t"
        "s_waitcnt lgkmcnt(3)\n\t"
        "v_mfma_f32_16x16x32_bf16 v[4:7], v[20:23], v[192:195], 0\n\t"
        "v_mfma_f32_16x16x32_bf16 v[12:15], v[20:23], v[208:211], 0\n\t"
        "ds_read_b128 v[20:23], v59 offset:4096\n\t"
        "s_waitcnt lgkmcnt(3)\n\t"
        "v_mfma_f32_16x16x32_bf16 v[4:7], v[24:27], v[196:199], v[4:7]\n\t"
        "v_mfma_f32_16x16x32_bf16 v[12:15], v[24:27], v[212:215], v[12:15]\n\t"
        "ds_read_b128 v[24:27], v59 offset:5120\n\t"
        "s_waitcnt lgkmcnt(3)\n\t"
        "v_mfma_f32_16x16x32_bf16 v[4:7], v[28:31], v[200:203], v[4:7]\n\t"
        "v_mfma_f32_16x16x32_bf16 v[12:15], v[28:31], v[216:219], v[12:15]\n\t"
        "ds_read_b128 v[28:31], v59 offset:6144\n\t"
        "buffer_store_dwordx4 v[40:43], v0, %[rs], s63 offen sc1\n\t"
        "s_waitcnt lgkmcnt(3)\n\t"
        "v_mfma_f32_16x16x32_bf16 v[4:7], v[32:35], v[204:207], v[4:7]\n\t"
        "v_mfma_f32_16x16x32_bf16 v[12:15], v[32:35], v[220:223], v[12:15]\n\t"
        "ds_read_b128 v[32:35], v59 offset:7168\n\t"
        "s_waitcnt lgkmcnt(3)\n\t"
        "v_mfma_f32_16x16x32_bf16 v[8:11], v[20:23], v[192:195], 0\n\t"
        "v_mfma_f32_16x16x32_bf16 v[16:19], v[20:23], v[208:211], 0\n\t"
        "ds_read_b128 v[20:23], v59 offset:0\n\t"
        "s_waitcnt lgkmcnt(3)\n\t"
        "v_mfma_f32_16x16x32_bf16 v[8:11], v[24:27], v[196:199], v[8:11]\n\t"
        "v_mfma_f32_16x16x32_bf16 v[16:19], v[24:27], v[212:215], v[16:19]\n\t"
        "ds_read_b128 v[24:27], v59 offset:1024\n\t"
        "s_waitcnt lgkmcnt(3)\n\t"
        "v_mfma_f32_16x16x32_bf16 v[8:11], v[28:31], v[200:203], v[8:11]\n\t"
        "v_mfma_f32_16x16x32_bf16 v[16:19], v[28:31], v[216:219], v[16:19]\n\t"
        "ds_read_b128 v[28:31], v59 offset:2048\n\t"
        "buffer_store_dwordx4 v[44:47], v0, %[rs], s64 offen sc1\n\t"
        "s_waitcnt lgkmcnt(3)\n\t"
        "v_mfma_f32_16x16x32_bf16 v[8:11], v[32:35], v[204:207], v[8:11]\n\t"
        "v_mfma_f32_16x16x32_bf16 v[16:19], v[32:35], v[220:223], v[16:19]\n\t"
        "ds_read_b128 v[32:35], v59 offset:3072\n\t"
        "s_nop 5\n\t"
        "v_cvt_pk_bf16_f32 v36, v4, v5\n\t"
        "v_cvt_pk_bf16_f32 v37, v6, v7\n\t"
        "v_cvt_pk_bf16_f32 v40, v12, v13\n\t"
        "v_cvt_pk_bf16_f32 v41, v14, v15\n\t"
        "v_cvt_pk_bf16_f32 v38, v8, v9\n\t"
        "v_cvt_pk_bf16_f32 v39, v10, v11\n\t"
        "v_cvt_pk_bf16_f32 v42, v16, v17\n\t"
        "v_cvt_pk_bf16_f32 v43, v18, v19\n\t"
        "s_waitcnt lgkmcnt(3)\n\t"
        "v_mfma_f32_16x16x32_bf16 v[4:7], v[20:23], v[224:227], 0\n\t"
        "v_mfma_f32_16x16x32_bf16 v[12:15], v[20:23], v[240:243], 0\n\t"
        "ds_read_b128 v[20:23], v59 offset:4096\n\t"
        "s_waitcnt lgkmcnt(3)\n\t"
        "v_mfma_f32_16x16x32_bf16 v[4:7], v[24:27], v[228:231], v[4:7]\n\t"
        "v_mfma_f32_16x16x32_bf16 v[12:15], v[24:27], v[244:247], v[12:15]\n\t"
        "ds_read_b128 v[24:27], v59 offset:5120\n\t"
        "s_waitcnt lgkmcnt(3)\n\t"
        "v_mfma_f32_16x16x32_bf16 v[4:7], v[28:31], v[232:235], v[4:7]\n\t"
        "v_mfma_f32_16x16x32_bf16 v[12:15], v[28:31], v[248:251], v[12:15]\n\t"
        "ds_read_b128 v[28:31], v59 offset:6144\n\t"
        "buffer_store_dwordx4 v[48:51], v0, %[rs], s65 offen sc1\n\t"
        "s_waitcnt lgkmcnt(3)\n\t"
        "v_mfma_f32_16x16x32_bf16 v[4:7], v[32:35], v[236:239], v[4:7]\n\t"
        "v_mfma_f32_16x16x32_bf16 v[12:15], v[32:35], v[252:255], v[12:15]\n\t"
        "ds_read_b128 v[32:35], v59 offset:7168\n\t"
        "s_waitcnt lgkmcnt(3)\n\t"
        "v_mfma_f32_16x16x32_bf16 v[8:11], v[20:23], v[224:227], 0\n\t"
        "v_mfma_f32_16x16x32_bf16 v[16:19], v[20:23], v[240:243], 0\n\t"
        "s_waitcnt lgkmcnt(2)\n\t"
        "v_mfma_f32_16x16x32_bf16 v[8:11], v[24:27], v[228:231], v[8:11]\n\t"
        "v_mfma_f32_16x16x32_bf16 v[16:19], v[24:27], v[244:247], v[16:19]\n\t"
        "s_waitcnt lgkmcnt(1)\n\t"
        "v_mfma_f32_16x16x32_bf16 v[8:11], v[28:31], v[232:235], v[8:11]\n\t"
        "v_mfma_f32_16x16x32_bf16 v[16:19], v[28:31], v[248:251], v[16:19]\n\t"
        "s_waitcnt lgkmcnt(0)\n\t"
        "v_mfma_f32_16x16x32_bf16 v[8:11], v[32:35], v[236:239], v[8:11]\n\t"
        "v_mfma_f32_16x16x32_bf16 v[16:19], v[32:35], v[252:255], v[16:19]\n\t"
        "s_nop 5\n\t"
        "v_cvt_pk_bf16_f32 v44, v4, v5\n\t"
        "v_cvt_pk_bf16_f32 v45, v6, v7\n\t"
        "v_cvt_pk_bf16_f32 v48, v12, v13\n\t"
        "v_cvt_pk_bf16_f32 v49, v14, v15\n\t"
        "v_cvt_pk_bf16_f32 v46, v8, v9\n\t"
        "v_cvt_pk_bf16_f32 v47, v10, v11\n\t"
        "v_cvt_pk_bf16_f32 v50, v16, v17\n\t"
        "v_cvt_pk_bf16_f32 v51, v18, v19\n\t"
        "s_mov_b32 s62, s52\n\t"
        "s_add_u32 s63, s52, 65536\n\t"
        "s_add_u32 s64, s52, 131072\n\t"
        "s_add_u32 s65, s52, 196608\n\t"
        "s_add_u32 s53, s53, 1\n\t"
        "s_add_u32 s52, s52, 4194304\n\t"
        "s_and_b32 s70, s53, 7\n\t"
        "s_cselect_b32 s70, 0, 33554432\n\t"
        "s_sub_u32 s52, s52, s70\n\t"
        "s_setpc_b64 s[58:59]\n\t"
        ".Lv_yxl_%=:\n\t"
        "buffer_store_dwordx4 v[36:39], v0, %[rs], s62 offen sc1\n\t"
        "buffer_store_dwordx4 v[40:43], v0, %[rs], s63 offen sc1\n\t"
        "buffer_store_dwordx4 v[44:47], v0, %[rs], s64 offen sc1\n\t"
        "buffer_store_dwordx4 v[48:51], v0, %[rs], s65 offen sc1\n\t"
        "s_mov_b32 s62, 0x7f000000\n\t"
        "s_mov_b32 s63, 0x7f000000\n\t"
        "s_mov_b32 s64, 0x7f000000\n\t"
        "s_mov_b32 s65, 0x7f000000\n\t"
        "s_setpc_b64 s[58:59]\n\t"
        ".Lv_u0f_%=:\n\t"
        "s_cmp_eq_u32 s45, 0\n\t"
        "s_cbranch_scc1 .Lv_u0k_%=\n\t"
        "s_add_u32 s79, s79, 1\n\t"
        "buffer_load_dwordx4 v[20:23], v0, %[rs], s47 offen sc1\n\t"
        "buffer_load_dwordx4 v[24:27], v0, %[rs], s47 offen offset:1024 sc1\n\t"
        "s_nop 7\n\t"
        "v_mov_b64 v[4:5], 0\n\t"
        "v_mov_b64 v[6:7], 0\n\t"
        "s_waitcnt vmcnt(0)\n\t"
        "s_sub_u32 s46, s46, 1\n\t"
        "s_cbranch_scc0 .Lv_u0r_%=\n\t"
        "s_mov_b32 s46, 0\n\t"
        "s_mov_b32 s46, 0\n\t"
        "v_mov_b32 v52, 0\n\t"
        "s_add_u32 s68, s45, 1000\n\t"
        "v_mov_b32 v53, s68\n\t"
        "global_store_dword v52, v53, %[tm] sc1\n\t"
        "s_branch .Lv_u0k_%=\n\t"
        ".Lv_f0p_%=:\n\t"
        "s_add_u32 s70, 1, 3\n\t"
        "s_andn2_b32 s70, s70, 3\n\t"
        "s_cmp_le_u32 1, 16\n\t"
        "s_cselect_b32 s70, 1, s70\n\t"
        "s_lshl_b32 s70, s70, 7\n\t"
        "v_mov_b32 v20, 0\n\t"
        "buffer_load_dwordx2 v[20:21], v20, %[rf], s70 offen sc1\n\t"
        "s_waitcnt vmcnt(0)\n\t"
        "v_readfirstlane_b32 s68, v20\n\t"
        "v_readfirstlane_b32 s69, v21\n\t"
        "s_xor_b32 s68, s68, s54\n\t"
        "s_xor_b32 s69, s69, s55\n\t"
        "s_lshr_b32 s70, s68, 8\n\t"
        "s_or_b32 s70, s70, s69\n\t"
        "s_cmp_eq_u32 s70, 0\n\t"
        "s_cselect_b32 s68, s68, 0\n\t"
        "s_and_b32 s69, s68, 127\n\t"
        "s_max_u32 s51, s51, s69\n\t"
        "s_cmp_ge_u32 s51, 1\n\t"
        "s_cbranch_scc1 .Lv_f0k_%=\n\t"
        "s_sleep 4\n\t"
        "s_sub_u32 s46, s46, 1\n\t"
        "s_cbranch_scc0 .Lv_f0p_%=\n\t"
        "s_mov_b32 s46, 0\n\t"
        "s_mov_b32 s46, 0\n\t"
        "v_mov_b32 v20, 0\n\t"
        "s_add_u32 s68, s45, 3000\n\t"
        "v_mov_b32 v21, s68\n\t"
        "global_store_dword v20, v21, %[tm] sc1\n\t"
        "s_branch .Lv_f0k_%=\n\t"
        ".Lv_g1p_%=:\n\t"
        "s_add_u32 s70, s71, 3\n\t"
        "s_andn2_b32 s70, s70, 3\n\t"
        "s_cmp_le_u32 s71, 16\n\t"
        "s_cselect_b32 s70, s71, s70\n\t"
        "s_lshl_b32 s70, s70, 7\n\t"
        "v_mov_b32 v20, 0\n\t"
        "buffer_load_dwordx2 v[20:21], v20, %[rf], s70 offen sc1\n\t"
        "s_waitcnt vmcnt(0)\n\t"
        "v_readfirstlane_b32 s68, v20\n\t"
        "v_readfirstlane_b32 s69, v21\n\t"
        "s_xor_b32 s68, s68, s54\n\t"
        "s_xor_b32 s69, s69, s55\n\t"
        "s_lshr_b32 s70, s68, 8\n\t"
        "s_or_b32 s70, s70, s69\n\t"
        "s_cmp_eq_u32 s70, 0\n\t"
        "s_cselect_b32 s68, s68, 0\n\t"
        "s_and_b32 s69, s68, 127\n\t"
        "s_max_u32 s51, s51, s69\n\t"
        "s_cmp_ge_u32 s51, s71\n\t"
        "s_cbranch_scc1 .Lv_g1k_%=\n\t"
        "s_sleep 4\n\t"
        "s_sub_u32 s46, s46, 1\n\t"
        "s_cbranch_scc0 .Lv_g1p_%=\n\t"
        "s_mov_b32 s46, 0\n\t"
        "s_mov_b32 s46, 0\n\t"
        "v_mov_b32 v20, 0\n\t"
        "s_add_u32 s68, s45, 3000\n\t"
        "v_mov_b32 v21, s68\n\t"
        "global_store_dword v20, v21, %[tm] sc1\n\t"
        "s_branch .Lv_g1k_%=\n\t"
        ".Lv_v0f_%=:\n\t"
        "s_cmp_eq_u32 s45, 0\n\t"
        "s_cbranch_scc1 .Lv_v0k_%=\n\t"
        "s_add_u32 s79, s79, 1\n\t"
        "buffer_load_dwordx4 v[20:23], v0, %[rs], s47 offen sc1\n\t"
        "buffer_load_dwordx4 v[24:27], v0, %[rs], s47 offen offset:1024 sc1\n\t"
        "s_nop 7\n\t"
        "v_mov_b64 v[4:5], 0\n\t"
        "v_mov_b64 v[6:7], 0\n\t"
        "s_waitcnt vmcnt(0)\n\t"
        "s_sub_u32 s46, s46, 1\n\t"
        "s_cbranch_scc0 .Lv_v0r_%=\n\t"
        "s_mov_b32 s46, 0\n\t"
        "s_mov_b32 s46, 0\n\t"
        "v_mov_b32 v52, 0\n\t"
        "s_add_u32 s68, s45, 1000\n\t"
        "v_mov_b32 v53, s68\n\t"
        "global_store_dword v52, v53, %[tm] sc1\n\t"
        "s_branch .Lv_v0k_%=\n\t"
        ".Lv_g2p_%=:\n\t"
        "s_add_u32 s70, s71, 3\n\t"
        "s_andn2_b32 s70, s70, 3\n\t"
        "s_cmp_le_u32 s71, 16\n\t"
        "s_cselect_b32 s70, s71, s70\n\t"
        "s_lshl_b32 s70, s70, 7\n\t"
        "v_mov_b32 v20, 0\n\t"
        "buffer_load_dwordx2 v[20:21], v20, %[rf], s70 offen sc1\n\t"
        "s_waitcnt vmcnt(0)\n\t"
        "v_readfirstlane_b32 s68, v20\n\t"
        "v_readfirstlane_b32 s69, v21\n\t"
        "s_xor_b32 s68, s68, s54\n\t"
        "s_xor_b32 s69, s69, s55\n\t"
        "s_lshr_b32 s70, s68, 8\n\t"
        "s_or_b32 s70, s70, s69\n\t"
        "s_cmp_eq_u32 s70, 0\n\t"
        "s_cselect_b32 s68, s68, 0\n\t"
        "s_and_b32 s69, s68, 127\n\t"
        "s_max_u32 s51, s51, s69\n\t"
        "s_cmp_ge_u32 s51, s71\n\t"
        "s_cbranch_scc1 .Lv_g2k_%=\n\t"
        "s_sleep 4\n\t"
        "s_sub_u32 s46, s46, 1\n\t"
        "s_cbranch_scc0 .Lv_g2p_%=\n\t"
        "s_mov_b32 s46, 0\n\t"
        "s_mov_b32 s46, 0\n\t"
        "v_mov_b32 v20, 0\n\t"
        "s_add_u32 s68, s45, 3000\n\t"
        "v_mov_b32 v21, s68\n\t"
        "global_store_dword v20, v21, %[tm] sc1\n\t"
        "s_branch .Lv_g2k_%=\n\t"
        ".Lv_twx_%=:\n\t"
        "s_add_u32 s79, s79, 0x1000000\n\t"
        "s_sleep 4\n\t"
        "buffer_load_dword v54, v52, %[rs], s76 offen sc1\n\t"
        "s_waitcnt vmcnt(0)\n\t"
        "s_sub_u32 s46, s46, 1\n\t"
        "s_cbranch_scc0 .Lv_twm_%=\n\t"
        "s_mov_b32 s46, 0\n\t"
        "s_branch .Lv_twy_%=\n\t"
        ".Lv_g3p_%=:\n\t"
        "s_add_u32 s70, s71, 3\n\t"
        "s_andn2_b32 s70, s70, 3\n\t"
        "s_cmp_le_u32 s71, 16\n\t"
        "s_cselect_b32 s70, s71, s70\n\t"
        "s_lshl_b32 s70, s70, 7\n\t"
        "v_mov_b32 v20, 0\n\t"
        "buffer_load_dwordx2 v[20:21], v20, %[rf], s70 offen sc1\n\t"
        "s_waitcnt vmcnt(0)\n\t"
        "v_readfirstlane_b32 s68, v20\n\t"
        "v_readfirstlane_b32 s69, v21\n\t"
        "s_xor_b32 s68, s68, s54\n\t"
        "s_xor_b32 s69, s69, s55\n\t"
        "s_lshr_b32 s70, s68, 8\n\t"
        "s_or_b32 s70, s70, s69\n\t"
        "s_cmp_eq_u32 s70, 0\n\t"
        "s_cselect_b32 s68, s68, 0\n\t"
        "s_and_b32 s69, s68, 127\n\t"
        "s_max_u32 s51, s51, s69\n\t"
        "s_cmp_ge_u32 s51, s71\n\t"
        "s_cbranch_scc1 .Lv_g3k_%=\n\t"
        "s_sleep 4\n\t"
        "s_sub_u32 s46, s46, 1\n\t"
        "s_cbranch_scc0 .Lv_g3p_%=\n\t"
        "s_mov_b32 s46, 0\n\t"
        "s_mov_b32 s46, 0\n\t"
        "v_mov_b32 v20, 0\n\t"
        "s_add_u32 s68, s45, 3000\n\t"
        "v_mov_b32 v21, s68\n\t"
        "global_store_dword v20, v21, %[tm] sc1\n\t"
        "s_branch .Lv_g3k_%=\n\t"
        ".Lv_g4p_%=:\n\t"
        "s_add_u32 s70, s71, 3\n\t"
        "s_andn2_b32 s70, s70, 3\n\t"
        "s_cmp_le_u32 s71, 16\n\t"
        "s_cselect_b32 s70, s71, s70\n\t"
        "s_lshl_b32 s70, s70, 7\n\t"
        "v_mov_b32 v20, 0\n\t"
        "buffer_load_dwordx2 v[20:21], v20, %[rf], s70 offen sc1\n\t"
        "s_waitcnt vmcnt(0)\n\t"
        "v_readfirstlane_b32 s68, v20\n\t"
        "v_readfirstlane_b32 s69, v21\n\t"
        "s_xor_b32 s68, s68, s54\n\t"
        "s_xor_b32 s69, s69, s55\n\t"
        "s_lshr_b32 s70, s68, 8\n\t"
        "s_or_b32 s70, s70, s69\n\t"
        "s_cmp_eq_u32 s70, 0\n\t"
        "s_cselect_b32 s68, s68, 0\n\t"
        "s_and_b32 s69, s68, 127\n\t"
        "s_max_u32 s51, s51, s69\n\t"
        "s_cmp_ge_u32 s51, s71\n\t"
        "s_cbranch_scc1 .Lv_g4k_%=\n\t"
        "s_sleep 4\n\t"
        "s_sub_u32 s46, s46, 1\n\t"
        "s_cbranch_scc0 .Lv_g4p_%=\n\t"
        "s_mov_b32 s46, 0\n\t"
        "s_mov_b32 s46, 0\n\t"
        "v_mov_b32 v20, 0\n\t"
        "s_add_u32 s68, s45, 3000\n\t"
        "v_mov_b32 v21, s68\n\t"
        "global_store_dword v20, v21, %[tm] sc1\n\t"
        "s_branch .Lv_g4k_%=\n\t"
        ".Lv_g5p_%=:\n\t"
        "s_add_u32 s70, s71, 3\n\t"
        "s_andn2_b32 s70, s70, 3\n\t"
        "s_cmp_le_u32 s71, 16\n\t"
        "s_cselect_b32 s70, s71, s70\n\t"
        "s_lshl_b32 s70, s70, 7\n\t"
        "v_mov_b32 v20, 0\n\t"
        "buffer_load_dwordx2 v[20:21], v20, %[rf], s70 offen sc1\n\t"
        "s_waitcnt vmcnt(0)\n\t"
        "v_readfirstlane_b32 s68, v20\n\t"
        "v_readfirstlane_b32 s69, v21\n\t"
        "s_xor_b32 s68, s68, s54\n\t"
        "s_xor_b32 s69, s69, s55\n\t"
        "s_lshr_b32 s70, s68, 8\n\t"
        "s_or_b32 s70, s70, s69\n\t"
        "s_cmp_eq_u32 s70, 0\n\t"
        "s_cselect_b32 s68, s68, 0\n\t"
        "s_and_b32 s69, s68, 127\n\t"
        "s_max_u32 s51, s51, s69\n\t"
        "s_cmp_ge_u32 s51, s71\n\t"
        "s_cbranch_scc1 .Lv_g5k_%=\n\t"
        "s_sleep 4\n\t"
        "s_sub_u32 s46, s46, 1\n\t"
        "s_cbranch_scc0 .Lv_g5p_%=\n\t"
        "s_mov_b32 s46, 0\n\t"
        "s_mov_b32 s46, 0\n\t"
        "v_mov_b32 v20, 0\n\t"
        "s_add_u32 s68, s45, 3000\n\t"
        "v_mov_b32 v21, s68\n\t"
        "global_store_dword v20, v21, %[tm] sc1\n\t"
        "s_branch .Lv_g5k_%=\n\t"
        ".Lv_g6p_%=:\n\t"
        "s_add_u32 s70, s71, 3\n\t"
        "s_andn2_b32 s70, s70, 3\n\t"
        "s_cmp_le_u32 s71, 16\n\t"
        "s_cselect_b32 s70, s71, s70\n\t"
        "s_lshl_b32 s70, s70, 7\n\t"
        "v_mov_b32 v20, 0\n\t"
        "buffer_load_dwordx2 v[20:21], v20, %[rf], s70 offen sc1\n\t"
        "s_waitcnt vmcnt(0)\n\t"
        "v_readfirstlane_b32 s68, v20\n\t"
        "v_readfirstlane_b32 s69, v21\n\t"
        "s_xor_b32 s68, s68, s54\n\t"
        "s_xor_b32 s69, s69, s55\n\t"
        "s_lshr_b32 s70, s68, 8\n\t"
        "s_or_b32 s70, s70, s69\n\t"
        "s_cmp_eq_u32 s70, 0\n\t"
        "s_cselect_b32 s68, s68, 0\n\t"
        "s_and_b32 s69, s68, 127\n\t"
        "s_max_u32 s51, s51, s69\n\t"
        "s_cmp_ge_u32 s51, s71\n\t"
        "s_cbranch_scc1 .Lv_g6k_%=\n\t"
        "s_sleep 4\n\t"
        "s_sub_u32 s46, s46, 1\n\t"
        "s_cbranch_scc0 .Lv_g6p_%=\n\t"
        "s_mov_b32 s46, 0\n\t"
        "s_mov_b32 s46, 0\n\t"
        "v_mov_b32 v20, 0\n\t"
        "s_add_u32 s68, s45, 3000\n\t"
        "v_mov_b32 v21, s68\n\t"
        "global_store_dword v20, v21, %[tm] sc1\n\t"
        "s_branch .Lv_g6k_%=\n\t"
        ".Lcode_v1:\n\t"
        ".Lv_end_%=:\n\t"
        "s_endpgm\n\t"
        :: [rs] "s"(RSRC(arena(a.slots), ARENA_BYTES)), [rw] "s"(RSRC(a.Wv + co, (DM * HD - co) * 2)), [rx] "s"(RSRC(a.Wo + oo, (DM * HD - oo) * 2)), [rm] "s"(RSRC(a.mem0, 8 * DM * 2)),
           [ep] "s"(a.boot + 144), [rf] "s"(PROG(64 + h)), [tm] "s"(a.sync + 40 * 32), [w] "s"(w), [wa] "s"(w ^ (4 * half)), [cv] "s"(cls_v(h, half)), [ra] "s"((4 + 2 * rep) * 16384),
           [ov] "s"(KV_OFF + h * 16384 + (cls_s(h) ^ 1) * 8192 + w * 1024), [om] "s"((64 + j0) * 16384 + cls_r(j0) * 8192 + h * 256),
           [oy] "s"(YX_OFF + (4 * j0 + (h >> 3)) * 16384 + (cls_r(j0) ^ half ^ YXC) * 8192 + (h & 7) * 1024), [tw] "s"(TW(cls_v(h, half))), [of] "s"(21 * 16384 + j0 * 128 + h * 4) STIN(w ? 251 : 32 + 2 * h + half)
        : "memory", "vcc", "scc", ALLV, ALLS);
    __builtin_unreachable();
}
__device__ __forceinline__ void column_wg(const int w, const int j, const unsigned t0, const unsigned t1) {
    const Args a = get_args();
    asm volatile(
        ".Lcode_r0:\n\t"
        "v_mbcnt_lo_u32_b32 v5, -1, 0\n\t"
        "v_mbcnt_hi_u32_b32 v5, -1, v5\n\t"
        "s_mov_b32 s79, 0\n\t"
        "s_mov_b32 s45, 0\n\t"
        "s_mov_b32 s46, 200000\n\t"
        "v_lshlrev_b32 v0, 4, v5\n\t"
        "s_mov_b32 s66, 0xffbfffbf\n\t"
        "s_lshl_b32 s70, %[cr], 13\n\t"
        "s_xor_b32 s48, s70, %[t0]\n\t"
        "s_mov_b32 s49, %[t0]\n\t"
        "v_lshrrev_b32 v6, 4, v5\n\t"
        "s_mov_b32 s44, 0\n\t"
        "s_cmp_gt_u32 %[w], 2\n\t"
        "s_cbranch_scc1 .Lr_dma_%=\n\t"
        "s_cmp_gt_u32 %[w], 0\n\t"
        "s_cbranch_scc1 .Lr_ox_%=\n\t"
        "v_mov_b32 v8, 0x4444\n\t"
        "v_mov_b32 v7, 0xeeee\n\t"
        "v_and_b32 v1, 1, v5\n\t"
        "v_cmp_eq_u32 vcc, 0, v1\n\t"
        "s_mov_b32 s64, 0x55555555\n\t"
        "s_mov_b32 s65, 0x55555555\n\t"
        "v_cndmask_b32 v1, v7, v8, vcc\n\t"
        "v_lshlrev_b32 v2, 6, v6\n\t"
        "v_and_b32 v7, 1, v5\n\t"
        "v_lshl_add_u32 v2, v7, 5, v2\n\t"
        "v_bfe_u32 v7, v5, 1, 1\n\t"
        "v_lshl_add_u32 v2, v7, 4, v2\n\t"
        "v_bfe_u32 v7, v5, 2, 2\n\t"
        "v_lshl_add_u32 v2, v7, 2, v2\n\t"
        "s_setprio 3\n\t"
        "s_memtime s[62:63]\n\t"
        "s_add_u32 s52, %[al], s48\n\t"
        "s_mov_b32 s53, %[ah]\n\t"
        "s_mov_b32 s54, 2097152\n\t"
        "s_mov_b32 s55, 0x20000\n\t"
        "s_xor_b32 s56, s52, 0x2000\n\t"
        "s_mov_b32 s57, s53\n\t"
        "s_mov_b32 s58, 2097152\n\t"
        "s_mov_b32 s59, 0x20000\n\t"
        "s_mov_b32 s50, 4096\n\t"
        "s_mov_b32 s51, 16384\n\t"
        "s_mov_b32 s60, 20480\n\t"
        "s_mov_b32 s61, 32768\n\t"
        "s_add_u32 s47, %[om], 4096\n\t"
        "v_add_u32 v3, 98304, v0\n\t"
        "s_waitcnt lgkmcnt(0)\n\t"
        "s_add_u32 s72, %[oa], 65536\n\t"
        "s_add_u32 s73, %[oa], 98304\n\t"
        "s_add_u32 s74, %[oa], 131072\n\t"
        "s_add_u32 s75, %[oa], 163840\n\t"
        "v_mov_b64 v[8:9], 0\n\t"
        "v_mov_b64 v[10:11], 0\n\t"
        "s_mov_b64 vcc, s[64:65]\n\t"
        "v_mov_b32 v21, 0\n\t"
        "s_branch .Lr_st_%=\n\t"
        ".p2align 6\n\t"
        ".Lr_loop_%=:\n\t"
        ".Lr_twa_%=:\n\t"
        "s_memrealtime s[68:69]\n\t"
        "s_waitcnt lgkmcnt(0)\n\t"
        "s_sub_i32 s68, s67, s68\n\t"
        "s_sub_u32 s69, s68, 5\n\t"
        "s_cmp_lt_u32 s69, 400\n\t"
        "s_cbranch_scc1 .Lr_twa_%=\n\t"
        "s_cmp_lt_i32 s68, 1\n\t"
        "s_cbranch_scc1 .Lr_twb_%=\n\t"
        "s_nop 2\n\t"
        "s_cmp_lt_i32 s68, 2\n\t"
        "s_cbranch_scc1 .Lr_twb_%=\n\t"
        "s_nop 2\n\t"
        "s_cmp_lt_i32 s68, 3\n\t"
        "s_cbranch_scc1 .Lr_twb_%=\n\t"
        "s_nop 2\n\t"
        "s_cmp_lt_i32 s68, 4\n\t"
        "s_cbranch_scc1 .Lr_twb_%=\n\t"
        "s_nop 2\n\t"
        "s_branch .Lr_twb_%=\n\t"
        ".Lr_twb_%=:\n\t"
        ".Lr_pp_%=:\n\t"
        "buffer_load_dwordx4 v[224:227], v0, s[52:55], %[om] offen offset:0 sc1\n\t"
        "buffer_load_dwordx4 v[228:231], v0, s[52:55], %[om] offen offset:1024 sc1\n\t"
        "buffer_load_dwordx4 v[232:235], v0, s[52:55], %[om] offen offset:2048 sc1\n\t"
        "buffer_load_dwordx4 v[236:239], v0, s[52:55], %[om] offen offset:3072 sc1\n\t"
        "buffer_load_dwordx4 v[240:243], v0, s[52:55], s47 offen offset:0 sc1\n\t"
        "buffer_load_dwordx4 v[244:247], v0, s[52:55], s47 offen offset:1024 sc1\n\t"
        "buffer_load_dwordx4 v[248:251], v0, s[52:55], s47 offen offset:2048 sc1\n\t"
        "buffer_load_dwordx4 v[252:255], v0, s[52:55], s47 offen offset:3072 sc1\n\t"
        "s_nop 9\n\t"
        "buffer_load_dwordx4 v[16:19], v0, s[52:55], 0 offen offset:0 sc1\n\t"
        "buffer_load_dwordx4 v[20:23], v0, s[52:55], 0 offen offset:1024 sc1\n\t"
        "buffer_load_dwordx4 v[24:27], v0, s[52:55], 0 offen offset:2048 sc1\n\t"
        "buffer_load_dwordx4 v[28:31], v0, s[52:55], 0 offen offset:3072 sc1\n\t"
        "buffer_load_dwordx4 v[32:35], v0, s[52:55], s50 offen offset:0 sc1\n\t"
        "buffer_load_dwordx4 v[36:39], v0, s[52:55], s50 offen offset:1024 sc1\n\t"
        "buffer_load_dwordx4 v[40:43], v0, s[52:55], s50 offen offset:2048 sc1\n\t"
        "buffer_load_dwordx4 v[44:47], v0, s[52:55], s50 offen offset:3072 sc1\n\t"
        "buffer_load_dwordx4 v[48:51], v0, s[52:55], s51 offen offset:0 sc1\n\t"
        "buffer_load_dwordx4 v[52:55], v0, s[52:55], s51 offen offset:1024 sc1\n\t"
        "buffer_load_dwordx4 v[56:59], v0, s[52:55], s51 offen offset:2048 sc1\n\t"
        "buffer_load_dwordx4 v[60:63], v0, s[52:55], s51 offen offset:3072 sc1\n\t"
        "buffer_load_dwordx4 v[64:67], v0, s[52:55], s60 offen offset:0 sc1\n\t"
        "buffer_load_dwordx4 v[68:71], v0, s[52:55], s60 offen offset:1024 sc1\n\t"
        "buffer_load_dwordx4 v[72:75], v0, s[52:55], s60 offen offset:2048 sc1\n\t"
        "buffer_load_dwordx4 v[76:79], v0, s[52:55], s60 offen offset:3072 sc1\n\t"
        "buffer_load_dwordx4 v[80:83], v0, s[52:55], s61 offen offset:0 sc1\n\t"
        "buffer_load_dwordx4 v[84:87], v0, s[52:55], s61 offen offset:1024 sc1\n\t"
        "buffer_load_dwordx4 v[88:91], v0, s[52:55], s61 offen offset:2048 sc1\n\t"
        "buffer_load_dwordx4 v[92:95], v0, s[52:55], s61 offen offset:3072 sc1\n\t"
        "s_waitcnt vmcnt(18)\n\t"
        "v_smfmac_f32_16x16x64_bf16 v[8:11], v[16:19], v[96:103], v1\n\t"
        "v_smfmac_f32_16x16x64_bf16 v[8:11], v[20:23], v[104:111], v1\n\t"
        "s_waitcnt vmcnt(16)\n\t"
        "v_smfmac_f32_16x16x64_bf16 v[8:11], v[24:27], v[112:119], v1\n\t"
        "v_smfmac_f32_16x16x64_bf16 v[8:11], v[28:31], v[120:127], v1\n\t"
        "s_waitcnt vmcnt(14)\n\t"
        "v_smfmac_f32_16x16x64_bf16 v[8:11], v[32:35], v[128:135], v1\n\t"
        "v_smfmac_f32_16x16x64_bf16 v[8:11], v[36:39], v[136:143], v1\n\t"
        "s_waitcnt vmcnt(12)\n\t"
        "v_smfmac_f32_16x16x64_bf16 v[8:11], v[40:43], v[144:151], v1\n\t"
        "v_smfmac_f32_16x16x64_bf16 v[8:11], v[44:47], v[152:159], v1\n\t"
        "s_waitcnt vmcnt(10)\n\t"
        "v_smfmac_f32_16x16x64_bf16 v[8:11], v[48:51], v[160:167], v1\n\t"
        "v_smfmac_f32_16x16x64_bf16 v[8:11], v[52:55], v[168:175], v1\n\t"
        "s_waitcnt vmcnt(8)\n\t"
        "v_smfmac_f32_16x16x64_bf16 v[8:11], v[56:59], v[176:183], v1\n\t"
        "v_smfmac_f32_16x16x64_bf16 v[8:11], v[60:63], v[184:191], v1\n\t"
        "s_waitcnt vmcnt(6)\n\t"
        "v_smfmac_f32_16x16x64_bf16 v[8:11], v[64:67], v[192:199], v1\n\t"
        "v_smfmac_f32_16x16x64_bf16 v[8:11], v[68:71], v[200:207], v1\n\t"
        "s_waitcnt vmcnt(4)\n\t"
        "v_smfmac_f32_16x16x64_bf16 v[8:11], v[72:75], v[208:215], v1\n\t"
        "v_smfmac_f32_16x16x64_bf16 v[8:11], v[76:79], v[216:223], v1\n\t"
        "s_waitcnt vmcnt(3)\n\t"
        "v_smfmac_f32_16x16x64_bf16 v[8:11], v[80:83], v[224:231], v1\n\t"
        "s_waitcnt vmcnt(2)\n\t"
        "v_smfmac_f32_16x16x64_bf16 v[8:11], v[84:87], v[232:239], v1\n\t"
        "s_waitcnt vmcnt(1)\n\t"
        "v_smfmac_f32_16x16x64_bf16 v[8:11], v[88:91], v[240:247], v1\n\t"
        "s_waitcnt vmcnt(0)\n\t"
        "v_smfmac_f32_16x16x64_bf16 v[8:11], v[92:95], v[248:255], v1\n\t"
        "s_nop 7\n\t"
        "v_add_f32 v5, v8, v9\n\t"
        "v_add_f32 v6, v10, v11\n\t"
        "v_cmp_u_f32 s[68:69], v5, v6\n\t"
        "s_nop 0\n\t"
        "v_cndmask_b32_dpp v7, v6, v5, vcc quad_perm:[0,0,2,2] row_mask:0xf bank_mask:0xf\n\t"
        "s_not_b64 vcc, vcc\n\t"
        "v_cndmask_b32_dpp v12, v5, v6, vcc quad_perm:[1,1,3,3] row_mask:0xf bank_mask:0xf\n\t"
        "s_cmp_lg_u64 s[68:69], 0\n\t"
        "v_cvt_pk_bf16_f32 v7, v7, v12\n\t"
        "s_cbranch_scc1 .Lr_pf_%=\n\t"
        ".Lr_pk_%=:\n\t"
        "buffer_store_dword v7, v2, s[56:59], s72 offen sc1\n\t"
        "buffer_store_dword v7, v2, s[52:55], s72 offen sc1\n\t"
        "buffer_store_dword v7, v2, s[56:59], s73 offen sc1\n\t"
        "buffer_store_dword v7, v2, s[52:55], s73 offen sc1\n\t"
        "buffer_store_dword v7, v2, s[56:59], s74 offen sc1\n\t"
        "buffer_store_dword v7, v2, s[52:55], s74 offen sc1\n\t"
        "buffer_store_dword v7, v2, s[56:59], s75 offen sc1\n\t"
        "buffer_store_dword v7, v2, s[52:55], s75 offen sc1\n\t"
        "s_memtime s[62:63]\n\t"
        "s_barrier\n\t"
        ".Lr_pl_%=:\n\t"
        "ds_write_b128 v3, v[224:227] offset:0\n\t"
        "ds_write_b128 v3, v[228:231] offset:1024\n\t"
        "ds_write_b128 v3, v[232:235] offset:2048\n\t"
        "ds_write_b128 v3, v[236:239] offset:3072\n\t"
        "ds_write_b128 v3, v[240:243] offset:4096\n\t"
        "ds_write_b128 v3, v[244:247] offset:5120\n\t"
        "ds_write_b128 v3, v[248:251] offset:6144\n\t"
        "ds_write_b128 v3, v[252:255] offset:7168\n\t"
        "s_add_u32 s70, s45, 1\n\t"
        "v_mov_b32 v5, 114720\n\t"
        "v_mov_b32 v6, s70\n\t"
        "ds_write_b32 v5, v6\n\t"
        "v_xor_b32 v3, 0x2000, v3\n\t"
        "s_add_u32 s48, s48, 2097152\n\t"
        "s_and_b32 s48, s48, 0xffffff\n\t"
        "s_xor_b32 s48, s48, 0x2000\n\t"
        "s_add_u32 s52, %[al], s48\n\t"
        "s_addc_u32 s53, %[ah], 0\n\t"
        "s_xor_b32 s56, s52, 0x2000\n\t"
        "s_mov_b32 s57, s53\n\t"
        "s_add_u32 s49, s49, 2097152\n\t"
        "s_and_b32 s49, s49, 0xffffff\n\t"
        "s_xor_b32 s49, s49, 0x2000\n\t"
        "v_mov_b64 v[8:9], 0\n\t"
        "v_mov_b64 v[10:11], 0\n\t"
        "s_mov_b64 vcc, s[64:65]\n\t"
        "s_add_u32 s44, s44, 32768\n\t"
        "s_cmp_lt_u32 s44, 98304\n\t"
        "s_cselect_b32 s44, s44, 0\n\t"
        "s_add_u32 s45, s45, 1\n\t"
        "s_cmp_lt_u32 s45, 64\n\t"
        "s_cbranch_scc0 .Lr_end_%=\n\t"
        "v_mov_b32 v21, 0\n\t"
        "s_cmp_eq_u32 s45, 63\n\t"
        "s_cbranch_scc1 .Lr_lastw_%=\n\t"
        "v_mov_b32 v5, 114704\n\t"
        "v_add_u32 v12, s44, v0\n\t"
        ".Lr_fl0_%=:\n\t"
        "ds_read_b32 v13, v5 offset:0\n\t"
        "s_waitcnt lgkmcnt(0)\n\t"
        "v_cmp_lt_u32 s[68:69], s45, v13\n\t"
        "s_nop 3\n\t"
        "s_cmp_lg_u64 s[68:69], 0\n\t"
        "s_cbranch_scc0 .Lr_fl0_%=\n\t"
        "ds_read_b128 v[96:99], v12 offset:0\n\t"
        "ds_read_b128 v[100:103], v12 offset:1024\n\t"
        "ds_read_b128 v[104:107], v12 offset:2048\n\t"
        "ds_read_b128 v[108:111], v12 offset:3072\n\t"
        "ds_read_b128 v[112:115], v12 offset:4096\n\t"
        "ds_read_b128 v[116:119], v12 offset:5120\n\t"
        "ds_read_b128 v[120:123], v12 offset:6144\n\t"
        "ds_read_b128 v[124:127], v12 offset:7168\n\t"
        ".Lr_fl1_%=:\n\t"
        "ds_read_b32 v13, v5 offset:4\n\t"
        "s_waitcnt lgkmcnt(0)\n\t"
        "v_cmp_lt_u32 s[68:69], s45, v13\n\t"
        "s_nop 3\n\t"
        "s_cmp_lg_u64 s[68:69], 0\n\t"
        "s_cbranch_scc0 .Lr_fl1_%=\n\t"
        "ds_read_b128 v[128:131], v12 offset:8192\n\t"
        "ds_read_b128 v[132:135], v12 offset:9216\n\t"
        "ds_read_b128 v[136:139], v12 offset:10240\n\t"
        "ds_read_b128 v[140:143], v12 offset:11264\n\t"
        "ds_read_b128 v[144:147], v12 offset:12288\n\t"
        "ds_read_b128 v[148:151], v12 offset:13312\n\t"
        "ds_read_b128 v[152:155], v12 offset:14336\n\t"
        "ds_read_b128 v[156:159], v12 offset:15360\n\t"
        ".Lr_fl2_%=:\n\t"
        "ds_read_b32 v13, v5 offset:8\n\t"
        "s_waitcnt lgkmcnt(0)\n\t"
        "v_cmp_lt_u32 s[68:69], s45, v13\n\t"
        "s_nop 3\n\t"
        "s_cmp_lg_u64 s[68:69], 0\n\t"
        "s_cbranch_scc0 .Lr_fl2_%=\n\t"
        "ds_read_b128 v[160:163], v12 offset:16384\n\t"
        "ds_read_b128 v[164:167], v12 offset:17408\n\t"
        "ds_read_b128 v[168:171], v12 offset:18432\n\t"
        "ds_read_b128 v[172:175], v12 offset:19456\n\t"
        "ds_read_b128 v[176:179], v12 offset:20480\n\t"
        "ds_read_b128 v[180:183], v12 offset:21504\n\t"
        "ds_read_b128 v[184:187], v12 offset:22528\n\t"
        "ds_read_b128 v[188:191], v12 offset:23552\n\t"
        ".Lr_fl3_%=:\n\t"
        "ds_read_b32 v13, v5 offset:12\n\t"
        "s_waitcnt lgkmcnt(0)\n\t"
        "v_cmp_lt_u32 s[68:69], s45, v13\n\t"
        "s_nop 3\n\t"
        "s_cmp_lg_u64 s[68:69], 0\n\t"
        "s_cbranch_scc0 .Lr_fl3_%=\n\t"
        "ds_read_b128 v[192:195], v12 offset:24576\n\t"
        "ds_read_b128 v[196:199], v12 offset:25600\n\t"
        "ds_read_b128 v[200:203], v12 offset:26624\n\t"
        "ds_read_b128 v[204:207], v12 offset:27648\n\t"
        "ds_read_b128 v[208:211], v12 offset:28672\n\t"
        "ds_read_b128 v[212:215], v12 offset:29696\n\t"
        "ds_read_b128 v[216:219], v12 offset:30720\n\t"
        "ds_read_b128 v[220:223], v12 offset:31744\n\t"
        "s_waitcnt lgkmcnt(0)\n\t"
        "s_memtime s[68:69]\n\t"
        "s_add_u32 s70, s62, 1900\n\t"
        "s_waitcnt lgkmcnt(0)\n\t"
        "s_sub_i32 s70, s70, s68\n\t"
        "s_max_i32 s70, s70, 0\n\t"
        "s_min_i32 s70, s70, 8000\n\t"
        ".Lr_tr_%=:\n\t"
        "s_sub_u32 s70, s70, 33\n\t"
        "s_cbranch_scc0 .Lr_tr_%=\n\t"
        "s_xor_b32 s67, s49, %[tw]\n\t"
        "v_and_b32 v16, 0x1f0, v0\n\t"
        "v_lshlrev_b32 v16, 1, v16\n\t"
        "buffer_load_dword v18, v16, %[rs], s67 offen sc1\n\t"
        ".Lr_s1_%=:\n\t"
        ".Lr_twm_%=:\n\t"
        "s_waitcnt vmcnt(0)\n\t"
        "v_cmp_eq_u32 s[68:69], 0, v18\n\t"
        "v_max_u32_dpp v18, v18, v18 row_shr:1 row_mask:0xf bank_mask:0xf\n\t"
        "s_nop 1\n\t"
        "v_max_u32_dpp v18, v18, v18 row_shr:2 row_mask:0xf bank_mask:0xf\n\t"
        "s_nop 1\n\t"
        "v_max_u32_dpp v18, v18, v18 row_shr:4 row_mask:0xf bank_mask:0xf\n\t"
        "s_nop 1\n\t"
        "v_max_u32_dpp v18, v18, v18 row_shr:8 row_mask:0xf bank_mask:0xf\n\t"
        "s_nop 1\n\t"
        "v_max_u32_dpp v18, v18, v18 row_bcast:15 row_mask:0xf bank_mask:0xf\n\t"
        "s_nop 1\n\t"
        "v_readlane_b32 s70, v18, 63\n\t"
        "s_cmp_lg_u64 s[68:69], 0\n\t"
        "v_readfirstlane_b32 s68, v21\n\t"
        "s_cbranch_scc1 .Lr_twx_%=\n\t"
        ".Lr_twy_%=:\n\t"
        "s_add_i32 s70, s70, 5\n\t"
        "s_add_u32 s67, s70, s68\n\t"
        "s_branch .Lr_loop_%=\n\t"
        ".Lr_lastw_%=:\n\t"
        "s_memtime s[68:69]\n\t"
        "s_add_u32 s70, s62, 1700\n\t"
        "s_waitcnt lgkmcnt(0)\n\t"
        "s_sub_i32 s70, s70, s68\n\t"
        "s_max_i32 s70, s70, 0\n\t"
        "s_min_i32 s70, s70, 8000\n\t"
        ".Lr_tq_%=:\n\t"
        "s_sub_u32 s70, s70, 33\n\t"
        "s_cbranch_scc0 .Lr_tq_%=\n\t"
        "s_xor_b32 s67, s49, %[tw]\n\t"
        "v_and_b32 v16, 0x1f0, v0\n\t"
        "v_lshlrev_b32 v16, 1, v16\n\t"
        "buffer_load_dword v18, v16, %[rs], s67 offen sc1\n\t"
        ".Lr_tlm_%=:\n\t"
        "s_waitcnt vmcnt(0)\n\t"
        "v_cmp_eq_u32 s[68:69], 0, v18\n\t"
        "v_max_u32_dpp v18, v18, v18 row_shr:1 row_mask:0xf bank_mask:0xf\n\t"
        "s_nop 1\n\t"
        "v_max_u32_dpp v18, v18, v18 row_shr:2 row_mask:0xf bank_mask:0xf\n\t"
        "s_nop 1\n\t"
        "v_max_u32_dpp v18, v18, v18 row_shr:4 row_mask:0xf bank_mask:0xf\n\t"
        "s_nop 1\n\t"
        "v_max_u32_dpp v18, v18, v18 row_shr:8 row_mask:0xf bank_mask:0xf\n\t"
        "s_nop 1\n\t"
        "v_max_u32_dpp v18, v18, v18 row_bcast:15 row_mask:0xf bank_mask:0xf\n\t"
        "s_nop 1\n\t"
        "v_readlane_b32 s70, v18, 63\n\t"
        "s_cmp_lg_u64 s[68:69], 0\n\t"
        "v_readfirstlane_b32 s68, v21\n\t"
        "s_cbranch_scc1 .Lr_tlx_%=\n\t"
        ".Lr_tly_%=:\n\t"
        "s_add_i32 s70, s70, -25\n\t"
        "s_add_u32 s67, s70, s68\n\t"
        "v_mov_b32 v5, 114704\n\t"
        "v_add_u32 v12, s44, v0\n\t"
        ".Lr_fl30_%=:\n\t"
        "ds_read_b32 v13, v5 offset:0\n\t"
        "s_waitcnt lgkmcnt(0)\n\t"
        "v_cmp_lt_u32 s[68:69], s45, v13\n\t"
        "s_nop 3\n\t"
        "s_cmp_lg_u64 s[68:69], 0\n\t"
        "s_cbranch_scc0 .Lr_fl30_%=\n\t"
        ".Lr_fl31_%=:\n\t"
        "ds_read_b32 v13, v5 offset:4\n\t"
        "s_waitcnt lgkmcnt(0)\n\t"
        "v_cmp_lt_u32 s[68:69], s45, v13\n\t"
        "s_nop 3\n\t"
        "s_cmp_lg_u64 s[68:69], 0\n\t"
        "s_cbranch_scc0 .Lr_fl31_%=\n\t"
        ".Lr_fl32_%=:\n\t"
        "ds_read_b32 v13, v5 offset:8\n\t"
        "s_waitcnt lgkmcnt(0)\n\t"
        "v_cmp_lt_u32 s[68:69], s45, v13\n\t"
        "s_nop 3\n\t"
        "s_cmp_lg_u64 s[68:69], 0\n\t"
        "s_cbranch_scc0 .Lr_fl32_%=\n\t"
        ".Lr_fl33_%=:\n\t"
        "ds_read_b32 v13, v5 offset:12\n\t"
        "s_waitcnt lgkmcnt(0)\n\t"
        "v_cmp_lt_u32 s[68:69], s45, v13\n\t"
        "s_nop 3\n\t"
        "s_cmp_lg_u64 s[68:69], 0\n\t"
        "s_cbranch_scc0 .Lr_fl33_%=\n\t"
        "s_waitcnt lgkmcnt(0)\n\t"
        ".Lr_tla_%=:\n\t"
        "s_memrealtime s[68:69]\n\t"
        "s_waitcnt lgkmcnt(0)\n\t"
        "s_sub_i32 s68, s67, s68\n\t"
        "s_sub_u32 s69, s68, 5\n\t"
        "s_cmp_lt_u32 s69, 400\n\t"
        "s_cbranch_scc1 .Lr_tla_%=\n\t"
        "s_cmp_lt_i32 s68, 1\n\t"
        "s_cbranch_scc1 .Lr_tlb_%=\n\t"
        "s_nop 2\n\t"
        "s_cmp_lt_i32 s68, 2\n\t"
        "s_cbranch_scc1 .Lr_tlb_%=\n\t"
        "s_nop 2\n\t"
        "s_cmp_lt_i32 s68, 3\n\t"
        "s_cbranch_scc1 .Lr_tlb_%=\n\t"
        "s_nop 2\n\t"
        "s_cmp_lt_i32 s68, 4\n\t"
        "s_cbranch_scc1 .Lr_tlb_%=\n\t"
        "s_nop 2\n\t"
        "s_branch .Lr_tlb_%=\n\t"
        ".Lr_tlb_%=:\n\t"
        "buffer_load_dwordx4 v[224:227], v0, s[52:55], %[om] offen offset:0 sc1\n\t"
        "buffer_load_dwordx4 v[228:231], v0, s[52:55], %[om] offen offset:1024 sc1\n\t"
        "buffer_load_dwordx4 v[232:235], v0, s[52:55], %[om] offen offset:2048 sc1\n\t"
        "buffer_load_dwordx4 v[236:239], v0, s[52:55], %[om] offen offset:3072 sc1\n\t"
        "buffer_load_dwordx4 v[240:243], v0, s[52:55], s47 offen offset:0 sc1\n\t"
        "buffer_load_dwordx4 v[244:247], v0, s[52:55], s47 offen offset:1024 sc1\n\t"
        "buffer_load_dwordx4 v[248:251], v0, s[52:55], s47 offen offset:2048 sc1\n\t"
        "buffer_load_dwordx4 v[252:255], v0, s[52:55], s47 offen offset:3072 sc1\n\t"
        "s_barrier\n\t"
        "s_branch .Lr_last2_%=\n\t"
        ".Lr_last_%=:\n\t"
        "buffer_load_dwordx4 v[224:227], v0, s[52:55], %[om] offen offset:0 sc1\n\t"
        "buffer_load_dwordx4 v[228:231], v0, s[52:55], %[om] offen offset:1024 sc1\n\t"
        "buffer_load_dwordx4 v[232:235], v0, s[52:55], %[om] offen offset:2048 sc1\n\t"
        "buffer_load_dwordx4 v[236:239], v0, s[52:55], %[om] offen offset:3072 sc1\n\t"
        "buffer_load_dwordx4 v[240:243], v0, s[52:55], s47 offen offset:0 sc1\n\t"
        "buffer_load_dwordx4 v[244:247], v0, s[52:55], s47 offen offset:1024 sc1\n\t"
        "buffer_load_dwordx4 v[248:251], v0, s[52:55], s47 offen offset:2048 sc1\n\t"
        "buffer_load_dwordx4 v[252:255], v0, s[52:55], s47 offen offset:3072 sc1\n\t"
        ".Lr_last2_%=:\n\t"
        "s_waitcnt vmcnt(0)\n\t"
        "v_max_u32 v4, v224, v225\n\t"
        "v_max3_u32 v4, v4, v226, v227\n\t"
        "v_max3_u32 v4, v4, v228, v229\n\t"
        "v_max3_u32 v4, v4, v230, v231\n\t"
        "v_max3_u32 v4, v4, v232, v233\n\t"
        "v_max3_u32 v4, v4, v234, v235\n\t"
        "v_max3_u32 v4, v4, v236, v237\n\t"
        "v_max3_u32 v4, v4, v238, v239\n\t"
        "v_max3_u32 v4, v4, v240, v241\n\t"
        "v_max3_u32 v4, v4, v242, v243\n\t"
        "v_max3_u32 v4, v4, v244, v245\n\t"
        "v_max3_u32 v4, v4, v246, v247\n\t"
        "v_max3_u32 v4, v4, v248, v249\n\t"
        "v_max3_u32 v4, v4, v250, v251\n\t"
        "v_max3_u32 v4, v4, v252, v253\n\t"
        "v_max3_u32 v4, v4, v254, v255\n\t"
        "v_cmp_eq_u32 s[68:69], s66, v4\n\t"
        "s_nop 3\n\t"
        "s_cmp_lg_u64 s[68:69], 0\n\t"
        "s_cbranch_scc0 .Lr_pl_%=\n\t"
        "s_sub_u32 s46, s46, 1\n\t"
        "s_cbranch_scc0 .Lr_last_%=\n\t"
        "s_mov_b32 s46, 0\n\t"
        "s_branch .Lr_pl_%=\n\t"
        ".Lr_dma_%=:\n\t"
        "s_sub_u32 s52, %[w], 4\n\t"
        "s_max_i32 s52, s52, 0\n\t"
        "s_xor_b32 s50, %[oy], %[t0]\n\t"
        "s_lshl_b32 s70, s52, 14\n\t"
        "s_add_u32 s50, s50, s70\n\t"
        "s_lshl_b32 s70, s52, 2\n\t"
        "s_add_u32 s70, s70, 114704\n\t"
        "v_mov_b32 v10, s70\n\t"
        "s_lshl_b32 s52, s52, 13\n\t"
        "v_add_u32 v11, s52, v0\n\t"
        "s_setprio 2\n\t"
        "v_mov_b32 v12, s66\n\t"
        "v_mov_b32 v13, s66\n\t"
        "v_mov_b32 v14, s66\n\t"
        "v_mov_b32 v15, s66\n\t"
        "v_and_b32 v5, 0xf0, v0\n\t"
        "s_lshl_b32 s57, %[oo], 2\n\t"
        "s_add_u32 s57, s57, 344064\n\t"
        "s_mov_b32 s58, 0\n\t"
        "s_mov_b32 s59, 0\n\t"
        "v_bfe_u32 v48, v0, 2, 7\n\t"
        "s_add_u32 s64, s49, 2097152\n\t"
        "s_and_b32 s64, s64, 0xffffff\n\t"
        "s_add_u32 s71, s64, %[oa]\n\t"
        "s_add_u32 s71, s71, 65536\n\t"
        "s_add_u32 s72, s64, %[oa]\n\t"
        "s_add_u32 s72, s72, 65536\n\t"
        "s_xor_b32 s72, s72, 0x2000\n\t"
        "s_add_u32 s73, s64, %[oa]\n\t"
        "s_add_u32 s73, s73, 98304\n\t"
        "s_add_u32 s74, s64, %[oa]\n\t"
        "s_add_u32 s74, s74, 98304\n\t"
        "s_xor_b32 s74, s74, 0x2000\n\t"
        "s_add_u32 s75, s64, %[oa]\n\t"
        "s_add_u32 s75, s75, 131072\n\t"
        "s_add_u32 s76, s64, %[oa]\n\t"
        "s_add_u32 s76, s76, 131072\n\t"
        "s_xor_b32 s76, s76, 0x2000\n\t"
        "s_add_u32 s77, s64, %[oa]\n\t"
        "s_add_u32 s77, s77, 163840\n\t"
        "s_add_u32 s78, s64, %[oa]\n\t"
        "s_add_u32 s78, s78, 163840\n\t"
        "s_xor_b32 s78, s78, 0x2000\n\t"
        "s_add_u32 s67, s48, 2097152\n\t"
        "s_and_b32 s67, s67, 0xffffff\n\t"
        "s_cmp_lt_u32 s45, 61\n\t"
        "s_cselect_b32 s65, 0, 0x7f000000\n\t"
        "s_xor_b32 s67, s67, 8192\n\t"
        "s_add_u32 s67, s67, %[om]\n\t"
        "s_lshr_b32 s68, s52, 2\n\t"
        "s_add_u32 s67, s67, s68\n\t"
        "s_add_u32 s67, s67, s65\n\t"
        "s_cmp_eq_u32 %[w], 3\n\t"
        "s_cselect_b32 s65, s65, 0x7f000000\n\t"
        "v_mov_b32 v8, 0x7f000000\n\t"
        "s_xor_b32 s70, s71, 8192\n\t"
        "v_cmp_eq_u32 vcc, 0, v6\n\t"
        "v_mov_b32 v4, s70\n\t"
        "s_nop 0\n\t"
        "v_cndmask_b32 v8, v8, v4, vcc\n\t"
        "s_xor_b32 s70, s72, 8192\n\t"
        "v_cmp_eq_u32 vcc, 1, v6\n\t"
        "v_mov_b32 v4, s70\n\t"
        "s_nop 0\n\t"
        "v_cndmask_b32 v8, v8, v4, vcc\n\t"
        "s_xor_b32 s70, s73, 8192\n\t"
        "v_cmp_eq_u32 vcc, 2, v6\n\t"
        "v_mov_b32 v4, s70\n\t"
        "s_nop 0\n\t"
        "v_cndmask_b32 v8, v8, v4, vcc\n\t"
        "s_xor_b32 s70, s74, 8192\n\t"
        "v_cmp_eq_u32 vcc, 3, v6\n\t"
        "v_mov_b32 v4, s70\n\t"
        "s_nop 0\n\t"
        "v_cndmask_b32 v8, v8, v4, vcc\n\t"
        "v_add_u32 v8, v8, v5\n\t"
        "v_mov_b32 v9, 0x7f000000\n\t"
        "s_xor_b32 s70, s75, 8192\n\t"
        "v_cmp_eq_u32 vcc, 0, v6\n\t"
        "v_mov_b32 v4, s70\n\t"
        "s_nop 0\n\t"
        "v_cndmask_b32 v9, v9, v4, vcc\n\t"
        "s_xor_b32 s70, s76, 8192\n\t"
        "v_cmp_eq_u32 vcc, 1, v6\n\t"
        "v_mov_b32 v4, s70\n\t"
        "s_nop 0\n\t"
        "v_cndmask_b32 v9, v9, v4, vcc\n\t"
        "s_xor_b32 s70, s77, 8192\n\t"
        "v_cmp_eq_u32 vcc, 2, v6\n\t"
        "v_mov_b32 v4, s70\n\t"
        "s_nop 0\n\t"
        "v_cndmask_b32 v9, v9, v4, vcc\n\t"
        "s_xor_b32 s70, s78, 8192\n\t"
        "v_cmp_eq_u32 vcc, 3, v6\n\t"
        "v_mov_b32 v4, s70\n\t"
        "s_nop 0\n\t"
        "v_cndmask_b32 v9, v9, v4, vcc\n\t"
        "v_add_u32 v9, v9, v5\n\t"
        "buffer_store_dwordx4 v[12:15], v8, %[rs], s65 offen sc1\n\t"
        "buffer_store_dwordx4 v[12:15], v9, %[rs], s65 offen sc1\n\t"
        "buffer_store_dwordx4 v[12:15], v0, %[rs], s67 offen sc1\n\t"
        "buffer_store_dwordx4 v[12:15], v0, %[rs], s67 offen offset:1024 sc1\n\t"
        "s_add_u32 s64, s49, 4194304\n\t"
        "s_and_b32 s64, s64, 0xffffff\n\t"
        "s_add_u32 s71, s64, %[oa]\n\t"
        "s_add_u32 s71, s71, 65536\n\t"
        "s_add_u32 s72, s64, %[oa]\n\t"
        "s_add_u32 s72, s72, 65536\n\t"
        "s_xor_b32 s72, s72, 0x2000\n\t"
        "s_add_u32 s73, s64, %[oa]\n\t"
        "s_add_u32 s73, s73, 98304\n\t"
        "s_add_u32 s74, s64, %[oa]\n\t"
        "s_add_u32 s74, s74, 98304\n\t"
        "s_xor_b32 s74, s74, 0x2000\n\t"
        "s_add_u32 s75, s64, %[oa]\n\t"
        "s_add_u32 s75, s75, 131072\n\t"
        "s_add_u32 s76, s64, %[oa]\n\t"
        "s_add_u32 s76, s76, 131072\n\t"
        "s_xor_b32 s76, s76, 0x2000\n\t"
        "s_add_u32 s77, s64, %[oa]\n\t"
        "s_add_u32 s77, s77, 163840\n\t"
        "s_add_u32 s78, s64, %[oa]\n\t"
        "s_add_u32 s78, s78, 163840\n\t"
        "s_xor_b32 s78, s78, 0x2000\n\t"
        "s_add_u32 s67, s48, 4194304\n\t"
        "s_and_b32 s67, s67, 0xffffff\n\t"
        "s_cmp_lt_u32 s45, 61\n\t"
        "s_cselect_b32 s65, 0, 0x7f000000\n\t"
        "s_xor_b32 s67, s67, 0\n\t"
        "s_add_u32 s67, s67, %[om]\n\t"
        "s_lshr_b32 s68, s52, 2\n\t"
        "s_add_u32 s67, s67, s68\n\t"
        "s_add_u32 s67, s67, s65\n\t"
        "s_cmp_eq_u32 %[w], 3\n\t"
        "s_cselect_b32 s65, s65, 0x7f000000\n\t"
        "v_mov_b32 v8, 0x7f000000\n\t"
        "s_xor_b32 s70, s71, 0\n\t"
        "v_cmp_eq_u32 vcc, 0, v6\n\t"
        "v_mov_b32 v4, s70\n\t"
        "s_nop 0\n\t"
        "v_cndmask_b32 v8, v8, v4, vcc\n\t"
        "s_xor_b32 s70, s72, 0\n\t"
        "v_cmp_eq_u32 vcc, 1, v6\n\t"
        "v_mov_b32 v4, s70\n\t"
        "s_nop 0\n\t"
        "v_cndmask_b32 v8, v8, v4, vcc\n\t"
        "s_xor_b32 s70, s73, 0\n\t"
        "v_cmp_eq_u32 vcc, 2, v6\n\t"
        "v_mov_b32 v4, s70\n\t"
        "s_nop 0\n\t"
        "v_cndmask_b32 v8, v8, v4, vcc\n\t"
        "s_xor_b32 s70, s74, 0\n\t"
        "v_cmp_eq_u32 vcc, 3, v6\n\t"
        "v_mov_b32 v4, s70\n\t"
        "s_nop 0\n\t"
        "v_cndmask_b32 v8, v8, v4, vcc\n\t"
        "v_add_u32 v8, v8, v5\n\t"
        "v_mov_b32 v9, 0x7f000000\n\t"
        "s_xor_b32 s70, s75, 0\n\t"
        "v_cmp_eq_u32 vcc, 0, v6\n\t"
        "v_mov_b32 v4, s70\n\t"
        "s_nop 0\n\t"
        "v_cndmask_b32 v9, v9, v4, vcc\n\t"
        "s_xor_b32 s70, s76, 0\n\t"
        "v_cmp_eq_u32 vcc, 1, v6\n\t"
        "v_mov_b32 v4, s70\n\t"
        "s_nop 0\n\t"
        "v_cndmask_b32 v9, v9, v4, vcc\n\t"
        "s_xor_b32 s70, s77, 0\n\t"
        "v_cmp_eq_u32 vcc, 2, v6\n\t"
        "v_mov_b32 v4, s70\n\t"
        "s_nop 0\n\t"
        "v_cndmask_b32 v9, v9, v4, vcc\n\t"
        "s_xor_b32 s70, s78, 0\n\t"
        "v_cmp_eq_u32 vcc, 3, v6\n\t"
        "v_mov_b32 v4, s70\n\t"
        "s_nop 0\n\t"
        "v_cndmask_b32 v9, v9, v4, vcc\n\t"
        "v_add_u32 v9, v9, v5\n\t"
        "buffer_store_dwordx4 v[12:15], v8, %[rs], s65 offen sc1\n\t"
        "buffer_store_dwordx4 v[12:15], v9, %[rs], s65 offen sc1\n\t"
        "buffer_store_dwordx4 v[12:15], v0, %[rs], s67 offen sc1\n\t"
        "buffer_store_dwordx4 v[12:15], v0, %[rs], s67 offen offset:1024 sc1\n\t"
        ".Lr_an0_%=:\n\t"
        "buffer_load_dword v49, v48, %[rs], s57 offen sc1\n\t"
        "s_waitcnt vmcnt(0)\n\t"
        "v_cmp_eq_u32 vcc, s66, v49\n\t"
        "s_cbranch_vccnz .Lr_an0f_%=\n\t"
        ".Lr_an0k_%=:\n\t"
        "s_add_u32 s57, s57, 8192\n\t"
        "s_add_u32 s69, s50, 4096\n\t"
        "buffer_load_dwordx4 v[16:19], v0, %[rs], s50 offen offset:0 sc1\n\t"
        "buffer_load_dwordx4 v[20:23], v0, %[rs], s50 offen offset:1024 sc1\n\t"
        "buffer_load_dwordx4 v[24:27], v0, %[rs], s50 offen offset:2048 sc1\n\t"
        "buffer_load_dwordx4 v[28:31], v0, %[rs], s50 offen offset:3072 sc1\n\t"
        "buffer_load_dwordx4 v[32:35], v0, %[rs], s69 offen offset:0 sc1\n\t"
        "buffer_load_dwordx4 v[36:39], v0, %[rs], s69 offen offset:1024 sc1\n\t"
        "buffer_load_dwordx4 v[40:43], v0, %[rs], s69 offen offset:2048 sc1\n\t"
        "buffer_load_dwordx4 v[44:47], v0, %[rs], s69 offen offset:3072 sc1\n\t"
        "v_add_u32 v7, s59, v11\n\t"
        "s_add_u32 s59, s59, 32768\n\t"
        "s_cmp_lt_u32 s59, 98304\n\t"
        "s_cselect_b32 s59, s59, 0\n\t"
        "s_add_u32 s58, s58, 1\n\t"
        "v_mov_b32 v4, s58\n\t"
        "s_add_u32 s50, s50, 4194304\n\t"
        "s_and_b32 s70, s58, 7\n\t"
        "s_cselect_b32 s70, 0, 33554432\n\t"
        "s_sub_u32 s50, s50, s70\n\t"
        "s_waitcnt vmcnt(7)\n\t"
        "ds_write_b128 v7, v[16:19] offset:0\n\t"
        "s_waitcnt vmcnt(6)\n\t"
        "ds_write_b128 v7, v[20:23] offset:1024\n\t"
        "s_waitcnt vmcnt(5)\n\t"
        "ds_write_b128 v7, v[24:27] offset:2048\n\t"
        "s_waitcnt vmcnt(4)\n\t"
        "ds_write_b128 v7, v[28:31] offset:3072\n\t"
        "s_waitcnt vmcnt(3)\n\t"
        "ds_write_b128 v7, v[32:35] offset:4096\n\t"
        "s_waitcnt vmcnt(2)\n\t"
        "ds_write_b128 v7, v[36:39] offset:5120\n\t"
        "s_waitcnt vmcnt(1)\n\t"
        "ds_write_b128 v7, v[40:43] offset:6144\n\t"
        "s_waitcnt vmcnt(0)\n\t"
        "ds_write_b128 v7, v[44:47] offset:7168\n\t"
        "ds_write_b32 v10, v4\n\t"
        ".Lr_an1_%=:\n\t"
        "buffer_load_dword v49, v48, %[rs], s57 offen sc1\n\t"
        "s_waitcnt vmcnt(0)\n\t"
        "v_cmp_eq_u32 vcc, s66, v49\n\t"
        "s_cbranch_vccnz .Lr_an1f_%=\n\t"
        ".Lr_an1k_%=:\n\t"
        "s_add_u32 s57, s57, 8192\n\t"
        "s_add_u32 s69, s50, 4096\n\t"
        "buffer_load_dwordx4 v[16:19], v0, %[rs], s50 offen offset:0 sc1\n\t"
        "buffer_load_dwordx4 v[20:23], v0, %[rs], s50 offen offset:1024 sc1\n\t"
        "buffer_load_dwordx4 v[24:27], v0, %[rs], s50 offen offset:2048 sc1\n\t"
        "buffer_load_dwordx4 v[28:31], v0, %[rs], s50 offen offset:3072 sc1\n\t"
        "buffer_load_dwordx4 v[32:35], v0, %[rs], s69 offen offset:0 sc1\n\t"
        "buffer_load_dwordx4 v[36:39], v0, %[rs], s69 offen offset:1024 sc1\n\t"
        "buffer_load_dwordx4 v[40:43], v0, %[rs], s69 offen offset:2048 sc1\n\t"
        "buffer_load_dwordx4 v[44:47], v0, %[rs], s69 offen offset:3072 sc1\n\t"
        "v_add_u32 v7, s59, v11\n\t"
        "s_add_u32 s59, s59, 32768\n\t"
        "s_cmp_lt_u32 s59, 98304\n\t"
        "s_cselect_b32 s59, s59, 0\n\t"
        "s_add_u32 s58, s58, 1\n\t"
        "v_mov_b32 v4, s58\n\t"
        "s_add_u32 s50, s50, 4194304\n\t"
        "s_and_b32 s70, s58, 7\n\t"
        "s_cselect_b32 s70, 0, 33554432\n\t"
        "s_sub_u32 s50, s50, s70\n\t"
        "s_waitcnt vmcnt(7)\n\t"
        "ds_write_b128 v7, v[16:19] offset:0\n\t"
        "s_waitcnt vmcnt(6)\n\t"
        "ds_write_b128 v7, v[20:23] offset:1024\n\t"
        "s_waitcnt vmcnt(5)\n\t"
        "ds_write_b128 v7, v[24:27] offset:2048\n\t"
        "s_waitcnt vmcnt(4)\n\t"
        "ds_write_b128 v7, v[28:31] offset:3072\n\t"
        "s_waitcnt vmcnt(3)\n\t"
        "ds_write_b128 v7, v[32:35] offset:4096\n\t"
        "s_waitcnt vmcnt(2)\n\t"
        "ds_write_b128 v7, v[36:39] offset:5120\n\t"
        "s_waitcnt vmcnt(1)\n\t"
        "ds_write_b128 v7, v[40:43] offset:6144\n\t"
        "s_waitcnt vmcnt(0)\n\t"
        "ds_write_b128 v7, v[44:47] offset:7168\n\t"
        "ds_write_b32 v10, v4\n\t"
        ".Lr_dloop_%=:\n\t"
        "s_add_u32 s64, s49, 6291456\n\t"
        "s_and_b32 s64, s64, 0xffffff\n\t"
        "s_add_u32 s71, s64, %[oa]\n\t"
        "s_add_u32 s71, s71, 65536\n\t"
        "s_add_u32 s72, s64, %[oa]\n\t"
        "s_add_u32 s72, s72, 65536\n\t"
        "s_xor_b32 s72, s72, 0x2000\n\t"
        "s_add_u32 s73, s64, %[oa]\n\t"
        "s_add_u32 s73, s73, 98304\n\t"
        "s_add_u32 s74, s64, %[oa]\n\t"
        "s_add_u32 s74, s74, 98304\n\t"
        "s_xor_b32 s74, s74, 0x2000\n\t"
        "s_add_u32 s75, s64, %[oa]\n\t"
        "s_add_u32 s75, s75, 131072\n\t"
        "s_add_u32 s76, s64, %[oa]\n\t"
        "s_add_u32 s76, s76, 131072\n\t"
        "s_xor_b32 s76, s76, 0x2000\n\t"
        "s_add_u32 s77, s64, %[oa]\n\t"
        "s_add_u32 s77, s77, 163840\n\t"
        "s_add_u32 s78, s64, %[oa]\n\t"
        "s_add_u32 s78, s78, 163840\n\t"
        "s_xor_b32 s78, s78, 0x2000\n\t"
        "s_add_u32 s67, s48, 6291456\n\t"
        "s_and_b32 s67, s67, 0xffffff\n\t"
        "s_cmp_lt_u32 s45, 61\n\t"
        "s_cselect_b32 s65, 0, 0x7f000000\n\t"
        "s_xor_b32 s67, s67, 8192\n\t"
        "s_add_u32 s67, s67, %[om]\n\t"
        "s_lshr_b32 s68, s52, 2\n\t"
        "s_add_u32 s67, s67, s68\n\t"
        "s_add_u32 s67, s67, s65\n\t"
        "s_cmp_eq_u32 %[w], 3\n\t"
        "s_cselect_b32 s65, s65, 0x7f000000\n\t"
        "v_mov_b32 v8, 0x7f000000\n\t"
        "s_xor_b32 s70, s71, 8192\n\t"
        "v_cmp_eq_u32 vcc, 0, v6\n\t"
        "v_mov_b32 v4, s70\n\t"
        "s_nop 0\n\t"
        "v_cndmask_b32 v8, v8, v4, vcc\n\t"
        "s_xor_b32 s70, s72, 8192\n\t"
        "v_cmp_eq_u32 vcc, 1, v6\n\t"
        "v_mov_b32 v4, s70\n\t"
        "s_nop 0\n\t"
        "v_cndmask_b32 v8, v8, v4, vcc\n\t"
        "s_xor_b32 s70, s73, 8192\n\t"
        "v_cmp_eq_u32 vcc, 2, v6\n\t"
        "v_mov_b32 v4, s70\n\t"
        "s_nop 0\n\t"
        "v_cndmask_b32 v8, v8, v4, vcc\n\t"
        "s_xor_b32 s70, s74, 8192\n\t"
        "v_cmp_eq_u32 vcc, 3, v6\n\t"
        "v_mov_b32 v4, s70\n\t"
        "s_nop 0\n\t"
        "v_cndmask_b32 v8, v8, v4, vcc\n\t"
        "v_add_u32 v8, v8, v5\n\t"
        "v_mov_b32 v9, 0x7f000000\n\t"
        "s_xor_b32 s70, s75, 8192\n\t"
        "v_cmp_eq_u32 vcc, 0, v6\n\t"
        "v_mov_b32 v4, s70\n\t"
        "s_nop 0\n\t"
        "v_cndmask_b32 v9, v9, v4, vcc\n\t"
        "s_xor_b32 s70, s76, 8192\n\t"
        "v_cmp_eq_u32 vcc, 1, v6\n\t"
        "v_mov_b32 v4, s70\n\t"
        "s_nop 0\n\t"
        "v_cndmask_b32 v9, v9, v4, vcc\n\t"
        "s_xor_b32 s70, s77, 8192\n\t"
        "v_cmp_eq_u32 vcc, 2, v6\n\t"
        "v_mov_b32 v4, s70\n\t"
        "s_nop 0\n\t"
        "v_cndmask_b32 v9, v9, v4, vcc\n\t"
        "s_xor_b32 s70, s78, 8192\n\t"
        "v_cmp_eq_u32 vcc, 3, v6\n\t"
        "v_mov_b32 v4, s70\n\t"
        "s_nop 0\n\t"
        "v_cndmask_b32 v9, v9, v4, vcc\n\t"
        "v_add_u32 v9, v9, v5\n\t"
        "s_cmp_lt_u32 s58, 64\n\t"
        "s_barrier\n\t"
        "s_cbranch_scc0 .Lr_d2_%=\n\t"
        "s_cmp_lt_u32 s45, 4\n\t"
        "s_cbranch_scc1 .Lr_d4_%=\n\t"
        "s_nop 15\n\t"
        "s_nop 15\n\t"
        "s_nop 15\n\t"
        "s_nop 15\n\t"
        "s_nop 15\n\t"
        "s_nop 15\n\t"
        "s_nop 15\n\t"
        "s_nop 15\n\t"
        "s_nop 15\n\t"
        "s_nop 15\n\t"
        "s_nop 15\n\t"
        "s_nop 15\n\t"
        "s_nop 15\n\t"
        "s_nop 15\n\t"
        "s_nop 15\n\t"
        "s_nop 15\n\t"
        "s_nop 15\n\t"
        "s_nop 15\n\t"
        "s_nop 15\n\t"
        "s_nop 15\n\t"
        "s_nop 15\n\t"
        "s_nop 13\n\t"
        ".Lr_d3_%=:\n\t"
        "s_add_u32 s69, s50, 4096\n\t"
        "buffer_load_dwordx4 v[16:19], v0, %[rs], s50 offen offset:0 sc1\n\t"
        "buffer_load_dwordx4 v[20:23], v0, %[rs], s50 offen offset:1024 sc1\n\t"
        "buffer_load_dwordx4 v[24:27], v0, %[rs], s50 offen offset:2048 sc1\n\t"
        "buffer_load_dwordx4 v[28:31], v0, %[rs], s50 offen offset:3072 sc1\n\t"
        "buffer_load_dwordx4 v[32:35], v0, %[rs], s69 offen offset:0 sc1\n\t"
        "buffer_load_dwordx4 v[36:39], v0, %[rs], s69 offen offset:1024 sc1\n\t"
        "buffer_load_dwordx4 v[40:43], v0, %[rs], s69 offen offset:2048 sc1\n\t"
        "buffer_load_dwordx4 v[44:47], v0, %[rs], s69 offen offset:3072 sc1\n\t"
        "v_add_u32 v7, s59, v11\n\t"
        "s_add_u32 s59, s59, 32768\n\t"
        "s_cmp_lt_u32 s59, 98304\n\t"
        "s_cselect_b32 s59, s59, 0\n\t"
        "s_add_u32 s58, s58, 1\n\t"
        "v_mov_b32 v4, s58\n\t"
        "s_add_u32 s50, s50, 4194304\n\t"
        "s_and_b32 s70, s58, 7\n\t"
        "s_cselect_b32 s70, 0, 33554432\n\t"
        "s_sub_u32 s50, s50, s70\n\t"
        "s_waitcnt vmcnt(7)\n\t"
        "ds_write_b128 v7, v[16:19] offset:0\n\t"
        "s_waitcnt vmcnt(6)\n\t"
        "ds_write_b128 v7, v[20:23] offset:1024\n\t"
        "s_waitcnt vmcnt(5)\n\t"
        "ds_write_b128 v7, v[24:27] offset:2048\n\t"
        "s_waitcnt vmcnt(4)\n\t"
        "ds_write_b128 v7, v[28:31] offset:3072\n\t"
        "s_waitcnt vmcnt(3)\n\t"
        "ds_write_b128 v7, v[32:35] offset:4096\n\t"
        "s_waitcnt vmcnt(2)\n\t"
        "ds_write_b128 v7, v[36:39] offset:5120\n\t"
        "s_waitcnt vmcnt(1)\n\t"
        "ds_write_b128 v7, v[40:43] offset:6144\n\t"
        "s_waitcnt vmcnt(0)\n\t"
        "ds_write_b128 v7, v[44:47] offset:7168\n\t"
        "ds_write_b32 v10, v4\n\t"
        ".Lr_d2_%=:\n\t"
        "buffer_store_dwordx4 v[12:15], v8, %[rs], s65 offen sc1\n\t"
        "buffer_store_dwordx4 v[12:15], v9, %[rs], s65 offen sc1\n\t"
        "buffer_store_dwordx4 v[12:15], v0, %[rs], s67 offen sc1\n\t"
        "buffer_store_dwordx4 v[12:15], v0, %[rs], s67 offen offset:1024 sc1\n\t"
        "s_waitcnt vmcnt(0)\n\t"
        "s_add_u32 s44, s44, 32768\n\t"
        "s_cmp_lt_u32 s44, 98304\n\t"
        "s_cselect_b32 s44, s44, 0\n\t"
        "s_add_u32 s48, s48, 2097152\n\t"
        "s_and_b32 s48, s48, 0xffffff\n\t"
        "s_xor_b32 s48, s48, 0x2000\n\t"
        "s_add_u32 s49, s49, 2097152\n\t"
        "s_and_b32 s49, s49, 0xffffff\n\t"
        "s_xor_b32 s49, s49, 0x2000\n\t"
        "s_add_u32 s45, s45, 1\n\t"
        "s_cmp_lt_u32 s45, 64\n\t"
        "s_cbranch_scc1 .Lr_dloop_%=\n\t"
        "s_branch .Lr_end_%=\n\t"
        ".Lr_d4_%=:\n\t"
        "s_cmp_eq_u32 s45, 0\n\t"
        "s_cbranch_scc1 .Lr_d2_%=\n\t"
        "s_nop 15\n\t"
        "s_nop 15\n\t"
        "s_nop 15\n\t"
        "s_nop 15\n\t"
        "s_nop 15\n\t"
        "s_nop 15\n\t"
        "s_nop 15\n\t"
        "s_nop 12\n\t"
        "s_cmp_lt_u32 s45, 3\n\t"
        "s_cbranch_scc1 .Lr_d3_%=\n\t"
        "s_add_u32 s69, s50, 4096\n\t"
        "buffer_load_dwordx4 v[16:19], v0, %[rs], s50 offen offset:0 sc1\n\t"
        "buffer_load_dwordx4 v[20:23], v0, %[rs], s50 offen offset:1024 sc1\n\t"
        "buffer_load_dwordx4 v[24:27], v0, %[rs], s50 offen offset:2048 sc1\n\t"
        "buffer_load_dwordx4 v[28:31], v0, %[rs], s50 offen offset:3072 sc1\n\t"
        "buffer_load_dwordx4 v[32:35], v0, %[rs], s69 offen offset:0 sc1\n\t"
        "buffer_load_dwordx4 v[36:39], v0, %[rs], s69 offen offset:1024 sc1\n\t"
        "buffer_load_dwordx4 v[40:43], v0, %[rs], s69 offen offset:2048 sc1\n\t"
        "buffer_load_dwordx4 v[44:47], v0, %[rs], s69 offen offset:3072 sc1\n\t"
        "v_add_u32 v7, s59, v11\n\t"
        "s_add_u32 s59, s59, 32768\n\t"
        "s_cmp_lt_u32 s59, 98304\n\t"
        "s_cselect_b32 s59, s59, 0\n\t"
        "s_add_u32 s58, s58, 1\n\t"
        "v_mov_b32 v4, s58\n\t"
        "s_add_u32 s50, s50, 4194304\n\t"
        "s_and_b32 s70, s58, 7\n\t"
        "s_cselect_b32 s70, 0, 33554432\n\t"
        "s_sub_u32 s50, s50, s70\n\t"
        "s_waitcnt vmcnt(7)\n\t"
        "ds_write_b128 v7, v[16:19] offset:0\n\t"
        "s_waitcnt vmcnt(6)\n\t"
        "ds_write_b128 v7, v[20:23] offset:1024\n\t"
        "s_waitcnt vmcnt(5)\n\t"
        "ds_write_b128 v7, v[24:27] offset:2048\n\t"
        "s_waitcnt vmcnt(4)\n\t"
        "ds_write_b128 v7, v[28:31] offset:3072\n\t"
        "s_waitcnt vmcnt(3)\n\t"
        "ds_write_b128 v7, v[32:35] offset:4096\n\t"
        "s_waitcnt vmcnt(2)\n\t"
        "ds_write_b128 v7, v[36:39] offset:5120\n\t"
        "s_waitcnt vmcnt(1)\n\t"
        "ds_write_b128 v7, v[40:43] offset:6144\n\t"
        "s_waitcnt vmcnt(0)\n\t"
        "ds_write_b128 v7, v[44:47] offset:7168\n\t"
        "ds_write_b32 v10, v4\n\t"
        "s_branch .Lr_d3_%=\n\t"
        ".Lr_ox_%=:\n\t"
        "v_and_b32 v7, 15, v5\n\t"
        "v_lshlrev_b32 v3, 12, v7\n\t"
        "v_lshl_add_u32 v3, v6, 3, v3\n\t"
        "s_mov_b32 s52, %[oo]\n\t"
        "s_cmp_eq_u32 %[w], 2\n\t"
        "s_cbranch_scc1 .Lr_oloop0_%=\n\t"
        "s_add_u32 s52, s52, 65536\n\t"
        ".Lr_oloop1_%=:\n\t"
        "s_and_b32 s70, s45, 1\n\t"
        "v_add_u32 v1, s44, v0\n\t"
        "s_lshl_b32 s70, s70, 13\n\t"
        "s_add_u32 s70, s70, 98304\n\t"
        "v_add_u32 v2, s70, v0\n\t"
        "v_mov_b32 v5, 114720\n\t"
        "s_cmp_eq_u32 s45, 63\n\t"
        "s_barrier\n\t"
        "s_cbranch_scc1 .Lr_ol1_%=\n\t"
        "s_nop 15\n\t"
        "s_nop 15\n\t"
        "s_nop 15\n\t"
        "s_nop 15\n\t"
        "s_nop 10\n\t"
        ".Lr_om1_%=:\n\t"
        "s_add_u32 s70, s48, 196608\n\t"
        "buffer_load_dwordx4 v[96:99], v0, %[rs], s70 offen offset:0 sc1\n\t"
        "buffer_load_dwordx4 v[100:103], v0, %[rs], s70 offen offset:1024 sc1\n\t"
        "buffer_load_dwordx4 v[104:107], v0, %[rs], s70 offen offset:2048 sc1\n\t"
        "buffer_load_dwordx4 v[108:111], v0, %[rs], s70 offen offset:3072 sc1\n\t"
        "s_add_u32 s70, s48, 200704\n\t"
        "buffer_load_dwordx4 v[112:115], v0, %[rs], s70 offen offset:0 sc1\n\t"
        "buffer_load_dwordx4 v[116:119], v0, %[rs], s70 offen offset:1024 sc1\n\t"
        "buffer_load_dwordx4 v[120:123], v0, %[rs], s70 offen offset:2048 sc1\n\t"
        "buffer_load_dwordx4 v[124:127], v0, %[rs], s70 offen offset:3072 sc1\n\t"
        "s_add_u32 s70, s48, 229376\n\t"
        "buffer_load_dwordx4 v[128:131], v0, %[rs], s70 offen offset:0 sc1\n\t"
        "buffer_load_dwordx4 v[132:135], v0, %[rs], s70 offen offset:1024 sc1\n\t"
        "buffer_load_dwordx4 v[136:139], v0, %[rs], s70 offen offset:2048 sc1\n\t"
        "buffer_load_dwordx4 v[140:143], v0, %[rs], s70 offen offset:3072 sc1\n\t"
        "s_add_u32 s70, s48, 233472\n\t"
        "buffer_load_dwordx4 v[144:147], v0, %[rs], s70 offen offset:0 sc1\n\t"
        "buffer_load_dwordx4 v[148:151], v0, %[rs], s70 offen offset:1024 sc1\n\t"
        "buffer_load_dwordx4 v[152:155], v0, %[rs], s70 offen offset:2048 sc1\n\t"
        "buffer_load_dwordx4 v[156:159], v0, %[rs], s70 offen offset:3072 sc1\n\t"
        "s_add_u32 s70, s48, 262144\n\t"
        "buffer_load_dwordx4 v[160:163], v0, %[rs], s70 offen offset:0 sc1\n\t"
        "buffer_load_dwordx4 v[164:167], v0, %[rs], s70 offen offset:1024 sc1\n\t"
        "buffer_load_dwordx4 v[168:171], v0, %[rs], s70 offen offset:2048 sc1\n\t"
        "buffer_load_dwordx4 v[172:175], v0, %[rs], s70 offen offset:3072 sc1\n\t"
        "s_add_u32 s70, s48, 266240\n\t"
        "buffer_load_dwordx4 v[176:179], v0, %[rs], s70 offen offset:0 sc1\n\t"
        "buffer_load_dwordx4 v[180:183], v0, %[rs], s70 offen offset:1024 sc1\n\t"
        "buffer_load_dwordx4 v[184:187], v0, %[rs], s70 offen offset:2048 sc1\n\t"
        "buffer_load_dwordx4 v[188:191], v0, %[rs], s70 offen offset:3072 sc1\n\t"
        "ds_read_b128 v[16:19], v1 offset:0\n\t"
        "ds_read_b128 v[20:23], v1 offset:1024\n\t"
        "ds_read_b128 v[24:27], v1 offset:2048\n\t"
        "ds_read_b128 v[28:31], v1 offset:3072\n\t"
        "ds_read_b128 v[32:35], v1 offset:4096\n\t"
        "ds_read_b128 v[36:39], v1 offset:5120\n\t"
        "ds_read_b128 v[40:43], v1 offset:6144\n\t"
        "ds_read_b128 v[44:47], v1 offset:7168\n\t"
        "ds_read_b128 v[48:51], v1 offset:8192\n\t"
        "ds_read_b128 v[52:55], v1 offset:9216\n\t"
        "ds_read_b128 v[56:59], v1 offset:10240\n\t"
        "ds_read_b128 v[60:63], v1 offset:11264\n\t"
        "s_waitcnt vmcnt(23)\n\t"
        "s_waitcnt lgkmcnt(10)\n\t"
        "v_mfma_f32_16x16x32_bf16 v[72:75], v[16:19], v[96:99], 0\n\t"
        "s_add_u32 s70, s48, 294912\n\t"
        "buffer_load_dwordx4 v[192:195], v0, %[rs], s70 offen offset:0 sc1\n\t"
        "ds_read_b128 v[16:19], v1 offset:12288\n\t"
        "s_waitcnt vmcnt(23)\n\t"
        "v_mfma_f32_16x16x32_bf16 v[8:11], v[20:23], v[100:103], 0\n\t"
        "buffer_load_dwordx4 v[196:199], v0, %[rs], s70 offen offset:1024 sc1\n\t"
        "ds_read_b128 v[20:23], v1 offset:13312\n\t"
        "s_waitcnt vmcnt(23)\n\t"
        "s_waitcnt lgkmcnt(10)\n\t"
        "v_mfma_f32_16x16x32_bf16 v[72:75], v[24:27], v[104:107], v[72:75]\n\t"
        "buffer_load_dwordx4 v[200:203], v0, %[rs], s70 offen offset:2048 sc1\n\t"
        "ds_read_b128 v[24:27], v1 offset:14336\n\t"
        "s_waitcnt vmcnt(23)\n\t"
        "v_mfma_f32_16x16x32_bf16 v[8:11], v[28:31], v[108:111], v[8:11]\n\t"
        "buffer_load_dwordx4 v[204:207], v0, %[rs], s70 offen offset:3072 sc1\n\t"
        "ds_read_b128 v[28:31], v1 offset:15360\n\t"
        "s_waitcnt vmcnt(23)\n\t"
        "s_waitcnt lgkmcnt(10)\n\t"
        "v_mfma_f32_16x16x32_bf16 v[72:75], v[32:35], v[112:115], v[72:75]\n\t"
        "s_add_u32 s70, s48, 299008\n\t"
        "buffer_load_dwordx4 v[208:211], v0, %[rs], s70 offen offset:0 sc1\n\t"
        "ds_read_b128 v[32:35], v1 offset:16384\n\t"
        "s_waitcnt vmcnt(23)\n\t"
        "v_mfma_f32_16x16x32_bf16 v[8:11], v[36:39], v[116:119], v[8:11]\n\t"
        "buffer_load_dwordx4 v[212:215], v0, %[rs], s70 offen offset:1024 sc1\n\t"
        "ds_read_b128 v[36:39], v1 offset:17408\n\t"
        "s_waitcnt vmcnt(23)\n\t"
        "s_waitcnt lgkmcnt(10)\n\t"
        "v_mfma_f32_16x16x32_bf16 v[72:75], v[40:43], v[120:123], v[72:75]\n\t"
        "buffer_load_dwordx4 v[216:219], v0, %[rs], s70 offen offset:2048 sc1\n\t"
        "ds_read_b128 v[40:43], v1 offset:18432\n\t"
        "s_waitcnt vmcnt(23)\n\t"
        "v_mfma_f32_16x16x32_bf16 v[8:11], v[44:47], v[124:127], v[8:11]\n\t"
        "buffer_load_dwordx4 v[220:223], v0, %[rs], s70 offen offset:3072 sc1\n\t"
        "ds_read_b128 v[44:47], v1 offset:19456\n\t"
        "s_waitcnt vmcnt(23)\n\t"
        "s_waitcnt lgkmcnt(10)\n\t"
        "v_mfma_f32_16x16x32_bf16 v[72:75], v[48:51], v[128:131], v[72:75]\n\t"
        "s_add_u32 s70, s48, 217088\n\t"
        "buffer_load_dwordx4 v[224:227], v0, %[rs], s70 offen offset:2048 sc1\n\t"
        "ds_read_b128 v[48:51], v1 offset:20480\n\t"
        "s_waitcnt vmcnt(23)\n\t"
        "v_mfma_f32_16x16x32_bf16 v[8:11], v[52:55], v[132:135], v[8:11]\n\t"
        "buffer_load_dwordx4 v[228:231], v0, %[rs], s70 offen offset:3072 sc1\n\t"
        "ds_read_b128 v[52:55], v1 offset:21504\n\t"
        "s_waitcnt vmcnt(23)\n\t"
        "s_waitcnt lgkmcnt(10)\n\t"
        "v_mfma_f32_16x16x32_bf16 v[72:75], v[56:59], v[136:139], v[72:75]\n\t"
        "s_add_u32 s70, s48, 249856\n\t"
        "buffer_load_dwordx4 v[232:235], v0, %[rs], s70 offen offset:2048 sc1\n\t"
        "ds_read_b128 v[56:59], v1 offset:22528\n\t"
        "s_waitcnt vmcnt(23)\n\t"
        "v_mfma_f32_16x16x32_bf16 v[8:11], v[60:63], v[140:143], v[8:11]\n\t"
        "buffer_load_dwordx4 v[236:239], v0, %[rs], s70 offen offset:3072 sc1\n\t"
        "ds_read_b128 v[60:63], v1 offset:23552\n\t"
        "s_waitcnt vmcnt(23)\n\t"
        "s_waitcnt lgkmcnt(10)\n\t"
        "v_mfma_f32_16x16x32_bf16 v[72:75], v[16:19], v[144:147], v[72:75]\n\t"
        "s_add_u32 s70, s48, 282624\n\t"
        "buffer_load_dwordx4 v[240:243], v0, %[rs], s70 offen offset:2048 sc1\n\t"
        "ds_read_b128 v[16:19], v1 offset:24576\n\t"
        "s_waitcnt vmcnt(23)\n\t"
        "v_mfma_f32_16x16x32_bf16 v[8:11], v[20:23], v[148:151], v[8:11]\n\t"
        "buffer_load_dwordx4 v[244:247], v0, %[rs], s70 offen offset:3072 sc1\n\t"
        "ds_read_b128 v[20:23], v1 offset:25600\n\t"
        "s_waitcnt vmcnt(23)\n\t"
        "s_waitcnt lgkmcnt(10)\n\t"
        "v_mfma_f32_16x16x32_bf16 v[72:75], v[24:27], v[152:155], v[72:75]\n\t"
        "s_add_u32 s70, s48, 315392\n\t"
        "buffer_load_dwordx4 v[248:251], v0, %[rs], s70 offen offset:2048 sc1\n\t"
        "ds_read_b128 v[24:27], v1 offset:26624\n\t"
        "s_waitcnt vmcnt(23)\n\t"
        "v_mfma_f32_16x16x32_bf16 v[8:11], v[28:31], v[156:159], v[8:11]\n\t"
        "buffer_load_dwordx4 v[252:255], v0, %[rs], s70 offen offset:3072 sc1\n\t"
        "ds_read_b128 v[28:31], v1 offset:27648\n\t"
        "s_waitcnt vmcnt(23)\n\t"
        "s_waitcnt lgkmcnt(10)\n\t"
        "v_mfma_f32_16x16x32_bf16 v[72:75], v[32:35], v[160:163], v[72:75]\n\t"
        "ds_read_b128 v[32:35], v1 offset:28672\n\t"
        "s_waitcnt vmcnt(22)\n\t"
        "v_mfma_f32_16x16x32_bf16 v[8:11], v[36:39], v[164:167], v[8:11]\n\t"
        "ds_read_b128 v[36:39], v1 offset:29696\n\t"
        "s_waitcnt vmcnt(21)\n\t"
        "s_waitcnt lgkmcnt(10)\n\t"
        "v_mfma_f32_16x16x32_bf16 v[72:75], v[40:43], v[168:171], v[72:75]\n\t"
        "ds_read_b128 v[40:43], v1 offset:30720\n\t"
        "s_waitcnt vmcnt(20)\n\t"
        "v_mfma_f32_16x16x32_bf16 v[8:11], v[44:47], v[172:175], v[8:11]\n\t"
        "ds_read_b128 v[44:47], v1 offset:31744\n\t"
        "s_waitcnt vmcnt(19)\n\t"
        "s_waitcnt lgkmcnt(10)\n\t"
        "v_mfma_f32_16x16x32_bf16 v[72:75], v[48:51], v[176:179], v[72:75]\n\t"
        ".Lr_o31_%=:\n\t"
        "ds_read_b32 v4, v5\n\t"
        "s_waitcnt lgkmcnt(0)\n\t"
        "v_cmp_lt_u32 vcc, s45, v4\n\t"
        "s_cbranch_vccz .Lr_o31_%=\n\t"
        "ds_read_b128 v[48:51], v2 offset:0\n\t"
        "s_waitcnt vmcnt(18)\n\t"
        "v_mfma_f32_16x16x32_bf16 v[8:11], v[52:55], v[180:183], v[8:11]\n\t"
        "ds_read_b128 v[52:55], v2 offset:1024\n\t"
        "s_waitcnt vmcnt(17)\n\t"
        "v_mfma_f32_16x16x32_bf16 v[72:75], v[56:59], v[184:187], v[72:75]\n\t"
        "ds_read_b128 v[56:59], v2 offset:2048\n\t"
        "s_waitcnt vmcnt(16)\n\t"
        "v_mfma_f32_16x16x32_bf16 v[8:11], v[60:63], v[188:191], v[8:11]\n\t"
        "ds_read_b128 v[60:63], v2 offset:3072\n\t"
        "s_waitcnt vmcnt(15)\n\t"
        "v_mfma_f32_16x16x32_bf16 v[72:75], v[16:19], v[192:195], v[72:75]\n\t"
        "ds_read_b128 v[16:19], v2 offset:4096\n\t"
        "s_waitcnt vmcnt(14)\n\t"
        "v_mfma_f32_16x16x32_bf16 v[8:11], v[20:23], v[196:199], v[8:11]\n\t"
        "ds_read_b128 v[20:23], v2 offset:5120\n\t"
        "s_waitcnt vmcnt(13)\n\t"
        "v_mfma_f32_16x16x32_bf16 v[72:75], v[24:27], v[200:203], v[72:75]\n\t"
        "ds_read_b128 v[24:27], v2 offset:6144\n\t"
        "s_waitcnt vmcnt(12)\n\t"
        "v_mfma_f32_16x16x32_bf16 v[8:11], v[28:31], v[204:207], v[8:11]\n\t"
        "ds_read_b128 v[28:31], v2 offset:7168\n\t"
        "s_waitcnt vmcnt(11)\n\t"
        "v_mfma_f32_16x16x32_bf16 v[72:75], v[32:35], v[208:211], v[72:75]\n\t"
        "s_waitcnt vmcnt(10)\n\t"
        "v_mfma_f32_16x16x32_bf16 v[8:11], v[36:39], v[212:215], v[8:11]\n\t"
        "s_waitcnt vmcnt(9)\n\t"
        "v_mfma_f32_16x16x32_bf16 v[72:75], v[40:43], v[216:219], v[72:75]\n\t"
        "s_waitcnt vmcnt(8)\n\t"
        "v_mfma_f32_16x16x32_bf16 v[8:11], v[44:47], v[220:223], v[8:11]\n\t"
        "s_waitcnt vmcnt(7)\n\t"
        "s_waitcnt lgkmcnt(6)\n\t"
        "v_mfma_f32_16x16x32_bf16 v[72:75], v[48:51], v[224:227], v[72:75]\n\t"
        "s_waitcnt vmcnt(6)\n\t"
        "v_mfma_f32_16x16x32_bf16 v[8:11], v[52:55], v[228:231], v[8:11]\n\t"
        "s_waitcnt vmcnt(5)\n\t"
        "s_waitcnt lgkmcnt(4)\n\t"
        "v_mfma_f32_16x16x32_bf16 v[72:75], v[56:59], v[232:235], v[72:75]\n\t"
        "s_waitcnt vmcnt(4)\n\t"
        "v_mfma_f32_16x16x32_bf16 v[8:11], v[60:63], v[236:239], v[8:11]\n\t"
        "s_waitcnt vmcnt(3)\n\t"
        "s_waitcnt lgkmcnt(2)\n\t"
        "v_mfma_f32_16x16x32_bf16 v[72:75], v[16:19], v[240:243], v[72:75]\n\t"
        "s_waitcnt vmcnt(2)\n\t"
        "v_mfma_f32_16x16x32_bf16 v[8:11], v[20:23], v[244:247], v[8:11]\n\t"
        "s_waitcnt vmcnt(1)\n\t"
        "s_waitcnt lgkmcnt(0)\n\t"
        "v_mfma_f32_16x16x32_bf16 v[72:75], v[24:27], v[248:251], v[72:75]\n\t"
        "s_waitcnt vmcnt(0)\n\t"
        "v_mfma_f32_16x16x32_bf16 v[8:11], v[28:31], v[252:255], v[8:11]\n\t"
        "s_nop 7\n\t"
        "v_add_f32 v8, v8, v72\n\t"
        "v_add_f32 v9, v9, v73\n\t"
        "v_add_f32 v10, v10, v74\n\t"
        "v_add_f32 v11, v11, v75\n\t"
        ".Lr_o21_%=:\n\t"
        "v_cmp_u_f32 vcc, v8, v9\n\t"
        "v_cvt_pk_bf16_f32 v4, v8, v9\n\t"
        "v_cvt_pk_bf16_f32 v5, v10, v11\n\t"
        "s_cbranch_vccnz .Lr_of1_%=\n\t"
        ".Lr_ok1_%=:\n\t"
        "buffer_store_dwordx2 v[4:5], v3, %[ro], s52 offen\n\t"
        "s_add_u32 s52, s52, 131072\n\t"
        "s_add_u32 s44, s44, 32768\n\t"
        "s_cmp_lt_u32 s44, 98304\n\t"
        "s_cselect_b32 s44, s44, 0\n\t"
        "s_add_u32 s48, s48, 2097152\n\t"
        "s_and_b32 s48, s48, 0xffffff\n\t"
        "s_xor_b32 s48, s48, 0x2000\n\t"
        "s_add_u32 s49, s49, 2097152\n\t"
        "s_and_b32 s49, s49, 0xffffff\n\t"
        "s_xor_b32 s49, s49, 0x2000\n\t"
        "s_add_u32 s45, s45, 1\n\t"
        "s_cmp_lt_u32 s45, 64\n\t"
        "s_cbranch_scc1 .Lr_oloop1_%=\n\t"
        "s_branch .Lr_end_%=\n\t"
        ".Lr_ol1_%=:\n\t"
        "s_branch .Lr_om1_%=\n\t"
        ".Lr_of1_%=:\n\t"
        "v_max_u32 v6, v96, v97\n\t"
        "v_max3_u32 v6, v6, v98, v99\n\t"
        "v_max3_u32 v6, v6, v100, v101\n\t"
        "v_max3_u32 v6, v6, v102, v103\n\t"
        "v_max3_u32 v6, v6, v104, v105\n\t"
        "v_max3_u32 v6, v6, v106, v107\n\t"
        "v_max3_u32 v6, v6, v108, v109\n\t"
        "v_max3_u32 v6, v6, v110, v111\n\t"
        "v_max3_u32 v6, v6, v112, v113\n\t"
        "v_max3_u32 v6, v6, v114, v115\n\t"
        "v_max3_u32 v6, v6, v116, v117\n\t"
        "v_max3_u32 v6, v6, v118, v119\n\t"
        "v_max3_u32 v6, v6, v120, v121\n\t"
        "v_max3_u32 v6, v6, v122, v123\n\t"
        "v_max3_u32 v6, v6, v124, v125\n\t"
        "v_max3_u32 v6, v6, v126, v127\n\t"
        "v_max3_u32 v6, v6, v128, v129\n\t"
        "v_max3_u32 v6, v6, v130, v131\n\t"
        "v_max3_u32 v6, v6, v132, v133\n\t"
        "v_max3_u32 v6, v6, v134, v135\n\t"
        "v_max3_u32 v6, v6, v136, v137\n\t"
        "v_max3_u32 v6, v6, v138, v139\n\t"
        "v_max3_u32 v6, v6, v140, v141\n\t"
        "v_max3_u32 v6, v6, v142, v143\n\t"
        "v_max3_u32 v6, v6, v144, v145\n\t"
        "v_max3_u32 v6, v6, v146, v147\n\t"
        "v_max3_u32 v6, v6, v148, v149\n\t"
        "v_max3_u32 v6, v6, v150, v151\n\t"
        "v_max3_u32 v6, v6, v152, v153\n\t"
        "v_max3_u32 v6, v6, v154, v155\n\t"
        "v_max3_u32 v6, v6, v156, v157\n\t"
        "v_max3_u32 v6, v6, v158, v159\n\t"
        "v_max3_u32 v6, v6, v160, v161\n\t"
        "v_max3_u32 v6, v6, v162, v163\n\t"
        "v_max3_u32 v6, v6, v164, v165\n\t"
        "v_max3_u32 v6, v6, v166, v167\n\t"
        "v_max3_u32 v6, v6, v168, v169\n\t"
        "v_max3_u32 v6, v6, v170, v171\n\t"
        "v_max3_u32 v6, v6, v172, v173\n\t"
        "v_max3_u32 v6, v6, v174, v175\n\t"
        "v_max3_u32 v6, v6, v176, v177\n\t"
        "v_max3_u32 v6, v6, v178, v179\n\t"
        "v_max3_u32 v6, v6, v180, v181\n\t"
        "v_max3_u32 v6, v6, v182, v183\n\t"
        "v_max3_u32 v6, v6, v184, v185\n\t"
        "v_max3_u32 v6, v6, v186, v187\n\t"
        "v_max3_u32 v6, v6, v188, v189\n\t"
        "v_max3_u32 v6, v6, v190, v191\n\t"
        "v_max3_u32 v6, v6, v192, v193\n\t"
        "v_max3_u32 v6, v6, v194, v195\n\t"
        "v_max3_u32 v6, v6, v196, v197\n\t"
        "v_max3_u32 v6, v6, v198, v199\n\t"
        "v_max3_u32 v6, v6, v200, v201\n\t"
        "v_max3_u32 v6, v6, v202, v203\n\t"
        "v_max3_u32 v6, v6, v204, v205\n\t"
        "v_max3_u32 v6, v6, v206, v207\n\t"
        "v_max3_u32 v6, v6, v208, v209\n\t"
        "v_max3_u32 v6, v6, v210, v211\n\t"
        "v_max3_u32 v6, v6, v212, v213\n\t"
        "v_max3_u32 v6, v6, v214, v215\n\t"
        "v_max3_u32 v6, v6, v216, v217\n\t"
        "v_max3_u32 v6, v6, v218, v219\n\t"
        "v_max3_u32 v6, v6, v220, v221\n\t"
        "v_max3_u32 v6, v6, v222, v223\n\t"
        "v_max3_u32 v6, v6, v224, v225\n\t"
        "v_max3_u32 v6, v6, v226, v227\n\t"
        "v_max3_u32 v6, v6, v228, v229\n\t"
        "v_max3_u32 v6, v6, v230, v231\n\t"
        "v_max3_u32 v6, v6, v232, v233\n\t"
        "v_max3_u32 v6, v6, v234, v235\n\t"
        "v_max3_u32 v6, v6, v236, v237\n\t"
        "v_max3_u32 v6, v6, v238, v239\n\t"
        "v_max3_u32 v6, v6, v240, v241\n\t"
        "v_max3_u32 v6, v6, v242, v243\n\t"
        "v_max3_u32 v6, v6, v244, v245\n\t"
        "v_max3_u32 v6, v6, v246, v247\n\t"
        "v_max3_u32 v6, v6, v248, v249\n\t"
        "v_max3_u32 v6, v6, v250, v251\n\t"
        "v_max3_u32 v6, v6, v252, v253\n\t"
        "v_max3_u32 v6, v6, v254, v255\n\t"
        "v_cmp_eq_u32 vcc, s66, v6\n\t"
        "s_cbranch_vccz .Lr_ok1_%=\n\t"
        "s_add_u32 s79, s79, 1\n\t"
        "s_sub_u32 s46, s46, 1\n\t"
        "s_cbranch_scc1 .Lr_og1_%=\n\t"
        "s_add_u32 s70, s48, 196608\n\t"
        "buffer_load_dwordx4 v[96:99], v0, %[rs], s70 offen offset:0 sc1\n\t"
        "buffer_load_dwordx4 v[100:103], v0, %[rs], s70 offen offset:1024 sc1\n\t"
        "buffer_load_dwordx4 v[104:107], v0, %[rs], s70 offen offset:2048 sc1\n\t"
        "buffer_load_dwordx4 v[108:111], v0, %[rs], s70 offen offset:3072 sc1\n\t"
        "s_add_u32 s70, s48, 200704\n\t"
        "buffer_load_dwordx4 v[112:115], v0, %[rs], s70 offen offset:0 sc1\n\t"
        "buffer_load_dwordx4 v[116:119], v0, %[rs], s70 offen offset:1024 sc1\n\t"
        "buffer_load_dwordx4 v[120:123], v0, %[rs], s70 offen offset:2048 sc1\n\t"
        "buffer_load_dwordx4 v[124:127], v0, %[rs], s70 offen offset:3072 sc1\n\t"
        "s_add_u32 s70, s48, 229376\n\t"
        "buffer_load_dwordx4 v[128:131], v0, %[rs], s70 offen offset:0 sc1\n\t"
        "buffer_load_dwordx4 v[132:135], v0, %[rs], s70 offen offset:1024 sc1\n\t"
        "buffer_load_dwordx4 v[136:139], v0, %[rs], s70 offen offset:2048 sc1\n\t"
        "buffer_load_dwordx4 v[140:143], v0, %[rs], s70 offen offset:3072 sc1\n\t"
        "s_add_u32 s70, s48, 233472\n\t"
        "buffer_load_dwordx4 v[144:147], v0, %[rs], s70 offen offset:0 sc1\n\t"
        "buffer_load_dwordx4 v[148:151], v0, %[rs], s70 offen offset:1024 sc1\n\t"
        "buffer_load_dwordx4 v[152:155], v0, %[rs], s70 offen offset:2048 sc1\n\t"
        "buffer_load_dwordx4 v[156:159], v0, %[rs], s70 offen offset:3072 sc1\n\t"
        "s_add_u32 s70, s48, 262144\n\t"
        "buffer_load_dwordx4 v[160:163], v0, %[rs], s70 offen offset:0 sc1\n\t"
        "buffer_load_dwordx4 v[164:167], v0, %[rs], s70 offen offset:1024 sc1\n\t"
        "buffer_load_dwordx4 v[168:171], v0, %[rs], s70 offen offset:2048 sc1\n\t"
        "buffer_load_dwordx4 v[172:175], v0, %[rs], s70 offen offset:3072 sc1\n\t"
        "s_add_u32 s70, s48, 266240\n\t"
        "buffer_load_dwordx4 v[176:179], v0, %[rs], s70 offen offset:0 sc1\n\t"
        "buffer_load_dwordx4 v[180:183], v0, %[rs], s70 offen offset:1024 sc1\n\t"
        "buffer_load_dwordx4 v[184:187], v0, %[rs], s70 offen offset:2048 sc1\n\t"
        "buffer_load_dwordx4 v[188:191], v0, %[rs], s70 offen offset:3072 sc1\n\t"
        "s_add_u32 s70, s48, 294912\n\t"
        "buffer_load_dwordx4 v[192:195], v0, %[rs], s70 offen offset:0 sc1\n\t"
        "buffer_load_dwordx4 v[196:199], v0, %[rs], s70 offen offset:1024 sc1\n\t"
        "buffer_load_dwordx4 v[200:203], v0, %[rs], s70 offen offset:2048 sc1\n\t"
        "buffer_load_dwordx4 v[204:207], v0, %[rs], s70 offen offset:3072 sc1\n\t"
        "s_add_u32 s70, s48, 299008\n\t"
        "buffer_load_dwordx4 v[208:211], v0, %[rs], s70 offen offset:0 sc1\n\t"
        "buffer_load_dwordx4 v[212:215], v0, %[rs], s70 offen offset:1024 sc1\n\t"
        "buffer_load_dwordx4 v[216:219], v0, %[rs], s70 offen offset:2048 sc1\n\t"
        "buffer_load_dwordx4 v[220:223], v0, %[rs], s70 offen offset:3072 sc1\n\t"
        "s_add_u32 s70, s48, 217088\n\t"
        "buffer_load_dwordx4 v[224:227], v0, %[rs], s70 offen offset:2048 sc1\n\t"
        "buffer_load_dwordx4 v[228:231], v0, %[rs], s70 offen offset:3072 sc1\n\t"
        "s_add_u32 s70, s48, 249856\n\t"
        "buffer_load_dwordx4 v[232:235], v0, %[rs], s70 offen offset:2048 sc1\n\t"
        "buffer_load_dwordx4 v[236:239], v0, %[rs], s70 offen offset:3072 sc1\n\t"
        "s_add_u32 s70, s48, 282624\n\t"
        "buffer_load_dwordx4 v[240:243], v0, %[rs], s70 offen offset:2048 sc1\n\t"
        "buffer_load_dwordx4 v[244:247], v0, %[rs], s70 offen offset:3072 sc1\n\t"
        "s_add_u32 s70, s48, 315392\n\t"
        "buffer_load_dwordx4 v[248:251], v0, %[rs], s70 offen offset:2048 sc1\n\t"
        "buffer_load_dwordx4 v[252:255], v0, %[rs], s70 offen offset:3072 sc1\n\t"
        "ds_read_b128 v[16:19], v1 offset:0\n\t"
        "ds_read_b128 v[20:23], v1 offset:1024\n\t"
        "ds_read_b128 v[24:27], v1 offset:2048\n\t"
        "ds_read_b128 v[28:31], v1 offset:3072\n\t"
        "ds_read_b128 v[32:35], v1 offset:4096\n\t"
        "ds_read_b128 v[36:39], v1 offset:5120\n\t"
        "ds_read_b128 v[40:43], v1 offset:6144\n\t"
        "ds_read_b128 v[44:47], v1 offset:7168\n\t"
        "ds_read_b128 v[48:51], v1 offset:8192\n\t"
        "ds_read_b128 v[52:55], v1 offset:9216\n\t"
        "ds_read_b128 v[56:59], v1 offset:10240\n\t"
        "ds_read_b128 v[60:63], v1 offset:11264\n\t"
        "s_waitcnt vmcnt(39)\n\t"
        "s_waitcnt lgkmcnt(10)\n\t"
        "v_mfma_f32_16x16x32_bf16 v[72:75], v[16:19], v[96:99], 0\n\t"
        "ds_read_b128 v[16:19], v1 offset:12288\n\t"
        "s_waitcnt vmcnt(38)\n\t"
        "v_mfma_f32_16x16x32_bf16 v[8:11], v[20:23], v[100:103], 0\n\t"
        "ds_read_b128 v[20:23], v1 offset:13312\n\t"
        "s_waitcnt vmcnt(37)\n\t"
        "s_waitcnt lgkmcnt(10)\n\t"
        "v_mfma_f32_16x16x32_bf16 v[72:75], v[24:27], v[104:107], v[72:75]\n\t"
        "ds_read_b128 v[24:27], v1 offset:14336\n\t"
        "s_waitcnt vmcnt(36)\n\t"
        "v_mfma_f32_16x16x32_bf16 v[8:11], v[28:31], v[108:111], v[8:11]\n\t"
        "ds_read_b128 v[28:31], v1 offset:15360\n\t"
        "s_waitcnt vmcnt(35)\n\t"
        "s_waitcnt lgkmcnt(10)\n\t"
        "v_mfma_f32_16x16x32_bf16 v[72:75], v[32:35], v[112:115], v[72:75]\n\t"
        "ds_read_b128 v[32:35], v1 offset:16384\n\t"
        "s_waitcnt vmcnt(34)\n\t"
        "v_mfma_f32_16x16x32_bf16 v[8:11], v[36:39], v[116:119], v[8:11]\n\t"
        "ds_read_b128 v[36:39], v1 offset:17408\n\t"
        "s_waitcnt vmcnt(33)\n\t"
        "s_waitcnt lgkmcnt(10)\n\t"
        "v_mfma_f32_16x16x32_bf16 v[72:75], v[40:43], v[120:123], v[72:75]\n\t"
        "ds_read_b128 v[40:43], v1 offset:18432\n\t"
        "s_waitcnt vmcnt(32)\n\t"
        "v_mfma_f32_16x16x32_bf16 v[8:11], v[44:47], v[124:127], v[8:11]\n\t"
        "ds_read_b128 v[44:47], v1 offset:19456\n\t"
        "s_waitcnt vmcnt(31)\n\t"
        "s_waitcnt lgkmcnt(10)\n\t"
        "v_mfma_f32_16x16x32_bf16 v[72:75], v[48:51], v[128:131], v[72:75]\n\t"
        "ds_read_b128 v[48:51], v1 offset:20480\n\t"
        "s_waitcnt vmcnt(30)\n\t"
        "v_mfma_f32_16x16x32_bf16 v[8:11], v[52:55], v[132:135], v[8:11]\n\t"
        "ds_read_b128 v[52:55], v1 offset:21504\n\t"
        "s_waitcnt vmcnt(29)\n\t"
        "s_waitcnt lgkmcnt(10)\n\t"
        "v_mfma_f32_16x16x32_bf16 v[72:75], v[56:59], v[136:139], v[72:75]\n\t"
        "ds_read_b128 v[56:59], v1 offset:22528\n\t"
        "s_waitcnt vmcnt(28)\n\t"
        "v_mfma_f32_16x16x32_bf16 v[8:11], v[60:63], v[140:143], v[8:11]\n\t"
        "ds_read_b128 v[60:63], v1 offset:23552\n\t"
        "s_waitcnt vmcnt(27)\n\t"
        "s_waitcnt lgkmcnt(10)\n\t"
        "v_mfma_f32_16x16x32_bf16 v[72:75], v[16:19], v[144:147], v[72:75]\n\t"
        "ds_read_b128 v[16:19], v1 offset:24576\n\t"
        "s_waitcnt vmcnt(26)\n\t"
        "v_mfma_f32_16x16x32_bf16 v[8:11], v[20:23], v[148:151], v[8:11]\n\t"
        "ds_read_b128 v[20:23], v1 offset:25600\n\t"
        "s_waitcnt vmcnt(25)\n\t"
        "s_waitcnt lgkmcnt(10)\n\t"
        "v_mfma_f32_16x16x32_bf16 v[72:75], v[24:27], v[152:155], v[72:75]\n\t"
        "ds_read_b128 v[24:27], v1 offset:26624\n\t"
        "s_waitcnt vmcnt(24)\n\t"
        "v_mfma_f32_16x16x32_bf16 v[8:11], v[28:31], v[156:159], v[8:11]\n\t"
        "ds_read_b128 v[28:31], v1 offset:27648\n\t"
        "s_waitcnt vmcnt(23)\n\t"
        "s_waitcnt lgkmcnt(10)\n\t"
        "v_mfma_f32_16x16x32_bf16 v[72:75], v[32:35], v[160:163], v[72:75]\n\t"
        "ds_read_b128 v[32:35], v1 offset:28672\n\t"
        "s_waitcnt vmcnt(22)\n\t"
        "v_mfma_f32_16x16x32_bf16 v[8:11], v[36:39], v[164:167], v[8:11]\n\t"
        "ds_read_b128 v[36:39], v1 offset:29696\n\t"
        "s_waitcnt vmcnt(21)\n\t"
        "s_waitcnt lgkmcnt(10)\n\t"
        "v_mfma_f32_16x16x32_bf16 v[72:75], v[40:43], v[168:171], v[72:75]\n\t"
        "ds_read_b128 v[40:43], v1 offset:30720\n\t"
        "s_waitcnt vmcnt(20)\n\t"
        "v_mfma_f32_16x16x32_bf16 v[8:11], v[44:47], v[172:175], v[8:11]\n\t"
        "ds_read_b128 v[44:47], v1 offset:31744\n\t"
        "s_waitcnt vmcnt(19)\n\t"
        "s_waitcnt lgkmcnt(10)\n\t"
        "v_mfma_f32_16x16x32_bf16 v[72:75], v[48:51], v[176:179], v[72:75]\n\t"
        "ds_read_b128 v[48:51], v2 offset:0\n\t"
        "s_waitcnt vmcnt(18)\n\t"
        "v_mfma_f32_16x16x32_bf16 v[8:11], v[52:55], v[180:183], v[8:11]\n\t"
        "ds_read_b128 v[52:55], v2 offset:1024\n\t"
        "s_waitcnt vmcnt(17)\n\t"
        "s_waitcnt lgkmcnt(10)\n\t"
        "v_mfma_f32_16x16x32_bf16 v[72:75], v[56:59], v[184:187], v[72:75]\n\t"
        "ds_read_b128 v[56:59], v2 offset:2048\n\t"
        "s_waitcnt vmcnt(16)\n\t"
        "v_mfma_f32_16x16x32_bf16 v[8:11], v[60:63], v[188:191], v[8:11]\n\t"
        "ds_read_b128 v[60:63], v2 offset:3072\n\t"
        "s_waitcnt vmcnt(15)\n\t"
        "s_waitcnt lgkmcnt(10)\n\t"
        "v_mfma_f32_16x16x32_bf16 v[72:75], v[16:19], v[192:195], v[72:75]\n\t"
        "ds_read_b128 v[16:19], v2 offset:4096\n\t"
        "s_waitcnt vmcnt(14)\n\t"
        "v_mfma_f32_16x16x32_bf16 v[8:11], v[20:23], v[196:199], v[8:11]\n\t"
        "ds_read_b128 v[20:23], v2 offset:5120\n\t"
        "s_waitcnt vmcnt(13)\n\t"
        "s_waitcnt lgkmcnt(10)\n\t"
        "v_mfma_f32_16x16x32_bf16 v[72:75], v[24:27], v[200:203], v[72:75]\n\t"
        "ds_read_b128 v[24:27], v2 offset:6144\n\t"
        "s_waitcnt vmcnt(12)\n\t"
        "v_mfma_f32_16x16x32_bf16 v[8:11], v[28:31], v[204:207], v[8:11]\n\t"
        "ds_read_b128 v[28:31], v2 offset:7168\n\t"
        "s_waitcnt vmcnt(11)\n\t"
        "s_waitcnt lgkmcnt(10)\n\t"
        "v_mfma_f32_16x16x32_bf16 v[72:75], v[32:35], v[208:211], v[72:75]\n\t"
        "s_waitcnt vmcnt(10)\n\t"
        "v_mfma_f32_16x16x32_bf16 v[8:11], v[36:39], v[212:215], v[8:11]\n\t"
        "s_waitcnt vmcnt(9)\n\t"
        "s_waitcnt lgkmcnt(8)\n\t"
        "v_mfma_f32_16x16x32_bf16 v[72:75], v[40:43], v[216:219], v[72:75]\n\t"
        "s_waitcnt vmcnt(8)\n\t"
        "v_mfma_f32_16x16x32_bf16 v[8:11], v[44:47], v[220:223], v[8:11]\n\t"
        "s_waitcnt vmcnt(7)\n\t"
        "s_waitcnt lgkmcnt(6)\n\t"
        "v_mfma_f32_16x16x32_bf16 v[72:75], v[48:51], v[224:227], v[72:75]\n\t"
        "s_waitcnt vmcnt(6)\n\t"
        "v_mfma_f32_16x16x32_bf16 v[8:11], v[52:55], v[228:231], v[8:11]\n\t"
        "s_waitcnt vmcnt(5)\n\t"
        "s_waitcnt lgkmcnt(4)\n\t"
        "v_mfma_f32_16x16x32_bf16 v[72:75], v[56:59], v[232:235], v[72:75]\n\t"
        "s_waitcnt vmcnt(4)\n\t"
        "v_mfma_f32_16x16x32_bf16 v[8:11], v[60:63], v[236:239], v[8:11]\n\t"
        "s_waitcnt vmcnt(3)\n\t"
        "s_waitcnt lgkmcnt(2)\n\t"
        "v_mfma_f32_16x16x32_bf16 v[72:75], v[16:19], v[240:243], v[72:75]\n\t"
        "s_waitcnt vmcnt(2)\n\t"
        "v_mfma_f32_16x16x32_bf16 v[8:11], v[20:23], v[244:247], v[8:11]\n\t"
        "s_waitcnt vmcnt(1)\n\t"
        "s_waitcnt lgkmcnt(0)\n\t"
        "v_mfma_f32_16x16x32_bf16 v[72:75], v[24:27], v[248:251], v[72:75]\n\t"
        "s_waitcnt vmcnt(0)\n\t"
        "v_mfma_f32_16x16x32_bf16 v[8:11], v[28:31], v[252:255], v[8:11]\n\t"
        "s_nop 7\n\t"
        "v_add_f32 v8, v8, v72\n\t"
        "v_add_f32 v9, v9, v73\n\t"
        "v_add_f32 v10, v10, v74\n\t"
        "v_add_f32 v11, v11, v75\n\t"
        "s_branch .Lr_o21_%=\n\t"
        ".Lr_og1_%=:\n\t"
        "s_mov_b32 s46, 0\n\t"
        "s_branch .Lr_ok1_%=\n\t"
        ".Lr_oloop0_%=:\n\t"
        "s_and_b32 s70, s45, 1\n\t"
        "v_add_u32 v1, s44, v0\n\t"
        "s_lshl_b32 s70, s70, 13\n\t"
        "s_add_u32 s70, s70, 98304\n\t"
        "v_add_u32 v2, s70, v0\n\t"
        "v_mov_b32 v5, 114720\n\t"
        "s_cmp_eq_u32 s45, 63\n\t"
        "s_barrier\n\t"
        "s_cbranch_scc1 .Lr_ol0_%=\n\t"
        "s_nop 15\n\t"
        "s_nop 15\n\t"
        "s_nop 15\n\t"
        "s_nop 15\n\t"
        "s_nop 10\n\t"
        ".Lr_om0_%=:\n\t"
        "s_add_u32 s70, s48, 212992\n\t"
        "buffer_load_dwordx4 v[96:99], v0, %[rs], s70 offen offset:0 sc1\n\t"
        "buffer_load_dwordx4 v[100:103], v0, %[rs], s70 offen offset:1024 sc1\n\t"
        "buffer_load_dwordx4 v[104:107], v0, %[rs], s70 offen offset:2048 sc1\n\t"
        "buffer_load_dwordx4 v[108:111], v0, %[rs], s70 offen offset:3072 sc1\n\t"
        "s_add_u32 s70, s48, 245760\n\t"
        "buffer_load_dwordx4 v[112:115], v0, %[rs], s70 offen offset:0 sc1\n\t"
        "buffer_load_dwordx4 v[116:119], v0, %[rs], s70 offen offset:1024 sc1\n\t"
        "buffer_load_dwordx4 v[120:123], v0, %[rs], s70 offen offset:2048 sc1\n\t"
        "buffer_load_dwordx4 v[124:127], v0, %[rs], s70 offen offset:3072 sc1\n\t"
        "s_add_u32 s70, s48, 278528\n\t"
        "buffer_load_dwordx4 v[128:131], v0, %[rs], s70 offen offset:0 sc1\n\t"
        "buffer_load_dwordx4 v[132:135], v0, %[rs], s70 offen offset:1024 sc1\n\t"
        "buffer_load_dwordx4 v[136:139], v0, %[rs], s70 offen offset:2048 sc1\n\t"
        "buffer_load_dwordx4 v[140:143], v0, %[rs], s70 offen offset:3072 sc1\n\t"
        "s_add_u32 s70, s48, 311296\n\t"
        "buffer_load_dwordx4 v[144:147], v0, %[rs], s70 offen offset:0 sc1\n\t"
        "buffer_load_dwordx4 v[148:151], v0, %[rs], s70 offen offset:1024 sc1\n\t"
        "buffer_load_dwordx4 v[152:155], v0, %[rs], s70 offen offset:2048 sc1\n\t"
        "buffer_load_dwordx4 v[156:159], v0, %[rs], s70 offen offset:3072 sc1\n\t"
        "s_add_u32 s70, s48, 217088\n\t"
        "buffer_load_dwordx4 v[160:163], v0, %[rs], s70 offen offset:0 sc1\n\t"
        "buffer_load_dwordx4 v[164:167], v0, %[rs], s70 offen offset:1024 sc1\n\t"
        "s_add_u32 s70, s48, 249856\n\t"
        "buffer_load_dwordx4 v[168:171], v0, %[rs], s70 offen offset:0 sc1\n\t"
        "buffer_load_dwordx4 v[172:175], v0, %[rs], s70 offen offset:1024 sc1\n\t"
        "s_add_u32 s70, s48, 282624\n\t"
        "buffer_load_dwordx4 v[176:179], v0, %[rs], s70 offen offset:0 sc1\n\t"
        "buffer_load_dwordx4 v[180:183], v0, %[rs], s70 offen offset:1024 sc1\n\t"
        "s_add_u32 s70, s48, 315392\n\t"
        "buffer_load_dwordx4 v[184:187], v0, %[rs], s70 offen offset:0 sc1\n\t"
        "buffer_load_dwordx4 v[188:191], v0, %[rs], s70 offen offset:1024 sc1\n\t"
        "ds_read_b64 v[16:17], v1 offset:0\n\t"
        "ds_read_b64 v[20:21], v1 offset:1024\n\t"
        "ds_read_b64 v[24:25], v1 offset:2048\n\t"
        "ds_read_b64 v[28:29], v1 offset:3072\n\t"
        "ds_read_b64 v[32:33], v1 offset:4096\n\t"
        "ds_read_b64 v[36:37], v1 offset:5120\n\t"
        "ds_read_b64 v[40:41], v1 offset:6144\n\t"
        "ds_read_b64 v[44:45], v1 offset:7168\n\t"
        "ds_read_b64 v[48:49], v1 offset:8192\n\t"
        "ds_read_b64 v[52:53], v1 offset:9216\n\t"
        "ds_read_b64 v[56:57], v1 offset:10240\n\t"
        "ds_read_b64 v[60:61], v1 offset:11264\n\t"
        "s_waitcnt vmcnt(23)\n\t"
        "s_waitcnt lgkmcnt(10)\n\t"
        "v_mfma_f32_16x16x16_bf16 v[76:79], v[16:17], v[96:97], 0\n\t"
        "ds_read_b64 v[16:17], v1 offset:12288\n\t"
        "v_mfma_f32_16x16x16_bf16 v[12:15], v[20:21], v[98:99], 0\n\t"
        "ds_read_b64 v[20:21], v1 offset:13312\n\t"
        "s_waitcnt vmcnt(22)\n\t"
        "s_waitcnt lgkmcnt(10)\n\t"
        "v_mfma_f32_16x16x16_bf16 v[76:79], v[24:25], v[100:101], v[76:79]\n\t"
        "ds_read_b64 v[24:25], v1 offset:14336\n\t"
        "v_mfma_f32_16x16x16_bf16 v[12:15], v[28:29], v[102:103], v[12:15]\n\t"
        "ds_read_b64 v[28:29], v1 offset:15360\n\t"
        "s_waitcnt vmcnt(21)\n\t"
        "s_waitcnt lgkmcnt(10)\n\t"
        "v_mfma_f32_16x16x16_bf16 v[76:79], v[32:33], v[104:105], v[76:79]\n\t"
        "ds_read_b64 v[32:33], v1 offset:16384\n\t"
        "v_mfma_f32_16x16x16_bf16 v[12:15], v[36:37], v[106:107], v[12:15]\n\t"
        "ds_read_b64 v[36:37], v1 offset:17408\n\t"
        "s_waitcnt vmcnt(20)\n\t"
        "s_waitcnt lgkmcnt(10)\n\t"
        "v_mfma_f32_16x16x16_bf16 v[76:79], v[40:41], v[108:109], v[76:79]\n\t"
        "ds_read_b64 v[40:41], v1 offset:18432\n\t"
        "v_mfma_f32_16x16x16_bf16 v[12:15], v[44:45], v[110:111], v[12:15]\n\t"
        "ds_read_b64 v[44:45], v1 offset:19456\n\t"
        "s_waitcnt vmcnt(19)\n\t"
        "s_waitcnt lgkmcnt(10)\n\t"
        "v_mfma_f32_16x16x16_bf16 v[76:79], v[48:49], v[112:113], v[76:79]\n\t"
        "ds_read_b64 v[48:49], v1 offset:20480\n\t"
        "v_mfma_f32_16x16x16_bf16 v[12:15], v[52:53], v[114:115], v[12:15]\n\t"
        "ds_read_b64 v[52:53], v1 offset:21504\n\t"
        "s_waitcnt vmcnt(18)\n\t"
        "s_waitcnt lgkmcnt(10)\n\t"
        "v_mfma_f32_16x16x16_bf16 v[76:79], v[56:57], v[116:117], v[76:79]\n\t"
        "ds_read_b64 v[56:57], v1 offset:22528\n\t"
        "v_mfma_f32_16x16x16_bf16 v[12:15], v[60:61], v[118:119], v[12:15]\n\t"
        "ds_read_b64 v[60:61], v1 offset:23552\n\t"
        "s_waitcnt vmcnt(17)\n\t"
        "s_waitcnt lgkmcnt(10)\n\t"
        "v_mfma_f32_16x16x16_bf16 v[76:79], v[16:17], v[120:121], v[76:79]\n\t"
        "ds_read_b64 v[16:17], v1 offset:24576\n\t"
        "v_mfma_f32_16x16x16_bf16 v[12:15], v[20:21], v[122:123], v[12:15]\n\t"
        "ds_read_b64 v[20:21], v1 offset:25600\n\t"
        "s_waitcnt vmcnt(16)\n\t"
        "s_waitcnt lgkmcnt(10)\n\t"
        "v_mfma_f32_16x16x16_bf16 v[76:79], v[24:25], v[124:125], v[76:79]\n\t"
        "ds_read_b64 v[24:25], v1 offset:26624\n\t"
        "v_mfma_f32_16x16x16_bf16 v[12:15], v[28:29], v[126:127], v[12:15]\n\t"
        "ds_read_b64 v[28:29], v1 offset:27648\n\t"
        "s_waitcnt vmcnt(15)\n\t"
        "s_waitcnt lgkmcnt(10)\n\t"
        "v_mfma_f32_16x16x16_bf16 v[76:79], v[32:33], v[128:129], v[76:79]\n\t"
        "ds_read_b64 v[32:33], v1 offset:28672\n\t"
        "v_mfma_f32_16x16x16_bf16 v[12:15], v[36:37], v[130:131], v[12:15]\n\t"
        "ds_read_b64 v[36:37], v1 offset:29696\n\t"
        "s_waitcnt vmcnt(14)\n\t"
        "s_waitcnt lgkmcnt(10)\n\t"
        "v_mfma_f32_16x16x16_bf16 v[76:79], v[40:41], v[132:133], v[76:79]\n\t"
        "ds_read_b64 v[40:41], v1 offset:30720\n\t"
        "v_mfma_f32_16x16x16_bf16 v[12:15], v[44:45], v[134:135], v[12:15]\n\t"
        "ds_read_b64 v[44:45], v1 offset:31744\n\t"
        "s_waitcnt vmcnt(13)\n\t"
        "s_waitcnt lgkmcnt(10)\n\t"
        "v_mfma_f32_16x16x16_bf16 v[76:79], v[48:49], v[136:137], v[76:79]\n\t"
        ".Lr_o30_%=:\n\t"
        "ds_read_b32 v4, v5\n\t"
        "s_waitcnt lgkmcnt(0)\n\t"
        "v_cmp_lt_u32 vcc, s45, v4\n\t"
        "s_cbranch_vccz .Lr_o30_%=\n\t"
        "ds_read_b128 v[48:51], v2 offset:0\n\t"
        "v_mfma_f32_16x16x16_bf16 v[12:15], v[52:53], v[138:139], v[12:15]\n\t"
        "ds_read_b128 v[52:55], v2 offset:1024\n\t"
        "s_waitcnt vmcnt(12)\n\t"
        "v_mfma_f32_16x16x16_bf16 v[76:79], v[56:57], v[140:141], v[76:79]\n\t"
        "ds_read_b128 v[56:59], v2 offset:2048\n\t"
        "v_mfma_f32_16x16x16_bf16 v[12:15], v[60:61], v[142:143], v[12:15]\n\t"
        "ds_read_b128 v[60:63], v2 offset:3072\n\t"
        "s_waitcnt vmcnt(11)\n\t"
        "v_mfma_f32_16x16x16_bf16 v[76:79], v[16:17], v[144:145], v[76:79]\n\t"
        "ds_read_b128 v[16:19], v2 offset:4096\n\t"
        "v_mfma_f32_16x16x16_bf16 v[12:15], v[20:21], v[146:147], v[12:15]\n\t"
        "ds_read_b128 v[20:23], v2 offset:5120\n\t"
        "s_waitcnt vmcnt(10)\n\t"
        "v_mfma_f32_16x16x16_bf16 v[76:79], v[24:25], v[148:149], v[76:79]\n\t"
        "ds_read_b128 v[24:27], v2 offset:6144\n\t"
        "v_mfma_f32_16x16x16_bf16 v[12:15], v[28:29], v[150:151], v[12:15]\n\t"
        "ds_read_b128 v[28:31], v2 offset:7168\n\t"
        "s_waitcnt vmcnt(9)\n\t"
        "v_mfma_f32_16x16x16_bf16 v[76:79], v[32:33], v[152:153], v[76:79]\n\t"
        "v_mfma_f32_16x16x16_bf16 v[12:15], v[36:37], v[154:155], v[12:15]\n\t"
        "s_waitcnt vmcnt(8)\n\t"
        "v_mfma_f32_16x16x16_bf16 v[76:79], v[40:41], v[156:157], v[76:79]\n\t"
        "v_mfma_f32_16x16x16_bf16 v[12:15], v[44:45], v[158:159], v[12:15]\n\t"
        "s_waitcnt vmcnt(7)\n\t"
        "s_waitcnt lgkmcnt(6)\n\t"
        "v_mfma_f32_16x16x32_bf16 v[72:75], v[48:51], v[160:163], 0\n\t"
        "s_waitcnt vmcnt(6)\n\t"
        "v_mfma_f32_16x16x32_bf16 v[8:11], v[52:55], v[164:167], 0\n\t"
        "s_waitcnt vmcnt(5)\n\t"
        "s_waitcnt lgkmcnt(4)\n\t"
        "v_mfma_f32_16x16x32_bf16 v[72:75], v[56:59], v[168:171], v[72:75]\n\t"
        "s_waitcnt vmcnt(4)\n\t"
        "v_mfma_f32_16x16x32_bf16 v[8:11], v[60:63], v[172:175], v[8:11]\n\t"
        "s_waitcnt vmcnt(3)\n\t"
        "s_waitcnt lgkmcnt(2)\n\t"
        "v_mfma_f32_16x16x32_bf16 v[72:75], v[16:19], v[176:179], v[72:75]\n\t"
        "s_waitcnt vmcnt(2)\n\t"
        "v_mfma_f32_16x16x32_bf16 v[8:11], v[20:23], v[180:183], v[8:11]\n\t"
        "s_waitcnt vmcnt(1)\n\t"
        "s_waitcnt lgkmcnt(0)\n\t"
        "v_mfma_f32_16x16x32_bf16 v[72:75], v[24:27], v[184:187], v[72:75]\n\t"
        "s_waitcnt vmcnt(0)\n\t"
        "v_mfma_f32_16x16x32_bf16 v[8:11], v[28:31], v[188:191], v[8:11]\n\t"
        "s_nop 7\n\t"
        "v_add_f32 v8, v8, v72\n\t"
        "v_add_f32 v9, v9, v73\n\t"
        "v_add_f32 v10, v10, v74\n\t"
        "v_add_f32 v11, v11, v75\n\t"
        "v_add_f32 v12, v12, v76\n\t"
        "v_add_f32 v13, v13, v77\n\t"
        "v_add_f32 v14, v14, v78\n\t"
        "v_add_f32 v15, v15, v79\n\t"
        "v_add_f32 v8, v8, v12\n\t"
        "v_add_f32 v9, v9, v13\n\t"
        "v_add_f32 v10, v10, v14\n\t"
        "v_add_f32 v11, v11, v15\n\t"
        ".Lr_o20_%=:\n\t"
        "v_cmp_u_f32 vcc, v8, v9\n\t"
        "v_cvt_pk_bf16_f32 v4, v8, v9\n\t"
        "v_cvt_pk_bf16_f32 v5, v10, v11\n\t"
        "s_cbranch_vccnz .Lr_of0_%=\n\t"
        ".Lr_ok0_%=:\n\t"
        "buffer_store_dwordx2 v[4:5], v3, %[ro], s52 offen\n\t"
        "s_add_u32 s52, s52, 131072\n\t"
        "s_add_u32 s44, s44, 32768\n\t"
        "s_cmp_lt_u32 s44, 98304\n\t"
        "s_cselect_b32 s44, s44, 0\n\t"
        "s_add_u32 s48, s48, 2097152\n\t"
        "s_and_b32 s48, s48, 0xffffff\n\t"
        "s_xor_b32 s48, s48, 0x2000\n\t"
        "s_add_u32 s49, s49, 2097152\n\t"
        "s_and_b32 s49, s49, 0xffffff\n\t"
        "s_xor_b32 s49, s49, 0x2000\n\t"
        "s_add_u32 s45, s45, 1\n\t"
        "s_cmp_lt_u32 s45, 64\n\t"
        "s_cbranch_scc1 .Lr_oloop0_%=\n\t"
        "s_branch .Lr_end_%=\n\t"
        ".Lr_ol0_%=:\n\t"
        "s_branch .Lr_om0_%=\n\t"
        ".Lr_of0_%=:\n\t"
        "v_max_u32 v6, v96, v97\n\t"
        "v_max3_u32 v6, v6, v98, v99\n\t"
        "v_max3_u32 v6, v6, v100, v101\n\t"
        "v_max3_u32 v6, v6, v102, v103\n\t"
        "v_max3_u32 v6, v6, v104, v105\n\t"
        "v_max3_u32 v6, v6, v106, v107\n\t"
        "v_max3_u32 v6, v6, v108, v109\n\t"
        "v_max3_u32 v6, v6, v110, v111\n\t"
        "v_max3_u32 v6, v6, v112, v113\n\t"
        "v_max3_u32 v6, v6, v114, v115\n\t"
        "v_max3_u32 v6, v6, v116, v117\n\t"
        "v_max3_u32 v6, v6, v118, v119\n\t"
        "v_max3_u32 v6, v6, v120, v121\n\t"
        "v_max3_u32 v6, v6, v122, v123\n\t"
        "v_max3_u32 v6, v6, v124, v125\n\t"
        "v_max3_u32 v6, v6, v126, v127\n\t"
        "v_max3_u32 v6, v6, v128, v129\n\t"
        "v_max3_u32 v6, v6, v130, v131\n\t"
        "v_max3_u32 v6, v6, v132, v133\n\t"
        "v_max3_u32 v6, v6, v134, v135\n\t"
        "v_max3_u32 v6, v6, v136, v137\n\t"
        "v_max3_u32 v6, v6, v138, v139\n\t"
        "v_max3_u32 v6, v6, v140, v141\n\t"
        "v_max3_u32 v6, v6, v142, v143\n\t"
        "v_max3_u32 v6, v6, v144, v145\n\t"
        "v_max3_u32 v6, v6, v146, v147\n\t"
        "v_max3_u32 v6, v6, v148, v149\n\t"
        "v_max3_u32 v6, v6, v150, v151\n\t"
        "v_max3_u32 v6, v6, v152, v153\n\t"
        "v_max3_u32 v6, v6, v154, v155\n\t"
        "v_max3_u32 v6, v6, v156, v157\n\t"
        "v_max3_u32 v6, v6, v158, v159\n\t"
        "v_max3_u32 v6, v6, v160, v161\n\t"
        "v_max3_u32 v6, v6, v162, v163\n\t"
        "v_max3_u32 v6, v6, v164, v165\n\t"
        "v_max3_u32 v6, v6, v166, v167\n\t"
        "v_max3_u32 v6, v6, v168, v169\n\t"
        "v_max3_u32 v6, v6, v170, v171\n\t"
        "v_max3_u32 v6, v6, v172, v173\n\t"
        "v_max3_u32 v6, v6, v174, v175\n\t"
        "v_max3_u32 v6, v6, v176, v177\n\t"
        "v_max3_u32 v6, v6, v178, v179\n\t"
        "v_max3_u32 v6, v6, v180, v181\n\t"
        "v_max3_u32 v6, v6, v182, v183\n\t"
        "v_max3_u32 v6, v6, v184, v185\n\t"
        "v_max3_u32 v6, v6, v186, v187\n\t"
        "v_max3_u32 v6, v6, v188, v189\n\t"
        "v_max3_u32 v6, v6, v190, v191\n\t"
        "v_cmp_eq_u32 vcc, s66, v6\n\t"
        "s_cbranch_vccz .Lr_ok0_%=\n\t"
        "s_add_u32 s79, s79, 1\n\t"
        "s_sub_u32 s46, s46, 1\n\t"
        "s_cbranch_scc1 .Lr_og0_%=\n\t"
        "s_add_u32 s70, s48, 212992\n\t"
        "buffer_load_dwordx4 v[96:99], v0, %[rs], s70 offen offset:0 sc1\n\t"
        "buffer_load_dwordx4 v[100:103], v0, %[rs], s70 offen offset:1024 sc1\n\t"
        "buffer_load_dwordx4 v[104:107], v0, %[rs], s70 offen offset:2048 sc1\n\t"
        "buffer_load_dwordx4 v[108:111], v0, %[rs], s70 offen offset:3072 sc1\n\t"
        "s_add_u32 s70, s48, 245760\n\t"
        "buffer_load_dwordx4 v[112:115], v0, %[rs], s70 offen offset:0 sc1\n\t"
        "buffer_load_dwordx4 v[116:119], v0, %[rs], s70 offen offset:1024 sc1\n\t"
        "buffer_load_dwordx4 v[120:123], v0, %[rs], s70 offen offset:2048 sc1\n\t"
        "buffer_load_dwordx4 v[124:127], v0, %[rs], s70 offen offset:3072 sc1\n\t"
        "s_add_u32 s70, s48, 278528\n\t"
        "buffer_load_dwordx4 v[128:131], v0, %[rs], s70 offen offset:0 sc1\n\t"
        "buffer_load_dwordx4 v[132:135], v0, %[rs], s70 offen offset:1024 sc1\n\t"
        "buffer_load_dwordx4 v[136:139], v0, %[rs], s70 offen offset:2048 sc1\n\t"
        "buffer_load_dwordx4 v[140:143], v0, %[rs], s70 offen offset:3072 sc1\n\t"
        "s_add_u32 s70, s48, 311296\n\t"
        "buffer_load_dwordx4 v[144:147], v0, %[rs], s70 offen offset:0 sc1\n\t"
        "buffer_load_dwordx4 v[148:151], v0, %[rs], s70 offen offset:1024 sc1\n\t"
        "buffer_load_dwordx4 v[152:155], v0, %[rs], s70 offen offset:2048 sc1\n\t"
        "buffer_load_dwordx4 v[156:159], v0, %[rs], s70 offen offset:3072 sc1\n\t"
        "s_add_u32 s70, s48, 217088\n\t"
        "buffer_load_dwordx4 v[160:163], v0, %[rs], s70 offen offset:0 sc1\n\t"
        "buffer_load_dwordx4 v[164:167], v0, %[rs], s70 offen offset:1024 sc1\n\t"
        "s_add_u32 s70, s48, 249856\n\t"
        "buffer_load_dwordx4 v[168:171], v0, %[rs], s70 offen offset:0 sc1\n\t"
        "buffer_load_dwordx4 v[172:175], v0, %[rs], s70 offen offset:1024 sc1\n\t"
        "s_add_u32 s70, s48, 282624\n\t"
        "buffer_load_dwordx4 v[176:179], v0, %[rs], s70 offen offset:0 sc1\n\t"
        "buffer_load_dwordx4 v[180:183], v0, %[rs], s70 offen offset:1024 sc1\n\t"
        "s_add_u32 s70, s48, 315392\n\t"
        "buffer_load_dwordx4 v[184:187], v0, %[rs], s70 offen offset:0 sc1\n\t"
        "buffer_load_dwordx4 v[188:191], v0, %[rs], s70 offen offset:1024 sc1\n\t"
        "ds_read_b64 v[16:17], v1 offset:0\n\t"
        "ds_read_b64 v[20:21], v1 offset:1024\n\t"
        "ds_read_b64 v[24:25], v1 offset:2048\n\t"
        "ds_read_b64 v[28:29], v1 offset:3072\n\t"
        "ds_read_b64 v[32:33], v1 offset:4096\n\t"
        "ds_read_b64 v[36:37], v1 offset:5120\n\t"
        "ds_read_b64 v[40:41], v1 offset:6144\n\t"
        "ds_read_b64 v[44:45], v1 offset:7168\n\t"
        "ds_read_b64 v[48:49], v1 offset:8192\n\t"
        "ds_read_b64 v[52:53], v1 offset:9216\n\t"
        "ds_read_b64 v[56:57], v1 offset:10240\n\t"
        "ds_read_b64 v[60:61], v1 offset:11264\n\t"
        "s_waitcnt vmcnt(23)\n\t"
        "s_waitcnt lgkmcnt(10)\n\t"
        "v_mfma_f32_16x16x16_bf16 v[76:79], v[16:17], v[96:97], 0\n\t"
        "ds_read_b64 v[16:17], v1 offset:12288\n\t"
        "v_mfma_f32_16x16x16_bf16 v[12:15], v[20:21], v[98:99], 0\n\t"
        "ds_read_b64 v[20:21], v1 offset:13312\n\t"
        "s_waitcnt vmcnt(22)\n\t"
        "s_waitcnt lgkmcnt(10)\n\t"
        "v_mfma_f32_16x16x16_bf16 v[76:79], v[24:25], v[100:101], v[76:79]\n\t"
        "ds_read_b64 v[24:25], v1 offset:14336\n\t"
        "v_mfma_f32_16x16x16_bf16 v[12:15], v[28:29], v[102:103], v[12:15]\n\t"
        "ds_read_b64 v[28:29], v1 offset:15360\n\t"
        "s_waitcnt vmcnt(21)\n\t"
        "s_waitcnt lgkmcnt(10)\n\t"
        "v_mfma_f32_16x16x16_bf16 v[76:79], v[32:33], v[104:105], v[76:79]\n\t"
        "ds_read_b64 v[32:33], v1 offset:16384\n\t"
        "v_mfma_f32_16x16x16_bf16 v[12:15], v[36:37], v[106:107], v[12:15]\n\t"
        "ds_read_b64 v[36:37], v1 offset:17408\n\t"
        "s_waitcnt vmcnt(20)\n\t"
        "s_waitcnt lgkmcnt(10)\n\t"
        "v_mfma_f32_16x16x16_bf16 v[76:79], v[40:41], v[108:109], v[76:79]\n\t"
        "ds_read_b64 v[40:41], v1 offset:18432\n\t"
        "v_mfma_f32_16x16x16_bf16 v[12:15], v[44:45], v[110:111], v[12:15]\n\t"
        "ds_read_b64 v[44:45], v1 offset:19456\n\t"
        "s_waitcnt vmcnt(19)\n\t"
        "s_waitcnt lgkmcnt(10)\n\t"
        "v_mfma_f32_16x16x16_bf16 v[76:79], v[48:49], v[112:113], v[76:79]\n\t"
        "ds_read_b64 v[48:49], v1 offset:20480\n\t"
        "v_mfma_f32_16x16x16_bf16 v[12:15], v[52:53], v[114:115], v[12:15]\n\t"
        "ds_read_b64 v[52:53], v1 offset:21504\n\t"
        "s_waitcnt vmcnt(18)\n\t"
        "s_waitcnt lgkmcnt(10)\n\t"
        "v_mfma_f32_16x16x16_bf16 v[76:79], v[56:57], v[116:117], v[76:79]\n\t"
        "ds_read_b64 v[56:57], v1 offset:22528\n\t"
        "v_mfma_f32_16x16x16_bf16 v[12:15], v[60:61], v[118:119], v[12:15]\n\t"
        "ds_read_b64 v[60:61], v1 offset:23552\n\t"
        "s_waitcnt vmcnt(17)\n\t"
        "s_waitcnt lgkmcnt(10)\n\t"
        "v_mfma_f32_16x16x16_bf16 v[76:79], v[16:17], v[120:121], v[76:79]\n\t"
        "ds_read_b64 v[16:17], v1 offset:24576\n\t"
        "v_mfma_f32_16x16x16_bf16 v[12:15], v[20:21], v[122:123], v[12:15]\n\t"
        "ds_read_b64 v[20:21], v1 offset:25600\n\t"
        "s_waitcnt vmcnt(16)\n\t"
        "s_waitcnt lgkmcnt(10)\n\t"
        "v_mfma_f32_16x16x16_bf16 v[76:79], v[24:25], v[124:125], v[76:79]\n\t"
        "ds_read_b64 v[24:25], v1 offset:26624\n\t"
        "v_mfma_f32_16x16x16_bf16 v[12:15], v[28:29], v[126:127], v[12:15]\n\t"
        "ds_read_b64 v[28:29], v1 offset:27648\n\t"
        "s_waitcnt vmcnt(15)\n\t"
        "s_waitcnt lgkmcnt(10)\n\t"
        "v_mfma_f32_16x16x16_bf16 v[76:79], v[32:33], v[128:129], v[76:79]\n\t"
        "ds_read_b64 v[32:33], v1 offset:28672\n\t"
        "v_mfma_f32_16x16x16_bf16 v[12:15], v[36:37], v[130:131], v[12:15]\n\t"
        "ds_read_b64 v[36:37], v1 offset:29696\n\t"
        "s_waitcnt vmcnt(14)\n\t"
        "s_waitcnt lgkmcnt(10)\n\t"
        "v_mfma_f32_16x16x16_bf16 v[76:79], v[40:41], v[132:133], v[76:79]\n\t"
        "ds_read_b64 v[40:41], v1 offset:30720\n\t"
        "v_mfma_f32_16x16x16_bf16 v[12:15], v[44:45], v[134:135], v[12:15]\n\t"
        "ds_read_b64 v[44:45], v1 offset:31744\n\t"
        "s_waitcnt vmcnt(13)\n\t"
        "s_waitcnt lgkmcnt(10)\n\t"
        "v_mfma_f32_16x16x16_bf16 v[76:79], v[48:49], v[136:137], v[76:79]\n\t"
        "ds_read_b128 v[48:51], v2 offset:0\n\t"
        "v_mfma_f32_16x16x16_bf16 v[12:15], v[52:53], v[138:139], v[12:15]\n\t"
        "ds_read_b128 v[52:55], v2 offset:1024\n\t"
        "s_waitcnt vmcnt(12)\n\t"
        "s_waitcnt lgkmcnt(10)\n\t"
        "v_mfma_f32_16x16x16_bf16 v[76:79], v[56:57], v[140:141], v[76:79]\n\t"
        "ds_read_b128 v[56:59], v2 offset:2048\n\t"
        "v_mfma_f32_16x16x16_bf16 v[12:15], v[60:61], v[142:143], v[12:15]\n\t"
        "ds_read_b128 v[60:63], v2 offset:3072\n\t"
        "s_waitcnt vmcnt(11)\n\t"
        "s_waitcnt lgkmcnt(10)\n\t"
        "v_mfma_f32_16x16x16_bf16 v[76:79], v[16:17], v[144:145], v[76:79]\n\t"
        "ds_read_b128 v[16:19], v2 offset:4096\n\t"
        "v_mfma_f32_16x16x16_bf16 v[12:15], v[20:21], v[146:147], v[12:15]\n\t"
        "ds_read_b128 v[20:23], v2 offset:5120\n\t"
        "s_waitcnt vmcnt(10)\n\t"
        "s_waitcnt lgkmcnt(10)\n\t"
        "v_mfma_f32_16x16x16_bf16 v[76:79], v[24:25], v[148:149], v[76:79]\n\t"
        "ds_read_b128 v[24:27], v2 offset:6144\n\t"
        "v_mfma_f32_16x16x16_bf16 v[12:15], v[28:29], v[150:151], v[12:15]\n\t"
        "ds_read_b128 v[28:31], v2 offset:7168\n\t"
        "s_waitcnt vmcnt(9)\n\t"
        "s_waitcnt lgkmcnt(10)\n\t"
        "v_mfma_f32_16x16x16_bf16 v[76:79], v[32:33], v[152:153], v[76:79]\n\t"
        "v_mfma_f32_16x16x16_bf16 v[12:15], v[36:37], v[154:155], v[12:15]\n\t"
        "s_waitcnt vmcnt(8)\n\t"
        "s_waitcnt lgkmcnt(8)\n\t"
        "v_mfma_f32_16x16x16_bf16 v[76:79], v[40:41], v[156:157], v[76:79]\n\t"
        "v_mfma_f32_16x16x16_bf16 v[12:15], v[44:45], v[158:159], v[12:15]\n\t"
        "s_waitcnt vmcnt(7)\n\t"
        "s_waitcnt lgkmcnt(6)\n\t"
        "v_mfma_f32_16x16x32_bf16 v[72:75], v[48:51], v[160:163], 0\n\t"
        "s_waitcnt vmcnt(6)\n\t"
        "v_mfma_f32_16x16x32_bf16 v[8:11], v[52:55], v[164:167], 0\n\t"
        "s_waitcnt vmcnt(5)\n\t"
        "s_waitcnt lgkmcnt(4)\n\t"
        "v_mfma_f32_16x16x32_bf16 v[72:75], v[56:59], v[168:171], v[72:75]\n\t"
        "s_waitcnt vmcnt(4)\n\t"
        "v_mfma_f32_16x16x32_bf16 v[8:11], v[60:63], v[172:175], v[8:11]\n\t"
        "s_waitcnt vmcnt(3)\n\t"
        "s_waitcnt lgkmcnt(2)\n\t"
        "v_mfma_f32_16x16x32_bf16 v[72:75], v[16:19], v[176:179], v[72:75]\n\t"
        "s_waitcnt vmcnt(2)\n\t"
        "v_mfma_f32_16x16x32_bf16 v[8:11], v[20:23], v[180:183], v[8:11]\n\t"
        "s_waitcnt vmcnt(1)\n\t"
        "s_waitcnt lgkmcnt(0)\n\t"
        "v_mfma_f32_16x16x32_bf16 v[72:75], v[24:27], v[184:187], v[72:75]\n\t"
        "s_waitcnt vmcnt(0)\n\t"
        "v_mfma_f32_16x16x32_bf16 v[8:11], v[28:31], v[188:191], v[8:11]\n\t"
        "s_nop 7\n\t"
        "v_add_f32 v8, v8, v72\n\t"
        "v_add_f32 v9, v9, v73\n\t"
        "v_add_f32 v10, v10, v74\n\t"
        "v_add_f32 v11, v11, v75\n\t"
        "v_add_f32 v12, v12, v76\n\t"
        "v_add_f32 v13, v13, v77\n\t"
        "v_add_f32 v14, v14, v78\n\t"
        "v_add_f32 v15, v15, v79\n\t"
        "v_add_f32 v8, v8, v12\n\t"
        "v_add_f32 v9, v9, v13\n\t"
        "v_add_f32 v10, v10, v14\n\t"
        "v_add_f32 v11, v11, v15\n\t"
        "s_branch .Lr_o20_%=\n\t"
        ".Lr_og0_%=:\n\t"
        "s_mov_b32 s46, 0\n\t"
        "s_branch .Lr_ok0_%=\n\t"
        ".Lr_twx_%=:\n\t"
        "s_add_u32 s79, s79, 0x1000000\n\t"
        "s_sleep 4\n\t"
        "buffer_load_dword v18, v16, %[rs], s67 offen sc1\n\t"
        "s_waitcnt vmcnt(0)\n\t"
        "s_sub_u32 s46, s46, 1\n\t"
        "s_cbranch_scc0 .Lr_twm_%=\n\t"
        "s_mov_b32 s46, 0\n\t"
        "s_branch .Lr_twy_%=\n\t"
        ".Lr_tlx_%=:\n\t"
        "s_add_u32 s79, s79, 0x1000000\n\t"
        "s_sleep 4\n\t"
        "buffer_load_dword v18, v16, %[rs], s67 offen sc1\n\t"
        "s_waitcnt vmcnt(0)\n\t"
        "s_sub_u32 s46, s46, 1\n\t"
        "s_cbranch_scc0 .Lr_tlm_%=\n\t"
        "s_mov_b32 s46, 0\n\t"
        "s_branch .Lr_tly_%=\n\t"
        ".Lr_pf_%=:\n\t"
        "v_max_u32 v4, v224, v225\n\t"
        "v_max3_u32 v4, v4, v226, v227\n\t"
        "v_max3_u32 v4, v4, v228, v229\n\t"
        "v_max3_u32 v4, v4, v230, v231\n\t"
        "v_max3_u32 v4, v4, v232, v233\n\t"
        "v_max3_u32 v4, v4, v234, v235\n\t"
        "v_max3_u32 v4, v4, v236, v237\n\t"
        "v_max3_u32 v4, v4, v238, v239\n\t"
        "v_max3_u32 v4, v4, v240, v241\n\t"
        "v_max3_u32 v4, v4, v242, v243\n\t"
        "v_max3_u32 v4, v4, v244, v245\n\t"
        "v_max3_u32 v4, v4, v246, v247\n\t"
        "v_max3_u32 v4, v4, v248, v249\n\t"
        "v_max3_u32 v4, v4, v250, v251\n\t"
        "v_max3_u32 v4, v4, v252, v253\n\t"
        "v_max3_u32 v4, v4, v254, v255\n\t"
        "v_max3_u32 v4, v4, v16, v17\n\t"
        "v_max3_u32 v4, v4, v18, v19\n\t"
        "v_max3_u32 v4, v4, v20, v21\n\t"
        "v_max3_u32 v4, v4, v22, v23\n\t"
        "v_max3_u32 v4, v4, v24, v25\n\t"
        "v_max3_u32 v4, v4, v26, v27\n\t"
        "v_max3_u32 v4, v4, v28, v29\n\t"
        "v_max3_u32 v4, v4, v30, v31\n\t"
        "v_max3_u32 v4, v4, v32, v33\n\t"
        "v_max3_u32 v4, v4, v34, v35\n\t"
        "v_max3_u32 v4, v4, v36, v37\n\t"
        "v_max3_u32 v4, v4, v38, v39\n\t"
        "v_max3_u32 v4, v4, v40, v41\n\t"
        "v_max3_u32 v4, v4, v42, v43\n\t"
        "v_max3_u32 v4, v4, v44, v45\n\t"
        "v_max3_u32 v4, v4, v46, v47\n\t"
        "v_max3_u32 v4, v4, v48, v49\n\t"
        "v_max3_u32 v4, v4, v50, v51\n\t"
        "v_max3_u32 v4, v4, v52, v53\n\t"
        "v_max3_u32 v4, v4, v54, v55\n\t"
        "v_max3_u32 v4, v4, v56, v57\n\t"
        "v_max3_u32 v4, v4, v58, v59\n\t"
        "v_max3_u32 v4, v4, v60, v61\n\t"
        "v_max3_u32 v4, v4, v62, v63\n\t"
        "v_max3_u32 v4, v4, v64, v65\n\t"
        "v_max3_u32 v4, v4, v66, v67\n\t"
        "v_max3_u32 v4, v4, v68, v69\n\t"
        "v_max3_u32 v4, v4, v70, v71\n\t"
        "v_max3_u32 v4, v4, v72, v73\n\t"
        "v_max3_u32 v4, v4, v74, v75\n\t"
        "v_max3_u32 v4, v4, v76, v77\n\t"
        "v_max3_u32 v4, v4, v78, v79\n\t"
        "v_max3_u32 v4, v4, v80, v81\n\t"
        "v_max3_u32 v4, v4, v82, v83\n\t"
        "v_max3_u32 v4, v4, v84, v85\n\t"
        "v_max3_u32 v4, v4, v86, v87\n\t"
        "v_max3_u32 v4, v4, v88, v89\n\t"
        "v_max3_u32 v4, v4, v90, v91\n\t"
        "v_max3_u32 v4, v4, v92, v93\n\t"
        "v_max3_u32 v4, v4, v94, v95\n\t"
        "v_cmp_eq_u32 s[68:69], s66, v4\n\t"
        "s_nop 3\n\t"
        "s_cmp_lg_u64 s[68:69], 0\n\t"
        "s_cbranch_scc0 .Lr_pk_%=\n\t"
        "v_mov_b64 v[8:9], 0\n\t"
        "v_mov_b64 v[10:11], 0\n\t"
        "s_mov_b64 vcc, s[64:65]\n\t"
        "s_sub_u32 s46, s46, 1\n\t"
        "s_cbranch_scc0 .Lr_pp_%=\n\t"
        "s_mov_b32 s46, 0\n\t"
        "v_mov_b32 v5, 0\n\t"
        "s_add_u32 s68, s45, 2100\n\t"
        "v_mov_b32 v6, s68\n\t"
        "global_store_dword v5, v6, %[tm] sc1\n\t"
        "s_branch .Lr_pk_%=\n\t"
        ".Lr_st_%=:\n\t"
        "v_mov_b32 v5, 114704\n\t"
        "v_add_u32 v12, s44, v0\n\t"
        ".Lr_fl20_%=:\n\t"
        "ds_read_b32 v13, v5 offset:0\n\t"
        "s_waitcnt lgkmcnt(0)\n\t"
        "v_cmp_lt_u32 s[68:69], s45, v13\n\t"
        "s_nop 3\n\t"
        "s_cmp_lg_u64 s[68:69], 0\n\t"
        "s_cbranch_scc0 .Lr_fl20_%=\n\t"
        "ds_read_b128 v[96:99], v12 offset:0\n\t"
        "ds_read_b128 v[100:103], v12 offset:1024\n\t"
        "ds_read_b128 v[104:107], v12 offset:2048\n\t"
        "ds_read_b128 v[108:111], v12 offset:3072\n\t"
        "ds_read_b128 v[112:115], v12 offset:4096\n\t"
        "ds_read_b128 v[116:119], v12 offset:5120\n\t"
        "ds_read_b128 v[120:123], v12 offset:6144\n\t"
        "ds_read_b128 v[124:127], v12 offset:7168\n\t"
        ".Lr_fl21_%=:\n\t"
        "ds_read_b32 v13, v5 offset:4\n\t"
        "s_waitcnt lgkmcnt(0)\n\t"
        "v_cmp_lt_u32 s[68:69], s45, v13\n\t"
        "s_nop 3\n\t"
        "s_cmp_lg_u64 s[68:69], 0\n\t"
        "s_cbranch_scc0 .Lr_fl21_%=\n\t"
        "ds_read_b128 v[128:131], v12 offset:8192\n\t"
        "ds_read_b128 v[132:135], v12 offset:9216\n\t"
        "ds_read_b128 v[136:139], v12 offset:10240\n\t"
        "ds_read_b128 v[140:143], v12 offset:11264\n\t"
        "ds_read_b128 v[144:147], v12 offset:12288\n\t"
        "ds_read_b128 v[148:151], v12 offset:13312\n\t"
        "ds_read_b128 v[152:155], v12 offset:14336\n\t"
        "ds_read_b128 v[156:159], v12 offset:15360\n\t"
        ".Lr_fl22_%=:\n\t"
        "ds_read_b32 v13, v5 offset:8\n\t"
        "s_waitcnt lgkmcnt(0)\n\t"
        "v_cmp_lt_u32 s[68:69], s45, v13\n\t"
        "s_nop 3\n\t"
        "s_cmp_lg_u64 s[68:69], 0\n\t"
        "s_cbranch_scc0 .Lr_fl22_%=\n\t"
        "ds_read_b128 v[160:163], v12 offset:16384\n\t"
        "ds_read_b128 v[164:167], v12 offset:17408\n\t"
        "ds_read_b128 v[168:171], v12 offset:18432\n\t"
        "ds_read_b128 v[172:175], v12 offset:19456\n\t"
        "ds_read_b128 v[176:179], v12 offset:20480\n\t"
        "ds_read_b128 v[180:183], v12 offset:21504\n\t"
        "ds_read_b128 v[184:187], v12 offset:22528\n\t"
        "ds_read_b128 v[188:191], v12 offset:23552\n\t"
        ".Lr_fl23_%=:\n\t"
        "ds_read_b32 v13, v5 offset:12\n\t"
        "s_waitcnt lgkmcnt(0)\n\t"
        "v_cmp_lt_u32 s[68:69], s45, v13\n\t"
        "s_nop 3\n\t"
        "s_cmp_lg_u64 s[68:69], 0\n\t"
        "s_cbranch_scc0 .Lr_fl23_%=\n\t"
        "ds_read_b128 v[192:195], v12 offset:24576\n\t"
        "ds_read_b128 v[196:199], v12 offset:25600\n\t"
        "ds_read_b128 v[200:203], v12 offset:26624\n\t"
        "ds_read_b128 v[204:207], v12 offset:27648\n\t"
        "ds_read_b128 v[208:211], v12 offset:28672\n\t"
        "ds_read_b128 v[212:215], v12 offset:29696\n\t"
        "ds_read_b128 v[216:219], v12 offset:30720\n\t"
        "ds_read_b128 v[220:223], v12 offset:31744\n\t"
        "s_waitcnt lgkmcnt(0)\n\t"
        "s_xor_b32 s67, s49, %[tw]\n\t"
        "v_and_b32 v16, 0x1f0, v0\n\t"
        "v_lshlrev_b32 v16, 1, v16\n\t"
        "buffer_load_dword v18, v16, %[rs], s67 offen sc1\n\t"
        "s_branch .Lr_s1_%=\n\t"
        ".Lr_an0f_%=:\n\t"
        "s_add_u32 s79, s79, 1\n\t"
        "s_sleep 4\n\t"
        "s_sub_u32 s46, s46, 1\n\t"
        "s_cbranch_scc0 .Lr_an0_%=\n\t"
        "s_mov_b32 s46, 0\n\t"
        "s_branch .Lr_an0k_%=\n\t"
        ".Lr_an1f_%=:\n\t"
        "s_add_u32 s79, s79, 1\n\t"
        "s_sleep 4\n\t"
        "s_sub_u32 s46, s46, 1\n\t"
        "s_cbranch_scc0 .Lr_an1_%=\n\t"
        "s_mov_b32 s46, 0\n\t"
        "s_branch .Lr_an1k_%=\n\t"
        ".Lcode_r1:\n\t"
        ".Lr_end_%=:\n\t"
        "s_or_b32 s70, %[oo], %[w]\n\t"
        "s_cmp_eq_u32 s70, 0\n\t"
        "s_cbranch_scc0 .Lr_fin_%=\n\t"
        "s_load_dwordx2 s[54:55], %[ep], 0\n\t"
        "v_mov_b32 v4, 0\n\t"
        "s_waitcnt lgkmcnt(0)\n\t"
        "s_add_u32 s54, s54, 1\n\t"
        "s_addc_u32 s55, s55, 0\n\t"
        "v_mov_b32 v6, s54\n\t"
        "v_mov_b32 v7, s55\n\t"
        "s_mov_b64 exec, 1\n\t"
        "global_store_dwordx2 v4, v[6:7], %[ep] sc1\n\t"
        ".Lr_fin_%=:\n\t"
        "s_endpgm\n\t"
        :: [rs] "s"(RSRC(arena(a.slots), ARENA_BYTES)), [ro] "s"(RSRC(a.out, S_LEN * OUTW * 2)), [tm] "s"(a.sync + 40 * 32), [ep] "s"(a.boot + 144), [w] "s"(w), [cr] "s"(cls_r(j)), [t0] "s"(t0), DGIN [om] "s"((64 + j) * 16384),
           [oy] "s"(YX_OFF + 4 * j * 16384 + (cls_r(j) ^ (j >> 5) ^ YXC) * 8192), [oa] "s"((j >> 5) * 16384 + (j & 31) * 256), [oo] "s"(j * 32), [tw] "s"(TW(cls_r(j))),
           [al] "s"((unsigned)(uintptr_t)arena(a.slots)), [ah] "s"((unsigned)((uintptr_t)arena(a.slots) >> 32) & 0xffffu) STIN(w == 0 ? 96 + j : w == 1 ? 160 + (j & 31) : w == 3 ? 288 + (j & 31) : 251)
        : "memory", "vcc", "scc", ALLV, ALLS);
    __builtin_unreachable();
}

struct cpx { float x, y; };
__device__ __forceinline__ cpx cmul(cpx a, cpx b) { return {a.x * b.x - a.y * b.y, a.x * b.y + a.y * b.x}; }
__device__ __forceinline__ cpx cadd(cpx a, cpx b) { return {a.x + b.x, a.y + b.y}; }
__device__ __forceinline__ cpx csub(cpx a, cpx b) { return {a.x - b.x, a.y - b.y}; }
template <int DIR> __device__ __forceinline__ cpx mul_i(cpx a) {
    return (DIR < 0) ? cpx{a.y, -a.x} : cpx{-a.y, a.x};
}
template <int DIR> __device__ __forceinline__ void r4(cpx& a, cpx& b, cpx& c, cpx& d) {
    cpx t0 = cadd(a, c), t1 = csub(a, c), t2 = cadd(b, d), t3 = mul_i<DIR>(csub(b, d));
    a = cadd(t0, t2); b = cadd(t1, t3); c = csub(t0, t2); d = csub(t1, t3);
}
template <int DIR> __device__ __forceinline__ cpx twc(float c, float s) { return cpx{c, (float)DIR * s}; }
template <int DIR> __device__ __forceinline__ void dft16(cpx (&v)[16]) {
#pragma unroll
    for (int a = 0; a < 4; ++a) r4<DIR>(v[a], v[a + 4], v[a + 8], v[a + 12]);
    const float C1 = 0.92387953251128674f, S1 = 0.38268343236508977f, R2 = 0.70710678118654752f;
    v[1 + 4] = cmul(v[1 + 4], twc<DIR>(C1, S1));
    v[1 + 8] = cmul(v[1 + 8], twc<DIR>(R2, R2));
    v[1 + 12] = cmul(v[1 + 12], twc<DIR>(S1, C1));
    v[2 + 4] = cmul(v[2 + 4], twc<DIR>(R2, R2));
    v[2 + 8] = mul_i<DIR>(v[2 + 8]);
    v[2 + 12] = cmul(v[2 + 12], twc<DIR>(-R2, R2));
    v[3 + 4] = cmul(v[3 + 4], twc<DIR>(S1, C1));
    v[3 + 8] = cmul(v[3 + 8], twc<DIR>(-R2, R2));
    v[3 + 12] = cmul(v[3 + 12], twc<DIR>(-C1, -S1));
#pragma unroll
    for (int d = 0; d < 4; ++d) r4<DIR>(v[4 * d], v[4 * d + 1], v[4 * d + 2], v[4 * d + 3]);
}
__device__ __forceinline__ constexpr int x16(int q) { return 4 * (q & 3) + (q >> 2); }
template <int DIR> __device__ __forceinline__ void dft8(cpx (&v)[8]) {
    r4<DIR>(v[0], v[2], v[4], v[6]);
    r4<DIR>(v[1], v[3], v[5], v[7]);
    const float R2 = 0.70710678118654752f;
    v[3] = cmul(v[3], twc<DIR>(R2, R2));
    v[5] = mul_i<DIR>(v[5]);
    v[7] = cmul(v[7], twc<DIR>(-R2, R2));
#pragma unroll
    for (int d = 0; d < 4; ++d) { cpx p = v[2 * d], q = v[2 * d + 1]; v[2 * d] = cadd(p, q); v[2 * d + 1] = csub(p, q); }
}
__device__ __forceinline__ int pad(int i) { return i + (i >> 4); }
__device__ __forceinline__ cpx expi(float x) { float s, c; sincospif(x, &s, &c); return cpx{c, s}; }
template <int R> __device__ __forceinline__ void powers(cpx* w, cpx w1) {
    w[1] = w1;
#pragma unroll
    for (int r = 2; r < R; ++r) w[r] = (r & 1) ? cmul(w[r - 1], w1) : cmul(w[r / 2], w[r / 2]);
}
template <int R, int DIR> __device__ __forceinline__ void rotate(cpx* v, const cpx* w) {
#pragma unroll
    for (int r = 1; r < R; ++r) v[r] = DIR < 0 ? cmul(v[r], w[r]) : cpx{v[r].x * w[r].x + v[r].y * w[r].y, v[r].y * w[r].x - v[r].x * w[r].y};
}
struct TwBase { cpx b; cpx c[4]; };
template <int N> __device__ __forceinline__ TwBase make_tw(int t) {
    TwBase tb;
    tb.b = expi(-(float)(t & 15) * (1.0f / 128.0f));
#pragma unroll
    for (int q = 0; q < 4; ++q) tb.c[q] = expi(-(float)(t + 64 * q) * (2.0f / (float)N));
    return tb;
}
__device__ __forceinline__ void wave_sync() { __builtin_amdgcn_fence(__ATOMIC_ACQ_REL, "wavefront"); __builtin_amdgcn_wave_barrier(); }
template <int N, int DIR, bool RIN, bool ROUT>
__device__ __forceinline__ void fft_reg(cpx* b0, cpx* b1, const TwBase& tb, const int t, cpx (&v)[2][16]) {
    constexpr int T = N / 16;
    cpx* const bf[2] = {b0, b1};
    const int tt[2] = {t, N == 2048 ? t + 64 : t};
    if constexpr (!RIN) {
#pragma unroll
        for (int e = 0; e < 2; ++e)
#pragma unroll
            for (int r = 0; r < 16; ++r) v[e][r] = bf[e][pad(tt[e] + T * r)];
        wave_sync();
    }
#pragma unroll
    for (int e = 0; e < 2; ++e) {
        dft16<DIR>(v[e]);
#pragma unroll
        for (int q = 0; q < 16; ++q) bf[e][pad(16 * tt[e] + x16(q))] = v[e][q];
    }
    wave_sync();
    {
#pragma unroll
        for (int e = 0; e < 2; ++e)
#pragma unroll
            for (int r = 0; r < 16; ++r) v[e][r] = bf[e][pad(tt[e] + T * r)];
        wave_sync();
        cpx w[16];
        powers<16>(w, tb.b);
#pragma unroll
        for (int e = 0; e < 2; ++e) {
            rotate<16, DIR>(v[e], w);
            dft16<DIR>(v[e]);
            const int o = (tt[e] >> 4) * 256 + (t & 15);
#pragma unroll
            for (int q = 0; q < 16; ++q) bf[e][pad(o + 16 * x16(q))] = v[e][q];
        }
        wave_sync();
    }
    if constexpr (N == 1024) {
#pragma unroll
        for (int e = 0; e < 2; ++e)
#pragma unroll
            for (int q = 0; q < 4; ++q)
#pragma unroll
                for (int r = 0; r < 4; ++r) v[e][4 * q + r] = bf[e][pad(t + 64 * q + 256 * r)];
#pragma unroll
        for (int q = 0; q < 4; ++q) {
            cpx w[4];
            powers<4>(w, tb.c[q]);
#pragma unroll
            for (int e = 0; e < 2; ++e) {
                rotate<4, DIR>(&v[e][4 * q], w);
                r4<DIR>(v[e][4 * q], v[e][4 * q + 1], v[e][4 * q + 2], v[e][4 * q + 3]);
#pragma unroll
                for (int r = 0; r < 4; ++r) bf[e][pad(t + 64 * q + 256 * r)] = v[e][4 * q + r];
            }
        }
        wave_sync();
    } else {
        cpx u[4][8];
#pragma unroll
        for (int q = 0; q < 4; ++q)
#pragma unroll
            for (int r = 0; r < 8; ++r) u[q][r] = b0[pad(t + 64 * q + 256 * r)];
        if constexpr (ROUT) wave_sync();
#pragma unroll
        for (int q = 0; q < 4; ++q) {
            cpx w[8];
            powers<8>(w, tb.c[q]);
            rotate<8, DIR>(u[q], w);
            dft8<DIR>(u[q]);
#pragma unroll
            for (int d = 0; d < 4; ++d) {
                if constexpr (ROUT) { v[q & 1][(q >> 1) + 2 * d] = u[q][2 * d]; v[q & 1][(q >> 1) + 2 * d + 8] = u[q][2 * d + 1]; }
                else { b0[pad(t + 64 * q + 256 * d)] = u[q][2 * d]; b0[pad(t + 64 * q + 256 * (d + 4))] = u[q][2 * d + 1]; }
            }
        }
        if constexpr (!ROUT) wave_sync();
    }
}
constexpr int WP = 2048 + 96;
constexpr int FP = 514;
constexpr int PB1 = 1024 + 64, PB2 = 2048 + 128;
typedef float f32x4v __attribute__((ext_vector_type(4)));
typedef unsigned u32x4v __attribute__((ext_vector_type(4)));

__device__ __forceinline__ void fft_rows_fwd(const u16* __restrict__ x, rsrc_t rsF, const int g, char* smem, const int tid) {
    cpx (*buf)[PB1] = (cpx (*)[PB1])smem;
    const int f = tid >> 6, t = tid & 63;
    const u16* p0 = x + (size_t)(g * 32 + 4 * f) * 1024 + 16 * t;
    u32x4v a[4][2];
#pragma unroll
    for (int r = 0; r < 4; ++r) { a[r][0] = *(const u32x4v*)(p0 + r * 1024); a[r][1] = *(const u32x4v*)(p0 + r * 1024 + 8); }
    const TwBase tb = make_tw<1024>(t);
#pragma unroll
    for (int e = 0; e < 2; ++e) {
        cpx* d = &buf[2 * f + e][pad(16 * t)];
#pragma unroll
        for (int c = 0; c < 8; ++c) {
            const unsigned p = a[2 * e][c >> 2][c & 3], q = a[2 * e + 1][c >> 2][c & 3];
            d[2 * c] = cpx{__uint_as_float(p << 16), __uint_as_float(q << 16)};
            d[2 * c + 1] = cpx{__uint_as_float(p & 0xFFFF0000u), __uint_as_float(q & 0xFFFF0000u)};
        }
    }
    wave_sync();
    cpx z[2][16];
    fft_reg<1024, -1, false, false>(buf[2 * f], buf[2 * f + 1], tb, t, z);
    __syncthreads();
    const int bl = tid >> 7;
    for (int idx = tid & 127; idx < 513 * 4; idx += 128) {
        const int k = idx >> 2, ff = 4 * bl + (idx & 3);
        const cpx zk = buf[ff][pad(k)], zn = buf[ff][pad((1024 - k) & 1023)];
        const f32x4v o = {0.5f * (zk.x + zn.x), 0.5f * (zk.y - zn.y), 0.5f * (zk.y + zn.y), -0.5f * (zk.x - zn.x)};
        st_sc1(rsF, (4 * g + bl) * FP * 64 + idx * 16, __builtin_bit_cast(v4u, o));
    }
}
__device__ __forceinline__ void filt_transpose(const u16* __restrict__ wr, const u16* __restrict__ wi, rsrc_t rsW, const int tb, char* smem, const int tid) {
    const v4u* s0 = (const v4u*)(wr + (size_t)tb * 32 * 513);
    const v4u* s1 = (const v4u*)(wi + (size_t)tb * 32 * 513);
    v4u r0[5], r1[5];
#pragma unroll
    for (int e = 0; e < 5; ++e) {
        const int idx = tid + 512 * e, ic = idx < 2052 ? idx : 2051;
        r0[e] = s0[ic]; r1[e] = s1[ic];
    }
#pragma unroll
    for (int e = 0; e < 5; ++e) {
        const int idx = tid + 512 * e;
        if (idx < 2052) { *(v4u*)(smem + idx * 16) = r0[e]; *(v4u*)(smem + 32832 + idx * 16) = r1[e]; }
    }
    __syncthreads();
    const u16* a0 = (const u16*)smem;
    const u16* a1 = (const u16*)(smem + 32832);
    for (int idx = tid; idx < 513 * 8; idx += 512) {
        const int k = idx >> 3, n4 = (idx & 7) * 4;
        v4u v;
#pragma unroll
        for (int j = 0; j < 4; ++j) v[j] = (unsigned)a0[(n4 + j) * 513 + k] | ((unsigned)a1[(n4 + j) * 513 + k] << 16);
        st_sc1(rsW, (k * WP + tb * 32 + n4) * 4, v);
    }
}

__device__ __forceinline__ void fft_col(rsrc_t rsF, rsrc_t rsW, const int k, char* smem, const int t) {
    cpx* buf = (cpx*)smem;
    cpx z[2][16];
    unsigned wv[2][16];
#pragma unroll
    for (int e = 0; e < 2; ++e)
#pragma unroll
        for (int r = 0; r < 16; ++r) z[e][r] = __builtin_bit_cast(cpx, __builtin_amdgcn_raw_buffer_load_b64(rsF, (t >> 3) * FP * 64 + (t & 7) * 8, ((8 * e + 16 * r) * FP + k) * 64, AUX_SC1));
#pragma unroll
    for (int e = 0; e < 2; ++e)
#pragma unroll
        for (int r = 0; r < 16; ++r) wv[e][r] = __builtin_amdgcn_raw_buffer_load_b32(rsW, (t + 64 * e + 128 * r) * 4, k * WP * 4, AUX_SC1);
    FST(8)
    const TwBase tb = make_tw<2048>(t);
    FWT FST(9)
    fft_reg<2048, -1, true, true>(buf, buf, tb, t, z);
    FST(10)
#pragma unroll
    for (int e = 0; e < 2; ++e)
#pragma unroll
        for (int r = 0; r < 16; ++r) z[e][r] = cmul(z[e][r], cpx{__uint_as_float(wv[e][r] << 16), __uint_as_float(wv[e][r] & 0xFFFF0000u)});
    fft_reg<2048, 1, true, false>(buf, buf, tb, t, z);
    FST(11)
    FST(12)
}

__device__ __forceinline__ void fft_col_out(rsrc_t rsF, const int k, const char* smem, const int t) {
    const cpx* buf = (const cpx*)smem + ((t >> 2) & 1) * PB2;
    const float sc = 1.0f / 2048.0f;
#pragma unroll
    for (int m = 0; m < 16; ++m) {
        const int rb = (k & 1) * 128 + (t >> 3) + 8 * m;
        const cpx* d = &buf[pad(rb * 8 + 2 * (t & 3))];
        st_sc1(rsF, (rb * FP + (k & ~1)) * 64 + (t & 7) * 16, __builtin_bit_cast(v4u, f32x4v{d[0].x * sc, d[0].y * sc, d[1].x * sc, d[1].y * sc}));
    }
}

__device__ __forceinline__ void fft_rows_inv(rsrc_t rsF, u16* __restrict__ out, const int g, char* smem, const int tid) {
    cpx (*buf)[PB1] = (cpx (*)[PB1])smem;
    const int f = tid >> 6, t = tid & 63, bl = tid >> 7, u = tid & 127;
    f32x4v v[17];
    const int src = (4 * g + bl) * FP * 64;
#pragma unroll
    for (int m = 0; m < 16; ++m) v[m] = __builtin_bit_cast(f32x4v, __builtin_amdgcn_raw_buffer_load_b128(rsF, src + u * 16, m * 2048, AUX_SC1));
    v[16] = __builtin_bit_cast(f32x4v, __builtin_amdgcn_raw_buffer_load_b128(rsF, src + (u & 3) * 16, 32768, AUX_SC1));
    const TwBase tb = make_tw<1024>(t);
    cpx* bf = buf[4 * bl + (u & 3)];
#pragma unroll
    for (int m = 0; m < 16; ++m) {
        const int k = (u >> 2) + 32 * m;
        if (m == 0 && k == 0) { bf[0] = cpx{v[0][0], v[0][2]}; bf[pad(512)] = cpx{v[16][0], v[16][2]}; }
        else { bf[pad(k)] = cpx{v[m][0] - v[m][3], v[m][1] + v[m][2]}; bf[pad(1024 - k)] = cpx{v[m][0] + v[m][3], v[m][2] - v[m][1]}; }
    }
    __syncthreads();
    cpx z[2][16];
    fft_reg<1024, 1, false, false>(buf[2 * f], buf[2 * f + 1], tb, t, z);
    const float sc = 1.0f / 1024.0f;
#pragma unroll
    for (int e = 0; e < 2; ++e) {
        u32x4v a[2], b[2];
#pragma unroll
        for (int h = 0; h < 2; ++h) {
            const cpx* d = &buf[2 * f + e][pad(512 * h + 8 * t)];
#pragma unroll
            for (int c = 0; c < 4; ++c) {
                cpx p = d[2 * c], q = d[2 * c + 1];
                a[h][c] = cvt_pk_bf16(p.x * sc, q.x * sc);
                b[h][c] = cvt_pk_bf16(p.y * sc, q.y * sc);
            }
        }
        u16* o0 = out + (size_t)(g * 32 + 4 * f + 2 * e) * OUTW + 1024 + 8 * t;
        *(u32x4v*)o0 = a[0]; *(u32x4v*)(o0 + 512) = a[1];
        *(u32x4v*)(o0 + OUTW) = b[0]; *(u32x4v*)(o0 + OUTW + 512) = b[1];
    }
}

#define K8_BAR do { asm volatile("" ::: "memory"); __builtin_amdgcn_s_barrier(); asm volatile("" ::: "memory"); } while (0)
#define K8_WV(n) asm volatile("s_waitcnt vmcnt(" #n ")" ::: "memory")
#define K8_WL(n) asm volatile("s_waitcnt lgkmcnt(" #n ")" ::: "memory")
#define K8_SCHED __builtin_amdgcn_sched_barrier(0)
__device__ __forceinline__ void xw_stream(const int w, const int idx) {
    const Args a = get_args();
    const int kind = idx >> 5, h = idx & 31;
    asm volatile(
        ".Lcode_x0:\n\t"
        "v_mbcnt_lo_u32_b32 v1, -1, 0\n\t"
        "v_mbcnt_hi_u32_b32 v1, -1, v1\n\t"
        "v_lshlrev_b32 v0, 4, v1\n\t"
        "v_lshrrev_b32 v2, 3, v1\n\t"
        "v_and_b32 v3, 7, v1\n\t"
        "v_xor_b32 v3, v3, v2\n\t"
        "v_lshlrev_b32 v3, 4, v3\n\t"
        "v_lshl_add_u32 v4, v2, 11, v3\n\t"
        "s_lshl_b32 s70, %[w], 8\n\t"
        "s_add_u32 s70, s70, 4096\n\t"
        "v_add_u32 v4, s70, v4\n\t"
        "v_add_u32 v5, 15360, v4\n\t"
        "v_add_u32 v6, -1920, v4\n\t"
        "v_add_u32 v7, 13440, v4\n\t"
        "v_lshrrev_b32 v2, 4, v1\n\t"
        "v_and_b32 v3, 15, v1\n\t"
        "s_and_b32 s70, %[w], 1\n\t"
        "s_lshl_b32 s70, s70, 2\n\t"
        "v_or_b32 v8, s70, v2\n\t"
        "v_lshlrev_b32 v8, 1, v8\n\t"
        "v_xor_b32 v8, v8, v3\n\t"
        "v_lshlrev_b32 v8, 4, v8\n\t"
        "v_lshl_add_u32 v8, v2, 13, v8\n\t"
        "s_lshr_b32 s44, %[w], 2\n\t"
        "s_and_b32 s45, %[w], 3\n\t"
        "s_lshl_b32 s46, s44, 22\n\t"
        "s_lshl_b32 s70, s45, 16\n\t"
        "s_add_u32 s46, s46, s70\n\t"
        "s_lshl_b32 s47, %[w], 12\n\t"
        "s_lshl_b32 s48, %[w], 11\n\t"
        "s_add_u32 s48, s48, 65536\n\t"
        "s_mov_b32 s49, 0\n\t"
        "s_add_u32 m0, s48, 0\n\t"
        "s_add_u32 s70, s46, 0\n\t"
        "s_add_u32 s71, s46, 31744\n\t"
        "buffer_load_dwordx4 v8, %[rw], s70 offen lds\n\t"
        "buffer_load_dwordx4 v8, %[rw], s71 offen offset:1024 lds\n\t"
        "s_add_u32 m0, s48, 16384\n\t"
        "s_add_u32 s70, s46, 1048576\n\t"
        "s_add_u32 s71, s46, 1080320\n\t"
        "buffer_load_dwordx4 v8, %[rw], s70 offen lds\n\t"
        "buffer_load_dwordx4 v8, %[rw], s71 offen offset:1024 lds\n\t"
        "s_add_u32 m0, s47, 0\n\t"
        "s_nop 0\n\t"
        "buffer_load_dwordx4 v4, %[rx], s49 offen offset:0 lds\n\t"
        "buffer_load_dwordx4 v5, %[rx], s49 offen offset:1024 lds\n\t"
        "buffer_load_dwordx4 v6, %[rx], s49 offen offset:2048 lds\n\t"
        "buffer_load_dwordx4 v7, %[rx], s49 offen offset:3072 lds\n\t"
        "s_add_u32 s49, s49, 32768\n\t"
        "s_add_u32 m0, s47, 32768\n\t"
        "s_nop 0\n\t"
        "buffer_load_dwordx4 v4, %[rx], s49 offen offset:0 lds\n\t"
        "buffer_load_dwordx4 v5, %[rx], s49 offen offset:1024 lds\n\t"
        "buffer_load_dwordx4 v6, %[rx], s49 offen offset:2048 lds\n\t"
        "buffer_load_dwordx4 v7, %[rx], s49 offen offset:3072 lds\n\t"
        "s_add_u32 s49, s49, 32768\n\t"
        "s_add_u32 m0, s48, 32768\n\t"
        "s_add_u32 s70, s46, 2097152\n\t"
        "s_add_u32 s71, s46, 2128896\n\t"
        "buffer_load_dwordx4 v8, %[rw], s70 offen lds\n\t"
        "buffer_load_dwordx4 v8, %[rw], s71 offen offset:1024 lds\n\t"
        "s_cmp_eq_u32 %[kn], 0\n\t"
        "s_cselect_b64 s[56:57], -1, 0\n\t"
        "v_and_b32 v72, 1, v2\n\t"
        "v_lshlrev_b32 v72, 1, v72\n\t"
        "v_lshrrev_b32 v73, 1, v2\n\t"
        "v_add_u32 v72, v72, v73\n\t"
        "v_lshlrev_b32 v72, 8, v72\n\t"
        "v_lshl_add_u32 v72, v3, 4, v72\n\t"
        "v_cndmask_b32_e64 v79, v0, v72, s[56:57]\n\t"
        "v_and_b32 v9, 7, v1\n\t"
        "v_xor_b32 v10, v2, v9\n\t"
        "v_lshlrev_b32 v10, 4, v10\n\t"
        "v_lshl_add_u32 v10, v3, 7, v10\n\t"
        "s_lshl_b32 s70, s44, 14\n\t"
        "v_add_u32 v10, s70, v10\n\t"
        "v_xor_b32 v11, 64, v10\n\t"
        "v_add_u32 v12, 65536, v10\n\t"
        "v_add_u32 v13, 65536, v11\n\t"
        "s_lshl_b32 s70, s45, 11\n\t"
        "s_add_u32 s70, s70, 131072\n\t"
        "v_add_u32 v14, s70, v0\n\t"
        "v_lshrrev_b32 v9, 2, v3\n\t"
        "v_lshl_add_u32 v15, v2, 3, v9\n\t"
        "v_lshlrev_b32 v15, 8, v15\n\t"
        "v_and_b32 v72, 1, v2\n\t"
        "v_lshl_add_u32 v9, v72, 2, v9\n\t"
        "s_lshl_b32 s70, s45, 1\n\t"
        "v_xor_b32 v9, s70, v9\n\t"
        "v_lshl_add_u32 v15, v9, 5, v15\n\t"
        "v_and_b32 v9, 3, v1\n\t"
        "v_lshl_add_u32 v15, v9, 3, v15\n\t"
        "s_lshl_b32 s70, s44, 13\n\t"
        "s_add_u32 s70, s70, 65536\n\t"
        "v_add_u32 v15, s70, v15\n\t"
        "v_xor_b32 v9, 32, v15\n\t"
        "s_waitcnt vmcnt(12)\n\t"
        "s_barrier\n\t"
        "s_add_u32 m0, s48, 49152\n\t"
        "s_add_u32 s70, s46, 3145728\n\t"
        "s_add_u32 s71, s46, 3177472\n\t"
        "buffer_load_dwordx4 v8, %[rw], s70 offen lds\n\t"
        "buffer_load_dwordx4 v8, %[rw], s71 offen offset:1024 lds\n\t"
        "ds_read_b64_tr_b16 v[128:129], v15 offset:0\n\t"
        "ds_read_b64_tr_b16 v[130:131], v15 offset:1024\n\t"
        "ds_read_b64_tr_b16 v[132:133], v9 offset:0\n\t"
        "ds_read_b64_tr_b16 v[134:135], v9 offset:1024\n\t"
        "s_waitcnt lgkmcnt(0)\n\t"
        "s_waitcnt vmcnt(4)\n\t"
        "s_barrier\n\t"
        "s_add_u32 m0, s48, 0\n\t"
        "s_add_u32 s70, s46, 262144\n\t"
        "s_add_u32 s71, s46, 293888\n\t"
        "buffer_load_dwordx4 v8, %[rw], s70 offen lds\n\t"
        "buffer_load_dwordx4 v8, %[rw], s71 offen offset:1024 lds\n\t"
        "ds_read_b64_tr_b16 v[160:161], v15 offset:16384\n\t"
        "ds_read_b64_tr_b16 v[162:163], v15 offset:17408\n\t"
        "ds_read_b64_tr_b16 v[164:165], v9 offset:16384\n\t"
        "ds_read_b64_tr_b16 v[166:167], v9 offset:17408\n\t"
        "ds_read_b128 v[16:19], v10 offset:0\n\t"
        "ds_read_b128 v[20:23], v10 offset:32768\n\t"
        "s_waitcnt lgkmcnt(0)\n\t"
        "v_mfma_f32_16x16x32_bf16 v[32:35], v[128:131], v[16:19], 0\n\t"
        "v_mfma_f32_16x16x32_bf16 v[36:39], v[132:135], v[16:19], 0\n\t"
        "v_mfma_f32_16x16x32_bf16 v[40:43], v[128:131], v[20:23], 0\n\t"
        "v_mfma_f32_16x16x32_bf16 v[44:47], v[132:135], v[20:23], 0\n\t"
        "s_waitcnt vmcnt(4)\n\t"
        "s_barrier\n\t"
        "s_add_u32 m0, s48, 16384\n\t"
        "s_add_u32 s70, s46, 1310720\n\t"
        "s_add_u32 s71, s46, 1342464\n\t"
        "buffer_load_dwordx4 v8, %[rw], s70 offen lds\n\t"
        "buffer_load_dwordx4 v8, %[rw], s71 offen offset:1024 lds\n\t"
        "ds_read_b64_tr_b16 v[192:193], v15 offset:32768\n\t"
        "ds_read_b64_tr_b16 v[194:195], v15 offset:33792\n\t"
        "ds_read_b64_tr_b16 v[196:197], v9 offset:32768\n\t"
        "ds_read_b64_tr_b16 v[198:199], v9 offset:33792\n\t"
        "ds_read_b128 v[16:19], v10 offset:4096\n\t"
        "ds_read_b128 v[20:23], v10 offset:36864\n\t"
        "s_waitcnt lgkmcnt(0)\n\t"
        "v_mfma_f32_16x16x32_bf16 v[32:35], v[160:163], v[16:19], v[32:35]\n\t"
        "v_mfma_f32_16x16x32_bf16 v[36:39], v[164:167], v[16:19], v[36:39]\n\t"
        "v_mfma_f32_16x16x32_bf16 v[40:43], v[160:163], v[20:23], v[40:43]\n\t"
        "v_mfma_f32_16x16x32_bf16 v[44:47], v[164:167], v[20:23], v[44:47]\n\t"
        "s_waitcnt vmcnt(4)\n\t"
        "s_barrier\n\t"
        "s_add_u32 m0, s48, 32768\n\t"
        "s_add_u32 s70, s46, 2359296\n\t"
        "s_add_u32 s71, s46, 2391040\n\t"
        "buffer_load_dwordx4 v8, %[rw], s70 offen lds\n\t"
        "buffer_load_dwordx4 v8, %[rw], s71 offen offset:1024 lds\n\t"
        "ds_read_b64_tr_b16 v[224:225], v15 offset:49152\n\t"
        "ds_read_b64_tr_b16 v[226:227], v15 offset:50176\n\t"
        "ds_read_b64_tr_b16 v[228:229], v9 offset:49152\n\t"
        "ds_read_b64_tr_b16 v[230:231], v9 offset:50176\n\t"
        "ds_read_b128 v[16:19], v10 offset:8192\n\t"
        "ds_read_b128 v[20:23], v10 offset:40960\n\t"
        "s_waitcnt lgkmcnt(0)\n\t"
        "v_mfma_f32_16x16x32_bf16 v[32:35], v[192:195], v[16:19], v[32:35]\n\t"
        "v_mfma_f32_16x16x32_bf16 v[36:39], v[196:199], v[16:19], v[36:39]\n\t"
        "v_mfma_f32_16x16x32_bf16 v[40:43], v[192:195], v[20:23], v[40:43]\n\t"
        "v_mfma_f32_16x16x32_bf16 v[44:47], v[196:199], v[20:23], v[44:47]\n\t"
        "s_waitcnt vmcnt(4)\n\t"
        "s_barrier\n\t"
        "s_add_u32 m0, s48, 49152\n\t"
        "s_add_u32 s70, s46, 3407872\n\t"
        "s_add_u32 s71, s46, 3439616\n\t"
        "buffer_load_dwordx4 v8, %[rw], s70 offen lds\n\t"
        "buffer_load_dwordx4 v8, %[rw], s71 offen offset:1024 lds\n\t"
        "ds_read_b64_tr_b16 v[136:137], v15 offset:0\n\t"
        "ds_read_b64_tr_b16 v[138:139], v15 offset:1024\n\t"
        "ds_read_b64_tr_b16 v[140:141], v9 offset:0\n\t"
        "ds_read_b64_tr_b16 v[142:143], v9 offset:1024\n\t"
        "ds_read_b128 v[16:19], v10 offset:12288\n\t"
        "ds_read_b128 v[20:23], v10 offset:45056\n\t"
        "s_waitcnt lgkmcnt(0)\n\t"
        "v_mfma_f32_16x16x32_bf16 v[32:35], v[224:227], v[16:19], v[32:35]\n\t"
        "v_mfma_f32_16x16x32_bf16 v[36:39], v[228:231], v[16:19], v[36:39]\n\t"
        "v_mfma_f32_16x16x32_bf16 v[40:43], v[224:227], v[20:23], v[40:43]\n\t"
        "v_mfma_f32_16x16x32_bf16 v[44:47], v[228:231], v[20:23], v[44:47]\n\t"
        "s_waitcnt vmcnt(4)\n\t"
        "s_barrier\n\t"
        "s_add_u32 m0, s48, 0\n\t"
        "s_add_u32 s70, s46, 524288\n\t"
        "s_add_u32 s71, s46, 556032\n\t"
        "buffer_load_dwordx4 v8, %[rw], s70 offen lds\n\t"
        "buffer_load_dwordx4 v8, %[rw], s71 offen offset:1024 lds\n\t"
        "ds_read_b64_tr_b16 v[168:169], v15 offset:16384\n\t"
        "ds_read_b64_tr_b16 v[170:171], v15 offset:17408\n\t"
        "ds_read_b64_tr_b16 v[172:173], v9 offset:16384\n\t"
        "ds_read_b64_tr_b16 v[174:175], v9 offset:17408\n\t"
        "ds_read_b128 v[16:19], v11 offset:0\n\t"
        "ds_read_b128 v[20:23], v11 offset:32768\n\t"
        "s_waitcnt lgkmcnt(0)\n\t"
        "v_mfma_f32_16x16x32_bf16 v[32:35], v[136:139], v[16:19], v[32:35]\n\t"
        "v_mfma_f32_16x16x32_bf16 v[36:39], v[140:143], v[16:19], v[36:39]\n\t"
        "v_mfma_f32_16x16x32_bf16 v[40:43], v[136:139], v[20:23], v[40:43]\n\t"
        "v_mfma_f32_16x16x32_bf16 v[44:47], v[140:143], v[20:23], v[44:47]\n\t"
        "s_waitcnt vmcnt(4)\n\t"
        "s_barrier\n\t"
        "s_add_u32 m0, s48, 16384\n\t"
        "s_add_u32 s70, s46, 1572864\n\t"
        "s_add_u32 s71, s46, 1604608\n\t"
        "buffer_load_dwordx4 v8, %[rw], s70 offen lds\n\t"
        "buffer_load_dwordx4 v8, %[rw], s71 offen offset:1024 lds\n\t"
        "ds_read_b64_tr_b16 v[200:201], v15 offset:32768\n\t"
        "ds_read_b64_tr_b16 v[202:203], v15 offset:33792\n\t"
        "ds_read_b64_tr_b16 v[204:205], v9 offset:32768\n\t"
        "ds_read_b64_tr_b16 v[206:207], v9 offset:33792\n\t"
        "ds_read_b128 v[16:19], v11 offset:4096\n\t"
        "ds_read_b128 v[20:23], v11 offset:36864\n\t"
        "s_waitcnt lgkmcnt(0)\n\t"
        "v_mfma_f32_16x16x32_bf16 v[32:35], v[168:171], v[16:19], v[32:35]\n\t"
        "v_mfma_f32_16x16x32_bf16 v[36:39], v[172:175], v[16:19], v[36:39]\n\t"
        "v_mfma_f32_16x16x32_bf16 v[40:43], v[168:171], v[20:23], v[40:43]\n\t"
        "v_mfma_f32_16x16x32_bf16 v[44:47], v[172:175], v[20:23], v[44:47]\n\t"
        "s_waitcnt vmcnt(4)\n\t"
        "s_barrier\n\t"
        "s_add_u32 m0, s48, 32768\n\t"
        "s_add_u32 s70, s46, 2621440\n\t"
        "s_add_u32 s71, s46, 2653184\n\t"
        "buffer_load_dwordx4 v8, %[rw], s70 offen lds\n\t"
        "buffer_load_dwordx4 v8, %[rw], s71 offen offset:1024 lds\n\t"
        "ds_read_b64_tr_b16 v[232:233], v15 offset:49152\n\t"
        "ds_read_b64_tr_b16 v[234:235], v15 offset:50176\n\t"
        "ds_read_b64_tr_b16 v[236:237], v9 offset:49152\n\t"
        "ds_read_b64_tr_b16 v[238:239], v9 offset:50176\n\t"
        "ds_read_b128 v[16:19], v11 offset:8192\n\t"
        "ds_read_b128 v[20:23], v11 offset:40960\n\t"
        "s_waitcnt lgkmcnt(0)\n\t"
        "v_mfma_f32_16x16x32_bf16 v[32:35], v[200:203], v[16:19], v[32:35]\n\t"
        "v_mfma_f32_16x16x32_bf16 v[36:39], v[204:207], v[16:19], v[36:39]\n\t"
        "v_mfma_f32_16x16x32_bf16 v[40:43], v[200:203], v[20:23], v[40:43]\n\t"
        "v_mfma_f32_16x16x32_bf16 v[44:47], v[204:207], v[20:23], v[44:47]\n\t"
        "s_waitcnt vmcnt(4)\n\t"
        "s_barrier\n\t"
        "s_add_u32 m0, s48, 49152\n\t"
        "s_add_u32 s70, s46, 3670016\n\t"
        "s_add_u32 s71, s46, 3701760\n\t"
        "buffer_load_dwordx4 v8, %[rw], s70 offen lds\n\t"
        "buffer_load_dwordx4 v8, %[rw], s71 offen offset:1024 lds\n\t"
        "ds_read_b64_tr_b16 v[144:145], v15 offset:0\n\t"
        "ds_read_b64_tr_b16 v[146:147], v15 offset:1024\n\t"
        "ds_read_b64_tr_b16 v[148:149], v9 offset:0\n\t"
        "ds_read_b64_tr_b16 v[150:151], v9 offset:1024\n\t"
        "ds_read_b128 v[16:19], v11 offset:12288\n\t"
        "ds_read_b128 v[20:23], v11 offset:45056\n\t"
        "s_waitcnt lgkmcnt(0)\n\t"
        "v_mfma_f32_16x16x32_bf16 v[32:35], v[232:235], v[16:19], v[32:35]\n\t"
        "v_mfma_f32_16x16x32_bf16 v[36:39], v[236:239], v[16:19], v[36:39]\n\t"
        "v_mfma_f32_16x16x32_bf16 v[40:43], v[232:235], v[20:23], v[40:43]\n\t"
        "v_mfma_f32_16x16x32_bf16 v[44:47], v[236:239], v[20:23], v[44:47]\n\t"
        "s_waitcnt vmcnt(4)\n\t"
        "s_barrier\n\t"
        "s_add_u32 m0, s48, 0\n\t"
        "s_add_u32 s70, s46, 786432\n\t"
        "s_add_u32 s71, s46, 818176\n\t"
        "buffer_load_dwordx4 v8, %[rw], s70 offen lds\n\t"
        "buffer_load_dwordx4 v8, %[rw], s71 offen offset:1024 lds\n\t"
        "ds_read_b64_tr_b16 v[176:177], v15 offset:16384\n\t"
        "ds_read_b64_tr_b16 v[178:179], v15 offset:17408\n\t"
        "ds_read_b64_tr_b16 v[180:181], v9 offset:16384\n\t"
        "ds_read_b64_tr_b16 v[182:183], v9 offset:17408\n\t"
        "ds_read_b128 v[16:19], v10 offset:2048\n\t"
        "ds_read_b128 v[20:23], v10 offset:34816\n\t"
        "s_waitcnt lgkmcnt(0)\n\t"
        "v_mfma_f32_16x16x32_bf16 v[32:35], v[144:147], v[16:19], v[32:35]\n\t"
        "v_mfma_f32_16x16x32_bf16 v[36:39], v[148:151], v[16:19], v[36:39]\n\t"
        "v_mfma_f32_16x16x32_bf16 v[40:43], v[144:147], v[20:23], v[40:43]\n\t"
        "v_mfma_f32_16x16x32_bf16 v[44:47], v[148:151], v[20:23], v[44:47]\n\t"
        "s_waitcnt vmcnt(4)\n\t"
        "s_barrier\n\t"
        "s_add_u32 m0, s48, 16384\n\t"
        "s_add_u32 s70, s46, 1835008\n\t"
        "s_add_u32 s71, s46, 1866752\n\t"
        "buffer_load_dwordx4 v8, %[rw], s70 offen lds\n\t"
        "buffer_load_dwordx4 v8, %[rw], s71 offen offset:1024 lds\n\t"
        "ds_read_b64_tr_b16 v[208:209], v15 offset:32768\n\t"
        "ds_read_b64_tr_b16 v[210:211], v15 offset:33792\n\t"
        "ds_read_b64_tr_b16 v[212:213], v9 offset:32768\n\t"
        "ds_read_b64_tr_b16 v[214:215], v9 offset:33792\n\t"
        "ds_read_b128 v[16:19], v10 offset:6144\n\t"
        "ds_read_b128 v[20:23], v10 offset:38912\n\t"
        "s_waitcnt lgkmcnt(0)\n\t"
        "v_mfma_f32_16x16x32_bf16 v[32:35], v[176:179], v[16:19], v[32:35]\n\t"
        "v_mfma_f32_16x16x32_bf16 v[36:39], v[180:183], v[16:19], v[36:39]\n\t"
        "v_mfma_f32_16x16x32_bf16 v[40:43], v[176:179], v[20:23], v[40:43]\n\t"
        "v_mfma_f32_16x16x32_bf16 v[44:47], v[180:183], v[20:23], v[44:47]\n\t"
        "s_waitcnt vmcnt(4)\n\t"
        "s_barrier\n\t"
        "s_add_u32 m0, s48, 32768\n\t"
        "s_add_u32 s70, s46, 2883584\n\t"
        "s_add_u32 s71, s46, 2915328\n\t"
        "buffer_load_dwordx4 v8, %[rw], s70 offen lds\n\t"
        "buffer_load_dwordx4 v8, %[rw], s71 offen offset:1024 lds\n\t"
        "ds_read_b64_tr_b16 v[240:241], v15 offset:49152\n\t"
        "ds_read_b64_tr_b16 v[242:243], v15 offset:50176\n\t"
        "ds_read_b64_tr_b16 v[244:245], v9 offset:49152\n\t"
        "ds_read_b64_tr_b16 v[246:247], v9 offset:50176\n\t"
        "ds_read_b128 v[16:19], v10 offset:10240\n\t"
        "ds_read_b128 v[20:23], v10 offset:43008\n\t"
        "s_waitcnt lgkmcnt(0)\n\t"
        "v_mfma_f32_16x16x32_bf16 v[32:35], v[208:211], v[16:19], v[32:35]\n\t"
        "v_mfma_f32_16x16x32_bf16 v[36:39], v[212:215], v[16:19], v[36:39]\n\t"
        "v_mfma_f32_16x16x32_bf16 v[40:43], v[208:211], v[20:23], v[40:43]\n\t"
        "v_mfma_f32_16x16x32_bf16 v[44:47], v[212:215], v[20:23], v[44:47]\n\t"
        "s_waitcnt vmcnt(4)\n\t"
        "s_barrier\n\t"
        "s_add_u32 m0, s48, 49152\n\t"
        "s_add_u32 s70, s46, 3932160\n\t"
        "s_add_u32 s71, s46, 3963904\n\t"
        "buffer_load_dwordx4 v8, %[rw], s70 offen lds\n\t"
        "buffer_load_dwordx4 v8, %[rw], s71 offen offset:1024 lds\n\t"
        "ds_read_b64_tr_b16 v[152:153], v15 offset:0\n\t"
        "ds_read_b64_tr_b16 v[154:155], v15 offset:1024\n\t"
        "ds_read_b64_tr_b16 v[156:157], v9 offset:0\n\t"
        "ds_read_b64_tr_b16 v[158:159], v9 offset:1024\n\t"
        "ds_read_b128 v[16:19], v10 offset:14336\n\t"
        "ds_read_b128 v[20:23], v10 offset:47104\n\t"
        "s_waitcnt lgkmcnt(0)\n\t"
        "v_mfma_f32_16x16x32_bf16 v[32:35], v[240:243], v[16:19], v[32:35]\n\t"
        "v_mfma_f32_16x16x32_bf16 v[36:39], v[244:247], v[16:19], v[36:39]\n\t"
        "v_mfma_f32_16x16x32_bf16 v[40:43], v[240:243], v[20:23], v[40:43]\n\t"
        "v_mfma_f32_16x16x32_bf16 v[44:47], v[244:247], v[20:23], v[44:47]\n\t"
        "s_waitcnt vmcnt(4)\n\t"
        "s_barrier\n\t"
        "ds_read_b64_tr_b16 v[184:185], v15 offset:16384\n\t"
        "ds_read_b64_tr_b16 v[186:187], v15 offset:17408\n\t"
        "ds_read_b64_tr_b16 v[188:189], v9 offset:16384\n\t"
        "ds_read_b64_tr_b16 v[190:191], v9 offset:17408\n\t"
        "ds_read_b128 v[16:19], v11 offset:2048\n\t"
        "ds_read_b128 v[20:23], v11 offset:34816\n\t"
        "s_waitcnt lgkmcnt(0)\n\t"
        "v_mfma_f32_16x16x32_bf16 v[32:35], v[152:155], v[16:19], v[32:35]\n\t"
        "v_mfma_f32_16x16x32_bf16 v[36:39], v[156:159], v[16:19], v[36:39]\n\t"
        "v_mfma_f32_16x16x32_bf16 v[40:43], v[152:155], v[20:23], v[40:43]\n\t"
        "v_mfma_f32_16x16x32_bf16 v[44:47], v[156:159], v[20:23], v[44:47]\n\t"
        "s_waitcnt vmcnt(2)\n\t"
        "s_barrier\n\t"
        "ds_read_b64_tr_b16 v[216:217], v15 offset:32768\n\t"
        "ds_read_b64_tr_b16 v[218:219], v15 offset:33792\n\t"
        "ds_read_b64_tr_b16 v[220:221], v9 offset:32768\n\t"
        "ds_read_b64_tr_b16 v[222:223], v9 offset:33792\n\t"
        "ds_read_b128 v[16:19], v11 offset:6144\n\t"
        "ds_read_b128 v[20:23], v11 offset:38912\n\t"
        "s_waitcnt lgkmcnt(0)\n\t"
        "v_mfma_f32_16x16x32_bf16 v[32:35], v[184:187], v[16:19], v[32:35]\n\t"
        "v_mfma_f32_16x16x32_bf16 v[36:39], v[188:191], v[16:19], v[36:39]\n\t"
        "v_mfma_f32_16x16x32_bf16 v[40:43], v[184:187], v[20:23], v[40:43]\n\t"
        "v_mfma_f32_16x16x32_bf16 v[44:47], v[188:191], v[20:23], v[44:47]\n\t"
        "s_waitcnt vmcnt(0)\n\t"
        "s_barrier\n\t"
        "ds_read_b64_tr_b16 v[248:249], v15 offset:49152\n\t"
        "ds_read_b64_tr_b16 v[250:251], v15 offset:50176\n\t"
        "ds_read_b64_tr_b16 v[252:253], v9 offset:49152\n\t"
        "ds_read_b64_tr_b16 v[254:255], v9 offset:50176\n\t"
        "ds_read_b128 v[16:19], v11 offset:10240\n\t"
        "ds_read_b128 v[20:23], v11 offset:43008\n\t"
        "s_waitcnt lgkmcnt(0)\n\t"
        "v_mfma_f32_16x16x32_bf16 v[32:35], v[216:219], v[16:19], v[32:35]\n\t"
        "v_mfma_f32_16x16x32_bf16 v[36:39], v[220:223], v[16:19], v[36:39]\n\t"
        "v_mfma_f32_16x16x32_bf16 v[40:43], v[216:219], v[20:23], v[40:43]\n\t"
        "v_mfma_f32_16x16x32_bf16 v[44:47], v[220:223], v[20:23], v[44:47]\n\t"
        "ds_read_b128 v[16:19], v11 offset:14336\n\t"
        "ds_read_b128 v[20:23], v11 offset:47104\n\t"
        "s_waitcnt lgkmcnt(0)\n\t"
        "v_mfma_f32_16x16x32_bf16 v[32:35], v[248:251], v[16:19], v[32:35]\n\t"
        "v_mfma_f32_16x16x32_bf16 v[36:39], v[252:255], v[16:19], v[36:39]\n\t"
        "v_mfma_f32_16x16x32_bf16 v[40:43], v[248:251], v[20:23], v[40:43]\n\t"
        "v_mfma_f32_16x16x32_bf16 v[44:47], v[252:255], v[20:23], v[44:47]\n\t"
        "s_cmp_eq_u32 %[w], 7\n\t"
        "s_cbranch_scc0 .Lx_nb_%=\n\t"
        "v_lshrrev_b32 v72, 1, v0\n\t"
        "v_mov_b32 v73, 0\n\t"
        "s_mov_b32 s70, 100000\n\t"
        ".Lx_bw_%=:\n\t"
        "global_load_dwordx2 v[74:75], v72, %[bt] sc1\n\t"
        "global_load_dwordx2 v[76:77], v73, %[bt] offset:1152 sc1\n\t"
        "s_waitcnt vmcnt(0)\n\t"
        "v_readfirstlane_b32 s54, v76\n\t"
        "v_readfirstlane_b32 s55, v77\n\t"
        "v_or_b32 v76, 1, v74\n\t"
        "s_add_u32 s54, s54, 1\n\t"
        "s_addc_u32 s55, s55, 0\n\t"
        "s_lshl_b64 s[72:73], s[54:55], 1\n\t"
        "s_or_b32 s72, s72, 1\n\t"
        "v_cmp_eq_u32 vcc, s72, v76\n\t"
        "v_cmp_eq_u32 s[68:69], s73, v75\n\t"
        "v_and_b32 v74, 1, v74\n\t"
        "s_and_b64 vcc, vcc, s[68:69]\n\t"
        "s_cmp_eq_u64 vcc, -1\n\t"
        "s_cbranch_scc1 .Lx_bk_%=\n\t"
        "s_sleep 16\n\t"
        "s_sub_u32 s70, s70, 1\n\t"
        "s_cbranch_scc0 .Lx_bw_%=\n\t"
        ".Lx_bk_%=:\n\t"
        "v_cmp_eq_u32 vcc, 1, v74\n\t"
        "s_lshl_b64 s[54:55], s[54:55], 8\n\t"
        "s_nop 1\n\t"
        "s_bcnt1_i32_b64 s70, vcc\n\t"
        "s_cmp_gt_u32 s70, 32\n\t"
        "s_cselect_b32 s70, 128, 0\n\t"
        "s_or_b32 s54, s54, s70\n\t"
        "v_mov_b32 v72, 147456\n\t"
        "v_mov_b32 v74, s54\n\t"
        "v_mov_b32 v75, s55\n\t"
        "ds_write_b64 v72, v[74:75]\n\t"
        ".Lx_nb_%=:\n\t"
        "s_cmp_eq_u32 s44, 0\n\t"
        "s_cbranch_scc0 .Lx_k1_%=\n\t"
        "s_nop 7\n\t"
        "ds_write_b128 v14, v[40:43] offset:8192\n\t"
        "ds_write_b128 v14, v[44:47] offset:9216\n\t"
        "s_waitcnt lgkmcnt(0)\n\t"
        "s_barrier\n\t"
        "v_mov_b32 v72, 147456\n\t"
        "ds_read_b64 v[76:77], v72\n\t"
        "ds_read_b128 v[64:67], v14 offset:0\n\t"
        "ds_read_b128 v[68:71], v14 offset:1024\n\t"
        "s_add_u32 m0, s47, 65536\n\t"
        "s_nop 0\n\t"
        "buffer_load_dwordx4 v4, %[rx], s49 offen offset:0 lds\n\t"
        "buffer_load_dwordx4 v5, %[rx], s49 offen offset:1024 lds\n\t"
        "buffer_load_dwordx4 v6, %[rx], s49 offen offset:2048 lds\n\t"
        "buffer_load_dwordx4 v7, %[rx], s49 offen offset:3072 lds\n\t"
        "s_add_u32 s49, s49, 32768\n\t"
        "s_add_u32 m0, s47, 98304\n\t"
        "s_nop 0\n\t"
        "buffer_load_dwordx4 v4, %[rx], s49 offen offset:0 lds\n\t"
        "buffer_load_dwordx4 v5, %[rx], s49 offen offset:1024 lds\n\t"
        "buffer_load_dwordx4 v6, %[rx], s49 offen offset:2048 lds\n\t"
        "buffer_load_dwordx4 v7, %[rx], s49 offen offset:3072 lds\n\t"
        "s_add_u32 s49, s49, 32768\n\t"
        "s_waitcnt lgkmcnt(0)\n\t"
        "v_readfirstlane_b32 s54, v76\n\t"
        "v_lshrrev_b32 v78, 4, v1\n\t"
        "v_lshlrev_b32 v78, 24, v78\n\t"
        "v_lshl_add_u32 v78, v3, 3, v78\n\t"
        "s_cmp_eq_u32 %[w], 0\n\t"
        "s_cselect_b32 s70, 0, 0x40000000\n\t"
        "v_add_u32 v78, s70, v78\n\t"
        "s_and_b32 s70, s54, 128\n\t"
        "s_lshl_b32 s70, s70, 6\n\t"
        "s_xor_b32 s50, %[co], s70\n\t"
        "s_lshl_b32 s70, %[w], 10\n\t"
        "s_add_u32 s50, s50, s70\n\t"
        "s_mov_b32 s51, 0\n\t"
        "v_add_f32 v64, v64, v32\n\t"
        "v_add_f32 v65, v65, v33\n\t"
        "v_add_f32 v66, v66, v34\n\t"
        "v_add_f32 v67, v67, v35\n\t"
        "v_add_f32 v68, v68, v36\n\t"
        "v_add_f32 v69, v69, v37\n\t"
        "v_add_f32 v70, v70, v38\n\t"
        "v_add_f32 v71, v71, v39\n\t"
        "v_cvt_pk_bf16_f32 v72, v64, v65\n\t"
        "v_cvt_pk_bf16_f32 v73, v68, v69\n\t"
        "v_cvt_pk_bf16_f32 v74, v66, v67\n\t"
        "v_cvt_pk_bf16_f32 v75, v70, v71\n\t"
        "s_mov_b64 exec, s[56:57]\n\t"
        "v_swap_b32 v73, v74\n\t"
        "s_nop 1\n\t"
        "v_permlane16_swap_b32 v72, v74\n\t"
        "v_permlane16_swap_b32 v73, v75\n\t"
        "s_mov_b64 exec, -1\n\t"
        "buffer_store_dwordx4 v[72:75], v79, %[rs], s50 offen sc1\n\t"
        "s_waitcnt vmcnt(0)\n\t"
        "s_barrier\n\t"
        "s_add_u32 s54, s54, 1\n\t"
        "v_mov_b32 v76, s54\n\t"
        "s_and_b32 s71, s54, 127\n\t"
        "s_lshl_b32 s71, s71, 7\n\t"
        "buffer_store_dwordx2 v[76:77], v78, %[rf], s71 offen sc1\n\t"
        "s_add_u32 m0, s47, 0\n\t"
        "s_nop 0\n\t"
        "buffer_load_dwordx4 v4, %[rx], s49 offen offset:0 lds\n\t"
        "buffer_load_dwordx4 v5, %[rx], s49 offen offset:1024 lds\n\t"
        "buffer_load_dwordx4 v6, %[rx], s49 offen offset:2048 lds\n\t"
        "buffer_load_dwordx4 v7, %[rx], s49 offen offset:3072 lds\n\t"
        "s_add_u32 s49, s49, 32768\n\t"
        "ds_read_b128 v[16:19], v12 offset:0\n\t"
        "ds_read_b128 v[20:23], v13 offset:0\n\t"
        "ds_read_b128 v[24:27], v12 offset:2048\n\t"
        "s_add_u32 m0, s47, 32768\n\t"
        "s_nop 0\n\t"
        "buffer_load_dwordx4 v4, %[rx], s49 offen offset:0 lds\n\t"
        "buffer_load_dwordx4 v5, %[rx], s49 offen offset:1024 lds\n\t"
        "buffer_load_dwordx4 v6, %[rx], s49 offen offset:2048 lds\n\t"
        "buffer_load_dwordx4 v7, %[rx], s49 offen offset:3072 lds\n\t"
        "s_add_u32 s49, s49, 32768\n\t"
        "ds_read_b128 v[28:31], v13 offset:2048\n\t"
        "s_waitcnt lgkmcnt(3)\n\t"
        "v_mfma_f32_16x16x32_bf16 v[48:51], v[128:131], v[16:19], 0\n\t"
        "v_mfma_f32_16x16x32_bf16 v[52:55], v[132:135], v[16:19], 0\n\t"
        "ds_read_b128 v[16:19], v12 offset:4096\n\t"
        "s_waitcnt lgkmcnt(3)\n\t"
        "v_mfma_f32_16x16x32_bf16 v[48:51], v[136:139], v[20:23], v[48:51]\n\t"
        "v_mfma_f32_16x16x32_bf16 v[52:55], v[140:143], v[20:23], v[52:55]\n\t"
        "ds_read_b128 v[20:23], v13 offset:4096\n\t"
        "s_waitcnt lgkmcnt(3)\n\t"
        "v_mfma_f32_16x16x32_bf16 v[48:51], v[144:147], v[24:27], v[48:51]\n\t"
        "v_mfma_f32_16x16x32_bf16 v[52:55], v[148:151], v[24:27], v[52:55]\n\t"
        "ds_read_b128 v[24:27], v12 offset:6144\n\t"
        "s_waitcnt lgkmcnt(3)\n\t"
        "v_mfma_f32_16x16x32_bf16 v[48:51], v[152:155], v[28:31], v[48:51]\n\t"
        "v_mfma_f32_16x16x32_bf16 v[52:55], v[156:159], v[28:31], v[52:55]\n\t"
        "ds_read_b128 v[28:31], v13 offset:6144\n\t"
        "s_waitcnt lgkmcnt(3)\n\t"
        "v_mfma_f32_16x16x32_bf16 v[48:51], v[160:163], v[16:19], v[48:51]\n\t"
        "v_mfma_f32_16x16x32_bf16 v[52:55], v[164:167], v[16:19], v[52:55]\n\t"
        "ds_read_b128 v[16:19], v12 offset:8192\n\t"
        "s_waitcnt lgkmcnt(3)\n\t"
        "v_mfma_f32_16x16x32_bf16 v[48:51], v[168:171], v[20:23], v[48:51]\n\t"
        "v_mfma_f32_16x16x32_bf16 v[52:55], v[172:175], v[20:23], v[52:55]\n\t"
        "ds_read_b128 v[20:23], v13 offset:8192\n\t"
        "s_waitcnt lgkmcnt(3)\n\t"
        "v_mfma_f32_16x16x32_bf16 v[48:51], v[176:179], v[24:27], v[48:51]\n\t"
        "v_mfma_f32_16x16x32_bf16 v[52:55], v[180:183], v[24:27], v[52:55]\n\t"
        "ds_read_b128 v[24:27], v12 offset:10240\n\t"
        "s_waitcnt lgkmcnt(3)\n\t"
        "v_mfma_f32_16x16x32_bf16 v[48:51], v[184:187], v[28:31], v[48:51]\n\t"
        "v_mfma_f32_16x16x32_bf16 v[52:55], v[188:191], v[28:31], v[52:55]\n\t"
        "ds_read_b128 v[28:31], v13 offset:10240\n\t"
        "s_waitcnt lgkmcnt(3)\n\t"
        "v_mfma_f32_16x16x32_bf16 v[48:51], v[192:195], v[16:19], v[48:51]\n\t"
        "v_mfma_f32_16x16x32_bf16 v[52:55], v[196:199], v[16:19], v[52:55]\n\t"
        "ds_read_b128 v[16:19], v12 offset:12288\n\t"
        "s_waitcnt lgkmcnt(3)\n\t"
        "v_mfma_f32_16x16x32_bf16 v[48:51], v[200:203], v[20:23], v[48:51]\n\t"
        "v_mfma_f32_16x16x32_bf16 v[52:55], v[204:207], v[20:23], v[52:55]\n\t"
        "ds_read_b128 v[20:23], v13 offset:12288\n\t"
        "s_waitcnt lgkmcnt(3)\n\t"
        "v_mfma_f32_16x16x32_bf16 v[48:51], v[208:211], v[24:27], v[48:51]\n\t"
        "v_mfma_f32_16x16x32_bf16 v[52:55], v[212:215], v[24:27], v[52:55]\n\t"
        "ds_read_b128 v[24:27], v12 offset:14336\n\t"
        "s_waitcnt lgkmcnt(3)\n\t"
        "v_mfma_f32_16x16x32_bf16 v[48:51], v[216:219], v[28:31], v[48:51]\n\t"
        "v_mfma_f32_16x16x32_bf16 v[52:55], v[220:223], v[28:31], v[52:55]\n\t"
        "ds_read_b128 v[28:31], v13 offset:14336\n\t"
        "s_waitcnt lgkmcnt(3)\n\t"
        "v_mfma_f32_16x16x32_bf16 v[48:51], v[224:227], v[16:19], v[48:51]\n\t"
        "v_mfma_f32_16x16x32_bf16 v[52:55], v[228:231], v[16:19], v[52:55]\n\t"
        "s_waitcnt lgkmcnt(2)\n\t"
        "v_mfma_f32_16x16x32_bf16 v[48:51], v[232:235], v[20:23], v[48:51]\n\t"
        "v_mfma_f32_16x16x32_bf16 v[52:55], v[236:239], v[20:23], v[52:55]\n\t"
        "s_waitcnt lgkmcnt(1)\n\t"
        "v_mfma_f32_16x16x32_bf16 v[48:51], v[240:243], v[24:27], v[48:51]\n\t"
        "v_mfma_f32_16x16x32_bf16 v[52:55], v[244:247], v[24:27], v[52:55]\n\t"
        "s_waitcnt lgkmcnt(0)\n\t"
        "v_mfma_f32_16x16x32_bf16 v[48:51], v[248:251], v[28:31], v[48:51]\n\t"
        "v_mfma_f32_16x16x32_bf16 v[52:55], v[252:255], v[28:31], v[52:55]\n\t"
        "s_waitcnt vmcnt(4) lgkmcnt(0)\n\t"
        "s_barrier\n\t"
        "ds_read_b128 v[16:19], v12 offset:32768\n\t"
        "ds_read_b128 v[20:23], v13 offset:32768\n\t"
        "ds_read_b128 v[24:27], v12 offset:34816\n\t"
        "ds_read_b128 v[64:67], v14 offset:0\n\t"
        "ds_read_b128 v[68:71], v14 offset:1024\n\t"
        "s_add_u32 s50, s50, 0x80000\n\t"
        "s_add_u32 s51, s51, 1\n\t"
        "s_and_b32 s70, s51, 3\n\t"
        "s_cselect_b32 s70, 0, 0x2000\n\t"
        "s_xor_b32 s50, s50, s70\n\t"
        "s_waitcnt lgkmcnt(0)\n\t"
        "v_add_f32 v64, v64, v48\n\t"
        "v_add_f32 v65, v65, v49\n\t"
        "v_add_f32 v66, v66, v50\n\t"
        "v_add_f32 v67, v67, v51\n\t"
        "v_add_f32 v68, v68, v52\n\t"
        "v_add_f32 v69, v69, v53\n\t"
        "v_add_f32 v70, v70, v54\n\t"
        "v_add_f32 v71, v71, v55\n\t"
        "v_cvt_pk_bf16_f32 v72, v64, v65\n\t"
        "v_cvt_pk_bf16_f32 v73, v68, v69\n\t"
        "v_cvt_pk_bf16_f32 v74, v66, v67\n\t"
        "v_cvt_pk_bf16_f32 v75, v70, v71\n\t"
        "s_mov_b64 exec, s[56:57]\n\t"
        "v_swap_b32 v73, v74\n\t"
        "s_nop 1\n\t"
        "v_permlane16_swap_b32 v72, v74\n\t"
        "v_permlane16_swap_b32 v73, v75\n\t"
        "s_mov_b64 exec, -1\n\t"
        "buffer_store_dwordx4 v[72:75], v79, %[rs], s50 offen sc1\n\t"
        "s_add_u32 m0, s47, 65536\n\t"
        "s_nop 0\n\t"
        "buffer_load_dwordx4 v4, %[rx], s49 offen offset:0 lds\n\t"
        "buffer_load_dwordx4 v5, %[rx], s49 offen offset:1024 lds\n\t"
        "buffer_load_dwordx4 v6, %[rx], s49 offen offset:2048 lds\n\t"
        "buffer_load_dwordx4 v7, %[rx], s49 offen offset:3072 lds\n\t"
        "s_add_u32 s49, s49, 32768\n\t"
        "ds_read_b128 v[28:31], v13 offset:34816\n\t"
        "v_mfma_f32_16x16x32_bf16 v[56:59], v[128:131], v[16:19], 0\n\t"
        "v_mfma_f32_16x16x32_bf16 v[60:63], v[132:135], v[16:19], 0\n\t"
        "ds_read_b128 v[16:19], v12 offset:36864\n\t"
        "v_mfma_f32_16x16x32_bf16 v[56:59], v[136:139], v[20:23], v[56:59]\n\t"
        "v_mfma_f32_16x16x32_bf16 v[60:63], v[140:143], v[20:23], v[60:63]\n\t"
        "ds_read_b128 v[20:23], v13 offset:36864\n\t"
        "v_mfma_f32_16x16x32_bf16 v[56:59], v[144:147], v[24:27], v[56:59]\n\t"
        "v_mfma_f32_16x16x32_bf16 v[60:63], v[148:151], v[24:27], v[60:63]\n\t"
        "ds_read_b128 v[24:27], v12 offset:38912\n\t"
        "s_waitcnt lgkmcnt(3)\n\t"
        "v_mfma_f32_16x16x32_bf16 v[56:59], v[152:155], v[28:31], v[56:59]\n\t"
        "v_mfma_f32_16x16x32_bf16 v[60:63], v[156:159], v[28:31], v[60:63]\n\t"
        "ds_read_b128 v[28:31], v13 offset:38912\n\t"
        "s_waitcnt lgkmcnt(3)\n\t"
        "v_mfma_f32_16x16x32_bf16 v[56:59], v[160:163], v[16:19], v[56:59]\n\t"
        "v_mfma_f32_16x16x32_bf16 v[60:63], v[164:167], v[16:19], v[60:63]\n\t"
        "ds_read_b128 v[16:19], v12 offset:40960\n\t"
        "s_waitcnt lgkmcnt(3)\n\t"
        "v_mfma_f32_16x16x32_bf16 v[56:59], v[168:171], v[20:23], v[56:59]\n\t"
        "v_mfma_f32_16x16x32_bf16 v[60:63], v[172:175], v[20:23], v[60:63]\n\t"
        "ds_read_b128 v[20:23], v13 offset:40960\n\t"
        "s_waitcnt lgkmcnt(3)\n\t"
        "v_mfma_f32_16x16x32_bf16 v[56:59], v[176:179], v[24:27], v[56:59]\n\t"
        "v_mfma_f32_16x16x32_bf16 v[60:63], v[180:183], v[24:27], v[60:63]\n\t"
        "ds_read_b128 v[24:27], v12 offset:43008\n\t"
        "s_waitcnt lgkmcnt(3)\n\t"
        "v_mfma_f32_16x16x32_bf16 v[56:59], v[184:187], v[28:31], v[56:59]\n\t"
        "v_mfma_f32_16x16x32_bf16 v[60:63], v[188:191], v[28:31], v[60:63]\n\t"
        "ds_read_b128 v[28:31], v13 offset:43008\n\t"
        "s_waitcnt lgkmcnt(3)\n\t"
        "v_mfma_f32_16x16x32_bf16 v[56:59], v[192:195], v[16:19], v[56:59]\n\t"
        "v_mfma_f32_16x16x32_bf16 v[60:63], v[196:199], v[16:19], v[60:63]\n\t"
        "ds_read_b128 v[16:19], v12 offset:45056\n\t"
        "s_waitcnt lgkmcnt(3)\n\t"
        "v_mfma_f32_16x16x32_bf16 v[56:59], v[200:203], v[20:23], v[56:59]\n\t"
        "v_mfma_f32_16x16x32_bf16 v[60:63], v[204:207], v[20:23], v[60:63]\n\t"
        "ds_read_b128 v[20:23], v13 offset:45056\n\t"
        "s_waitcnt lgkmcnt(3)\n\t"
        "v_mfma_f32_16x16x32_bf16 v[56:59], v[208:211], v[24:27], v[56:59]\n\t"
        "v_mfma_f32_16x16x32_bf16 v[60:63], v[212:215], v[24:27], v[60:63]\n\t"
        "ds_read_b128 v[24:27], v12 offset:47104\n\t"
        "s_waitcnt lgkmcnt(3)\n\t"
        "v_mfma_f32_16x16x32_bf16 v[56:59], v[216:219], v[28:31], v[56:59]\n\t"
        "v_mfma_f32_16x16x32_bf16 v[60:63], v[220:223], v[28:31], v[60:63]\n\t"
        "ds_read_b128 v[28:31], v13 offset:47104\n\t"
        "s_waitcnt lgkmcnt(3)\n\t"
        "v_mfma_f32_16x16x32_bf16 v[56:59], v[224:227], v[16:19], v[56:59]\n\t"
        "v_mfma_f32_16x16x32_bf16 v[60:63], v[228:231], v[16:19], v[60:63]\n\t"
        "s_waitcnt lgkmcnt(2)\n\t"
        "v_mfma_f32_16x16x32_bf16 v[56:59], v[232:235], v[20:23], v[56:59]\n\t"
        "v_mfma_f32_16x16x32_bf16 v[60:63], v[236:239], v[20:23], v[60:63]\n\t"
        "s_waitcnt lgkmcnt(1)\n\t"
        "v_mfma_f32_16x16x32_bf16 v[56:59], v[240:243], v[24:27], v[56:59]\n\t"
        "v_mfma_f32_16x16x32_bf16 v[60:63], v[244:247], v[24:27], v[60:63]\n\t"
        "s_waitcnt lgkmcnt(0)\n\t"
        "v_mfma_f32_16x16x32_bf16 v[56:59], v[248:251], v[28:31], v[56:59]\n\t"
        "v_mfma_f32_16x16x32_bf16 v[60:63], v[252:255], v[28:31], v[60:63]\n\t"
        "s_nop 7\n\t"
        "ds_write_b128 v14, v[56:59] offset:8192\n\t"
        "ds_write_b128 v14, v[60:63] offset:9216\n\t"
        "s_waitcnt vmcnt(4) lgkmcnt(0)\n\t"
        "s_barrier\n\t"
        "ds_read_b128 v[16:19], v10 offset:0\n\t"
        "ds_read_b128 v[20:23], v11 offset:0\n\t"
        "ds_read_b128 v[24:27], v10 offset:2048\n\t"
        "s_add_u32 m0, s47, 98304\n\t"
        "s_nop 0\n\t"
        "buffer_load_dwordx4 v4, %[rx], s49 offen offset:0 lds\n\t"
        "buffer_load_dwordx4 v5, %[rx], s49 offen offset:1024 lds\n\t"
        "buffer_load_dwordx4 v6, %[rx], s49 offen offset:2048 lds\n\t"
        "buffer_load_dwordx4 v7, %[rx], s49 offen offset:3072 lds\n\t"
        "s_add_u32 s49, s49, 32768\n\t"
        "ds_read_b128 v[28:31], v11 offset:2048\n\t"
        "s_waitcnt lgkmcnt(3)\n\t"
        "v_mfma_f32_16x16x32_bf16 v[32:35], v[128:131], v[16:19], 0\n\t"
        "v_mfma_f32_16x16x32_bf16 v[36:39], v[132:135], v[16:19], 0\n\t"
        "ds_read_b128 v[16:19], v10 offset:4096\n\t"
        "s_waitcnt lgkmcnt(3)\n\t"
        "v_mfma_f32_16x16x32_bf16 v[32:35], v[136:139], v[20:23], v[32:35]\n\t"
        "v_mfma_f32_16x16x32_bf16 v[36:39], v[140:143], v[20:23], v[36:39]\n\t"
        "ds_read_b128 v[20:23], v11 offset:4096\n\t"
        "s_waitcnt lgkmcnt(3)\n\t"
        "v_mfma_f32_16x16x32_bf16 v[32:35], v[144:147], v[24:27], v[32:35]\n\t"
        "v_mfma_f32_16x16x32_bf16 v[36:39], v[148:151], v[24:27], v[36:39]\n\t"
        "ds_read_b128 v[24:27], v10 offset:6144\n\t"
        "s_waitcnt lgkmcnt(3)\n\t"
        "v_mfma_f32_16x16x32_bf16 v[32:35], v[152:155], v[28:31], v[32:35]\n\t"
        "v_mfma_f32_16x16x32_bf16 v[36:39], v[156:159], v[28:31], v[36:39]\n\t"
        "ds_read_b128 v[28:31], v11 offset:6144\n\t"
        "s_waitcnt lgkmcnt(3)\n\t"
        "v_mfma_f32_16x16x32_bf16 v[32:35], v[160:163], v[16:19], v[32:35]\n\t"
        "v_mfma_f32_16x16x32_bf16 v[36:39], v[164:167], v[16:19], v[36:39]\n\t"
        "ds_read_b128 v[16:19], v10 offset:8192\n\t"
        "s_waitcnt lgkmcnt(3)\n\t"
        "v_mfma_f32_16x16x32_bf16 v[32:35], v[168:171], v[20:23], v[32:35]\n\t"
        "v_mfma_f32_16x16x32_bf16 v[36:39], v[172:175], v[20:23], v[36:39]\n\t"
        "ds_read_b128 v[20:23], v11 offset:8192\n\t"
        "s_waitcnt lgkmcnt(3)\n\t"
        "v_mfma_f32_16x16x32_bf16 v[32:35], v[176:179], v[24:27], v[32:35]\n\t"
        "v_mfma_f32_16x16x32_bf16 v[36:39], v[180:183], v[24:27], v[36:39]\n\t"
        "ds_read_b128 v[24:27], v10 offset:10240\n\t"
        "s_waitcnt lgkmcnt(3)\n\t"
        "v_mfma_f32_16x16x32_bf16 v[32:35], v[184:187], v[28:31], v[32:35]\n\t"
        "v_mfma_f32_16x16x32_bf16 v[36:39], v[188:191], v[28:31], v[36:39]\n\t"
        "ds_read_b128 v[28:31], v11 offset:10240\n\t"
        "s_waitcnt lgkmcnt(3)\n\t"
        "v_mfma_f32_16x16x32_bf16 v[32:35], v[192:195], v[16:19], v[32:35]\n\t"
        "v_mfma_f32_16x16x32_bf16 v[36:39], v[196:199], v[16:19], v[36:39]\n\t"
        "ds_read_b128 v[16:19], v10 offset:12288\n\t"
        "s_waitcnt lgkmcnt(3)\n\t"
        "v_mfma_f32_16x16x32_bf16 v[32:35], v[200:203], v[20:23], v[32:35]\n\t"
        "v_mfma_f32_16x16x32_bf16 v[36:39], v[204:207], v[20:23], v[36:39]\n\t"
        "ds_read_b128 v[20:23], v11 offset:12288\n\t"
        "s_waitcnt lgkmcnt(3)\n\t"
        "v_mfma_f32_16x16x32_bf16 v[32:35], v[208:211], v[24:27], v[32:35]\n\t"
        "v_mfma_f32_16x16x32_bf16 v[36:39], v[212:215], v[24:27], v[36:39]\n\t"
        "ds_read_b128 v[24:27], v10 offset:14336\n\t"
        "s_waitcnt lgkmcnt(3)\n\t"
        "v_mfma_f32_16x16x32_bf16 v[32:35], v[216:219], v[28:31], v[32:35]\n\t"
        "v_mfma_f32_16x16x32_bf16 v[36:39], v[220:223], v[28:31], v[36:39]\n\t"
        "ds_read_b128 v[28:31], v11 offset:14336\n\t"
        "s_waitcnt lgkmcnt(3)\n\t"
        "v_mfma_f32_16x16x32_bf16 v[32:35], v[224:227], v[16:19], v[32:35]\n\t"
        "v_mfma_f32_16x16x32_bf16 v[36:39], v[228:231], v[16:19], v[36:39]\n\t"
        "s_waitcnt lgkmcnt(2)\n\t"
        "v_mfma_f32_16x16x32_bf16 v[32:35], v[232:235], v[20:23], v[32:35]\n\t"
        "v_mfma_f32_16x16x32_bf16 v[36:39], v[236:239], v[20:23], v[36:39]\n\t"
        "s_waitcnt lgkmcnt(1)\n\t"
        "v_mfma_f32_16x16x32_bf16 v[32:35], v[240:243], v[24:27], v[32:35]\n\t"
        "v_mfma_f32_16x16x32_bf16 v[36:39], v[244:247], v[24:27], v[36:39]\n\t"
        "s_waitcnt lgkmcnt(0)\n\t"
        "v_mfma_f32_16x16x32_bf16 v[32:35], v[248:251], v[28:31], v[32:35]\n\t"
        "v_mfma_f32_16x16x32_bf16 v[36:39], v[252:255], v[28:31], v[36:39]\n\t"
        "s_waitcnt vmcnt(4) lgkmcnt(0)\n\t"
        "s_barrier\n\t"
        "s_add_u32 s54, s54, 1\n\t"
        "v_mov_b32 v76, s54\n\t"
        "s_and_b32 s71, s54, 127\n\t"
        "s_lshl_b32 s71, s71, 7\n\t"
        "buffer_store_dwordx2 v[76:77], v78, %[rf], s71 offen sc1\n\t"
        "ds_read_b128 v[16:19], v10 offset:32768\n\t"
        "ds_read_b128 v[20:23], v11 offset:32768\n\t"
        "ds_read_b128 v[24:27], v10 offset:34816\n\t"
        "ds_read_b128 v[64:67], v14 offset:0\n\t"
        "ds_read_b128 v[68:71], v14 offset:1024\n\t"
        "s_add_u32 s50, s50, 0x80000\n\t"
        "s_add_u32 s51, s51, 1\n\t"
        "s_and_b32 s70, s51, 3\n\t"
        "s_cselect_b32 s70, 0, 0x2000\n\t"
        "s_xor_b32 s50, s50, s70\n\t"
        "s_waitcnt lgkmcnt(0)\n\t"
        "v_add_f32 v64, v64, v32\n\t"
        "v_add_f32 v65, v65, v33\n\t"
        "v_add_f32 v66, v66, v34\n\t"
        "v_add_f32 v67, v67, v35\n\t"
        "v_add_f32 v68, v68, v36\n\t"
        "v_add_f32 v69, v69, v37\n\t"
        "v_add_f32 v70, v70, v38\n\t"
        "v_add_f32 v71, v71, v39\n\t"
        "v_cvt_pk_bf16_f32 v72, v64, v65\n\t"
        "v_cvt_pk_bf16_f32 v73, v68, v69\n\t"
        "v_cvt_pk_bf16_f32 v74, v66, v67\n\t"
        "v_cvt_pk_bf16_f32 v75, v70, v71\n\t"
        "s_mov_b64 exec, s[56:57]\n\t"
        "v_swap_b32 v73, v74\n\t"
        "s_nop 1\n\t"
        "v_permlane16_swap_b32 v72, v74\n\t"
        "v_permlane16_swap_b32 v73, v75\n\t"
        "s_mov_b64 exec, -1\n\t"
        "buffer_store_dwordx4 v[72:75], v79, %[rs], s50 offen sc1\n\t"
        "s_add_u32 m0, s47, 0\n\t"
        "s_nop 0\n\t"
        "buffer_load_dwordx4 v4, %[rx], s49 offen offset:0 lds\n\t"
        "buffer_load_dwordx4 v5, %[rx], s49 offen offset:1024 lds\n\t"
        "buffer_load_dwordx4 v6, %[rx], s49 offen offset:2048 lds\n\t"
        "buffer_load_dwordx4 v7, %[rx], s49 offen offset:3072 lds\n\t"
        "s_add_u32 s49, s49, 32768\n\t"
        "ds_read_b128 v[28:31], v11 offset:34816\n\t"
        "v_mfma_f32_16x16x32_bf16 v[40:43], v[128:131], v[16:19], 0\n\t"
        "v_mfma_f32_16x16x32_bf16 v[44:47], v[132:135], v[16:19], 0\n\t"
        "ds_read_b128 v[16:19], v10 offset:36864\n\t"
        "v_mfma_f32_16x16x32_bf16 v[40:43], v[136:139], v[20:23], v[40:43]\n\t"
        "v_mfma_f32_16x16x32_bf16 v[44:47], v[140:143], v[20:23], v[44:47]\n\t"
        "ds_read_b128 v[20:23], v11 offset:36864\n\t"
        "v_mfma_f32_16x16x32_bf16 v[40:43], v[144:147], v[24:27], v[40:43]\n\t"
        "v_mfma_f32_16x16x32_bf16 v[44:47], v[148:151], v[24:27], v[44:47]\n\t"
        "ds_read_b128 v[24:27], v10 offset:38912\n\t"
        "s_waitcnt lgkmcnt(3)\n\t"
        "v_mfma_f32_16x16x32_bf16 v[40:43], v[152:155], v[28:31], v[40:43]\n\t"
        "v_mfma_f32_16x16x32_bf16 v[44:47], v[156:159], v[28:31], v[44:47]\n\t"
        "ds_read_b128 v[28:31], v11 offset:38912\n\t"
        "s_waitcnt lgkmcnt(3)\n\t"
        "v_mfma_f32_16x16x32_bf16 v[40:43], v[160:163], v[16:19], v[40:43]\n\t"
        "v_mfma_f32_16x16x32_bf16 v[44:47], v[164:167], v[16:19], v[44:47]\n\t"
        "ds_read_b128 v[16:19], v10 offset:40960\n\t"
        "s_waitcnt lgkmcnt(3)\n\t"
        "v_mfma_f32_16x16x32_bf16 v[40:43], v[168:171], v[20:23], v[40:43]\n\t"
        "v_mfma_f32_16x16x32_bf16 v[44:47], v[172:175], v[20:23], v[44:47]\n\t"
        "ds_read_b128 v[20:23], v11 offset:40960\n\t"
        "s_waitcnt lgkmcnt(3)\n\t"
        "v_mfma_f32_16x16x32_bf16 v[40:43], v[176:179], v[24:27], v[40:43]\n\t"
        "v_mfma_f32_16x16x32_bf16 v[44:47], v[180:183], v[24:27], v[44:47]\n\t"
        "ds_read_b128 v[24:27], v10 offset:43008\n\t"
        "s_waitcnt lgkmcnt(3)\n\t"
        "v_mfma_f32_16x16x32_bf16 v[40:43], v[184:187], v[28:31], v[40:43]\n\t"
        "v_mfma_f32_16x16x32_bf16 v[44:47], v[188:191], v[28:31], v[44:47]\n\t"
        "ds_read_b128 v[28:31], v11 offset:43008\n\t"
        "s_waitcnt lgkmcnt(3)\n\t"
        "v_mfma_f32_16x16x32_bf16 v[40:43], v[192:195], v[16:19], v[40:43]\n\t"
        "v_mfma_f32_16x16x32_bf16 v[44:47], v[196:199], v[16:19], v[44:47]\n\t"
        "ds_read_b128 v[16:19], v10 offset:45056\n\t"
        "s_waitcnt lgkmcnt(3)\n\t"
        "v_mfma_f32_16x16x32_bf16 v[40:43], v[200:203], v[20:23], v[40:43]\n\t"
        "v_mfma_f32_16x16x32_bf16 v[44:47], v[204:207], v[20:23], v[44:47]\n\t"
        "ds_read_b128 v[20:23], v11 offset:45056\n\t"
        "s_waitcnt lgkmcnt(3)\n\t"
        "v_mfma_f32_16x16x32_bf16 v[40:43], v[208:211], v[24:27], v[40:43]\n\t"
        "v_mfma_f32_16x16x32_bf16 v[44:47], v[212:215], v[24:27], v[44:47]\n\t"
        "ds_read_b128 v[24:27], v10 offset:47104\n\t"
        "s_waitcnt lgkmcnt(3)\n\t"
        "v_mfma_f32_16x16x32_bf16 v[40:43], v[216:219], v[28:31], v[40:43]\n\t"
        "v_mfma_f32_16x16x32_bf16 v[44:47], v[220:223], v[28:31], v[44:47]\n\t"
        "ds_read_b128 v[28:31], v11 offset:47104\n\t"
        "s_waitcnt lgkmcnt(3)\n\t"
        "v_mfma_f32_16x16x32_bf16 v[40:43], v[224:227], v[16:19], v[40:43]\n\t"
        "v_mfma_f32_16x16x32_bf16 v[44:47], v[228:231], v[16:19], v[44:47]\n\t"
        "s_waitcnt lgkmcnt(2)\n\t"
        "v_mfma_f32_16x16x32_bf16 v[40:43], v[232:235], v[20:23], v[40:43]\n\t"
        "v_mfma_f32_16x16x32_bf16 v[44:47], v[236:239], v[20:23], v[44:47]\n\t"
        "s_waitcnt lgkmcnt(1)\n\t"
        "v_mfma_f32_16x16x32_bf16 v[40:43], v[240:243], v[24:27], v[40:43]\n\t"
        "v_mfma_f32_16x16x32_bf16 v[44:47], v[244:247], v[24:27], v[44:47]\n\t"
        "s_waitcnt lgkmcnt(0)\n\t"
        "v_mfma_f32_16x16x32_bf16 v[40:43], v[248:251], v[28:31], v[40:43]\n\t"
        "v_mfma_f32_16x16x32_bf16 v[44:47], v[252:255], v[28:31], v[44:47]\n\t"
        "s_nop 7\n\t"
        "ds_write_b128 v14, v[40:43] offset:8192\n\t"
        "ds_write_b128 v14, v[44:47] offset:9216\n\t"
        "s_waitcnt vmcnt(4) lgkmcnt(0)\n\t"
        "s_barrier\n\t"
        "ds_read_b128 v[16:19], v12 offset:0\n\t"
        "ds_read_b128 v[20:23], v13 offset:0\n\t"
        "ds_read_b128 v[24:27], v12 offset:2048\n\t"
        "s_add_u32 m0, s47, 32768\n\t"
        "s_nop 0\n\t"
        "buffer_load_dwordx4 v4, %[rx], s49 offen offset:0 lds\n\t"
        "buffer_load_dwordx4 v5, %[rx], s49 offen offset:1024 lds\n\t"
        "buffer_load_dwordx4 v6, %[rx], s49 offen offset:2048 lds\n\t"
        "buffer_load_dwordx4 v7, %[rx], s49 offen offset:3072 lds\n\t"
        "s_add_u32 s49, s49, 32768\n\t"
        "ds_read_b128 v[28:31], v13 offset:2048\n\t"
        "s_waitcnt lgkmcnt(3)\n\t"
        "v_mfma_f32_16x16x32_bf16 v[48:51], v[128:131], v[16:19], 0\n\t"
        "v_mfma_f32_16x16x32_bf16 v[52:55], v[132:135], v[16:19], 0\n\t"
        "ds_read_b128 v[16:19], v12 offset:4096\n\t"
        "s_waitcnt lgkmcnt(3)\n\t"
        "v_mfma_f32_16x16x32_bf16 v[48:51], v[136:139], v[20:23], v[48:51]\n\t"
        "v_mfma_f32_16x16x32_bf16 v[52:55], v[140:143], v[20:23], v[52:55]\n\t"
        "ds_read_b128 v[20:23], v13 offset:4096\n\t"
        "s_waitcnt lgkmcnt(3)\n\t"
        "v_mfma_f32_16x16x32_bf16 v[48:51], v[144:147], v[24:27], v[48:51]\n\t"
        "v_mfma_f32_16x16x32_bf16 v[52:55], v[148:151], v[24:27], v[52:55]\n\t"
        "ds_read_b128 v[24:27], v12 offset:6144\n\t"
        "s_waitcnt lgkmcnt(3)\n\t"
        "v_mfma_f32_16x16x32_bf16 v[48:51], v[152:155], v[28:31], v[48:51]\n\t"
        "v_mfma_f32_16x16x32_bf16 v[52:55], v[156:159], v[28:31], v[52:55]\n\t"
        "ds_read_b128 v[28:31], v13 offset:6144\n\t"
        "s_waitcnt lgkmcnt(3)\n\t"
        "v_mfma_f32_16x16x32_bf16 v[48:51], v[160:163], v[16:19], v[48:51]\n\t"
        "v_mfma_f32_16x16x32_bf16 v[52:55], v[164:167], v[16:19], v[52:55]\n\t"
        "ds_read_b128 v[16:19], v12 offset:8192\n\t"
        "s_waitcnt lgkmcnt(3)\n\t"
        "v_mfma_f32_16x16x32_bf16 v[48:51], v[168:171], v[20:23], v[48:51]\n\t"
        "v_mfma_f32_16x16x32_bf16 v[52:55], v[172:175], v[20:23], v[52:55]\n\t"
        "ds_read_b128 v[20:23], v13 offset:8192\n\t"
        "s_waitcnt lgkmcnt(3)\n\t"
        "v_mfma_f32_16x16x32_bf16 v[48:51], v[176:179], v[24:27], v[48:51]\n\t"
        "v_mfma_f32_16x16x32_bf16 v[52:55], v[180:183], v[24:27], v[52:55]\n\t"
        "ds_read_b128 v[24:27], v12 offset:10240\n\t"
        "s_waitcnt lgkmcnt(3)\n\t"
        "v_mfma_f32_16x16x32_bf16 v[48:51], v[184:187], v[28:31], v[48:51]\n\t"
        "v_mfma_f32_16x16x32_bf16 v[52:55], v[188:191], v[28:31], v[52:55]\n\t"
        "ds_read_b128 v[28:31], v13 offset:10240\n\t"
        "s_waitcnt lgkmcnt(3)\n\t"
        "v_mfma_f32_16x16x32_bf16 v[48:51], v[192:195], v[16:19], v[48:51]\n\t"
        "v_mfma_f32_16x16x32_bf16 v[52:55], v[196:199], v[16:19], v[52:55]\n\t"
        "ds_read_b128 v[16:19], v12 offset:12288\n\t"
        "s_waitcnt lgkmcnt(3)\n\t"
        "v_mfma_f32_16x16x32_bf16 v[48:51], v[200:203], v[20:23], v[48:51]\n\t"
        "v_mfma_f32_16x16x32_bf16 v[52:55], v[204:207], v[20:23], v[52:55]\n\t"
        "ds_read_b128 v[20:23], v13 offset:12288\n\t"
        "s_waitcnt lgkmcnt(3)\n\t"
        "v_mfma_f32_16x16x32_bf16 v[48:51], v[208:211], v[24:27], v[48:51]\n\t"
        "v_mfma_f32_16x16x32_bf16 v[52:55], v[212:215], v[24:27], v[52:55]\n\t"
        "ds_read_b128 v[24:27], v12 offset:14336\n\t"
        "s_waitcnt lgkmcnt(3)\n\t"
        "v_mfma_f32_16x16x32_bf16 v[48:51], v[216:219], v[28:31], v[48:51]\n\t"
        "v_mfma_f32_16x16x32_bf16 v[52:55], v[220:223], v[28:31], v[52:55]\n\t"
        "ds_read_b128 v[28:31], v13 offset:14336\n\t"
        "s_waitcnt lgkmcnt(3)\n\t"
        "v_mfma_f32_16x16x32_bf16 v[48:51], v[224:227], v[16:19], v[48:51]\n\t"
        "v_mfma_f32_16x16x32_bf16 v[52:55], v[228:231], v[16:19], v[52:55]\n\t"
        "s_waitcnt lgkmcnt(2)\n\t"
        "v_mfma_f32_16x16x32_bf16 v[48:51], v[232:235], v[20:23], v[48:51]\n\t"
        "v_mfma_f32_16x16x32_bf16 v[52:55], v[236:239], v[20:23], v[52:55]\n\t"
        "s_waitcnt lgkmcnt(1)\n\t"
        "v_mfma_f32_16x16x32_bf16 v[48:51], v[240:243], v[24:27], v[48:51]\n\t"
        "v_mfma_f32_16x16x32_bf16 v[52:55], v[244:247], v[24:27], v[52:55]\n\t"
        "s_waitcnt lgkmcnt(0)\n\t"
        "v_mfma_f32_16x16x32_bf16 v[48:51], v[248:251], v[28:31], v[48:51]\n\t"
        "v_mfma_f32_16x16x32_bf16 v[52:55], v[252:255], v[28:31], v[52:55]\n\t"
        "s_waitcnt vmcnt(4) lgkmcnt(0)\n\t"
        "s_barrier\n\t"
        "s_add_u32 s54, s54, 1\n\t"
        "v_mov_b32 v76, s54\n\t"
        "s_and_b32 s71, s54, 127\n\t"
        "s_lshl_b32 s71, s71, 7\n\t"
        "buffer_store_dwordx2 v[76:77], v78, %[rf], s71 offen sc1\n\t"
        "ds_read_b128 v[16:19], v12 offset:32768\n\t"
        "ds_read_b128 v[20:23], v13 offset:32768\n\t"
        "ds_read_b128 v[24:27], v12 offset:34816\n\t"
        "ds_read_b128 v[64:67], v14 offset:0\n\t"
        "ds_read_b128 v[68:71], v14 offset:1024\n\t"
        "s_add_u32 s50, s50, 0x80000\n\t"
        "s_add_u32 s51, s51, 1\n\t"
        "s_and_b32 s70, s51, 3\n\t"
        "s_cselect_b32 s70, 0, 0x2000\n\t"
        "s_xor_b32 s50, s50, s70\n\t"
        "s_waitcnt lgkmcnt(0)\n\t"
        "v_add_f32 v64, v64, v48\n\t"
        "v_add_f32 v65, v65, v49\n\t"
        "v_add_f32 v66, v66, v50\n\t"
        "v_add_f32 v67, v67, v51\n\t"
        "v_add_f32 v68, v68, v52\n\t"
        "v_add_f32 v69, v69, v53\n\t"
        "v_add_f32 v70, v70, v54\n\t"
        "v_add_f32 v71, v71, v55\n\t"
        "v_cvt_pk_bf16_f32 v72, v64, v65\n\t"
        "v_cvt_pk_bf16_f32 v73, v68, v69\n\t"
        "v_cvt_pk_bf16_f32 v74, v66, v67\n\t"
        "v_cvt_pk_bf16_f32 v75, v70, v71\n\t"
        "s_mov_b64 exec, s[56:57]\n\t"
        "v_swap_b32 v73, v74\n\t"
        "s_nop 1\n\t"
        "v_permlane16_swap_b32 v72, v74\n\t"
        "v_permlane16_swap_b32 v73, v75\n\t"
        "s_mov_b64 exec, -1\n\t"
        "buffer_store_dwordx4 v[72:75], v79, %[rs], s50 offen sc1\n\t"
        "s_add_u32 m0, s47, 65536\n\t"
        "s_nop 0\n\t"
        "buffer_load_dwordx4 v4, %[rx], s49 offen offset:0 lds\n\t"
        "buffer_load_dwordx4 v5, %[rx], s49 offen offset:1024 lds\n\t"
        "buffer_load_dwordx4 v6, %[rx], s49 offen offset:2048 lds\n\t"
        "buffer_load_dwordx4 v7, %[rx], s49 offen offset:3072 lds\n\t"
        "s_add_u32 s49, s49, 32768\n\t"
        "ds_read_b128 v[28:31], v13 offset:34816\n\t"
        "v_mfma_f32_16x16x32_bf16 v[56:59], v[128:131], v[16:19], 0\n\t"
        "v_mfma_f32_16x16x32_bf16 v[60:63], v[132:135], v[16:19], 0\n\t"
        "ds_read_b128 v[16:19], v12 offset:36864\n\t"
        "v_mfma_f32_16x16x32_bf16 v[56:59], v[136:139], v[20:23], v[56:59]\n\t"
        "v_mfma_f32_16x16x32_bf16 v[60:63], v[140:143], v[20:23], v[60:63]\n\t"
        "ds_read_b128 v[20:23], v13 offset:36864\n\t"
        "v_mfma_f32_16x16x32_bf16 v[56:59], v[144:147], v[24:27], v[56:59]\n\t"
        "v_mfma_f32_16x16x32_bf16 v[60:63], v[148:151], v[24:27], v[60:63]\n\t"
        "ds_read_b128 v[24:27], v12 offset:38912\n\t"
        "s_waitcnt lgkmcnt(3)\n\t"
        "v_mfma_f32_16x16x32_bf16 v[56:59], v[152:155], v[28:31], v[56:59]\n\t"
        "v_mfma_f32_16x16x32_bf16 v[60:63], v[156:159], v[28:31], v[60:63]\n\t"
        "ds_read_b128 v[28:31], v13 offset:38912\n\t"
        "s_waitcnt lgkmcnt(3)\n\t"
        "v_mfma_f32_16x16x32_bf16 v[56:59], v[160:163], v[16:19], v[56:59]\n\t"
        "v_mfma_f32_16x16x32_bf16 v[60:63], v[164:167], v[16:19], v[60:63]\n\t"
        "ds_read_b128 v[16:19], v12 offset:40960\n\t"
        "s_waitcnt lgkmcnt(3)\n\t"
        "v_mfma_f32_16x16x32_bf16 v[56:59], v[168:171], v[20:23], v[56:59]\n\t"
        "v_mfma_f32_16x16x32_bf16 v[60:63], v[172:175], v[20:23], v[60:63]\n\t"
        "ds_read_b128 v[20:23], v13 offset:40960\n\t"
        "s_waitcnt lgkmcnt(3)\n\t"
        "v_mfma_f32_16x16x32_bf16 v[56:59], v[176:179], v[24:27], v[56:59]\n\t"
        "v_mfma_f32_16x16x32_bf16 v[60:63], v[180:183], v[24:27], v[60:63]\n\t"
        "ds_read_b128 v[24:27], v12 offset:43008\n\t"
        "s_waitcnt lgkmcnt(3)\n\t"
        "v_mfma_f32_16x16x32_bf16 v[56:59], v[184:187], v[28:31], v[56:59]\n\t"
        "v_mfma_f32_16x16x32_bf16 v[60:63], v[188:191], v[28:31], v[60:63]\n\t"
        "ds_read_b128 v[28:31], v13 offset:43008\n\t"
        "s_waitcnt lgkmcnt(3)\n\t"
        "v_mfma_f32_16x16x32_bf16 v[56:59], v[192:195], v[16:19], v[56:59]\n\t"
        "v_mfma_f32_16x16x32_bf16 v[60:63], v[196:199], v[16:19], v[60:63]\n\t"
        "ds_read_b128 v[16:19], v12 offset:45056\n\t"
        "s_waitcnt lgkmcnt(3)\n\t"
        "v_mfma_f32_16x16x32_bf16 v[56:59], v[200:203], v[20:23], v[56:59]\n\t"
        "v_mfma_f32_16x16x32_bf16 v[60:63], v[204:207], v[20:23], v[60:63]\n\t"
        "ds_read_b128 v[20:23], v13 offset:45056\n\t"
        "s_waitcnt lgkmcnt(3)\n\t"
        "v_mfma_f32_16x16x32_bf16 v[56:59], v[208:211], v[24:27], v[56:59]\n\t"
        "v_mfma_f32_16x16x32_bf16 v[60:63], v[212:215], v[24:27], v[60:63]\n\t"
        "ds_read_b128 v[24:27], v12 offset:47104\n\t"
        "s_waitcnt lgkmcnt(3)\n\t"
        "v_mfma_f32_16x16x32_bf16 v[56:59], v[216:219], v[28:31], v[56:59]\n\t"
        "v_mfma_f32_16x16x32_bf16 v[60:63], v[220:223], v[28:31], v[60:63]\n\t"
        "ds_read_b128 v[28:31], v13 offset:47104\n\t"
        "s_waitcnt lgkmcnt(3)\n\t"
        "v_mfma_f32_16x16x32_bf16 v[56:59], v[224:227], v[16:19], v[56:59]\n\t"
        "v_mfma_f32_16x16x32_bf16 v[60:63], v[228:231], v[16:19], v[60:63]\n\t"
        "s_waitcnt lgkmcnt(2)\n\t"
        "v_mfma_f32_16x16x32_bf16 v[56:59], v[232:235], v[20:23], v[56:59]\n\t"
        "v_mfma_f32_16x16x32_bf16 v[60:63], v[236:239], v[20:23], v[60:63]\n\t"
        "s_waitcnt lgkmcnt(1)\n\t"
        "v_mfma_f32_16x16x32_bf16 v[56:59], v[240:243], v[24:27], v[56:59]\n\t"
        "v_mfma_f32_16x16x32_bf16 v[60:63], v[244:247], v[24:27], v[60:63]\n\t"
        "s_waitcnt lgkmcnt(0)\n\t"
        "v_mfma_f32_16x16x32_bf16 v[56:59], v[248:251], v[28:31], v[56:59]\n\t"
        "v_mfma_f32_16x16x32_bf16 v[60:63], v[252:255], v[28:31], v[60:63]\n\t"
        "s_nop 7\n\t"
        "ds_write_b128 v14, v[56:59] offset:8192\n\t"
        "ds_write_b128 v14, v[60:63] offset:9216\n\t"
        "s_waitcnt vmcnt(4) lgkmcnt(0)\n\t"
        "s_barrier\n\t"
        "s_sub_u32 s50, s50, 0x80000\n\t"
        "s_and_b32 s70, s51, 3\n\t"
        "s_cselect_b32 s70, 0, 0x2000\n\t"
        "s_xor_b32 s50, s50, s70\n\t"
        "s_sub_u32 s51, s51, 1\n\t"
        "s_mov_b32 s52, 30\n\t"
        "s_sub_u32 s54, s54, 1\n\t"
        "v_add_u32 v81, s47, v0\n\t"
        "v_add_u32 v82, 65536, v81\n\t"
        "ds_read_b128 v[16:19], v10 offset:0\n\t"
        "ds_read_b128 v[20:23], v11 offset:0\n\t"
        "ds_read_b128 v[24:27], v10 offset:2048\n\t"
        ".p2align 6\n\t"
        ".Lx_loop0_%=:\n\t"
        "ds_read_b128 v[28:31], v11 offset:2048\n\t"
        "s_waitcnt lgkmcnt(3)\n\t"
        "v_mfma_f32_16x16x32_bf16 v[32:35], v[128:131], v[16:19], 0\n\t"
        "s_add_u32 m0, s47, 98304\n\t"
        "ds_read_b128 v[64:67], v14 offset:0\n\t"
        "v_mfma_f32_16x16x32_bf16 v[36:39], v[132:135], v[16:19], 0\n\t"
        "ds_read_b128 v[68:71], v14 offset:1024\n\t"
        "ds_read_b128 v[16:19], v10 offset:4096\n\t"
        "s_waitcnt lgkmcnt(5)\n\t"
        "v_mfma_f32_16x16x32_bf16 v[32:35], v[136:139], v[20:23], v[32:35]\n\t"
        "buffer_load_dwordx4 v4, %[rx], s49 offen offset:0 lds\n\t"
        "ds_write_b128 v14, v[56:59] offset:8192\n\t"
        "v_mfma_f32_16x16x32_bf16 v[36:39], v[140:143], v[20:23], v[36:39]\n\t"
        "s_add_u32 s50, s50, 0x80000\n\t"
        "s_add_u32 s51, s51, 1\n\t"
        "ds_read_b128 v[20:23], v11 offset:4096\n\t"
        "s_waitcnt lgkmcnt(6)\n\t"
        "v_mfma_f32_16x16x32_bf16 v[32:35], v[144:147], v[24:27], v[32:35]\n\t"
        "ds_write_b128 v14, v[60:63] offset:9216\n\t"
        "s_and_b32 s70, s51, 3\n\t"
        "v_mfma_f32_16x16x32_bf16 v[36:39], v[148:151], v[24:27], v[36:39]\n\t"
        "s_cselect_b32 s70, 0, 0x2000\n\t"
        "ds_read_b128 v[24:27], v10 offset:6144\n\t"
        "s_waitcnt lgkmcnt(7)\n\t"
        "v_mfma_f32_16x16x32_bf16 v[32:35], v[152:155], v[28:31], v[32:35]\n\t"
        "buffer_load_dwordx4 v5, %[rx], s49 offen offset:1024 lds\n\t"
        "v_mfma_f32_16x16x32_bf16 v[36:39], v[156:159], v[28:31], v[36:39]\n\t"
        "s_xor_b32 s50, s50, s70\n\t"
        "ds_read_b128 v[28:31], v11 offset:6144\n\t"
        "s_waitcnt lgkmcnt(5)\n\t"
        "v_mfma_f32_16x16x32_bf16 v[32:35], v[160:163], v[16:19], v[32:35]\n\t"
        "v_add_f32 v64, v64, v48\n\t"
        "v_mfma_f32_16x16x32_bf16 v[36:39], v[164:167], v[16:19], v[36:39]\n\t"
        "v_add_f32 v65, v65, v49\n\t"
        "ds_read_b128 v[16:19], v10 offset:8192\n\t"
        "s_waitcnt lgkmcnt(4)\n\t"
        "v_mfma_f32_16x16x32_bf16 v[32:35], v[168:171], v[20:23], v[32:35]\n\t"
        "buffer_load_dwordx4 v6, %[rx], s49 offen offset:2048 lds\n\t"
        "v_add_f32 v66, v66, v50\n\t"
        "v_mfma_f32_16x16x32_bf16 v[36:39], v[172:175], v[20:23], v[36:39]\n\t"
        "v_add_f32 v67, v67, v51\n\t"
        "ds_read_b128 v[20:23], v11 offset:8192\n\t"
        "s_waitcnt lgkmcnt(3)\n\t"
        "v_mfma_f32_16x16x32_bf16 v[32:35], v[176:179], v[24:27], v[32:35]\n\t"
        "v_add_f32 v68, v68, v52\n\t"
        "v_mfma_f32_16x16x32_bf16 v[36:39], v[180:183], v[24:27], v[36:39]\n\t"
        "v_add_f32 v69, v69, v53\n\t"
        "ds_read_b128 v[24:27], v10 offset:10240\n\t"
        "s_waitcnt lgkmcnt(3)\n\t"
        "v_mfma_f32_16x16x32_bf16 v[32:35], v[184:187], v[28:31], v[32:35]\n\t"
        "buffer_load_dwordx4 v7, %[rx], s49 offen offset:3072 lds\n\t"
        "v_add_f32 v70, v70, v54\n\t"
        "v_mfma_f32_16x16x32_bf16 v[36:39], v[188:191], v[28:31], v[36:39]\n\t"
        "v_add_f32 v71, v71, v55\n\t"
        "ds_read_b128 v[28:31], v11 offset:10240\n\t"
        "s_waitcnt lgkmcnt(3)\n\t"
        "v_mfma_f32_16x16x32_bf16 v[32:35], v[192:195], v[16:19], v[32:35]\n\t"
        "s_add_u32 s49, s49, 32768\n\t"
        "v_cvt_pk_bf16_f32 v72, v64, v65\n\t"
        "v_mfma_f32_16x16x32_bf16 v[36:39], v[196:199], v[16:19], v[36:39]\n\t"
        "v_cvt_pk_bf16_f32 v73, v68, v69\n\t"
        "ds_read_b128 v[16:19], v10 offset:12288\n\t"
        "s_waitcnt lgkmcnt(3)\n\t"
        "v_mfma_f32_16x16x32_bf16 v[32:35], v[200:203], v[20:23], v[32:35]\n\t"
        "v_cvt_pk_bf16_f32 v74, v66, v67\n\t"
        "v_mfma_f32_16x16x32_bf16 v[36:39], v[204:207], v[20:23], v[36:39]\n\t"
        "v_cvt_pk_bf16_f32 v75, v70, v71\n\t"
        "ds_read_b128 v[20:23], v11 offset:12288\n\t"
        "s_waitcnt lgkmcnt(3)\n\t"
        "v_mfma_f32_16x16x32_bf16 v[32:35], v[208:211], v[24:27], v[32:35]\n\t"
        "s_mov_b64 exec, s[56:57]\n\t"
        "v_swap_b32 v73, v74\n\t"
        "s_nop 1\n\t"
        "v_permlane16_swap_b32 v72, v74\n\t"
        "v_permlane16_swap_b32 v73, v75\n\t"
        "s_mov_b64 exec, -1\n\t"
        "v_mfma_f32_16x16x32_bf16 v[36:39], v[212:215], v[24:27], v[36:39]\n\t"
        "buffer_store_dwordx4 v[72:75], v79, %[rs], s50 offen sc1\n\t"
        "ds_read_b128 v[24:27], v10 offset:14336\n\t"
        "s_waitcnt lgkmcnt(3)\n\t"
        "v_mfma_f32_16x16x32_bf16 v[32:35], v[216:219], v[28:31], v[32:35]\n\t"
        "v_mfma_f32_16x16x32_bf16 v[36:39], v[220:223], v[28:31], v[36:39]\n\t"
        "ds_read_b128 v[28:31], v11 offset:14336\n\t"
        "s_waitcnt lgkmcnt(3)\n\t"
        "v_mfma_f32_16x16x32_bf16 v[32:35], v[224:227], v[16:19], v[32:35]\n\t"
        "v_mfma_f32_16x16x32_bf16 v[36:39], v[228:231], v[16:19], v[36:39]\n\t"
        "ds_read_b128 v[16:19], v10 offset:32768\n\t"
        "s_waitcnt lgkmcnt(3)\n\t"
        "v_mfma_f32_16x16x32_bf16 v[32:35], v[232:235], v[20:23], v[32:35]\n\t"
        "v_mfma_f32_16x16x32_bf16 v[36:39], v[236:239], v[20:23], v[36:39]\n\t"
        "ds_read_b128 v[20:23], v11 offset:32768\n\t"
        "s_waitcnt lgkmcnt(3)\n\t"
        "v_mfma_f32_16x16x32_bf16 v[32:35], v[240:243], v[24:27], v[32:35]\n\t"
        "v_mfma_f32_16x16x32_bf16 v[36:39], v[244:247], v[24:27], v[36:39]\n\t"
        "ds_read_b128 v[24:27], v10 offset:34816\n\t"
        "s_waitcnt lgkmcnt(3)\n\t"
        "v_mfma_f32_16x16x32_bf16 v[32:35], v[248:251], v[28:31], v[32:35]\n\t"
        "v_mfma_f32_16x16x32_bf16 v[36:39], v[252:255], v[28:31], v[36:39]\n\t"
        "s_waitcnt vmcnt(5)\n\t"
        "s_barrier\n\t"
        "ds_read_b128 v[28:31], v11 offset:34816\n\t"
        "s_waitcnt lgkmcnt(3)\n\t"
        "v_mfma_f32_16x16x32_bf16 v[40:43], v[128:131], v[16:19], 0\n\t"
        "s_add_u32 m0, s47, 0\n\t"
        "v_mfma_f32_16x16x32_bf16 v[44:47], v[132:135], v[16:19], 0\n\t"
        "ds_read_b128 v[16:19], v10 offset:36864\n\t"
        "s_waitcnt lgkmcnt(3)\n\t"
        "v_mfma_f32_16x16x32_bf16 v[40:43], v[136:139], v[20:23], v[40:43]\n\t"
        "buffer_load_dwordx4 v4, %[rx], s49 offen offset:0 lds\n\t"
        "v_mfma_f32_16x16x32_bf16 v[44:47], v[140:143], v[20:23], v[44:47]\n\t"
        "ds_read_b128 v[20:23], v11 offset:36864\n\t"
        "s_waitcnt lgkmcnt(3)\n\t"
        "v_mfma_f32_16x16x32_bf16 v[40:43], v[144:147], v[24:27], v[40:43]\n\t"
        "v_mfma_f32_16x16x32_bf16 v[44:47], v[148:151], v[24:27], v[44:47]\n\t"
        "ds_read_b128 v[24:27], v10 offset:38912\n\t"
        "s_waitcnt lgkmcnt(3)\n\t"
        "v_mfma_f32_16x16x32_bf16 v[40:43], v[152:155], v[28:31], v[40:43]\n\t"
        "buffer_load_dwordx4 v5, %[rx], s49 offen offset:1024 lds\n\t"
        "v_mfma_f32_16x16x32_bf16 v[44:47], v[156:159], v[28:31], v[44:47]\n\t"
        "ds_read_b128 v[28:31], v11 offset:38912\n\t"
        "s_waitcnt lgkmcnt(3)\n\t"
        "v_mfma_f32_16x16x32_bf16 v[40:43], v[160:163], v[16:19], v[40:43]\n\t"
        "v_mfma_f32_16x16x32_bf16 v[44:47], v[164:167], v[16:19], v[44:47]\n\t"
        "ds_read_b128 v[16:19], v10 offset:40960\n\t"
        "s_waitcnt lgkmcnt(3)\n\t"
        "v_mfma_f32_16x16x32_bf16 v[40:43], v[168:171], v[20:23], v[40:43]\n\t"
        "buffer_load_dwordx4 v6, %[rx], s49 offen offset:2048 lds\n\t"
        "v_mfma_f32_16x16x32_bf16 v[44:47], v[172:175], v[20:23], v[44:47]\n\t"
        "ds_read_b128 v[20:23], v11 offset:40960\n\t"
        "s_waitcnt lgkmcnt(3)\n\t"
        "v_mfma_f32_16x16x32_bf16 v[40:43], v[176:179], v[24:27], v[40:43]\n\t"
        "v_mfma_f32_16x16x32_bf16 v[44:47], v[180:183], v[24:27], v[44:47]\n\t"
        "ds_read_b128 v[24:27], v10 offset:43008\n\t"
        "s_waitcnt lgkmcnt(3)\n\t"
        "v_mfma_f32_16x16x32_bf16 v[40:43], v[184:187], v[28:31], v[40:43]\n\t"
        "buffer_load_dwordx4 v7, %[rx], s49 offen offset:3072 lds\n\t"
        "v_mfma_f32_16x16x32_bf16 v[44:47], v[188:191], v[28:31], v[44:47]\n\t"
        "ds_read_b128 v[28:31], v11 offset:43008\n\t"
        "s_waitcnt lgkmcnt(3)\n\t"
        "v_mfma_f32_16x16x32_bf16 v[40:43], v[192:195], v[16:19], v[40:43]\n\t"
        "s_add_u32 s49, s49, 32768\n\t"
        "v_mfma_f32_16x16x32_bf16 v[44:47], v[196:199], v[16:19], v[44:47]\n\t"
        "s_add_u32 s54, s54, 1\n\t"
        "ds_read_b128 v[16:19], v10 offset:45056\n\t"
        "s_waitcnt lgkmcnt(3)\n\t"
        "v_mfma_f32_16x16x32_bf16 v[40:43], v[200:203], v[20:23], v[40:43]\n\t"
        "v_mov_b32 v76, s54\n\t"
        "v_mfma_f32_16x16x32_bf16 v[44:47], v[204:207], v[20:23], v[44:47]\n\t"
        "ds_read_b128 v[20:23], v11 offset:45056\n\t"
        "s_waitcnt lgkmcnt(3)\n\t"
        "v_mfma_f32_16x16x32_bf16 v[40:43], v[208:211], v[24:27], v[40:43]\n\t"
        "s_and_b32 s71, s54, 127\n\t"
        "s_cmp_le_u32 s71, 16\n\t"
        "s_cselect_b32 s72, 0, 3\n\t"
        "s_mul_i32 s71, s71, 128\n\t"
        "s_and_b32 s72, s72, s54\n\t"
        "s_cselect_b32 s71, 0x70000000, s71\n\t"
        "v_mfma_f32_16x16x32_bf16 v[44:47], v[212:215], v[24:27], v[44:47]\n\t"
        "ds_read_b128 v[24:27], v10 offset:47104\n\t"
        "s_waitcnt lgkmcnt(3)\n\t"
        "v_mfma_f32_16x16x32_bf16 v[40:43], v[216:219], v[28:31], v[40:43]\n\t"
        "buffer_store_dwordx2 v[76:77], v78, %[rf], s71 offen sc1\n\t"
        "v_mfma_f32_16x16x32_bf16 v[44:47], v[220:223], v[28:31], v[44:47]\n\t"
        "ds_read_b128 v[28:31], v11 offset:47104\n\t"
        "s_waitcnt lgkmcnt(3)\n\t"
        "v_mfma_f32_16x16x32_bf16 v[40:43], v[224:227], v[16:19], v[40:43]\n\t"
        "v_mfma_f32_16x16x32_bf16 v[44:47], v[228:231], v[16:19], v[44:47]\n\t"
        "ds_read_b128 v[16:19], v12 offset:0\n\t"
        "s_waitcnt lgkmcnt(3)\n\t"
        "v_mfma_f32_16x16x32_bf16 v[40:43], v[232:235], v[20:23], v[40:43]\n\t"
        "v_mfma_f32_16x16x32_bf16 v[44:47], v[236:239], v[20:23], v[44:47]\n\t"
        "ds_read_b128 v[20:23], v13 offset:0\n\t"
        "s_waitcnt lgkmcnt(3)\n\t"
        "v_mfma_f32_16x16x32_bf16 v[40:43], v[240:243], v[24:27], v[40:43]\n\t"
        "v_mfma_f32_16x16x32_bf16 v[44:47], v[244:247], v[24:27], v[44:47]\n\t"
        "ds_read_b128 v[24:27], v12 offset:2048\n\t"
        "s_waitcnt lgkmcnt(3)\n\t"
        "v_mfma_f32_16x16x32_bf16 v[40:43], v[248:251], v[28:31], v[40:43]\n\t"
        "v_mfma_f32_16x16x32_bf16 v[44:47], v[252:255], v[28:31], v[44:47]\n\t"
        "s_waitcnt vmcnt(5)\n\t"
        "s_barrier\n\t"
        "ds_read_b128 v[28:31], v13 offset:2048\n\t"
        "s_waitcnt lgkmcnt(3)\n\t"
        "v_mfma_f32_16x16x32_bf16 v[48:51], v[128:131], v[16:19], 0\n\t"
        "s_add_u32 m0, s47, 32768\n\t"
        "ds_read_b128 v[64:67], v14 offset:0\n\t"
        "v_mfma_f32_16x16x32_bf16 v[52:55], v[132:135], v[16:19], 0\n\t"
        "ds_read_b128 v[68:71], v14 offset:1024\n\t"
        "ds_read_b128 v[16:19], v12 offset:4096\n\t"
        "s_waitcnt lgkmcnt(5)\n\t"
        "v_mfma_f32_16x16x32_bf16 v[48:51], v[136:139], v[20:23], v[48:51]\n\t"
        "buffer_load_dwordx4 v4, %[rx], s49 offen offset:0 lds\n\t"
        "ds_write_b128 v14, v[40:43] offset:8192\n\t"
        "v_mfma_f32_16x16x32_bf16 v[52:55], v[140:143], v[20:23], v[52:55]\n\t"
        "s_add_u32 s50, s50, 0x80000\n\t"
        "s_add_u32 s51, s51, 1\n\t"
        "ds_read_b128 v[20:23], v13 offset:4096\n\t"
        "s_waitcnt lgkmcnt(6)\n\t"
        "v_mfma_f32_16x16x32_bf16 v[48:51], v[144:147], v[24:27], v[48:51]\n\t"
        "ds_write_b128 v14, v[44:47] offset:9216\n\t"
        "s_and_b32 s70, s51, 3\n\t"
        "v_mfma_f32_16x16x32_bf16 v[52:55], v[148:151], v[24:27], v[52:55]\n\t"
        "s_cselect_b32 s70, 0, 0x2000\n\t"
        "ds_read_b128 v[24:27], v12 offset:6144\n\t"
        "s_waitcnt lgkmcnt(7)\n\t"
        "v_mfma_f32_16x16x32_bf16 v[48:51], v[152:155], v[28:31], v[48:51]\n\t"
        "buffer_load_dwordx4 v5, %[rx], s49 offen offset:1024 lds\n\t"
        "v_mfma_f32_16x16x32_bf16 v[52:55], v[156:159], v[28:31], v[52:55]\n\t"
        "s_xor_b32 s50, s50, s70\n\t"
        "ds_read_b128 v[28:31], v13 offset:6144\n\t"
        "s_waitcnt lgkmcnt(5)\n\t"
        "v_mfma_f32_16x16x32_bf16 v[48:51], v[160:163], v[16:19], v[48:51]\n\t"
        "v_add_f32 v64, v64, v32\n\t"
        "v_mfma_f32_16x16x32_bf16 v[52:55], v[164:167], v[16:19], v[52:55]\n\t"
        "v_add_f32 v65, v65, v33\n\t"
        "ds_read_b128 v[16:19], v12 offset:8192\n\t"
        "s_waitcnt lgkmcnt(4)\n\t"
        "v_mfma_f32_16x16x32_bf16 v[48:51], v[168:171], v[20:23], v[48:51]\n\t"
        "buffer_load_dwordx4 v6, %[rx], s49 offen offset:2048 lds\n\t"
        "v_add_f32 v66, v66, v34\n\t"
        "v_mfma_f32_16x16x32_bf16 v[52:55], v[172:175], v[20:23], v[52:55]\n\t"
        "v_add_f32 v67, v67, v35\n\t"
        "ds_read_b128 v[20:23], v13 offset:8192\n\t"
        "s_waitcnt lgkmcnt(3)\n\t"
        "v_mfma_f32_16x16x32_bf16 v[48:51], v[176:179], v[24:27], v[48:51]\n\t"
        "v_add_f32 v68, v68, v36\n\t"
        "v_mfma_f32_16x16x32_bf16 v[52:55], v[180:183], v[24:27], v[52:55]\n\t"
        "v_add_f32 v69, v69, v37\n\t"
        "ds_read_b128 v[24:27], v12 offset:10240\n\t"
        "s_waitcnt lgkmcnt(3)\n\t"
        "v_mfma_f32_16x16x32_bf16 v[48:51], v[184:187], v[28:31], v[48:51]\n\t"
        "buffer_load_dwordx4 v7, %[rx], s49 offen offset:3072 lds\n\t"
        "v_add_f32 v70, v70, v38\n\t"
        "v_mfma_f32_16x16x32_bf16 v[52:55], v[188:191], v[28:31], v[52:55]\n\t"
        "v_add_f32 v71, v71, v39\n\t"
        "ds_read_b128 v[28:31], v13 offset:10240\n\t"
        "s_waitcnt lgkmcnt(3)\n\t"
        "v_mfma_f32_16x16x32_bf16 v[48:51], v[192:195], v[16:19], v[48:51]\n\t"
        "s_add_u32 s49, s49, 32768\n\t"
        "v_cvt_pk_bf16_f32 v72, v64, v65\n\t"
        "v_mfma_f32_16x16x32_bf16 v[52:55], v[196:199], v[16:19], v[52:55]\n\t"
        "v_cvt_pk_bf16_f32 v73, v68, v69\n\t"
        "ds_read_b128 v[16:19], v12 offset:12288\n\t"
        "s_waitcnt lgkmcnt(3)\n\t"
        "v_mfma_f32_16x16x32_bf16 v[48:51], v[200:203], v[20:23], v[48:51]\n\t"
        "v_cvt_pk_bf16_f32 v74, v66, v67\n\t"
        "v_mfma_f32_16x16x32_bf16 v[52:55], v[204:207], v[20:23], v[52:55]\n\t"
        "v_cvt_pk_bf16_f32 v75, v70, v71\n\t"
        "ds_read_b128 v[20:23], v13 offset:12288\n\t"
        "s_waitcnt lgkmcnt(3)\n\t"
        "v_mfma_f32_16x16x32_bf16 v[48:51], v[208:211], v[24:27], v[48:51]\n\t"
        "s_mov_b64 exec, s[56:57]\n\t"
        "v_swap_b32 v73, v74\n\t"
        "s_nop 1\n\t"
        "v_permlane16_swap_b32 v72, v74\n\t"
        "v_permlane16_swap_b32 v73, v75\n\t"
        "s_mov_b64 exec, -1\n\t"
        "v_mfma_f32_16x16x32_bf16 v[52:55], v[212:215], v[24:27], v[52:55]\n\t"
        "buffer_store_dwordx4 v[72:75], v79, %[rs], s50 offen sc1\n\t"
        "ds_read_b128 v[24:27], v12 offset:14336\n\t"
        "s_waitcnt lgkmcnt(3)\n\t"
        "v_mfma_f32_16x16x32_bf16 v[48:51], v[216:219], v[28:31], v[48:51]\n\t"
        "v_mfma_f32_16x16x32_bf16 v[52:55], v[220:223], v[28:31], v[52:55]\n\t"
        "ds_read_b128 v[28:31], v13 offset:14336\n\t"
        "s_waitcnt lgkmcnt(3)\n\t"
        "v_mfma_f32_16x16x32_bf16 v[48:51], v[224:227], v[16:19], v[48:51]\n\t"
        "v_mfma_f32_16x16x32_bf16 v[52:55], v[228:231], v[16:19], v[52:55]\n\t"
        "ds_read_b128 v[16:19], v12 offset:32768\n\t"
        "s_waitcnt lgkmcnt(3)\n\t"
        "v_mfma_f32_16x16x32_bf16 v[48:51], v[232:235], v[20:23], v[48:51]\n\t"
        "v_mfma_f32_16x16x32_bf16 v[52:55], v[236:239], v[20:23], v[52:55]\n\t"
        "ds_read_b128 v[20:23], v13 offset:32768\n\t"
        "s_waitcnt lgkmcnt(3)\n\t"
        "v_mfma_f32_16x16x32_bf16 v[48:51], v[240:243], v[24:27], v[48:51]\n\t"
        "v_mfma_f32_16x16x32_bf16 v[52:55], v[244:247], v[24:27], v[52:55]\n\t"
        "ds_read_b128 v[24:27], v12 offset:34816\n\t"
        "s_waitcnt lgkmcnt(3)\n\t"
        "v_mfma_f32_16x16x32_bf16 v[48:51], v[248:251], v[28:31], v[48:51]\n\t"
        "v_mfma_f32_16x16x32_bf16 v[52:55], v[252:255], v[28:31], v[52:55]\n\t"
        "s_waitcnt vmcnt(5)\n\t"
        "s_barrier\n\t"
        "ds_read_b128 v[28:31], v13 offset:34816\n\t"
        "s_waitcnt lgkmcnt(3)\n\t"
        "v_mfma_f32_16x16x32_bf16 v[56:59], v[128:131], v[16:19], 0\n\t"
        "s_add_u32 m0, s47, 65536\n\t"
        "v_mfma_f32_16x16x32_bf16 v[60:63], v[132:135], v[16:19], 0\n\t"
        "ds_read_b128 v[16:19], v12 offset:36864\n\t"
        "s_waitcnt lgkmcnt(3)\n\t"
        "v_mfma_f32_16x16x32_bf16 v[56:59], v[136:139], v[20:23], v[56:59]\n\t"
        "buffer_load_dwordx4 v4, %[rx], s49 offen offset:0 lds\n\t"
        "v_mfma_f32_16x16x32_bf16 v[60:63], v[140:143], v[20:23], v[60:63]\n\t"
        "ds_read_b128 v[20:23], v13 offset:36864\n\t"
        "s_waitcnt lgkmcnt(3)\n\t"
        "v_mfma_f32_16x16x32_bf16 v[56:59], v[144:147], v[24:27], v[56:59]\n\t"
        "v_mfma_f32_16x16x32_bf16 v[60:63], v[148:151], v[24:27], v[60:63]\n\t"
        "ds_read_b128 v[24:27], v12 offset:38912\n\t"
        "s_waitcnt lgkmcnt(3)\n\t"
        "v_mfma_f32_16x16x32_bf16 v[56:59], v[152:155], v[28:31], v[56:59]\n\t"
        "buffer_load_dwordx4 v5, %[rx], s49 offen offset:1024 lds\n\t"
        "v_mfma_f32_16x16x32_bf16 v[60:63], v[156:159], v[28:31], v[60:63]\n\t"
        "ds_read_b128 v[28:31], v13 offset:38912\n\t"
        "s_waitcnt lgkmcnt(3)\n\t"
        "v_mfma_f32_16x16x32_bf16 v[56:59], v[160:163], v[16:19], v[56:59]\n\t"
        "v_mfma_f32_16x16x32_bf16 v[60:63], v[164:167], v[16:19], v[60:63]\n\t"
        "ds_read_b128 v[16:19], v12 offset:40960\n\t"
        "s_waitcnt lgkmcnt(3)\n\t"
        "v_mfma_f32_16x16x32_bf16 v[56:59], v[168:171], v[20:23], v[56:59]\n\t"
        "buffer_load_dwordx4 v6, %[rx], s49 offen offset:2048 lds\n\t"
        "v_mfma_f32_16x16x32_bf16 v[60:63], v[172:175], v[20:23], v[60:63]\n\t"
        "ds_read_b128 v[20:23], v13 offset:40960\n\t"
        "s_waitcnt lgkmcnt(3)\n\t"
        "v_mfma_f32_16x16x32_bf16 v[56:59], v[176:179], v[24:27], v[56:59]\n\t"
        "v_mfma_f32_16x16x32_bf16 v[60:63], v[180:183], v[24:27], v[60:63]\n\t"
        "ds_read_b128 v[24:27], v12 offset:43008\n\t"
        "s_waitcnt lgkmcnt(3)\n\t"
        "v_mfma_f32_16x16x32_bf16 v[56:59], v[184:187], v[28:31], v[56:59]\n\t"
        "buffer_load_dwordx4 v7, %[rx], s49 offen offset:3072 lds\n\t"
        "v_mfma_f32_16x16x32_bf16 v[60:63], v[188:191], v[28:31], v[60:63]\n\t"
        "ds_read_b128 v[28:31], v13 offset:43008\n\t"
        "s_waitcnt lgkmcnt(3)\n\t"
        "v_mfma_f32_16x16x32_bf16 v[56:59], v[192:195], v[16:19], v[56:59]\n\t"
        "s_add_u32 s49, s49, 32768\n\t"
        "v_mfma_f32_16x16x32_bf16 v[60:63], v[196:199], v[16:19], v[60:63]\n\t"
        "s_add_u32 s54, s54, 1\n\t"
        "ds_read_b128 v[16:19], v12 offset:45056\n\t"
        "s_waitcnt lgkmcnt(3)\n\t"
        "v_mfma_f32_16x16x32_bf16 v[56:59], v[200:203], v[20:23], v[56:59]\n\t"
        "v_mov_b32 v76, s54\n\t"
        "v_mfma_f32_16x16x32_bf16 v[60:63], v[204:207], v[20:23], v[60:63]\n\t"
        "ds_read_b128 v[20:23], v13 offset:45056\n\t"
        "s_waitcnt lgkmcnt(3)\n\t"
        "v_mfma_f32_16x16x32_bf16 v[56:59], v[208:211], v[24:27], v[56:59]\n\t"
        "s_and_b32 s71, s54, 127\n\t"
        "s_cmp_le_u32 s71, 16\n\t"
        "s_cselect_b32 s72, 0, 3\n\t"
        "s_mul_i32 s71, s71, 128\n\t"
        "s_and_b32 s72, s72, s54\n\t"
        "s_cselect_b32 s71, 0x70000000, s71\n\t"
        "v_mfma_f32_16x16x32_bf16 v[60:63], v[212:215], v[24:27], v[60:63]\n\t"
        "ds_read_b128 v[24:27], v12 offset:47104\n\t"
        "s_waitcnt lgkmcnt(3)\n\t"
        "v_mfma_f32_16x16x32_bf16 v[56:59], v[216:219], v[28:31], v[56:59]\n\t"
        "buffer_store_dwordx2 v[76:77], v78, %[rf], s71 offen sc1\n\t"
        "v_mfma_f32_16x16x32_bf16 v[60:63], v[220:223], v[28:31], v[60:63]\n\t"
        "ds_read_b128 v[28:31], v13 offset:47104\n\t"
        "s_waitcnt lgkmcnt(3)\n\t"
        "v_mfma_f32_16x16x32_bf16 v[56:59], v[224:227], v[16:19], v[56:59]\n\t"
        "v_mfma_f32_16x16x32_bf16 v[60:63], v[228:231], v[16:19], v[60:63]\n\t"
        "ds_read_b128 v[16:19], v10 offset:0\n\t"
        "s_waitcnt lgkmcnt(3)\n\t"
        "v_mfma_f32_16x16x32_bf16 v[56:59], v[232:235], v[20:23], v[56:59]\n\t"
        "v_mfma_f32_16x16x32_bf16 v[60:63], v[236:239], v[20:23], v[60:63]\n\t"
        "ds_read_b128 v[20:23], v11 offset:0\n\t"
        "s_waitcnt lgkmcnt(3)\n\t"
        "v_mfma_f32_16x16x32_bf16 v[56:59], v[240:243], v[24:27], v[56:59]\n\t"
        "v_mfma_f32_16x16x32_bf16 v[60:63], v[244:247], v[24:27], v[60:63]\n\t"
        "ds_read_b128 v[24:27], v10 offset:2048\n\t"
        "s_waitcnt lgkmcnt(3)\n\t"
        "v_mfma_f32_16x16x32_bf16 v[56:59], v[248:251], v[28:31], v[56:59]\n\t"
        "v_mfma_f32_16x16x32_bf16 v[60:63], v[252:255], v[28:31], v[60:63]\n\t"
        "s_sub_u32 s52, s52, 1\n\t"
        "s_waitcnt vmcnt(5)\n\t"
        "s_barrier\n\t"
        "s_cmp_lg_u32 s52, 0\n\t"
        "s_cbranch_scc1 .Lx_loop0_%=\n\t"
        "s_nop 7\n\t"
        "ds_write_b128 v14, v[56:59] offset:8192\n\t"
        "ds_write_b128 v14, v[60:63] offset:9216\n\t"
        "ds_read_b128 v[64:67], v14 offset:0\n\t"
        "ds_read_b128 v[68:71], v14 offset:1024\n\t"
        "s_waitcnt lgkmcnt(0)\n\t"
        "s_barrier\n\t"
        "s_add_u32 s50, s50, 0x80000\n\t"
        "s_add_u32 s51, s51, 1\n\t"
        "s_and_b32 s70, s51, 3\n\t"
        "s_cselect_b32 s70, 0, 0x2000\n\t"
        "s_xor_b32 s50, s50, s70\n\t"
        "s_waitcnt lgkmcnt(0)\n\t"
        "v_add_f32 v64, v64, v48\n\t"
        "v_add_f32 v65, v65, v49\n\t"
        "v_add_f32 v66, v66, v50\n\t"
        "v_add_f32 v67, v67, v51\n\t"
        "v_add_f32 v68, v68, v52\n\t"
        "v_add_f32 v69, v69, v53\n\t"
        "v_add_f32 v70, v70, v54\n\t"
        "v_add_f32 v71, v71, v55\n\t"
        "v_cvt_pk_bf16_f32 v72, v64, v65\n\t"
        "v_cvt_pk_bf16_f32 v73, v68, v69\n\t"
        "v_cvt_pk_bf16_f32 v74, v66, v67\n\t"
        "v_cvt_pk_bf16_f32 v75, v70, v71\n\t"
        "s_mov_b64 exec, s[56:57]\n\t"
        "v_swap_b32 v73, v74\n\t"
        "s_nop 1\n\t"
        "v_permlane16_swap_b32 v72, v74\n\t"
        "v_permlane16_swap_b32 v73, v75\n\t"
        "s_mov_b64 exec, -1\n\t"
        "buffer_store_dwordx4 v[72:75], v79, %[rs], s50 offen sc1\n\t"
        "s_waitcnt vmcnt(0)\n\t"
        "s_barrier\n\t"
        "s_and_b32 s54, s54, 0xffffff80\n\t"
        "s_add_u32 s54, s54, 63\n\t"
        "s_add_u32 s54, s54, 1\n\t"
        "v_mov_b32 v76, s54\n\t"
        "s_and_b32 s71, s54, 127\n\t"
        "s_lshl_b32 s71, s71, 7\n\t"
        "buffer_store_dwordx2 v[76:77], v78, %[rf], s71 offen sc1\n\t"
        "s_branch .Lx_end_%=\n\t"
        ".Lx_k1_%=:\n\t"
        "s_nop 7\n\t"
        "ds_write_b128 v14, v[32:35] offset:0\n\t"
        "ds_write_b128 v14, v[36:39] offset:1024\n\t"
        "s_waitcnt lgkmcnt(0)\n\t"
        "s_barrier\n\t"
        "v_mov_b32 v72, 147456\n\t"
        "ds_read_b64 v[76:77], v72\n\t"
        "ds_read_b128 v[64:67], v14 offset:8192\n\t"
        "ds_read_b128 v[68:71], v14 offset:9216\n\t"
        "s_add_u32 m0, s47, 65536\n\t"
        "s_nop 0\n\t"
        "buffer_load_dwordx4 v4, %[rx], s49 offen offset:0 lds\n\t"
        "buffer_load_dwordx4 v5, %[rx], s49 offen offset:1024 lds\n\t"
        "buffer_load_dwordx4 v6, %[rx], s49 offen offset:2048 lds\n\t"
        "buffer_load_dwordx4 v7, %[rx], s49 offen offset:3072 lds\n\t"
        "s_add_u32 s49, s49, 32768\n\t"
        "s_add_u32 m0, s47, 98304\n\t"
        "s_nop 0\n\t"
        "buffer_load_dwordx4 v4, %[rx], s49 offen offset:0 lds\n\t"
        "buffer_load_dwordx4 v5, %[rx], s49 offen offset:1024 lds\n\t"
        "buffer_load_dwordx4 v6, %[rx], s49 offen offset:2048 lds\n\t"
        "buffer_load_dwordx4 v7, %[rx], s49 offen offset:3072 lds\n\t"
        "s_add_u32 s49, s49, 32768\n\t"
        "s_waitcnt lgkmcnt(0)\n\t"
        "v_readfirstlane_b32 s54, v76\n\t"
        "v_lshrrev_b32 v78, 4, v1\n\t"
        "v_lshlrev_b32 v78, 24, v78\n\t"
        "v_lshl_add_u32 v78, v3, 3, v78\n\t"
        "s_cmp_eq_u32 %[w], 0\n\t"
        "s_cselect_b32 s70, 0, 0x40000000\n\t"
        "v_add_u32 v78, s70, v78\n\t"
        "s_and_b32 s70, s54, 128\n\t"
        "s_lshl_b32 s70, s70, 6\n\t"
        "s_xor_b32 s50, %[co], s70\n\t"
        "s_lshl_b32 s70, %[w], 10\n\t"
        "s_add_u32 s50, s50, s70\n\t"
        "s_mov_b32 s51, 0\n\t"
        "v_add_f32 v64, v64, v40\n\t"
        "v_add_f32 v65, v65, v41\n\t"
        "v_add_f32 v66, v66, v42\n\t"
        "v_add_f32 v67, v67, v43\n\t"
        "v_add_f32 v68, v68, v44\n\t"
        "v_add_f32 v69, v69, v45\n\t"
        "v_add_f32 v70, v70, v46\n\t"
        "v_add_f32 v71, v71, v47\n\t"
        "v_cvt_pk_bf16_f32 v72, v64, v65\n\t"
        "v_cvt_pk_bf16_f32 v73, v68, v69\n\t"
        "v_cvt_pk_bf16_f32 v74, v66, v67\n\t"
        "v_cvt_pk_bf16_f32 v75, v70, v71\n\t"
        "s_mov_b64 exec, s[56:57]\n\t"
        "v_swap_b32 v73, v74\n\t"
        "s_nop 1\n\t"
        "v_permlane16_swap_b32 v72, v74\n\t"
        "v_permlane16_swap_b32 v73, v75\n\t"
        "s_mov_b64 exec, -1\n\t"
        "buffer_store_dwordx4 v[72:75], v79, %[rs], s50 offen sc1\n\t"
        "s_waitcnt vmcnt(0)\n\t"
        "s_barrier\n\t"
        "s_add_u32 s54, s54, 1\n\t"
        "v_mov_b32 v76, s54\n\t"
        "s_and_b32 s71, s54, 127\n\t"
        "s_lshl_b32 s71, s71, 7\n\t"
        "buffer_store_dwordx2 v[76:77], v78, %[rf], s71 offen sc1\n\t"
        "s_add_u32 m0, s47, 0\n\t"
        "s_nop 0\n\t"
        "buffer_load_dwordx4 v4, %[rx], s49 offen offset:0 lds\n\t"
        "buffer_load_dwordx4 v5, %[rx], s49 offen offset:1024 lds\n\t"
        "buffer_load_dwordx4 v6, %[rx], s49 offen offset:2048 lds\n\t"
        "buffer_load_dwordx4 v7, %[rx], s49 offen offset:3072 lds\n\t"
        "s_add_u32 s49, s49, 32768\n\t"
        "ds_read_b128 v[16:19], v12 offset:0\n\t"
        "ds_read_b128 v[20:23], v13 offset:0\n\t"
        "ds_read_b128 v[24:27], v12 offset:2048\n\t"
        "ds_read_b128 v[28:31], v13 offset:2048\n\t"
        "s_waitcnt lgkmcnt(3)\n\t"
        "v_mfma_f32_16x16x32_bf16 v[48:51], v[128:131], v[16:19], 0\n\t"
        "v_mfma_f32_16x16x32_bf16 v[52:55], v[132:135], v[16:19], 0\n\t"
        "ds_read_b128 v[16:19], v12 offset:4096\n\t"
        "s_waitcnt lgkmcnt(3)\n\t"
        "v_mfma_f32_16x16x32_bf16 v[48:51], v[136:139], v[20:23], v[48:51]\n\t"
        "v_mfma_f32_16x16x32_bf16 v[52:55], v[140:143], v[20:23], v[52:55]\n\t"
        "ds_read_b128 v[20:23], v13 offset:4096\n\t"
        "s_waitcnt lgkmcnt(3)\n\t"
        "v_mfma_f32_16x16x32_bf16 v[48:51], v[144:147], v[24:27], v[48:51]\n\t"
        "v_mfma_f32_16x16x32_bf16 v[52:55], v[148:151], v[24:27], v[52:55]\n\t"
        "ds_read_b128 v[24:27], v12 offset:6144\n\t"
        "s_waitcnt lgkmcnt(3)\n\t"
        "v_mfma_f32_16x16x32_bf16 v[48:51], v[152:155], v[28:31], v[48:51]\n\t"
        "v_mfma_f32_16x16x32_bf16 v[52:55], v[156:159], v[28:31], v[52:55]\n\t"
        "ds_read_b128 v[28:31], v13 offset:6144\n\t"
        "s_waitcnt lgkmcnt(3)\n\t"
        "v_mfma_f32_16x16x32_bf16 v[48:51], v[160:163], v[16:19], v[48:51]\n\t"
        "v_mfma_f32_16x16x32_bf16 v[52:55], v[164:167], v[16:19], v[52:55]\n\t"
        "ds_read_b128 v[16:19], v12 offset:8192\n\t"
        "s_waitcnt lgkmcnt(3)\n\t"
        "v_mfma_f32_16x16x32_bf16 v[48:51], v[168:171], v[20:23], v[48:51]\n\t"
        "v_mfma_f32_16x16x32_bf16 v[52:55], v[172:175], v[20:23], v[52:55]\n\t"
        "ds_read_b128 v[20:23], v13 offset:8192\n\t"
        "s_waitcnt lgkmcnt(3)\n\t"
        "v_mfma_f32_16x16x32_bf16 v[48:51], v[176:179], v[24:27], v[48:51]\n\t"
        "v_mfma_f32_16x16x32_bf16 v[52:55], v[180:183], v[24:27], v[52:55]\n\t"
        "ds_read_b128 v[24:27], v12 offset:10240\n\t"
        "s_waitcnt lgkmcnt(3)\n\t"
        "v_mfma_f32_16x16x32_bf16 v[48:51], v[184:187], v[28:31], v[48:51]\n\t"
        "v_mfma_f32_16x16x32_bf16 v[52:55], v[188:191], v[28:31], v[52:55]\n\t"
        "ds_read_b128 v[28:31], v13 offset:10240\n\t"
        "s_waitcnt lgkmcnt(3)\n\t"
        "v_mfma_f32_16x16x32_bf16 v[48:51], v[192:195], v[16:19], v[48:51]\n\t"
        "v_mfma_f32_16x16x32_bf16 v[52:55], v[196:199], v[16:19], v[52:55]\n\t"
        "ds_read_b128 v[16:19], v12 offset:12288\n\t"
        "s_waitcnt lgkmcnt(3)\n\t"
        "v_mfma_f32_16x16x32_bf16 v[48:51], v[200:203], v[20:23], v[48:51]\n\t"
        "v_mfma_f32_16x16x32_bf16 v[52:55], v[204:207], v[20:23], v[52:55]\n\t"
        "ds_read_b128 v[20:23], v13 offset:12288\n\t"
        "s_waitcnt lgkmcnt(3)\n\t"
        "v_mfma_f32_16x16x32_bf16 v[48:51], v[208:211], v[24:27], v[48:51]\n\t"
        "v_mfma_f32_16x16x32_bf16 v[52:55], v[212:215], v[24:27], v[52:55]\n\t"
        "ds_read_b128 v[24:27], v12 offset:14336\n\t"
        "s_waitcnt lgkmcnt(3)\n\t"
        "v_mfma_f32_16x16x32_bf16 v[48:51], v[216:219], v[28:31], v[48:51]\n\t"
        "v_mfma_f32_16x16x32_bf16 v[52:55], v[220:223], v[28:31], v[52:55]\n\t"
        "ds_read_b128 v[28:31], v13 offset:14336\n\t"
        "s_waitcnt lgkmcnt(3)\n\t"
        "v_mfma_f32_16x16x32_bf16 v[48:51], v[224:227], v[16:19], v[48:51]\n\t"
        "v_mfma_f32_16x16x32_bf16 v[52:55], v[228:231], v[16:19], v[52:55]\n\t"
        "s_waitcnt lgkmcnt(2)\n\t"
        "v_mfma_f32_16x16x32_bf16 v[48:51], v[232:235], v[20:23], v[48:51]\n\t"
        "v_mfma_f32_16x16x32_bf16 v[52:55], v[236:239], v[20:23], v[52:55]\n\t"
        "s_waitcnt lgkmcnt(1)\n\t"
        "v_mfma_f32_16x16x32_bf16 v[48:51], v[240:243], v[24:27], v[48:51]\n\t"
        "v_mfma_f32_16x16x32_bf16 v[52:55], v[244:247], v[24:27], v[52:55]\n\t"
        "s_waitcnt lgkmcnt(0)\n\t"
        "v_mfma_f32_16x16x32_bf16 v[48:51], v[248:251], v[28:31], v[48:51]\n\t"
        "v_mfma_f32_16x16x32_bf16 v[52:55], v[252:255], v[28:31], v[52:55]\n\t"
        "s_add_u32 m0, s47, 32768\n\t"
        "s_nop 0\n\t"
        "buffer_load_dwordx4 v4, %[rx], s49 offen offset:0 lds\n\t"
        "buffer_load_dwordx4 v5, %[rx], s49 offen offset:1024 lds\n\t"
        "buffer_load_dwordx4 v6, %[rx], s49 offen offset:2048 lds\n\t"
        "buffer_load_dwordx4 v7, %[rx], s49 offen offset:3072 lds\n\t"
        "s_add_u32 s49, s49, 32768\n\t"
        "s_nop 7\n\t"
        "ds_write_b128 v14, v[48:51] offset:0\n\t"
        "ds_write_b128 v14, v[52:55] offset:1024\n\t"
        "s_waitcnt vmcnt(4) lgkmcnt(0)\n\t"
        "s_barrier\n\t"
        "ds_read_b128 v[16:19], v12 offset:32768\n\t"
        "ds_read_b128 v[20:23], v13 offset:32768\n\t"
        "ds_read_b128 v[24:27], v12 offset:34816\n\t"
        "ds_read_b128 v[28:31], v13 offset:34816\n\t"
        "s_waitcnt lgkmcnt(3)\n\t"
        "v_mfma_f32_16x16x32_bf16 v[56:59], v[128:131], v[16:19], 0\n\t"
        "v_mfma_f32_16x16x32_bf16 v[60:63], v[132:135], v[16:19], 0\n\t"
        "ds_read_b128 v[16:19], v12 offset:36864\n\t"
        "s_waitcnt lgkmcnt(3)\n\t"
        "v_mfma_f32_16x16x32_bf16 v[56:59], v[136:139], v[20:23], v[56:59]\n\t"
        "v_mfma_f32_16x16x32_bf16 v[60:63], v[140:143], v[20:23], v[60:63]\n\t"
        "ds_read_b128 v[20:23], v13 offset:36864\n\t"
        "s_waitcnt lgkmcnt(3)\n\t"
        "v_mfma_f32_16x16x32_bf16 v[56:59], v[144:147], v[24:27], v[56:59]\n\t"
        "v_mfma_f32_16x16x32_bf16 v[60:63], v[148:151], v[24:27], v[60:63]\n\t"
        "ds_read_b128 v[24:27], v12 offset:38912\n\t"
        "s_waitcnt lgkmcnt(3)\n\t"
        "v_mfma_f32_16x16x32_bf16 v[56:59], v[152:155], v[28:31], v[56:59]\n\t"
        "v_mfma_f32_16x16x32_bf16 v[60:63], v[156:159], v[28:31], v[60:63]\n\t"
        "ds_read_b128 v[28:31], v13 offset:38912\n\t"
        "s_waitcnt lgkmcnt(3)\n\t"
        "v_mfma_f32_16x16x32_bf16 v[56:59], v[160:163], v[16:19], v[56:59]\n\t"
        "v_mfma_f32_16x16x32_bf16 v[60:63], v[164:167], v[16:19], v[60:63]\n\t"
        "ds_read_b128 v[16:19], v12 offset:40960\n\t"
        "s_waitcnt lgkmcnt(3)\n\t"
        "v_mfma_f32_16x16x32_bf16 v[56:59], v[168:171], v[20:23], v[56:59]\n\t"
        "v_mfma_f32_16x16x32_bf16 v[60:63], v[172:175], v[20:23], v[60:63]\n\t"
        "ds_read_b128 v[20:23], v13 offset:40960\n\t"
        "s_waitcnt lgkmcnt(3)\n\t"
        "v_mfma_f32_16x16x32_bf16 v[56:59], v[176:179], v[24:27], v[56:59]\n\t"
        "v_mfma_f32_16x16x32_bf16 v[60:63], v[180:183], v[24:27], v[60:63]\n\t"
        "ds_read_b128 v[24:27], v12 offset:43008\n\t"
        "s_waitcnt lgkmcnt(3)\n\t"
        "v_mfma_f32_16x16x32_bf16 v[56:59], v[184:187], v[28:31], v[56:59]\n\t"
        "v_mfma_f32_16x16x32_bf16 v[60:63], v[188:191], v[28:31], v[60:63]\n\t"
        "ds_read_b128 v[28:31], v13 offset:43008\n\t"
        "s_waitcnt lgkmcnt(3)\n\t"
        "v_mfma_f32_16x16x32_bf16 v[56:59], v[192:195], v[16:19], v[56:59]\n\t"
        "v_mfma_f32_16x16x32_bf16 v[60:63], v[196:199], v[16:19], v[60:63]\n\t"
        "ds_read_b128 v[16:19], v12 offset:45056\n\t"
        "s_waitcnt lgkmcnt(3)\n\t"
        "v_mfma_f32_16x16x32_bf16 v[56:59], v[200:203], v[20:23], v[56:59]\n\t"
        "v_mfma_f32_16x16x32_bf16 v[60:63], v[204:207], v[20:23], v[60:63]\n\t"
        "ds_read_b128 v[20:23], v13 offset:45056\n\t"
        "s_waitcnt lgkmcnt(3)\n\t"
        "v_mfma_f32_16x16x32_bf16 v[56:59], v[208:211], v[24:27], v[56:59]\n\t"
        "v_mfma_f32_16x16x32_bf16 v[60:63], v[212:215], v[24:27], v[60:63]\n\t"
        "ds_read_b128 v[24:27], v12 offset:47104\n\t"
        "s_waitcnt lgkmcnt(3)\n\t"
        "v_mfma_f32_16x16x32_bf16 v[56:59], v[216:219], v[28:31], v[56:59]\n\t"
        "v_mfma_f32_16x16x32_bf16 v[60:63], v[220:223], v[28:31], v[60:63]\n\t"
        "ds_read_b128 v[28:31], v13 offset:47104\n\t"
        "s_waitcnt lgkmcnt(3)\n\t"
        "v_mfma_f32_16x16x32_bf16 v[56:59], v[224:227], v[16:19], v[56:59]\n\t"
        "v_mfma_f32_16x16x32_bf16 v[60:63], v[228:231], v[16:19], v[60:63]\n\t"
        "s_waitcnt lgkmcnt(2)\n\t"
        "v_mfma_f32_16x16x32_bf16 v[56:59], v[232:235], v[20:23], v[56:59]\n\t"
        "v_mfma_f32_16x16x32_bf16 v[60:63], v[236:239], v[20:23], v[60:63]\n\t"
        "s_waitcnt lgkmcnt(1)\n\t"
        "v_mfma_f32_16x16x32_bf16 v[56:59], v[240:243], v[24:27], v[56:59]\n\t"
        "v_mfma_f32_16x16x32_bf16 v[60:63], v[244:247], v[24:27], v[60:63]\n\t"
        "s_waitcnt lgkmcnt(0)\n\t"
        "v_mfma_f32_16x16x32_bf16 v[56:59], v[248:251], v[28:31], v[56:59]\n\t"
        "v_mfma_f32_16x16x32_bf16 v[60:63], v[252:255], v[28:31], v[60:63]\n\t"
        "s_add_u32 m0, s47, 65536\n\t"
        "s_nop 0\n\t"
        "buffer_load_dwordx4 v4, %[rx], s49 offen offset:0 lds\n\t"
        "buffer_load_dwordx4 v5, %[rx], s49 offen offset:1024 lds\n\t"
        "buffer_load_dwordx4 v6, %[rx], s49 offen offset:2048 lds\n\t"
        "buffer_load_dwordx4 v7, %[rx], s49 offen offset:3072 lds\n\t"
        "s_add_u32 s49, s49, 32768\n\t"
        "s_waitcnt vmcnt(4) lgkmcnt(0)\n\t"
        "s_barrier\n\t"
        "ds_read_b128 v[16:19], v10 offset:0\n\t"
        "ds_read_b128 v[20:23], v11 offset:0\n\t"
        "ds_read_b128 v[24:27], v10 offset:2048\n\t"
        "ds_read_b128 v[64:67], v14 offset:8192\n\t"
        "ds_read_b128 v[68:71], v14 offset:9216\n\t"
        "s_add_u32 s50, s50, 0x80000\n\t"
        "s_add_u32 s51, s51, 1\n\t"
        "s_and_b32 s70, s51, 3\n\t"
        "s_cselect_b32 s70, 0, 0x2000\n\t"
        "s_xor_b32 s50, s50, s70\n\t"
        "s_waitcnt lgkmcnt(0)\n\t"
        "v_add_f32 v64, v64, v56\n\t"
        "v_add_f32 v65, v65, v57\n\t"
        "v_add_f32 v66, v66, v58\n\t"
        "v_add_f32 v67, v67, v59\n\t"
        "v_add_f32 v68, v68, v60\n\t"
        "v_add_f32 v69, v69, v61\n\t"
        "v_add_f32 v70, v70, v62\n\t"
        "v_add_f32 v71, v71, v63\n\t"
        "v_cvt_pk_bf16_f32 v72, v64, v65\n\t"
        "v_cvt_pk_bf16_f32 v73, v68, v69\n\t"
        "v_cvt_pk_bf16_f32 v74, v66, v67\n\t"
        "v_cvt_pk_bf16_f32 v75, v70, v71\n\t"
        "s_mov_b64 exec, s[56:57]\n\t"
        "v_swap_b32 v73, v74\n\t"
        "s_nop 1\n\t"
        "v_permlane16_swap_b32 v72, v74\n\t"
        "v_permlane16_swap_b32 v73, v75\n\t"
        "s_mov_b64 exec, -1\n\t"
        "buffer_store_dwordx4 v[72:75], v79, %[rs], s50 offen sc1\n\t"
        "ds_read_b128 v[28:31], v11 offset:2048\n\t"
        "v_mfma_f32_16x16x32_bf16 v[32:35], v[128:131], v[16:19], 0\n\t"
        "v_mfma_f32_16x16x32_bf16 v[36:39], v[132:135], v[16:19], 0\n\t"
        "ds_read_b128 v[16:19], v10 offset:4096\n\t"
        "v_mfma_f32_16x16x32_bf16 v[32:35], v[136:139], v[20:23], v[32:35]\n\t"
        "v_mfma_f32_16x16x32_bf16 v[36:39], v[140:143], v[20:23], v[36:39]\n\t"
        "ds_read_b128 v[20:23], v11 offset:4096\n\t"
        "v_mfma_f32_16x16x32_bf16 v[32:35], v[144:147], v[24:27], v[32:35]\n\t"
        "v_mfma_f32_16x16x32_bf16 v[36:39], v[148:151], v[24:27], v[36:39]\n\t"
        "ds_read_b128 v[24:27], v10 offset:6144\n\t"
        "s_waitcnt lgkmcnt(3)\n\t"
        "v_mfma_f32_16x16x32_bf16 v[32:35], v[152:155], v[28:31], v[32:35]\n\t"
        "v_mfma_f32_16x16x32_bf16 v[36:39], v[156:159], v[28:31], v[36:39]\n\t"
        "ds_read_b128 v[28:31], v11 offset:6144\n\t"
        "s_waitcnt lgkmcnt(3)\n\t"
        "v_mfma_f32_16x16x32_bf16 v[32:35], v[160:163], v[16:19], v[32:35]\n\t"
        "v_mfma_f32_16x16x32_bf16 v[36:39], v[164:167], v[16:19], v[36:39]\n\t"
        "ds_read_b128 v[16:19], v10 offset:8192\n\t"
        "s_waitcnt lgkmcnt(3)\n\t"
        "v_mfma_f32_16x16x32_bf16 v[32:35], v[168:171], v[20:23], v[32:35]\n\t"
        "v_mfma_f32_16x16x32_bf16 v[36:39], v[172:175], v[20:23], v[36:39]\n\t"
        "ds_read_b128 v[20:23], v11 offset:8192\n\t"
        "s_waitcnt lgkmcnt(3)\n\t"
        "v_mfma_f32_16x16x32_bf16 v[32:35], v[176:179], v[24:27], v[32:35]\n\t"
        "v_mfma_f32_16x16x32_bf16 v[36:39], v[180:183], v[24:27], v[36:39]\n\t"
        "ds_read_b128 v[24:27], v10 offset:10240\n\t"
        "s_waitcnt lgkmcnt(3)\n\t"
        "v_mfma_f32_16x16x32_bf16 v[32:35], v[184:187], v[28:31], v[32:35]\n\t"
        "v_mfma_f32_16x16x32_bf16 v[36:39], v[188:191], v[28:31], v[36:39]\n\t"
        "ds_read_b128 v[28:31], v11 offset:10240\n\t"
        "s_waitcnt lgkmcnt(3)\n\t"
        "v_mfma_f32_16x16x32_bf16 v[32:35], v[192:195], v[16:19], v[32:35]\n\t"
        "v_mfma_f32_16x16x32_bf16 v[36:39], v[196:199], v[16:19], v[36:39]\n\t"
        "ds_read_b128 v[16:19], v10 offset:12288\n\t"
        "s_waitcnt lgkmcnt(3)\n\t"
        "v_mfma_f32_16x16x32_bf16 v[32:35], v[200:203], v[20:23], v[32:35]\n\t"
        "v_mfma_f32_16x16x32_bf16 v[36:39], v[204:207], v[20:23], v[36:39]\n\t"
        "ds_read_b128 v[20:23], v11 offset:12288\n\t"
        "s_waitcnt lgkmcnt(3)\n\t"
        "v_mfma_f32_16x16x32_bf16 v[32:35], v[208:211], v[24:27], v[32:35]\n\t"
        "v_mfma_f32_16x16x32_bf16 v[36:39], v[212:215], v[24:27], v[36:39]\n\t"
        "ds_read_b128 v[24:27], v10 offset:14336\n\t"
        "s_waitcnt lgkmcnt(3)\n\t"
        "v_mfma_f32_16x16x32_bf16 v[32:35], v[216:219], v[28:31], v[32:35]\n\t"
        "v_mfma_f32_16x16x32_bf16 v[36:39], v[220:223], v[28:31], v[36:39]\n\t"
        "ds_read_b128 v[28:31], v11 offset:14336\n\t"
        "s_waitcnt lgkmcnt(3)\n\t"
        "v_mfma_f32_16x16x32_bf16 v[32:35], v[224:227], v[16:19], v[32:35]\n\t"
        "v_mfma_f32_16x16x32_bf16 v[36:39], v[228:231], v[16:19], v[36:39]\n\t"
        "s_waitcnt lgkmcnt(2)\n\t"
        "v_mfma_f32_16x16x32_bf16 v[32:35], v[232:235], v[20:23], v[32:35]\n\t"
        "v_mfma_f32_16x16x32_bf16 v[36:39], v[236:239], v[20:23], v[36:39]\n\t"
        "s_waitcnt lgkmcnt(1)\n\t"
        "v_mfma_f32_16x16x32_bf16 v[32:35], v[240:243], v[24:27], v[32:35]\n\t"
        "v_mfma_f32_16x16x32_bf16 v[36:39], v[244:247], v[24:27], v[36:39]\n\t"
        "s_waitcnt lgkmcnt(0)\n\t"
        "v_mfma_f32_16x16x32_bf16 v[32:35], v[248:251], v[28:31], v[32:35]\n\t"
        "v_mfma_f32_16x16x32_bf16 v[36:39], v[252:255], v[28:31], v[36:39]\n\t"
        "s_add_u32 m0, s47, 98304\n\t"
        "s_nop 0\n\t"
        "buffer_load_dwordx4 v4, %[rx], s49 offen offset:0 lds\n\t"
        "buffer_load_dwordx4 v5, %[rx], s49 offen offset:1024 lds\n\t"
        "buffer_load_dwordx4 v6, %[rx], s49 offen offset:2048 lds\n\t"
        "buffer_load_dwordx4 v7, %[rx], s49 offen offset:3072 lds\n\t"
        "s_add_u32 s49, s49, 32768\n\t"
        "s_nop 7\n\t"
        "ds_write_b128 v14, v[32:35] offset:0\n\t"
        "ds_write_b128 v14, v[36:39] offset:1024\n\t"
        "s_waitcnt vmcnt(4) lgkmcnt(0)\n\t"
        "s_barrier\n\t"
        "s_add_u32 s54, s54, 1\n\t"
        "v_mov_b32 v76, s54\n\t"
        "s_and_b32 s71, s54, 127\n\t"
        "s_lshl_b32 s71, s71, 7\n\t"
        "buffer_store_dwordx2 v[76:77], v78, %[rf], s71 offen sc1\n\t"
        "ds_read_b128 v[16:19], v10 offset:32768\n\t"
        "ds_read_b128 v[20:23], v11 offset:32768\n\t"
        "ds_read_b128 v[24:27], v10 offset:34816\n\t"
        "ds_read_b128 v[28:31], v11 offset:34816\n\t"
        "s_waitcnt lgkmcnt(3)\n\t"
        "v_mfma_f32_16x16x32_bf16 v[40:43], v[128:131], v[16:19], 0\n\t"
        "v_mfma_f32_16x16x32_bf16 v[44:47], v[132:135], v[16:19], 0\n\t"
        "ds_read_b128 v[16:19], v10 offset:36864\n\t"
        "s_waitcnt lgkmcnt(3)\n\t"
        "v_mfma_f32_16x16x32_bf16 v[40:43], v[136:139], v[20:23], v[40:43]\n\t"
        "v_mfma_f32_16x16x32_bf16 v[44:47], v[140:143], v[20:23], v[44:47]\n\t"
        "ds_read_b128 v[20:23], v11 offset:36864\n\t"
        "s_waitcnt lgkmcnt(3)\n\t"
        "v_mfma_f32_16x16x32_bf16 v[40:43], v[144:147], v[24:27], v[40:43]\n\t"
        "v_mfma_f32_16x16x32_bf16 v[44:47], v[148:151], v[24:27], v[44:47]\n\t"
        "ds_read_b128 v[24:27], v10 offset:38912\n\t"
        "s_waitcnt lgkmcnt(3)\n\t"
        "v_mfma_f32_16x16x32_bf16 v[40:43], v[152:155], v[28:31], v[40:43]\n\t"
        "v_mfma_f32_16x16x32_bf16 v[44:47], v[156:159], v[28:31], v[44:47]\n\t"
        "ds_read_b128 v[28:31], v11 offset:38912\n\t"
        "s_waitcnt lgkmcnt(3)\n\t"
        "v_mfma_f32_16x16x32_bf16 v[40:43], v[160:163], v[16:19], v[40:43]\n\t"
        "v_mfma_f32_16x16x32_bf16 v[44:47], v[164:167], v[16:19], v[44:47]\n\t"
        "ds_read_b128 v[16:19], v10 offset:40960\n\t"
        "s_waitcnt lgkmcnt(3)\n\t"
        "v_mfma_f32_16x16x32_bf16 v[40:43], v[168:171], v[20:23], v[40:43]\n\t"
        "v_mfma_f32_16x16x32_bf16 v[44:47], v[172:175], v[20:23], v[44:47]\n\t"
        "ds_read_b128 v[20:23], v11 offset:40960\n\t"
        "s_waitcnt lgkmcnt(3)\n\t"
        "v_mfma_f32_16x16x32_bf16 v[40:43], v[176:179], v[24:27], v[40:43]\n\t"
        "v_mfma_f32_16x16x32_bf16 v[44:47], v[180:183], v[24:27], v[44:47]\n\t"
        "ds_read_b128 v[24:27], v10 offset:43008\n\t"
        "s_waitcnt lgkmcnt(3)\n\t"
        "v_mfma_f32_16x16x32_bf16 v[40:43], v[184:187], v[28:31], v[40:43]\n\t"
        "v_mfma_f32_16x16x32_bf16 v[44:47], v[188:191], v[28:31], v[44:47]\n\t"
        "ds_read_b128 v[28:31], v11 offset:43008\n\t"
        "s_waitcnt lgkmcnt(3)\n\t"
        "v_mfma_f32_16x16x32_bf16 v[40:43], v[192:195], v[16:19], v[40:43]\n\t"
        "v_mfma_f32_16x16x32_bf16 v[44:47], v[196:199], v[16:19], v[44:47]\n\t"
        "ds_read_b128 v[16:19], v10 offset:45056\n\t"
        "s_waitcnt lgkmcnt(3)\n\t"
        "v_mfma_f32_16x16x32_bf16 v[40:43], v[200:203], v[20:23], v[40:43]\n\t"
        "v_mfma_f32_16x16x32_bf16 v[44:47], v[204:207], v[20:23], v[44:47]\n\t"
        "ds_read_b128 v[20:23], v11 offset:45056\n\t"
        "s_waitcnt lgkmcnt(3)\n\t"
        "v_mfma_f32_16x16x32_bf16 v[40:43], v[208:211], v[24:27], v[40:43]\n\t"
        "v_mfma_f32_16x16x32_bf16 v[44:47], v[212:215], v[24:27], v[44:47]\n\t"
        "ds_read_b128 v[24:27], v10 offset:47104\n\t"
        "s_waitcnt lgkmcnt(3)\n\t"
        "v_mfma_f32_16x16x32_bf16 v[40:43], v[216:219], v[28:31], v[40:43]\n\t"
        "v_mfma_f32_16x16x32_bf16 v[44:47], v[220:223], v[28:31], v[44:47]\n\t"
        "ds_read_b128 v[28:31], v11 offset:47104\n\t"
        "s_waitcnt lgkmcnt(3)\n\t"
        "v_mfma_f32_16x16x32_bf16 v[40:43], v[224:227], v[16:19], v[40:43]\n\t"
        "v_mfma_f32_16x16x32_bf16 v[44:47], v[228:231], v[16:19], v[44:47]\n\t"
        "s_waitcnt lgkmcnt(2)\n\t"
        "v_mfma_f32_16x16x32_bf16 v[40:43], v[232:235], v[20:23], v[40:43]\n\t"
        "v_mfma_f32_16x16x32_bf16 v[44:47], v[236:239], v[20:23], v[44:47]\n\t"
        "s_waitcnt lgkmcnt(1)\n\t"
        "v_mfma_f32_16x16x32_bf16 v[40:43], v[240:243], v[24:27], v[40:43]\n\t"
        "v_mfma_f32_16x16x32_bf16 v[44:47], v[244:247], v[24:27], v[44:47]\n\t"
        "s_waitcnt lgkmcnt(0)\n\t"
        "v_mfma_f32_16x16x32_bf16 v[40:43], v[248:251], v[28:31], v[40:43]\n\t"
        "v_mfma_f32_16x16x32_bf16 v[44:47], v[252:255], v[28:31], v[44:47]\n\t"
        "s_add_u32 m0, s47, 0\n\t"
        "s_nop 0\n\t"
        "buffer_load_dwordx4 v4, %[rx], s49 offen offset:0 lds\n\t"
        "buffer_load_dwordx4 v5, %[rx], s49 offen offset:1024 lds\n\t"
        "buffer_load_dwordx4 v6, %[rx], s49 offen offset:2048 lds\n\t"
        "buffer_load_dwordx4 v7, %[rx], s49 offen offset:3072 lds\n\t"
        "s_add_u32 s49, s49, 32768\n\t"
        "s_waitcnt vmcnt(4) lgkmcnt(0)\n\t"
        "s_barrier\n\t"
        "ds_read_b128 v[16:19], v12 offset:0\n\t"
        "ds_read_b128 v[20:23], v13 offset:0\n\t"
        "ds_read_b128 v[24:27], v12 offset:2048\n\t"
        "ds_read_b128 v[64:67], v14 offset:8192\n\t"
        "ds_read_b128 v[68:71], v14 offset:9216\n\t"
        "s_add_u32 s50, s50, 0x80000\n\t"
        "s_add_u32 s51, s51, 1\n\t"
        "s_and_b32 s70, s51, 3\n\t"
        "s_cselect_b32 s70, 0, 0x2000\n\t"
        "s_xor_b32 s50, s50, s70\n\t"
        "s_waitcnt lgkmcnt(0)\n\t"
        "v_add_f32 v64, v64, v40\n\t"
        "v_add_f32 v65, v65, v41\n\t"
        "v_add_f32 v66, v66, v42\n\t"
        "v_add_f32 v67, v67, v43\n\t"
        "v_add_f32 v68, v68, v44\n\t"
        "v_add_f32 v69, v69, v45\n\t"
        "v_add_f32 v70, v70, v46\n\t"
        "v_add_f32 v71, v71, v47\n\t"
        "v_cvt_pk_bf16_f32 v72, v64, v65\n\t"
        "v_cvt_pk_bf16_f32 v73, v68, v69\n\t"
        "v_cvt_pk_bf16_f32 v74, v66, v67\n\t"
        "v_cvt_pk_bf16_f32 v75, v70, v71\n\t"
        "s_mov_b64 exec, s[56:57]\n\t"
        "v_swap_b32 v73, v74\n\t"
        "s_nop 1\n\t"
        "v_permlane16_swap_b32 v72, v74\n\t"
        "v_permlane16_swap_b32 v73, v75\n\t"
        "s_mov_b64 exec, -1\n\t"
        "buffer_store_dwordx4 v[72:75], v79, %[rs], s50 offen sc1\n\t"
        "ds_read_b128 v[28:31], v13 offset:2048\n\t"
        "v_mfma_f32_16x16x32_bf16 v[48:51], v[128:131], v[16:19], 0\n\t"
        "v_mfma_f32_16x16x32_bf16 v[52:55], v[132:135], v[16:19], 0\n\t"
        "ds_read_b128 v[16:19], v12 offset:4096\n\t"
        "v_mfma_f32_16x16x32_bf16 v[48:51], v[136:139], v[20:23], v[48:51]\n\t"
        "v_mfma_f32_16x16x32_bf16 v[52:55], v[140:143], v[20:23], v[52:55]\n\t"
        "ds_read_b128 v[20:23], v13 offset:4096\n\t"
        "v_mfma_f32_16x16x32_bf16 v[48:51], v[144:147], v[24:27], v[48:51]\n\t"
        "v_mfma_f32_16x16x32_bf16 v[52:55], v[148:151], v[24:27], v[52:55]\n\t"
        "ds_read_b128 v[24:27], v12 offset:6144\n\t"
        "s_waitcnt lgkmcnt(3)\n\t"
        "v_mfma_f32_16x16x32_bf16 v[48:51], v[152:155], v[28:31], v[48:51]\n\t"
        "v_mfma_f32_16x16x32_bf16 v[52:55], v[156:159], v[28:31], v[52:55]\n\t"
        "ds_read_b128 v[28:31], v13 offset:6144\n\t"
        "s_waitcnt lgkmcnt(3)\n\t"
        "v_mfma_f32_16x16x32_bf16 v[48:51], v[160:163], v[16:19], v[48:51]\n\t"
        "v_mfma_f32_16x16x32_bf16 v[52:55], v[164:167], v[16:19], v[52:55]\n\t"
        "ds_read_b128 v[16:19], v12 offset:8192\n\t"
        "s_waitcnt lgkmcnt(3)\n\t"
        "v_mfma_f32_16x16x32_bf16 v[48:51], v[168:171], v[20:23], v[48:51]\n\t"
        "v_mfma_f32_16x16x32_bf16 v[52:55], v[172:175], v[20:23], v[52:55]\n\t"
        "ds_read_b128 v[20:23], v13 offset:8192\n\t"
        "s_waitcnt lgkmcnt(3)\n\t"
        "v_mfma_f32_16x16x32_bf16 v[48:51], v[176:179], v[24:27], v[48:51]\n\t"
        "v_mfma_f32_16x16x32_bf16 v[52:55], v[180:183], v[24:27], v[52:55]\n\t"
        "ds_read_b128 v[24:27], v12 offset:10240\n\t"
        "s_waitcnt lgkmcnt(3)\n\t"
        "v_mfma_f32_16x16x32_bf16 v[48:51], v[184:187], v[28:31], v[48:51]\n\t"
        "v_mfma_f32_16x16x32_bf16 v[52:55], v[188:191], v[28:31], v[52:55]\n\t"
        "ds_read_b128 v[28:31], v13 offset:10240\n\t"
        "s_waitcnt lgkmcnt(3)\n\t"
        "v_mfma_f32_16x16x32_bf16 v[48:51], v[192:195], v[16:19], v[48:51]\n\t"
        "v_mfma_f32_16x16x32_bf16 v[52:55], v[196:199], v[16:19], v[52:55]\n\t"
        "ds_read_b128 v[16:19], v12 offset:12288\n\t"
        "s_waitcnt lgkmcnt(3)\n\t"
        "v_mfma_f32_16x16x32_bf16 v[48:51], v[200:203], v[20:23], v[48:51]\n\t"
        "v_mfma_f32_16x16x32_bf16 v[52:55], v[204:207], v[20:23], v[52:55]\n\t"
        "ds_read_b128 v[20:23], v13 offset:12288\n\t"
        "s_waitcnt lgkmcnt(3)\n\t"
        "v_mfma_f32_16x16x32_bf16 v[48:51], v[208:211], v[24:27], v[48:51]\n\t"
        "v_mfma_f32_16x16x32_bf16 v[52:55], v[212:215], v[24:27], v[52:55]\n\t"
        "ds_read_b128 v[24:27], v12 offset:14336\n\t"
        "s_waitcnt lgkmcnt(3)\n\t"
        "v_mfma_f32_16x16x32_bf16 v[48:51], v[216:219], v[28:31], v[48:51]\n\t"
        "v_mfma_f32_16x16x32_bf16 v[52:55], v[220:223], v[28:31], v[52:55]\n\t"
        "ds_read_b128 v[28:31], v13 offset:14336\n\t"
        "s_waitcnt lgkmcnt(3)\n\t"
        "v_mfma_f32_16x16x32_bf16 v[48:51], v[224:227], v[16:19], v[48:51]\n\t"
        "v_mfma_f32_16x16x32_bf16 v[52:55], v[228:231], v[16:19], v[52:55]\n\t"
        "s_waitcnt lgkmcnt(2)\n\t"
        "v_mfma_f32_16x16x32_bf16 v[48:51], v[232:235], v[20:23], v[48:51]\n\t"
        "v_mfma_f32_16x16x32_bf16 v[52:55], v[236:239], v[20:23], v[52:55]\n\t"
        "s_waitcnt lgkmcnt(1)\n\t"
        "v_mfma_f32_16x16x32_bf16 v[48:51], v[240:243], v[24:27], v[48:51]\n\t"
        "v_mfma_f32_16x16x32_bf16 v[52:55], v[244:247], v[24:27], v[52:55]\n\t"
        "s_waitcnt lgkmcnt(0)\n\t"
        "v_mfma_f32_16x16x32_bf16 v[48:51], v[248:251], v[28:31], v[48:51]\n\t"
        "v_mfma_f32_16x16x32_bf16 v[52:55], v[252:255], v[28:31], v[52:55]\n\t"
        "s_add_u32 m0, s47, 32768\n\t"
        "s_nop 0\n\t"
        "buffer_load_dwordx4 v4, %[rx], s49 offen offset:0 lds\n\t"
        "buffer_load_dwordx4 v5, %[rx], s49 offen offset:1024 lds\n\t"
        "buffer_load_dwordx4 v6, %[rx], s49 offen offset:2048 lds\n\t"
        "buffer_load_dwordx4 v7, %[rx], s49 offen offset:3072 lds\n\t"
        "s_add_u32 s49, s49, 32768\n\t"
        "s_nop 7\n\t"
        "ds_write_b128 v14, v[48:51] offset:0\n\t"
        "ds_write_b128 v14, v[52:55] offset:1024\n\t"
        "s_waitcnt vmcnt(4) lgkmcnt(0)\n\t"
        "s_barrier\n\t"
        "s_add_u32 s54, s54, 1\n\t"
        "v_mov_b32 v76, s54\n\t"
        "s_and_b32 s71, s54, 127\n\t"
        "s_lshl_b32 s71, s71, 7\n\t"
        "buffer_store_dwordx2 v[76:77], v78, %[rf], s71 offen sc1\n\t"
        "ds_read_b128 v[16:19], v12 offset:32768\n\t"
        "ds_read_b128 v[20:23], v13 offset:32768\n\t"
        "ds_read_b128 v[24:27], v12 offset:34816\n\t"
        "ds_read_b128 v[28:31], v13 offset:34816\n\t"
        "s_waitcnt lgkmcnt(3)\n\t"
        "v_mfma_f32_16x16x32_bf16 v[56:59], v[128:131], v[16:19], 0\n\t"
        "v_mfma_f32_16x16x32_bf16 v[60:63], v[132:135], v[16:19], 0\n\t"
        "ds_read_b128 v[16:19], v12 offset:36864\n\t"
        "s_waitcnt lgkmcnt(3)\n\t"
        "v_mfma_f32_16x16x32_bf16 v[56:59], v[136:139], v[20:23], v[56:59]\n\t"
        "v_mfma_f32_16x16x32_bf16 v[60:63], v[140:143], v[20:23], v[60:63]\n\t"
        "ds_read_b128 v[20:23], v13 offset:36864\n\t"
        "s_waitcnt lgkmcnt(3)\n\t"
        "v_mfma_f32_16x16x32_bf16 v[56:59], v[144:147], v[24:27], v[56:59]\n\t"
        "v_mfma_f32_16x16x32_bf16 v[60:63], v[148:151], v[24:27], v[60:63]\n\t"
        "ds_read_b128 v[24:27], v12 offset:38912\n\t"
        "s_waitcnt lgkmcnt(3)\n\t"
        "v_mfma_f32_16x16x32_bf16 v[56:59], v[152:155], v[28:31], v[56:59]\n\t"
        "v_mfma_f32_16x16x32_bf16 v[60:63], v[156:159], v[28:31], v[60:63]\n\t"
        "ds_read_b128 v[28:31], v13 offset:38912\n\t"
        "s_waitcnt lgkmcnt(3)\n\t"
        "v_mfma_f32_16x16x32_bf16 v[56:59], v[160:163], v[16:19], v[56:59]\n\t"
        "v_mfma_f32_16x16x32_bf16 v[60:63], v[164:167], v[16:19], v[60:63]\n\t"
        "ds_read_b128 v[16:19], v12 offset:40960\n\t"
        "s_waitcnt lgkmcnt(3)\n\t"
        "v_mfma_f32_16x16x32_bf16 v[56:59], v[168:171], v[20:23], v[56:59]\n\t"
        "v_mfma_f32_16x16x32_bf16 v[60:63], v[172:175], v[20:23], v[60:63]\n\t"
        "ds_read_b128 v[20:23], v13 offset:40960\n\t"
        "s_waitcnt lgkmcnt(3)\n\t"
        "v_mfma_f32_16x16x32_bf16 v[56:59], v[176:179], v[24:27], v[56:59]\n\t"
        "v_mfma_f32_16x16x32_bf16 v[60:63], v[180:183], v[24:27], v[60:63]\n\t"
        "ds_read_b128 v[24:27], v12 offset:43008\n\t"
        "s_waitcnt lgkmcnt(3)\n\t"
        "v_mfma_f32_16x16x32_bf16 v[56:59], v[184:187], v[28:31], v[56:59]\n\t"
        "v_mfma_f32_16x16x32_bf16 v[60:63], v[188:191], v[28:31], v[60:63]\n\t"
        "ds_read_b128 v[28:31], v13 offset:43008\n\t"
        "s_waitcnt lgkmcnt(3)\n\t"
        "v_mfma_f32_16x16x32_bf16 v[56:59], v[192:195], v[16:19], v[56:59]\n\t"
        "v_mfma_f32_16x16x32_bf16 v[60:63], v[196:199], v[16:19], v[60:63]\n\t"
        "ds_read_b128 v[16:19], v12 offset:45056\n\t"
        "s_waitcnt lgkmcnt(3)\n\t"
        "v_mfma_f32_16x16x32_bf16 v[56:59], v[200:203], v[20:23], v[56:59]\n\t"
        "v_mfma_f32_16x16x32_bf16 v[60:63], v[204:207], v[20:23], v[60:63]\n\t"
        "ds_read_b128 v[20:23], v13 offset:45056\n\t"
        "s_waitcnt lgkmcnt(3)\n\t"
        "v_mfma_f32_16x16x32_bf16 v[56:59], v[208:211], v[24:27], v[56:59]\n\t"
        "v_mfma_f32_16x16x32_bf16 v[60:63], v[212:215], v[24:27], v[60:63]\n\t"
        "ds_read_b128 v[24:27], v12 offset:47104\n\t"
        "s_waitcnt lgkmcnt(3)\n\t"
        "v_mfma_f32_16x16x32_bf16 v[56:59], v[216:219], v[28:31], v[56:59]\n\t"
        "v_mfma_f32_16x16x32_bf16 v[60:63], v[220:223], v[28:31], v[60:63]\n\t"
        "ds_read_b128 v[28:31], v13 offset:47104\n\t"
        "s_waitcnt lgkmcnt(3)\n\t"
        "v_mfma_f32_16x16x32_bf16 v[56:59], v[224:227], v[16:19], v[56:59]\n\t"
        "v_mfma_f32_16x16x32_bf16 v[60:63], v[228:231], v[16:19], v[60:63]\n\t"
        "s_waitcnt lgkmcnt(2)\n\t"
        "v_mfma_f32_16x16x32_bf16 v[56:59], v[232:235], v[20:23], v[56:59]\n\t"
        "v_mfma_f32_16x16x32_bf16 v[60:63], v[236:239], v[20:23], v[60:63]\n\t"
        "s_waitcnt lgkmcnt(1)\n\t"
        "v_mfma_f32_16x16x32_bf16 v[56:59], v[240:243], v[24:27], v[56:59]\n\t"
        "v_mfma_f32_16x16x32_bf16 v[60:63], v[244:247], v[24:27], v[60:63]\n\t"
        "s_waitcnt lgkmcnt(0)\n\t"
        "v_mfma_f32_16x16x32_bf16 v[56:59], v[248:251], v[28:31], v[56:59]\n\t"
        "v_mfma_f32_16x16x32_bf16 v[60:63], v[252:255], v[28:31], v[60:63]\n\t"
        "s_add_u32 m0, s47, 65536\n\t"
        "s_nop 0\n\t"
        "buffer_load_dwordx4 v4, %[rx], s49 offen offset:0 lds\n\t"
        "buffer_load_dwordx4 v5, %[rx], s49 offen offset:1024 lds\n\t"
        "buffer_load_dwordx4 v6, %[rx], s49 offen offset:2048 lds\n\t"
        "buffer_load_dwordx4 v7, %[rx], s49 offen offset:3072 lds\n\t"
        "s_add_u32 s49, s49, 32768\n\t"
        "s_waitcnt vmcnt(4) lgkmcnt(0)\n\t"
        "s_barrier\n\t"
        "s_mov_b32 s52, 30\n\t"
        "s_sub_u32 s54, s54, 1\n\t"
        "v_add_u32 v81, s47, v0\n\t"
        "v_add_u32 v82, 65536, v81\n\t"
        "ds_read_b128 v[16:19], v10 offset:0\n\t"
        "ds_read_b128 v[20:23], v11 offset:0\n\t"
        "ds_read_b128 v[24:27], v10 offset:2048\n\t"
        ".p2align 6\n\t"
        ".Lx_loop1_%=:\n\t"
        "ds_read_b128 v[28:31], v11 offset:2048\n\t"
        "s_waitcnt lgkmcnt(3)\n\t"
        "v_mfma_f32_16x16x32_bf16 v[32:35], v[128:131], v[16:19], 0\n\t"
        "s_add_u32 m0, s47, 98304\n\t"
        "v_mfma_f32_16x16x32_bf16 v[36:39], v[132:135], v[16:19], 0\n\t"
        "ds_read_b128 v[16:19], v10 offset:4096\n\t"
        "s_waitcnt lgkmcnt(3)\n\t"
        "v_mfma_f32_16x16x32_bf16 v[32:35], v[136:139], v[20:23], v[32:35]\n\t"
        "buffer_load_dwordx4 v4, %[rx], s49 offen offset:0 lds\n\t"
        "v_mfma_f32_16x16x32_bf16 v[36:39], v[140:143], v[20:23], v[36:39]\n\t"
        "ds_read_b128 v[20:23], v11 offset:4096\n\t"
        "s_waitcnt lgkmcnt(3)\n\t"
        "v_mfma_f32_16x16x32_bf16 v[32:35], v[144:147], v[24:27], v[32:35]\n\t"
        "v_mfma_f32_16x16x32_bf16 v[36:39], v[148:151], v[24:27], v[36:39]\n\t"
        "ds_read_b128 v[24:27], v10 offset:6144\n\t"
        "s_waitcnt lgkmcnt(3)\n\t"
        "v_mfma_f32_16x16x32_bf16 v[32:35], v[152:155], v[28:31], v[32:35]\n\t"
        "buffer_load_dwordx4 v5, %[rx], s49 offen offset:1024 lds\n\t"
        "v_mfma_f32_16x16x32_bf16 v[36:39], v[156:159], v[28:31], v[36:39]\n\t"
        "ds_read_b128 v[28:31], v11 offset:6144\n\t"
        "s_waitcnt lgkmcnt(3)\n\t"
        "v_mfma_f32_16x16x32_bf16 v[32:35], v[160:163], v[16:19], v[32:35]\n\t"
        "v_mfma_f32_16x16x32_bf16 v[36:39], v[164:167], v[16:19], v[36:39]\n\t"
        "ds_read_b128 v[16:19], v10 offset:8192\n\t"
        "s_waitcnt lgkmcnt(3)\n\t"
        "v_mfma_f32_16x16x32_bf16 v[32:35], v[168:171], v[20:23], v[32:35]\n\t"
        "buffer_load_dwordx4 v6, %[rx], s49 offen offset:2048 lds\n\t"
        "v_mfma_f32_16x16x32_bf16 v[36:39], v[172:175], v[20:23], v[36:39]\n\t"
        "ds_read_b128 v[20:23], v11 offset:8192\n\t"
        "s_waitcnt lgkmcnt(3)\n\t"
        "v_mfma_f32_16x16x32_bf16 v[32:35], v[176:179], v[24:27], v[32:35]\n\t"
        "v_mfma_f32_16x16x32_bf16 v[36:39], v[180:183], v[24:27], v[36:39]\n\t"
        "ds_read_b128 v[24:27], v10 offset:10240\n\t"
        "s_waitcnt lgkmcnt(3)\n\t"
        "v_mfma_f32_16x16x32_bf16 v[32:35], v[184:187], v[28:31], v[32:35]\n\t"
        "buffer_load_dwordx4 v7, %[rx], s49 offen offset:3072 lds\n\t"
        "v_mfma_f32_16x16x32_bf16 v[36:39], v[188:191], v[28:31], v[36:39]\n\t"
        "ds_read_b128 v[28:31], v11 offset:10240\n\t"
        "s_waitcnt lgkmcnt(3)\n\t"
        "v_mfma_f32_16x16x32_bf16 v[32:35], v[192:195], v[16:19], v[32:35]\n\t"
        "s_add_u32 s49, s49, 32768\n\t"
        "v_mfma_f32_16x16x32_bf16 v[36:39], v[196:199], v[16:19], v[36:39]\n\t"
        "ds_read_b128 v[16:19], v10 offset:12288\n\t"
        "s_waitcnt lgkmcnt(3)\n\t"
        "v_mfma_f32_16x16x32_bf16 v[32:35], v[200:203], v[20:23], v[32:35]\n\t"
        "v_mfma_f32_16x16x32_bf16 v[36:39], v[204:207], v[20:23], v[36:39]\n\t"
        "ds_read_b128 v[20:23], v11 offset:12288\n\t"
        "s_waitcnt lgkmcnt(3)\n\t"
        "v_mfma_f32_16x16x32_bf16 v[32:35], v[208:211], v[24:27], v[32:35]\n\t"
        "v_mfma_f32_16x16x32_bf16 v[36:39], v[212:215], v[24:27], v[36:39]\n\t"
        "ds_read_b128 v[24:27], v10 offset:14336\n\t"
        "s_waitcnt lgkmcnt(3)\n\t"
        "v_mfma_f32_16x16x32_bf16 v[32:35], v[216:219], v[28:31], v[32:35]\n\t"
        "v_mfma_f32_16x16x32_bf16 v[36:39], v[220:223], v[28:31], v[36:39]\n\t"
        "ds_read_b128 v[28:31], v11 offset:14336\n\t"
        "s_waitcnt lgkmcnt(3)\n\t"
        "v_mfma_f32_16x16x32_bf16 v[32:35], v[224:227], v[16:19], v[32:35]\n\t"
        "v_mfma_f32_16x16x32_bf16 v[36:39], v[228:231], v[16:19], v[36:39]\n\t"
        "ds_read_b128 v[16:19], v10 offset:32768\n\t"
        "s_waitcnt lgkmcnt(3)\n\t"
        "v_mfma_f32_16x16x32_bf16 v[32:35], v[232:235], v[20:23], v[32:35]\n\t"
        "v_mfma_f32_16x16x32_bf16 v[36:39], v[236:239], v[20:23], v[36:39]\n\t"
        "ds_read_b128 v[20:23], v11 offset:32768\n\t"
        "s_waitcnt lgkmcnt(3)\n\t"
        "v_mfma_f32_16x16x32_bf16 v[32:35], v[240:243], v[24:27], v[32:35]\n\t"
        "v_mfma_f32_16x16x32_bf16 v[36:39], v[244:247], v[24:27], v[36:39]\n\t"
        "ds_read_b128 v[24:27], v10 offset:34816\n\t"
        "s_waitcnt lgkmcnt(3)\n\t"
        "v_mfma_f32_16x16x32_bf16 v[32:35], v[248:251], v[28:31], v[32:35]\n\t"
        "v_mfma_f32_16x16x32_bf16 v[36:39], v[252:255], v[28:31], v[36:39]\n\t"
        "s_waitcnt vmcnt(4)\n\t"
        "s_barrier\n\t"
        "ds_read_b128 v[28:31], v11 offset:34816\n\t"
        "s_waitcnt lgkmcnt(3)\n\t"
        "v_mfma_f32_16x16x32_bf16 v[40:43], v[128:131], v[16:19], 0\n\t"
        "s_add_u32 m0, s47, 0\n\t"
        "ds_read_b128 v[64:67], v14 offset:8192\n\t"
        "v_mfma_f32_16x16x32_bf16 v[44:47], v[132:135], v[16:19], 0\n\t"
        "ds_read_b128 v[68:71], v14 offset:9216\n\t"
        "ds_read_b128 v[16:19], v10 offset:36864\n\t"
        "s_waitcnt lgkmcnt(5)\n\t"
        "v_mfma_f32_16x16x32_bf16 v[40:43], v[136:139], v[20:23], v[40:43]\n\t"
        "buffer_load_dwordx4 v4, %[rx], s49 offen offset:0 lds\n\t"
        "ds_write_b128 v14, v[32:35] offset:0\n\t"
        "v_mfma_f32_16x16x32_bf16 v[44:47], v[140:143], v[20:23], v[44:47]\n\t"
        "s_add_u32 s50, s50, 0x80000\n\t"
        "s_add_u32 s51, s51, 1\n\t"
        "ds_read_b128 v[20:23], v11 offset:36864\n\t"
        "s_waitcnt lgkmcnt(6)\n\t"
        "v_mfma_f32_16x16x32_bf16 v[40:43], v[144:147], v[24:27], v[40:43]\n\t"
        "ds_write_b128 v14, v[36:39] offset:1024\n\t"
        "s_and_b32 s70, s51, 3\n\t"
        "v_mfma_f32_16x16x32_bf16 v[44:47], v[148:151], v[24:27], v[44:47]\n\t"
        "s_cselect_b32 s70, 0, 0x2000\n\t"
        "ds_read_b128 v[24:27], v10 offset:38912\n\t"
        "s_waitcnt lgkmcnt(7)\n\t"
        "v_mfma_f32_16x16x32_bf16 v[40:43], v[152:155], v[28:31], v[40:43]\n\t"
        "buffer_load_dwordx4 v5, %[rx], s49 offen offset:1024 lds\n\t"
        "v_mfma_f32_16x16x32_bf16 v[44:47], v[156:159], v[28:31], v[44:47]\n\t"
        "s_xor_b32 s50, s50, s70\n\t"
        "ds_read_b128 v[28:31], v11 offset:38912\n\t"
        "s_waitcnt lgkmcnt(5)\n\t"
        "v_mfma_f32_16x16x32_bf16 v[40:43], v[160:163], v[16:19], v[40:43]\n\t"
        "v_add_f32 v64, v64, v56\n\t"
        "v_mfma_f32_16x16x32_bf16 v[44:47], v[164:167], v[16:19], v[44:47]\n\t"
        "v_add_f32 v65, v65, v57\n\t"
        "ds_read_b128 v[16:19], v10 offset:40960\n\t"
        "s_waitcnt lgkmcnt(4)\n\t"
        "v_mfma_f32_16x16x32_bf16 v[40:43], v[168:171], v[20:23], v[40:43]\n\t"
        "buffer_load_dwordx4 v6, %[rx], s49 offen offset:2048 lds\n\t"
        "v_add_f32 v66, v66, v58\n\t"
        "v_mfma_f32_16x16x32_bf16 v[44:47], v[172:175], v[20:23], v[44:47]\n\t"
        "v_add_f32 v67, v67, v59\n\t"
        "ds_read_b128 v[20:23], v11 offset:40960\n\t"
        "s_waitcnt lgkmcnt(3)\n\t"
        "v_mfma_f32_16x16x32_bf16 v[40:43], v[176:179], v[24:27], v[40:43]\n\t"
        "v_add_f32 v68, v68, v60\n\t"
        "v_mfma_f32_16x16x32_bf16 v[44:47], v[180:183], v[24:27], v[44:47]\n\t"
        "v_add_f32 v69, v69, v61\n\t"
        "ds_read_b128 v[24:27], v10 offset:43008\n\t"
        "s_waitcnt lgkmcnt(3)\n\t"
        "v_mfma_f32_16x16x32_bf16 v[40:43], v[184:187], v[28:31], v[40:43]\n\t"
        "buffer_load_dwordx4 v7, %[rx], s49 offen offset:3072 lds\n\t"
        "v_add_f32 v70, v70, v62\n\t"
        "v_mfma_f32_16x16x32_bf16 v[44:47], v[188:191], v[28:31], v[44:47]\n\t"
        "v_add_f32 v71, v71, v63\n\t"
        "ds_read_b128 v[28:31], v11 offset:43008\n\t"
        "s_waitcnt lgkmcnt(3)\n\t"
        "v_mfma_f32_16x16x32_bf16 v[40:43], v[192:195], v[16:19], v[40:43]\n\t"
        "s_add_u32 s49, s49, 32768\n\t"
        "v_cvt_pk_bf16_f32 v72, v64, v65\n\t"
        "v_mfma_f32_16x16x32_bf16 v[44:47], v[196:199], v[16:19], v[44:47]\n\t"
        "v_cvt_pk_bf16_f32 v73, v68, v69\n\t"
        "s_add_u32 s54, s54, 1\n\t"
        "ds_read_b128 v[16:19], v10 offset:45056\n\t"
        "s_waitcnt lgkmcnt(3)\n\t"
        "v_mfma_f32_16x16x32_bf16 v[40:43], v[200:203], v[20:23], v[40:43]\n\t"
        "v_cvt_pk_bf16_f32 v74, v66, v67\n\t"
        "v_cvt_pk_bf16_f32 v75, v70, v71\n\t"
        "v_mfma_f32_16x16x32_bf16 v[44:47], v[204:207], v[20:23], v[44:47]\n\t"
        "v_mov_b32 v76, s54\n\t"
        "ds_read_b128 v[20:23], v11 offset:45056\n\t"
        "s_waitcnt lgkmcnt(3)\n\t"
        "v_mfma_f32_16x16x32_bf16 v[40:43], v[208:211], v[24:27], v[40:43]\n\t"
        "s_mov_b64 exec, s[56:57]\n\t"
        "v_swap_b32 v73, v74\n\t"
        "s_nop 1\n\t"
        "v_permlane16_swap_b32 v72, v74\n\t"
        "v_permlane16_swap_b32 v73, v75\n\t"
        "s_mov_b64 exec, -1\n\t"
        "buffer_store_dwordx4 v[72:75], v79, %[rs], s50 offen sc1\n\t"
        "v_mfma_f32_16x16x32_bf16 v[44:47], v[212:215], v[24:27], v[44:47]\n\t"
        "s_and_b32 s71, s54, 127\n\t"
        "s_cmp_le_u32 s71, 16\n\t"
        "s_cselect_b32 s72, 0, 3\n\t"
        "s_mul_i32 s71, s71, 128\n\t"
        "s_and_b32 s72, s72, s54\n\t"
        "s_cselect_b32 s71, 0x70000000, s71\n\t"
        "ds_read_b128 v[24:27], v10 offset:47104\n\t"
        "s_waitcnt lgkmcnt(3)\n\t"
        "v_mfma_f32_16x16x32_bf16 v[40:43], v[216:219], v[28:31], v[40:43]\n\t"
        "buffer_store_dwordx2 v[76:77], v78, %[rf], s71 offen sc1\n\t"
        "v_mfma_f32_16x16x32_bf16 v[44:47], v[220:223], v[28:31], v[44:47]\n\t"
        "ds_read_b128 v[28:31], v11 offset:47104\n\t"
        "s_waitcnt lgkmcnt(3)\n\t"
        "v_mfma_f32_16x16x32_bf16 v[40:43], v[224:227], v[16:19], v[40:43]\n\t"
        "v_mfma_f32_16x16x32_bf16 v[44:47], v[228:231], v[16:19], v[44:47]\n\t"
        "ds_read_b128 v[16:19], v12 offset:0\n\t"
        "s_waitcnt lgkmcnt(3)\n\t"
        "v_mfma_f32_16x16x32_bf16 v[40:43], v[232:235], v[20:23], v[40:43]\n\t"
        "v_mfma_f32_16x16x32_bf16 v[44:47], v[236:239], v[20:23], v[44:47]\n\t"
        "ds_read_b128 v[20:23], v13 offset:0\n\t"
        "s_waitcnt lgkmcnt(3)\n\t"
        "v_mfma_f32_16x16x32_bf16 v[40:43], v[240:243], v[24:27], v[40:43]\n\t"
        "v_mfma_f32_16x16x32_bf16 v[44:47], v[244:247], v[24:27], v[44:47]\n\t"
        "ds_read_b128 v[24:27], v12 offset:2048\n\t"
        "s_waitcnt lgkmcnt(3)\n\t"
        "v_mfma_f32_16x16x32_bf16 v[40:43], v[248:251], v[28:31], v[40:43]\n\t"
        "v_mfma_f32_16x16x32_bf16 v[44:47], v[252:255], v[28:31], v[44:47]\n\t"
        "s_waitcnt vmcnt(6)\n\t"
        "s_barrier\n\t"
        "ds_read_b128 v[28:31], v13 offset:2048\n\t"
        "s_waitcnt lgkmcnt(3)\n\t"
        "v_mfma_f32_16x16x32_bf16 v[48:51], v[128:131], v[16:19], 0\n\t"
        "s_add_u32 m0, s47, 32768\n\t"
        "v_mfma_f32_16x16x32_bf16 v[52:55], v[132:135], v[16:19], 0\n\t"
        "ds_read_b128 v[16:19], v12 offset:4096\n\t"
        "s_waitcnt lgkmcnt(3)\n\t"
        "v_mfma_f32_16x16x32_bf16 v[48:51], v[136:139], v[20:23], v[48:51]\n\t"
        "buffer_load_dwordx4 v4, %[rx], s49 offen offset:0 lds\n\t"
        "v_mfma_f32_16x16x32_bf16 v[52:55], v[140:143], v[20:23], v[52:55]\n\t"
        "ds_read_b128 v[20:23], v13 offset:4096\n\t"
        "s_waitcnt lgkmcnt(3)\n\t"
        "v_mfma_f32_16x16x32_bf16 v[48:51], v[144:147], v[24:27], v[48:51]\n\t"
        "v_mfma_f32_16x16x32_bf16 v[52:55], v[148:151], v[24:27], v[52:55]\n\t"
        "ds_read_b128 v[24:27], v12 offset:6144\n\t"
        "s_waitcnt lgkmcnt(3)\n\t"
        "v_mfma_f32_16x16x32_bf16 v[48:51], v[152:155], v[28:31], v[48:51]\n\t"
        "buffer_load_dwordx4 v5, %[rx], s49 offen offset:1024 lds\n\t"
        "v_mfma_f32_16x16x32_bf16 v[52:55], v[156:159], v[28:31], v[52:55]\n\t"
        "ds_read_b128 v[28:31], v13 offset:6144\n\t"
        "s_waitcnt lgkmcnt(3)\n\t"
        "v_mfma_f32_16x16x32_bf16 v[48:51], v[160:163], v[16:19], v[48:51]\n\t"
        "v_mfma_f32_16x16x32_bf16 v[52:55], v[164:167], v[16:19], v[52:55]\n\t"
        "ds_read_b128 v[16:19], v12 offset:8192\n\t"
        "s_waitcnt lgkmcnt(3)\n\t"
        "v_mfma_f32_16x16x32_bf16 v[48:51], v[168:171], v[20:23], v[48:51]\n\t"
        "buffer_load_dwordx4 v6, %[rx], s49 offen offset:2048 lds\n\t"
        "v_mfma_f32_16x16x32_bf16 v[52:55], v[172:175], v[20:23], v[52:55]\n\t"
        "ds_read_b128 v[20:23], v13 offset:8192\n\t"
        "s_waitcnt lgkmcnt(3)\n\t"
        "v_mfma_f32_16x16x32_bf16 v[48:51], v[176:179], v[24:27], v[48:51]\n\t"
        "v_mfma_f32_16x16x32_bf16 v[52:55], v[180:183], v[24:27], v[52:55]\n\t"
        "ds_read_b128 v[24:27], v12 offset:10240\n\t"
        "s_waitcnt lgkmcnt(3)\n\t"
        "v_mfma_f32_16x16x32_bf16 v[48:51], v[184:187], v[28:31], v[48:51]\n\t"
        "buffer_load_dwordx4 v7, %[rx], s49 offen offset:3072 lds\n\t"
        "v_mfma_f32_16x16x32_bf16 v[52:55], v[188:191], v[28:31], v[52:55]\n\t"
        "ds_read_b128 v[28:31], v13 offset:10240\n\t"
        "s_waitcnt lgkmcnt(3)\n\t"
        "v_mfma_f32_16x16x32_bf16 v[48:51], v[192:195], v[16:19], v[48:51]\n\t"
        "s_add_u32 s49, s49, 32768\n\t"
        "v_mfma_f32_16x16x32_bf16 v[52:55], v[196:199], v[16:19], v[52:55]\n\t"
        "ds_read_b128 v[16:19], v12 offset:12288\n\t"
        "s_waitcnt lgkmcnt(3)\n\t"
        "v_mfma_f32_16x16x32_bf16 v[48:51], v[200:203], v[20:23], v[48:51]\n\t"
        "v_mfma_f32_16x16x32_bf16 v[52:55], v[204:207], v[20:23], v[52:55]\n\t"
        "ds_read_b128 v[20:23], v13 offset:12288\n\t"
        "s_waitcnt lgkmcnt(3)\n\t"
        "v_mfma_f32_16x16x32_bf16 v[48:51], v[208:211], v[24:27], v[48:51]\n\t"
        "v_mfma_f32_16x16x32_bf16 v[52:55], v[212:215], v[24:27], v[52:55]\n\t"
        "ds_read_b128 v[24:27], v12 offset:14336\n\t"
        "s_waitcnt lgkmcnt(3)\n\t"
        "v_mfma_f32_16x16x32_bf16 v[48:51], v[216:219], v[28:31], v[48:51]\n\t"
        "v_mfma_f32_16x16x32_bf16 v[52:55], v[220:223], v[28:31], v[52:55]\n\t"
        "ds_read_b128 v[28:31], v13 offset:14336\n\t"
        "s_waitcnt lgkmcnt(3)\n\t"
        "v_mfma_f32_16x16x32_bf16 v[48:51], v[224:227], v[16:19], v[48:51]\n\t"
        "v_mfma_f32_16x16x32_bf16 v[52:55], v[228:231], v[16:19], v[52:55]\n\t"
        "ds_read_b128 v[16:19], v12 offset:32768\n\t"
        "s_waitcnt lgkmcnt(3)\n\t"
        "v_mfma_f32_16x16x32_bf16 v[48:51], v[232:235], v[20:23], v[48:51]\n\t"
        "v_mfma_f32_16x16x32_bf16 v[52:55], v[236:239], v[20:23], v[52:55]\n\t"
        "ds_read_b128 v[20:23], v13 offset:32768\n\t"
        "s_waitcnt lgkmcnt(3)\n\t"
        "v_mfma_f32_16x16x32_bf16 v[48:51], v[240:243], v[24:27], v[48:51]\n\t"
        "v_mfma_f32_16x16x32_bf16 v[52:55], v[244:247], v[24:27], v[52:55]\n\t"
        "ds_read_b128 v[24:27], v12 offset:34816\n\t"
        "s_waitcnt lgkmcnt(3)\n\t"
        "v_mfma_f32_16x16x32_bf16 v[48:51], v[248:251], v[28:31], v[48:51]\n\t"
        "v_mfma_f32_16x16x32_bf16 v[52:55], v[252:255], v[28:31], v[52:55]\n\t"
        "s_waitcnt vmcnt(4)\n\t"
        "s_barrier\n\t"
        "ds_read_b128 v[28:31], v13 offset:34816\n\t"
        "s_waitcnt lgkmcnt(3)\n\t"
        "v_mfma_f32_16x16x32_bf16 v[56:59], v[128:131], v[16:19], 0\n\t"
        "s_add_u32 m0, s47, 65536\n\t"
        "ds_read_b128 v[64:67], v14 offset:8192\n\t"
        "v_mfma_f32_16x16x32_bf16 v[60:63], v[132:135], v[16:19], 0\n\t"
        "ds_read_b128 v[68:71], v14 offset:9216\n\t"
        "ds_read_b128 v[16:19], v12 offset:36864\n\t"
        "s_waitcnt lgkmcnt(5)\n\t"
        "v_mfma_f32_16x16x32_bf16 v[56:59], v[136:139], v[20:23], v[56:59]\n\t"
        "buffer_load_dwordx4 v4, %[rx], s49 offen offset:0 lds\n\t"
        "ds_write_b128 v14, v[48:51] offset:0\n\t"
        "v_mfma_f32_16x16x32_bf16 v[60:63], v[140:143], v[20:23], v[60:63]\n\t"
        "s_add_u32 s50, s50, 0x80000\n\t"
        "s_add_u32 s51, s51, 1\n\t"
        "ds_read_b128 v[20:23], v13 offset:36864\n\t"
        "s_waitcnt lgkmcnt(6)\n\t"
        "v_mfma_f32_16x16x32_bf16 v[56:59], v[144:147], v[24:27], v[56:59]\n\t"
        "ds_write_b128 v14, v[52:55] offset:1024\n\t"
        "s_and_b32 s70, s51, 3\n\t"
        "v_mfma_f32_16x16x32_bf16 v[60:63], v[148:151], v[24:27], v[60:63]\n\t"
        "s_cselect_b32 s70, 0, 0x2000\n\t"
        "ds_read_b128 v[24:27], v12 offset:38912\n\t"
        "s_waitcnt lgkmcnt(7)\n\t"
        "v_mfma_f32_16x16x32_bf16 v[56:59], v[152:155], v[28:31], v[56:59]\n\t"
        "buffer_load_dwordx4 v5, %[rx], s49 offen offset:1024 lds\n\t"
        "v_mfma_f32_16x16x32_bf16 v[60:63], v[156:159], v[28:31], v[60:63]\n\t"
        "s_xor_b32 s50, s50, s70\n\t"
        "ds_read_b128 v[28:31], v13 offset:38912\n\t"
        "s_waitcnt lgkmcnt(5)\n\t"
        "v_mfma_f32_16x16x32_bf16 v[56:59], v[160:163], v[16:19], v[56:59]\n\t"
        "v_add_f32 v64, v64, v40\n\t"
        "v_mfma_f32_16x16x32_bf16 v[60:63], v[164:167], v[16:19], v[60:63]\n\t"
        "v_add_f32 v65, v65, v41\n\t"
        "ds_read_b128 v[16:19], v12 offset:40960\n\t"
        "s_waitcnt lgkmcnt(4)\n\t"
        "v_mfma_f32_16x16x32_bf16 v[56:59], v[168:171], v[20:23], v[56:59]\n\t"
        "buffer_load_dwordx4 v6, %[rx], s49 offen offset:2048 lds\n\t"
        "v_add_f32 v66, v66, v42\n\t"
        "v_mfma_f32_16x16x32_bf16 v[60:63], v[172:175], v[20:23], v[60:63]\n\t"
        "v_add_f32 v67, v67, v43\n\t"
        "ds_read_b128 v[20:23], v13 offset:40960\n\t"
        "s_waitcnt lgkmcnt(3)\n\t"
        "v_mfma_f32_16x16x32_bf16 v[56:59], v[176:179], v[24:27], v[56:59]\n\t"
        "v_add_f32 v68, v68, v44\n\t"
        "v_mfma_f32_16x16x32_bf16 v[60:63], v[180:183], v[24:27], v[60:63]\n\t"
        "v_add_f32 v69, v69, v45\n\t"
        "ds_read_b128 v[24:27], v12 offset:43008\n\t"
        "s_waitcnt lgkmcnt(3)\n\t"
        "v_mfma_f32_16x16x32_bf16 v[56:59], v[184:187], v[28:31], v[56:59]\n\t"
        "buffer_load_dwordx4 v7, %[rx], s49 offen offset:3072 lds\n\t"
        "v_add_f32 v70, v70, v46\n\t"
        "v_mfma_f32_16x16x32_bf16 v[60:63], v[188:191], v[28:31], v[60:63]\n\t"
        "v_add_f32 v71, v71, v47\n\t"
        "ds_read_b128 v[28:31], v13 offset:43008\n\t"
        "s_waitcnt lgkmcnt(3)\n\t"
        "v_mfma_f32_16x16x32_bf16 v[56:59], v[192:195], v[16:19], v[56:59]\n\t"
        "s_add_u32 s49, s49, 32768\n\t"
        "v_cvt_pk_bf16_f32 v72, v64, v65\n\t"
        "v_mfma_f32_16x16x32_bf16 v[60:63], v[196:199], v[16:19], v[60:63]\n\t"
        "v_cvt_pk_bf16_f32 v73, v68, v69\n\t"
        "s_add_u32 s54, s54, 1\n\t"
        "ds_read_b128 v[16:19], v12 offset:45056\n\t"
        "s_waitcnt lgkmcnt(3)\n\t"
        "v_mfma_f32_16x16x32_bf16 v[56:59], v[200:203], v[20:23], v[56:59]\n\t"
        "v_cvt_pk_bf16_f32 v74, v66, v67\n\t"
        "v_cvt_pk_bf16_f32 v75, v70, v71\n\t"
        "v_mfma_f32_16x16x32_bf16 v[60:63], v[204:207], v[20:23], v[60:63]\n\t"
        "v_mov_b32 v76, s54\n\t"
        "ds_read_b128 v[20:23], v13 offset:45056\n\t"
        "s_waitcnt lgkmcnt(3)\n\t"
        "v_mfma_f32_16x16x32_bf16 v[56:59], v[208:211], v[24:27], v[56:59]\n\t"
        "s_mov_b64 exec, s[56:57]\n\t"
        "v_swap_b32 v73, v74\n\t"
        "s_nop 1\n\t"
        "v_permlane16_swap_b32 v72, v74\n\t"
        "v_permlane16_swap_b32 v73, v75\n\t"
        "s_mov_b64 exec, -1\n\t"
        "buffer_store_dwordx4 v[72:75], v79, %[rs], s50 offen sc1\n\t"
        "v_mfma_f32_16x16x32_bf16 v[60:63], v[212:215], v[24:27], v[60:63]\n\t"
        "s_and_b32 s71, s54, 127\n\t"
        "s_cmp_le_u32 s71, 16\n\t"
        "s_cselect_b32 s72, 0, 3\n\t"
        "s_mul_i32 s71, s71, 128\n\t"
        "s_and_b32 s72, s72, s54\n\t"
        "s_cselect_b32 s71, 0x70000000, s71\n\t"
        "ds_read_b128 v[24:27], v12 offset:47104\n\t"
        "s_waitcnt lgkmcnt(3)\n\t"
        "v_mfma_f32_16x16x32_bf16 v[56:59], v[216:219], v[28:31], v[56:59]\n\t"
        "buffer_store_dwordx2 v[76:77], v78, %[rf], s71 offen sc1\n\t"
        "v_mfma_f32_16x16x32_bf16 v[60:63], v[220:223], v[28:31], v[60:63]\n\t"
        "ds_read_b128 v[28:31], v13 offset:47104\n\t"
        "s_waitcnt lgkmcnt(3)\n\t"
        "v_mfma_f32_16x16x32_bf16 v[56:59], v[224:227], v[16:19], v[56:59]\n\t"
        "v_mfma_f32_16x16x32_bf16 v[60:63], v[228:231], v[16:19], v[60:63]\n\t"
        "ds_read_b128 v[16:19], v10 offset:0\n\t"
        "s_waitcnt lgkmcnt(3)\n\t"
        "v_mfma_f32_16x16x32_bf16 v[56:59], v[232:235], v[20:23], v[56:59]\n\t"
        "v_mfma_f32_16x16x32_bf16 v[60:63], v[236:239], v[20:23], v[60:63]\n\t"
        "ds_read_b128 v[20:23], v11 offset:0\n\t"
        "s_waitcnt lgkmcnt(3)\n\t"
        "v_mfma_f32_16x16x32_bf16 v[56:59], v[240:243], v[24:27], v[56:59]\n\t"
        "v_mfma_f32_16x16x32_bf16 v[60:63], v[244:247], v[24:27], v[60:63]\n\t"
        "ds_read_b128 v[24:27], v10 offset:2048\n\t"
        "s_waitcnt lgkmcnt(3)\n\t"
        "v_mfma_f32_16x16x32_bf16 v[56:59], v[248:251], v[28:31], v[56:59]\n\t"
        "v_mfma_f32_16x16x32_bf16 v[60:63], v[252:255], v[28:31], v[60:63]\n\t"
        "s_sub_u32 s52, s52, 1\n\t"
        "s_waitcnt vmcnt(6)\n\t"
        "s_barrier\n\t"
        "s_cmp_lg_u32 s52, 0\n\t"
        "s_cbranch_scc1 .Lx_loop1_%=\n\t"
        "s_nop 7\n\t"
        "s_barrier\n\t"
        "ds_read_b128 v[64:67], v14 offset:8192\n\t"
        "ds_read_b128 v[68:71], v14 offset:9216\n\t"
        "s_add_u32 s50, s50, 0x80000\n\t"
        "s_add_u32 s51, s51, 1\n\t"
        "s_and_b32 s70, s51, 3\n\t"
        "s_cselect_b32 s70, 0, 0x2000\n\t"
        "s_xor_b32 s50, s50, s70\n\t"
        "s_waitcnt lgkmcnt(0)\n\t"
        "v_add_f32 v64, v64, v56\n\t"
        "v_add_f32 v65, v65, v57\n\t"
        "v_add_f32 v66, v66, v58\n\t"
        "v_add_f32 v67, v67, v59\n\t"
        "v_add_f32 v68, v68, v60\n\t"
        "v_add_f32 v69, v69, v61\n\t"
        "v_add_f32 v70, v70, v62\n\t"
        "v_add_f32 v71, v71, v63\n\t"
        "v_cvt_pk_bf16_f32 v72, v64, v65\n\t"
        "v_cvt_pk_bf16_f32 v73, v68, v69\n\t"
        "v_cvt_pk_bf16_f32 v74, v66, v67\n\t"
        "v_cvt_pk_bf16_f32 v75, v70, v71\n\t"
        "s_mov_b64 exec, s[56:57]\n\t"
        "v_swap_b32 v73, v74\n\t"
        "s_nop 1\n\t"
        "v_permlane16_swap_b32 v72, v74\n\t"
        "v_permlane16_swap_b32 v73, v75\n\t"
        "s_mov_b64 exec, -1\n\t"
        "buffer_store_dwordx4 v[72:75], v79, %[rs], s50 offen sc1\n\t"
        "s_waitcnt vmcnt(0)\n\t"
        "s_barrier\n\t"
        "s_and_b32 s54, s54, 0xffffff80\n\t"
        "s_add_u32 s54, s54, 63\n\t"
        "s_add_u32 s54, s54, 1\n\t"
        "v_mov_b32 v76, s54\n\t"
        "s_and_b32 s71, s54, 127\n\t"
        "s_lshl_b32 s71, s71, 7\n\t"
        "buffer_store_dwordx2 v[76:77], v78, %[rf], s71 offen sc1\n\t"
        "s_branch .Lx_end_%=\n\t"
        ".Lcode_x1:\n\t"
        ".Lx_end_%=:\n\t"
        :: [rs] "s"(RSRC(arena(a.slots), ARENA_BYTES)), [rw] "s"(RSRC((kind == 0 ? a.Wk : kind == 1 ? a.Wq : a.Wv) + hcol(h), (DM * HD - hcol(h)) * 2)), [rx] "s"(RSRC(a.x - 2048, S_LEN * DM * 2 + 4096)),
           [rf] "s"(PROG(idx)), [bt] "s"(a.boot), [w] "s"(w), [kn] "s"(kind), FTIN [co] "s"((kind == 1 ? Q_OFF : KV_OFF) + h * 16384 + (cls_s(h) ^ (kind >> 1)) * 8192) XSTIN
        : "memory", "vcc", "scc", ALLV, ALLS);
}

constexpr int JUNK = 160256;
template <int R> __device__ __forceinline__ void warm(const int share, const int l) {
    unsigned long long pc; int o0, o1;
#define CODE(r) asm volatile("s_getpc_b64 %0\n1:\n\ts_mov_b32 %1, .Lcode_" r "0-1b\n\ts_mov_b32 %2, .Lcode_" r "1-1b" : "=s"(pc), "=s"(o0), "=s"(o1))
    if (R == 0) CODE("s"); else if (R == 1) CODE("v"); else if (R == 2) CODE("r"); else CODE("x");
    for (int o = o0 + share * 8192; o < o1; o += 64 * 8192) touch((const char*)pc + min(o + l * 128, o1 - 4), JUNK);
}
constexpr int MEGA_LDS = 163840;
__global__ __launch_bounds__(512, 2) void mega_kernel(Args) {
    extern __shared__ __attribute__((aligned(256))) char chsm[];
    const int b = BIDX, xi = b & 7, sl = b >> 3, x3 = xi & 3;
    int wv = __builtin_amdgcn_readfirstlane(TIDX >> 6);
    asm volatile("" : "+s"(wv));
    const unsigned tstart = DGT;
    int role, idx;
    if (PL == 0 && SE) {
        const int e = (xi & 1) == SE - 1, g = xi >> 1;
        if (sl < 8) { role = e ? 0 : 3; idx = (e ? 0 : 32) + 8 * g + sl; } else if (sl < 16) { role = 1; idx = 16 * x3 + 2 * (sl - 8) + (xi >> 2); } else if (sl < 24) { role = 2; idx = 8 * xi + sl - 16; }
        else { role = 3; idx = (e ? 0 : 64) + 8 * g + sl - 24; }
    } else if (PL == 0) {
        if (sl < 4) { role = 0; idx = 4 * xi + sl; } else if (sl < 12) { role = 1; idx = VNEAR ? 16 * x3 + 2 * (sl - 4) + (xi >> 2) : 8 * (xi ^ 4) + sl - 4; } else if (sl < 20) { role = 2; idx = 8 * xi + sl - 12; }
        else { role = 3; idx = sl < 28 ? ((sl - 20) >> 2) * 32 + 4 * xi + (sl & 3) : VNEAR ? 64 + 8 * x3 + 4 * (xi >> 2) + (sl & 3) : 64 + 4 * (xi ^ 4) + (sl & 3); }
    } else if (PL == 1) {
        if (xi < 4) { if (sl < 8) { role = 0; idx = 8 * xi + sl; } else if (sl < 16) { role = 3; idx = 8 * xi + sl - 8; } else { role = 2; idx = 16 * xi + sl - 16; } }
        else { if (sl < 16) { role = 1; idx = 16 * x3 + sl; } else { role = 3; idx = (sl < 24 ? 64 : 32) + 8 * x3 + (sl & 7); } }
    } else {
        role = 3; idx = 0;
    }
    if (role == 0) for (int k = 0; k < SD; k += 16) __builtin_amdgcn_s_sleep(16);
    if (role == 3 && idx < 64) for (int k = 0; k < (idx < 32 ? KD : QD); k += 16) __builtin_amdgcn_s_sleep(16);
    if (role == 0) score_wg(wv, idx, REP_S(xi));
    if (role == 1) value_wg(wv, idx >> 1, idx & 1, REP_V(xi));
    if (role == 2) {
        const Args a = get_args();
        const int t = tid_now(wv), l = t & 63, wu = __builtin_amdgcn_readfirstlane(t >> 6);
        const rsrc_t rs = RSRC(arena(a.slots), ARENA_BYTES);
        const unsigned long long tag = boot_tag(a.boot);
#pragma unroll
        for (int e = 0; e < 4; ++e) {
            const int u = wu + 8 * e, n = 3 * idx + u - 22;
            st_sc1s(rs, l * 16, u < 16 ? (64 + idx) * 16384 + u * 1024 : u < 21 ? (5 * idx + u - 16) * 1024 : u == 21 ? (idx < 32 ? 21 * 16384 + idx * 1024 : 0x7f000000) : u < 25 && n < 176 ? MB2 + (n < 48 ? n : n + 144) * 1024 : 0x7f000000, v4u{SENT, SENT, SENT, SENT});
        }
        const int cal = CAL_OFF + (idx >> 3) * 16384 + (idx & 7) * 1024;
        if (wu == 0) {
            if (l < 32) __hip_atomic_store((gu32*)(a.sync + idx * 32 + l), 0u, __ATOMIC_RELAXED, __HIP_MEMORY_SCOPE_AGENT);
            if (idx < 2) st_sc1s(rs, l * 16, 20 * 16384 + idx * 8192, v4u{0u, 0u, 0u, 0u});
            __builtin_amdgcn_raw_buffer_store_b32(0u, rs, (l & 3) * 256 + ((l >> 2) & 1) * 8192, cal, AUX_SC1);
        }
        K8_WV(0); K8_BAR;
        volatile unsigned* mb = (volatile unsigned*)(chsm + 160000);
        if (wu) {
            if (wu == 1) touch((const char*)a.x + (idx * 64 + l) * 128, JUNK);
            else if (wu == 2) touch_sc1(arena(a.slots) + (size_t)min(l, ARENA_BYTES / MB2 - 1) * MB2 + MB2 - 256, JUNK);
            else if (wu == 3) { touch((const char*)a.mem0 + l * 128, JUNK); touch((const char*)a.mem0 + 8192 + l * 128, JUNK); }
            PREFETCH
            CODEWARM
            if (!EF) __builtin_amdgcn_s_sleep(XSL);
            touch((const char*)a.x + (4096 + idx * 448 + t - 64) * 128, JUNK);
            if (EF) touch((const char*)(t & 1 ? a.wr : a.wi) + ((idx * 512 + t) >> 1) * 128, JUNK);
            if (OT) {
                __builtin_amdgcn_s_sleep(OT);
                const int n = idx * 512 + t;
                touch((const char*)a.out + (n >> 4) * 4096 + (n & 15) * 128, JUNK);
                if (wu == 1) touch((const char*)a.out + ((n - 64) >> 4) * 4096 + (n & 15) * 128, JUNK);
            }
            if (IT && !EF) {
                for (int k = 0; k < IT; k += 16) __builtin_amdgcn_s_sleep(16);
                const int n = idx * 512 + t;
                touch((const char*)a.bx + n * 128, JUNK); touch((const char*)(n & 1 ? a.wr : a.wi) + (n >> 1) * 128, JUNK);
                if (wu == 1) { touch((const char*)a.bx + (n - 64) * 128, JUNK); touch((const char*)(n & 1 ? a.wr : a.wi) + ((n - 64) >> 1) * 128, JUNK); }
            }
        } else {
            unsigned tm[2] = {~0u, ~0u};
            const char* pb = arena(a.slots) + cal;
#pragma unroll
            for (int k = 0; k < 4; ++k) {
                unsigned long long c0, c1; unsigned d;
                asm volatile("s_memtime %0\n\ts_waitcnt lgkmcnt(0)\n\ts_load_dword %2, %3, %4\n\ts_waitcnt lgkmcnt(0)\n\ts_memtime %1\n\ts_waitcnt lgkmcnt(0)"
                             : "=&s"(c0), "=&s"(c1), "=&s"(d) : "s"(pb), "i"((k >> 1) * 256 + (k & 1) * 8192) : "memory");
                tm[k & 1] = min(tm[k & 1], (unsigned)(c1 - c0));
            }
            if (l == 0) __hip_atomic_store((gu64*)(a.boot + idx), (tag << 1) | ((tm[0] > tm[1] ? 1u : 0u) ^ (unsigned)cls_r(idx)), __ATOMIC_RELAXED, __HIP_MEMORY_SCOPE_AGENT);
        }
        if (EF) {
            for (int k = 1; k < EF; k += 16) __builtin_amdgcn_s_sleep(16);
            fft_rows_fwd(a.bx, RSRC(a.F, 256 * FP * 64), idx, chsm, tid_now(wv));
        } else if (wv == 0) __builtin_amdgcn_s_sleep(64);
        if (wv == 0) { *mb = ((unsigned)boot_wait(a.boot, a.sync + 40 * 32) & 128u) << 6; K8_WL(0); }
        if (EF) wg_publish(wv, a.sync, SY_DONE, 128u); else K8_BAR;
        const unsigned t0 = __builtin_amdgcn_readfirstlane(*mb);
        if (wv == 4) return;
        if (wv == 0) { ((volatile unsigned*)(chsm + 114688))[lane_now() & 15] = 0u; K8_WL(0); }
        K8_BAR;
        column_wg(wv, idx, t0, tstart);
    }
    WST(0)
    xw_stream(wv, idx);
    WST(3)
    {
        const Args a = get_args();
        cpx* F = (cpx*)a.F;
        const rsrc_t rsF = RSRC(F, 256 * FP * 64), rsW = RSRC(a.Wt, 513 * WP * 4);
        auto stagger = [&](const int n) { for (int k = 0; k < n; k += 128) __builtin_amdgcn_s_sleep(2); };
        if (!EF) {
        stagger(idx * FD1);
        if (FAB & 1) {} else if (idx < 64) fft_rows_fwd(a.bx, rsF, idx, chsm, tid_now(wv));
        else {
            filt_transpose(a.wr, a.wi, rsW, idx - 64, chsm, tid_now(wv));
            __syncthreads();
            filt_transpose(a.wr, a.wi, rsW, idx - 32, chsm, tid_now(wv));
        }
        wg_publish(wv, a.sync, SY_DONE, 96u);
        }
        if (EF && idx < 64) { filt_transpose(a.wr, a.wi, rsW, idx, chsm, tid_now(wv)); wg_publish(wv, a.sync, SY_DONE, 128u); }
        WST(4)
        wg_wait(wv, a.sync + (SY_DONE + xi) * 32, a.sync + 40 * 32, 6000u);
        stagger(idx * FD2);
        if (!(FAB & 2) && wv < FCW && FCW * idx + wv < 513) fft_col(rsF, rsW, FCW * idx + wv, chsm + wv * PB2 * 8, lane_now());
        __syncthreads();
        if (!(FAB & 2) && wv < FCW && FCW * idx + wv < 514) fft_col_out(RSRC(OOP ? a.G : a.F, 256 * FP * 64), FCW * idx + wv, chsm + (wv & ~1) * PB2 * 8, lane_now());
        wg_publish(wv, a.sync, SY_DONE + 8, 96u);
        WST(5)
        if (idx < 64) {
            wg_wait(wv, a.sync + (SY_DONE + 8 + xi) * 32, a.sync + 40 * 32, 6001u);
            stagger(idx * FD3);
            if (!(FAB & 4)) fft_rows_inv(RSRC(OOP ? a.G : a.F, 256 * FP * 64), a.out, idx, chsm, tid_now(wv));
        }
        WST(6)
    }
}

#if defined(__HIP_DEVICE_COMPILE__)
#pragma clang attribute pop
#endif
extern "C" void kernel_launch(void* const* d_in, const int* in_sizes, int n_in,
                              void* d_out, int out_size, void* d_ws, size_t ws_size,
                              hipStream_t stream) {
    char* ws = (char*)d_ws;
    size_t off = 0;
    auto alloc = [&](size_t bytes) { void* p = ws + off; off += (bytes + 255) & ~(size_t)255; return p; };
    Args a;
    a.sync = (unsigned*)alloc(8192);
    a.boot = (unsigned long long*)alloc((256 + 1040 * 96) * 8);
    a.F = alloc((size_t)256 * FP * 8 * sizeof(cpx));
    a.G = alloc((size_t)256 * FP * 8 * sizeof(cpx));
    a.Wt = (unsigned*)alloc((size_t)513 * WP * 4);
    a.slots = (char*)alloc((size_t)ARENA_BYTES + (2u << 20));
    a.stamps = (unsigned*)(ws + ((size_t)STOFF << 20));
    a.x = (const u16*)d_in[0]; a.mem0 = (const u16*)d_in[1]; a.Wq = (const u16*)d_in[2]; a.Wk = (const u16*)d_in[3]; a.Wv = (const u16*)d_in[4]; a.Wo = (const u16*)d_in[5];
    a.bx = (const u16*)d_in[7]; a.wr = (const u16*)d_in[8]; a.wi = (const u16*)d_in[9]; a.out = (u16*)d_out;
    (void)hipFuncSetAttribute((const void*)mega_kernel, hipFuncAttributeMaxDynamicSharedMemorySize, MEGA_LDS);
    mega_kernel<<<256, 512, MEGA_LDS, stream>>>(a);
}
```
